```python
import torch

def _softmax(x, axis=-1):
    return torch.softmax(x, dim=axis)
def _gelu(x):
    return torch.nn.functional.gelu(x, approximate="tanh")
def _silu(x):
    return torch.nn.functional.silu(x)
def _sigmoid(x):
    return torch.sigmoid(x)
def _layernorm(x, axis=-1, eps=1e-5):
    m = x.mean(dim=axis, keepdim=True)
    v = x.var(dim=axis, keepdim=True, unbiased=False)
    return (x - m) / torch.sqrt(v + eps)
def _scatter_add(tgt, idx, src, axis=0):
    return tgt.clone().index_add_(axis, idx, src)
def _segment_sum(src, seg, n):
    out = torch.zeros((n,) + src.shape[1:], dtype=src.dtype)
    return out.index_add_(0, seg, src)
def _pad(x, pw):
    flat = tuple(v for pair in reversed(pw) for v in pair)
    return torch.nn.functional.pad(x, flat)

kernel_name = 'interleave_layers__linear_kernel_hedgehog__sliding_local_dilated__prompt'

def setup_inputs():

    _w = (lambda a: (lambda t: t.to(torch.bfloat16) if t.is_floating_point() else t)(torch.as_tensor(a)))
    def _A_setup():
        B, S, D, H = 1, 2048, 1024, 32
        Dh = D // H
        return {
            "x": _w((lambda *s: torch.randn(*s, dtype=torch.bfloat16) * 0.05)(B, S, D)),
            "wqkv": _w((lambda *s: torch.randn(*s, dtype=torch.bfloat16) * 0.2)(D, 3 * D)),
            "w_phi": _w((lambda *s: torch.randn(*s, dtype=torch.bfloat16) * 0.2)(H, Dh, Dh) * (Dh ** -0.5)),
            "wo": _w((lambda *s: torch.randn(*s, dtype=torch.bfloat16) * 0.05)(D, D)),
        }
    _A_in = _A_setup()

    def _B_setup():
        B, S, D = 1, 2048, 1024
        return {
            "x": _w((lambda *s: torch.randn(*s, dtype=torch.bfloat16) * 0.05)(B, S, D)),
            "wqkv": _w((lambda *s: torch.randn(*s, dtype=torch.bfloat16) * 0.1)(D, 3 * D)),
            "wo": _w((lambda *s: torch.randn(*s, dtype=torch.bfloat16) * 0.05)(D, D)),
        }
    _B_in = _B_setup()

    return {
        "A_x": _A_in["x"],
        "A_wqkv": _A_in["wqkv"],
        "A_w_phi": _A_in["w_phi"],
        "A_wo": _A_in["wo"],
        "B_x": _B_in["x"],
        "B_wqkv": _B_in["wqkv"],
        "B_wo": _B_in["wo"],
    }

def reference(A_x, A_wqkv, A_w_phi, A_wo, B_x, B_wqkv, B_wo):
    def _A_ref(x, wqkv, w_phi, wo):
        B, S, D = x.shape
        H = 32; Dh = D // H
        qkv = (lambda x, *d: x.permute(*d))((x @ wqkv).reshape(B, S, 3, H, Dh), 2, 0, 3, 1, 4)
        q, k, v = qkv[0], qkv[1], qkv[2]
        pq = torch.einsum("bhsd,hdf->bhsf", q, w_phi)
        pk = torch.einsum("bhsd,hdf->bhsf", k, w_phi)
        phi_q = torch.concatenate([_softmax(pq, axis=-1), _softmax(-pq, axis=-1)], axis=-1)
        phi_k = torch.concatenate([_softmax(pk, axis=-1), _softmax(-pk, axis=-1)], axis=-1)
        kv = torch.einsum("bhsf,bhsd->bhfd", phi_k, v)
        z  = torch.einsum("bhsf->bhf", phi_k)
        num = torch.einsum("bhqf,bhfd->bhqd", phi_q, kv)
        den = torch.einsum("bhqf,bhf->bhq", phi_q, z)[..., None] + 1e-6
        o = num / den
        return (lambda x, *d: x.permute(*d))(o, 0, 2, 1, 3).reshape(B, S, D) @ wo

    def _B_ref(x, wqkv, wo):
        B, S, D = x.shape
        H = 32; Dh = D // H
        W, dil = 8, 2
        qkv = (lambda x, *d: x.permute(*d))((x @ wqkv).reshape(B, S, 3, H, Dh), 2, 0, 3, 1, 4)
        q, k, v = qkv[0], qkv[1], qkv[2]
        scale = Dh ** -0.5
        pos = torch.arange(S)
        off = torch.arange(W) * dil
        idx = pos[:, None] - off[None, :]
        mask = idx >= 0
        idx_c = torch.where(mask, idx, 0).reshape(-1)
        kg = k[:, :, idx_c, :].reshape(B, H, S, W, Dh)
        vg = v[:, :, idx_c, :].reshape(B, H, S, W, Dh)
        logits = torch.einsum("bhsd,bhswd->bhsw", q, kg) * scale
        logits = torch.where(mask[None, None, :, :], logits, -1e9)
        attn = _softmax(logits, axis=-1)
        o = torch.einsum("bhsw,bhswd->bhsd", attn, vg)
        return (lambda x, *d: x.permute(*d))(o, 0, 2, 1, 3).reshape(B, S, D) @ wo

    x = A_x
    x = x + _A_ref(_layernorm(x, axis=-1), A_wqkv, A_w_phi, A_wo)
    x = x + _B_ref(_layernorm(x, axis=-1), B_wqkv, B_wo)
    x = x + _A_ref(_layernorm(x, axis=-1), A_wqkv, A_w_phi, A_wo)
    x = x + _B_ref(_layernorm(x, axis=-1), B_wqkv, B_wo)
    return x
```

```cpp
#include <hip/hip_runtime.h>
#include <stdint.h>
#include <type_traits>
#define SL 2
constexpr int LOOKAHEAD = 2;
constexpr int DRAIN_T = 5;
constexpr int NVAL = 4;
constexpr int PA1 = 6, PA2 = 6, PB1 = 4, PB2 = 4;

typedef unsigned short u16;
typedef unsigned long long u64;
typedef __attribute__((ext_vector_type(8))) short bf16x8;
typedef __attribute__((ext_vector_type(4))) short s16x4;
typedef __attribute__((ext_vector_type(4))) float f32x4;
typedef __attribute__((ext_vector_type(2))) float f32x2;
typedef __attribute__((ext_vector_type(4))) unsigned u32x4;
typedef __attribute__((ext_vector_type(2))) unsigned u32x2;
typedef __attribute__((ext_vector_type(2))) __bf16 bf16x2_t;
#define LAS __attribute__((address_space(3)))
typedef LAS s16x4* lds_s16x4_ptr;

constexpr unsigned NREP = 8, REP_STRIDE = 14 * 2048;
enum { FL_X = 0, FL_CS = 4, FL_CLR = 5, FL_CLC = 9, FL_PK = 13 };
__device__ __forceinline__ unsigned fl_line(int set, int a) { return (unsigned)(set * 16 + a) * 128; }
constexpr unsigned WS_EPOCH = 512u << 10;
constexpr unsigned WS_CS = 1u << 20;
constexpr unsigned WS_KVP = 4u << 20;
constexpr unsigned KVP_J = 512u << 10, KVP_G = 32u << 10, KVP_L = 16u << 10, KVP_HALO = 12u << 10;
constexpr unsigned SENT = 0xffffffffu;
constexpr unsigned WS_G = 16u << 20, G_STRIDE = 2u << 20;
__device__ __forceinline__ unsigned xb_off(int g, int l) { return WS_G + (unsigned)g * G_STRIDE + (unsigned)l * (256u << 10); }
__device__ __forceinline__ unsigned ob_off(int g, int j, int l) { return WS_G + (unsigned)g * G_STRIDE + (3u << 18) + (unsigned)j * (64u << 10) + (unsigned)l * (16u << 10); }
__device__ __forceinline__ unsigned st_off(int g, int l) { return WS_G + (unsigned)g * G_STRIDE + (7u << 18) + (unsigned)l * (16u << 10); }
constexpr unsigned WS_WP = 48u << 20;
constexpr unsigned WS_SPAN = 64u << 20;

constexpr unsigned L_R1 = 0;
constexpr unsigned L_R2 = 98304;
constexpr unsigned L_MISC = 153600;
constexpr unsigned L_MEAN = L_MISC;
constexpr unsigned L_RSTD = L_MISC + 576;
constexpr unsigned L_CS = L_MISC + 1152;
constexpr unsigned L_WPHI = L_MISC + 2688;
constexpr unsigned L_SINK = L_MISC + 6912;
constexpr unsigned L_CNT = L_MISC + 6784;
constexpr unsigned L_TOTAL = 163840;

#define JROT(j) ((j) & ~3)
#define JOBC(j, w) (((j) & ~3) + (((j) + (w) + 1) & 3))

struct Params {
  const u16* x;
  const u16* wqkvA;
  const u16* wphi;
  const u16* woA;
  const u16* wqkvB;
  const u16* woB;
  u16* out;
  char* ws;
};

#define SCHED __builtin_amdgcn_sched_barrier(0)
#define BAR __builtin_amdgcn_s_barrier()
#define SGB(m, n) __builtin_amdgcn_sched_group_barrier(m, n, 0)
#define WAITV(n) asm volatile("s_waitcnt vmcnt(%0)" ::"n"(n) : "memory")
#define WAITL0 asm volatile("s_waitcnt lgkmcnt(0)" ::: "memory")

#define STAMP5(i) do {} while (0)
#define STAMP(i) do {} while (0)
#define STAMPP(i) do {} while (0)
#define STAMPC(i) do {} while (0)
#define STAMP_INIT do {} while (0)
#define STAMP_DUMP do {} while (0)

__device__ __forceinline__ unsigned pk2(float a, float b) {
  f32x2 v = {a, b};
  return __builtin_bit_cast(unsigned, __builtin_convertvector(v, bf16x2_t));
}
__device__ __forceinline__ float lo16(unsigned u) { return __uint_as_float(u << 16); }
__device__ __forceinline__ float hi16(unsigned u) { return __uint_as_float(u & 0xffff0000u); }

typedef __amdgpu_buffer_rsrc_t rsrc_t;
__device__ __forceinline__ rsrc_t mk_rsrc(const void* p, unsigned bytes) { return __builtin_amdgcn_make_buffer_rsrc((void*)p, 0, bytes, 0x00020000); }
#define UNI(x) __builtin_amdgcn_readfirstlane(x)
#define DMA_HEAD "s_mov_b32 m0, %4\n\ts_mov_b32 %0, %3\n\ts_nop 2\n\tbuffer_load_dwordx4 %1, %2, %0 offen sc1 lds\n\t"
#define DMA_NEXT "s_add_u32 m0, m0, %6\n\ts_add_u32 %0, %0, %5\n\tbuffer_load_dwordx4 %1, %2, %0 offen sc1 lds\n\t"
#define DMA_OPS : "=&s"(t) : "v"(voff), "s"(r), "s"(UNI(soff)), "s"(UNI(lds)), "s"(UNI(sstep)), "i"(LSTEP) : "memory", "scc", "m0"
template <int LSTEP> __device__ __forceinline__ void dma4(rsrc_t r, unsigned voff, unsigned soff, unsigned sstep, unsigned lds) {
  unsigned t;
  asm volatile(DMA_HEAD DMA_NEXT DMA_NEXT DMA_NEXT DMA_OPS);
}
template <int LSTEP> __device__ __forceinline__ void dma3(rsrc_t r, unsigned voff, unsigned soff, unsigned sstep, unsigned lds) {
  unsigned t;
  asm volatile(DMA_HEAD DMA_NEXT DMA_NEXT DMA_OPS);
}
__device__ __forceinline__ void dma1(rsrc_t r, unsigned voff, unsigned soff, unsigned lds) {
  asm volatile("s_mov_b32 m0, %3\n\ts_nop 3\n\tbuffer_load_dwordx4 %0, %1, %2 offen sc1 lds" ::"v"(voff), "s"(r), "s"(UNI(soff)), "s"(UNI(lds)) : "memory", "m0");
}
__device__ __forceinline__ void touch(rsrc_t r, unsigned voff, unsigned lds) {
  asm volatile("s_mov_b32 m0, %2\n\ts_nop 3\n\tbuffer_load_dword %0, %1, 0 offen lds" ::"v"(voff), "s"(r), "s"(UNI(lds)) : "memory", "m0");
}
__device__ __forceinline__ void ld8_asm(u32x2& d, rsrc_t r, unsigned voff) {
  asm volatile("s_nop 4\n\tbuffer_load_dwordx2 %0, %1, %2, 0 offen sc1" : "=v"(d) : "v"(voff), "s"(r) : "memory");
}

template <typename T> __device__ __forceinline__ T lds_ld(unsigned a) { return *(LAS T*)(a); }
template <typename T> __device__ __forceinline__ void lds_st(unsigned a, T v) { *(LAS T*)(a) = v; }
__device__ __forceinline__ bf16x8 tr2(unsigned a0, unsigned a1) {
  s16x4 v0 = __builtin_amdgcn_ds_read_tr16_b64_v4i16((lds_s16x4_ptr)(a0));
  s16x4 v1 = __builtin_amdgcn_ds_read_tr16_b64_v4i16((lds_s16x4_ptr)(a1));
  return __builtin_shufflevector(v0, v1, 0, 1, 2, 3, 4, 5, 6, 7);
}
__device__ __forceinline__ bf16x8 as_frag(u32x4 v) { return __builtin_bit_cast(bf16x8, v); }
#define MFMA(a, b, c) __builtin_amdgcn_mfma_f32_16x16x32_bf16(a, b, c, 0, 0, 0)

__device__ __forceinline__ float vmax2(float a, float b) { float d; asm("v_max_f32 %0, %1, %2" : "=v"(d) : "v"(a), "v"(b)); return d; }
#define SWAP16(a, b) __builtin_amdgcn_permlane16_swap(__float_as_uint(a), __float_as_uint(b), false, false)
#define SWAP32(a, b) __builtin_amdgcn_permlane32_swap(__float_as_uint(a), __float_as_uint(b), false, false)
__device__ __forceinline__ void red2_max(float& a, float& b) {
  auto r = SWAP16(a, b);
  float x = vmax2(__uint_as_float(r[0]), __uint_as_float(r[1]));
  r = SWAP32(x, x);
  x = vmax2(__uint_as_float(r[0]), __uint_as_float(r[1]));
  r = SWAP16(x, x);
  a = __uint_as_float(r[0]); b = __uint_as_float(r[1]);
}
__device__ __forceinline__ void red2_add(float& a, float& b) {
  auto r = SWAP16(a, b);
  float x = __uint_as_float(r[0]) + __uint_as_float(r[1]);
  r = SWAP32(x, x);
  x = __uint_as_float(r[0]) + __uint_as_float(r[1]);
  r = SWAP16(x, x);
  a = __uint_as_float(r[0]); b = __uint_as_float(r[1]);
}
__device__ __forceinline__ float red_max4(float x) {
  auto r = __builtin_amdgcn_permlane16_swap(__float_as_uint(x), __float_as_uint(x), false, false);
  x = vmax2(__uint_as_float(r[0]), __uint_as_float(r[1]));
  r = __builtin_amdgcn_permlane32_swap(__float_as_uint(x), __float_as_uint(x), false, false);
  return vmax2(__uint_as_float(r[0]), __uint_as_float(r[1]));
}
__device__ __forceinline__ float red_add4(float x) {
  auto r = __builtin_amdgcn_permlane16_swap(__float_as_uint(x), __float_as_uint(x), false, false);
  x = __uint_as_float(r[0]) + __uint_as_float(r[1]);
  r = __builtin_amdgcn_permlane32_swap(__float_as_uint(x), __float_as_uint(x), false, false);
  return __uint_as_float(r[0]) + __uint_as_float(r[1]);
}

__device__ __forceinline__ void st16_sc1(rsrc_t r, unsigned voff, unsigned soff, u32x4 v) {
  __builtin_amdgcn_raw_buffer_store_b128(v, r, voff, soff, 16);
  asm volatile("s_nop 1" ::"v"(v) : "memory");
}
__device__ __forceinline__ void st8_sc1(rsrc_t r, unsigned voff, unsigned soff, u32x2 v) { __builtin_amdgcn_raw_buffer_store_b64(v, r, voff, soff, 16); }
__device__ __forceinline__ u32x4 ld16_sc1(rsrc_t r, unsigned voff, unsigned soff) { return __builtin_amdgcn_raw_buffer_load_b128(r, voff, soff, 16); }
__device__ __forceinline__ u32x2 ld8_sc1(rsrc_t r, unsigned voff, unsigned soff) { return __builtin_amdgcn_raw_buffer_load_b64(r, voff, soff, 16); }
__device__ __forceinline__ f32x4 ld4f_sc1(rsrc_t r, unsigned voff, unsigned soff) { return __builtin_bit_cast(f32x4, __builtin_amdgcn_raw_buffer_load_b128(r, voff, soff, 16)); }
#define OPAQUE_V(x) asm volatile("" : "+v"(x))
#define OPAQUE_S(x) asm volatile("" : "+s"(x))

#define CLOBBER asm volatile("" ::: "memory")
constexpr unsigned SPIN_CAP = 1u << 17;
__device__ __forceinline__ bool is_set(u32x2 v, u32x2 epoch) { return v[0] == epoch[0] && v[1] == epoch[1]; }
__device__ __forceinline__ void set_flags(rsrc_t rs, unsigned lineA, int colA, unsigned lineB, int colB, u32x2 epoch, int lane) {
  const unsigned off = (lane & 7) * REP_STRIDE + ((lane & 8) ? lineB + colB * 8 : lineA + colA * 8);
  if (lane < 16) st8_sc1(rs, off, 0, epoch);
}
__device__ __forceinline__ unsigned poll_need(rsrc_t rs, unsigned voff, u32x2 epoch, unsigned ready, unsigned need, bool& dead) {
  for (unsigned spins = 0; !dead; ++spins) {
    CLOBBER;
    ready |= (unsigned)__ballot(is_set(ld8_sc1(rs, voff, 0), epoch));
    if ((ready & need) == need) break;
    if (spins > SPIN_CAP) dead = true;
    __builtin_amdgcn_s_sleep(1);
  }
  CLOBBER;
  return dead ? ~0u : ready;
}

__device__ __forceinline__ void poll_all(rsrc_t rs, unsigned voff, u32x2 epoch, bool& dead) {
  for (unsigned spins = 0; !dead; ++spins) {
    CLOBBER;
    if (__all(is_set(ld8_sc1(rs, voff, 0), epoch))) break;
    if (spins > SPIN_CAP) dead = true;
    __builtin_amdgcn_s_sleep(1);
  }
  CLOBBER;
}

constexpr unsigned SC_QF = L_R1;
constexpr unsigned SC_KF = L_R1 + 16384;
constexpr unsigned SC_VIM = L_R1 + 32768;
constexpr unsigned SC_PHIK = L_R1 + 53248;
constexpr unsigned SC_KVF = L_R1 + 49152;
constexpr unsigned SC_ZF = L_R1 + 49152 + 8192;
constexpr unsigned SC_IMG = L_R1 + 16384;
constexpr unsigned SC_HV = L_R1 + 62464;
constexpr unsigned SC_XCH = L_R1 + 49152;
constexpr unsigned wo_slot(int s) {
  return s < 3 ? L_R2 + (unsigned)((s + 1) % 3) * 18432 : (s < 5 ? L_R1 + 65536 + (unsigned)(s - 3) * 16384 : L_R1 + (unsigned)(s - 5) * 16384);
}
__device__ __forceinline__ unsigned wo_src(int s, int j) { return (unsigned)(((JROT(j) >> 1) + 2 + s) & 7) * (128 * 2048); }
__device__ __forceinline__ unsigned umax3(unsigned a, unsigned b, unsigned c) { return max(max(a, b), c); }
__device__ __forceinline__ unsigned umax4v(unsigned m, const u32x4& v) { return umax3(umax3(m, v[0], v[1]), v[2], v[3]); }
__device__ __forceinline__ u32x4 nosent(u32x4 v) { return u32x4{min(v[0], SENT - 1), min(v[1], SENT - 1), min(v[2], SENT - 1), min(v[3], SENT - 1)}; }

template <typename F, typename G>
__device__ __forceinline__ void qkv_loop(bool isb, unsigned lds0, int wm, int h, int lane, unsigned sp, bool first, F publish, G rest) {
  f32x4 acc[4][6];
  const int fr = lane & 15, fq = lane >> 4;
  unsigned ab[2], bb[2];
  {
    const int r = wm * 64 + fr;
#pragma unroll
    for (int ks = 0; ks < 2; ++ks) ab[ks] = lds0 + L_R2 + r * 128 + (((ks * 4 + fq) ^ ((fr >> 1) & 7)) * 16);
    const int kr = fq * 8 + (fr >> 2);
    const int gs = ((kr >> 1) & 1) | (((kr >> 3) & 1) << 1);
#pragma unroll
    for (int c = 0; c < 2; ++c) bb[c] = lds0 + L_R1 + 3 * 24576 + kr * 128 + (((h * 2 + c) ^ gs) * 32) + (lane & 3) * 8;
  }
#pragma unroll
  for (int m = 0; m < 4; ++m)
#pragma unroll
    for (int n = 0; n < 6; ++n) acc[m][n] = f32x4{0.f, 0.f, 0.f, 0.f};
  bf16x8 af[2][4], bf[2][6];
  auto rds = [&](int ks, int p) {
#pragma unroll
    for (int m = 0; m < 4; ++m) af[p][m] = lds_ld<bf16x8>(ab[ks] + m * 2048);
#pragma unroll
    for (int n = 0; n < 6; ++n) {
      const unsigned a = bb[n & 1] + (n >> 1) * 8192 + ks * 4096;
      bf[p][n] = tr2(a, a + 512);
    }
  };
  auto mms = [&](int p) {
#pragma unroll
    for (int n = 0; n < 6; ++n)
#pragma unroll
      for (int mm = 0; mm < 4; ++mm) { const int m = (n & 1) ? 3 - mm : mm; acc[m][n] = MFMA(bf[p][n], af[p][m], acc[m][n]); }
  };
  auto ilv2 = [&]() {
#pragma unroll
    for (int i = 0; i < 16; ++i) { SGB(0x008, 1); SGB(0x100, 1); }
#pragma unroll
    for (int i = 0; i < 4; ++i) { SGB(0x008, 1); SGB(0x002, 1); }
    SGB(0x008, 4);
  };
  auto ilv3 = [&]() {
#pragma unroll
    for (int i = 0; i < 16; ++i) { SGB(0x008, 1); SGB(0x100, 1); }
#pragma unroll
    for (int i = 0; i < 7; ++i) { SGB(0x008, 1); SGB(0x004, 2); }
    SGB(0x008, 1);
  };
  if (!first) { WAITV(0); WAITL0; }
  BAR;
  u64 cyc0 = 0;
  STAMPP(0);
  rds(0, 0);
  int sa = 0, sb = 3;
  int da = 18432, db = -3 * 24576;
  constexpr int nd = DRAIN_T;
#pragma unroll 1
  for (int ph = 0; ph < 2; ++ph) {
    if (ph) WAITV(0);
#pragma unroll 1
    for (int t = ph ? 15 - nd : nd; t > 0; --t) {
      SCHED;
      rds(1, 1); mms(0);
      ab[0] += da; ab[1] += da;
      bb[0] += db; bb[1] += db;
      ilv2();
      SCHED;
      __builtin_amdgcn_s_waitcnt(0xC07F);
      BAR;
      SCHED;
      rds(0, 0);
      sa = (sa == 2) ? 0 : sa + 1;
      sb = (sb == 3) ? 0 : sb + 1;
      da = (sa == 2) ? -2 * 18432 : 18432;
      db = (sb == 3) ? -3 * 24576 : 24576;
      mms(1); ilv3();
    }
  }
  SCHED;
  f32x4 cs[6];
  float nmean[4], rstd[4];
#pragma unroll
  for (int m = 0; m < 4; ++m) nmean[m] = lds_ld<float>(lds0 + L_MEAN + (16 + wm * 64 + m * 16 + fr) * 4);
#pragma unroll
  for (int n = 0; n < 6; ++n) cs[n] = lds_ld<f32x4>(lds0 + L_CS + ((isb ? 192 : 0) + (n >> 1) * 64 + h * 32 + (n & 1) * 16 + fq * 4) * 4);
  rds(1, 1);
#pragma unroll
  for (int m = 0; m < 4; ++m) rstd[m] = lds_ld<float>(lds0 + L_RSTD + (16 + wm * 64 + m * 16 + fr) * 4);
  mms(0);
#pragma unroll
  for (int n = 0; n < 6; ++n)
#pragma unroll
    for (int m = 0; m < 4; ++m)
#pragma unroll
      for (int e = 0; e < 4; ++e)
        if (m != 1) acc[m][n][e] = __builtin_fmaf(nmean[m], cs[n][e], acc[m][n][e]);
#pragma unroll
  for (int i = 0; i < 6; ++i) { SGB(0x008, 1); SGB(0x100, 2); }
#pragma unroll
  for (int i = 0; i < 18; ++i) { SGB(0x008, 1); SGB(0x100, 1); SGB(0x002, 3); }
  SCHED;
  __builtin_amdgcn_s_waitcnt(0xC07F);
  BAR;
  SCHED;
  auto pack = [&](const f32x4& a, const f32x4& b) { return u32x4{pk2(a[0], a[1]), pk2(a[2], a[3]), pk2(b[0], b[1]), pk2(b[2], b[3])}; };
  auto mm6 = [&](int m) {
#pragma unroll
    for (int nn = 0; nn < 6; ++nn) { const int n = (nn + 4) % 6; acc[m][n] = MFMA(bf[1][n], af[1][m], acc[m][n]); }
  };
  auto ilv4 = [&]() {
#pragma unroll
    for (int i = 0; i < 3; ++i) { SGB(0x008, 1); SGB(0x002, 3); SGB(0x008, 1); SGB(0x002, 3); SGB(0x200, 1); }
  };
  auto fin = [&](auto isb_c) {
    constexpr bool ISB = decltype(isb_c)::value;
    u32x4 qk[4];
    int ln = lane; OPAQUE_V(ln);
    const int fr2 = ln & 15, fq2 = ln >> 4, hw = h * 2 + wm;
    constexpr unsigned d1s = ISB ? 16 * 80 : 1024, d2s = 16 * 80;
    const unsigned d1 = ISB ? lds0 + SC_IMG + h * (2 * 144 * 80) + (16 + wm * 64 + fr2) * 80 + fq2 * 16 : lds0 + SC_KF + hw * 4096 + ln * 16;
    const unsigned d2 = ISB ? d1 + 144 * 80 : lds0 + SC_VIM + (h * 128 + wm * 64 + fr2) * 80 + fq2 * 16;
    const unsigned d0 = lds0 + SC_QF + hw * 4096 + ln * 16;
    auto out3 = [&](int m) {
      if (ISB) lds_st<u32x4>(d2 + m * d2s, pack(acc[m][4], acc[m][5]));
      else lds_st<u32x4>(d2 + m * d2s, pack(acc[m][4] * rstd[m], acc[m][5] * rstd[m]));
      if (m < 2) qk[m] = pack(acc[m][0], acc[m][1]);
      else lds_st<u32x4>(d0 + m * 1024, pack(acc[m][0], acc[m][1]));
      const u32x4 kk = pack(acc[m][2], acc[m][3]);
      if (m < 2) qk[2 + m] = kk;
      if (m >= (ISB ? 1 : 2)) lds_st<u32x4>(d1 + m * d1s, kk);
    };
    mm6(3);
#pragma unroll
    for (int n = 0; n < 6; ++n)
#pragma unroll
      for (int e = 0; e < 4; ++e) acc[1][n][e] = __builtin_fmaf(nmean[1], cs[n][e], acc[1][n][e]);
#pragma unroll
    for (int i = 0; i < 6; ++i) { SGB(0x008, 1); SGB(0x002, 4); }
    SCHED;
    mm6(2);
    out3(3);
    ilv4();
    SCHED;
    if (ISB && wm) publish(pack(acc[3][2] * rstd[3], acc[3][3] * rstd[3]), pack(acc[3][4] * rstd[3], acc[3][5] * rstd[3]));
    SCHED;
    mm6(0);
    out3(2);
    ilv4();
    SCHED;
    if (!ISB) {
      WAITL0;
      BAR;
      SCHED;
    }
    mm6(1);
    out3(0);
    ilv4();
    SCHED;
    out3(1);
    SCHED;
    if (ISB) {
      WAITL0;
      BAR;
    }
    STAMPC(16);
    rest(isb_c, qk);
  };
  if (isb) fin(std::true_type{}); else fin(std::false_type{});
}

__device__ __forceinline__ void hedgehog_exp(const bf16x8& w0, const bf16x8& w1, u32x4 xin, float c, float (&ep)[8], float (&en)[8]) {
  const f32x4 zero = {0.f, 0.f, 0.f, 0.f};
  const f32x4 r0 = MFMA(w0, as_frag(xin), zero);
  const f32x4 r1 = MFMA(w1, as_frag(xin), zero);
  const float x[8] = {r0[0], r0[1], r0[2], r0[3], r1[0], r1[1], r1[2], r1[3]};
  float mx = fmaxf(fmaxf(fmaxf(fmaxf(x[0], x[1]), x[2]), fmaxf(fmaxf(x[3], x[4]), x[5])), fmaxf(x[6], x[7]));
  float nm = -fminf(fminf(fminf(fminf(x[0], x[1]), x[2]), fminf(fminf(x[3], x[4]), x[5])), fminf(x[6], x[7]));
  red2_max(mx, nm);
  const float mxl = mx * -c, mnl = nm * -c;
#pragma unroll
  for (int i = 0; i < 8; ++i) {
    ep[i] = __builtin_amdgcn_exp2f(__builtin_fmaf(x[i], c, mxl));
    en[i] = __builtin_amdgcn_exp2f(__builtin_fmaf(x[i], -c, mnl));
  }
}
__device__ __forceinline__ u32x4 pack8(const float (&e)[8], float f) {
  return u32x4{pk2(e[0] * f, e[1] * f), pk2(e[2] * f, e[3] * f), pk2(e[4] * f, e[5] * f), pk2(e[6] * f, e[7] * f)};
}
__device__ __forceinline__ u32x4 pack8(const float (&e)[8]) { return u32x4{pk2(e[0], e[1]), pk2(e[2], e[3]), pk2(e[4], e[5]), pk2(e[6], e[7])}; }

__device__ __forceinline__ void store_o(rsrc_t rs, unsigned tile_off, int mt, int h, int lane, u32x2 X, u32x2 Y) {
  st16_sc1(rs, lane * 16, tile_off + (unsigned)(mt * 2 + h) * 1024, nosent(u32x4{X[0], X[1], Y[0], Y[1]}));
}

__device__ __forceinline__ void job_colsum(rsrc_t rs, const u32x4 (&w)[8], int mat, int g, int jc, int hw, int lane) {
  const int fr = lane & 15;
  const unsigned one = (fr < 8) ? (0x3f80u << (16 * (fr & 1))) : 0u;
  const int e = (fr >> 1) & 3;
  const bf16x8 sel = as_frag(u32x4{e == 0 ? one : 0u, e == 1 ? one : 0u, e == 2 ? one : 0u, e == 3 ? one : 0u});
  f32x4 c = {0.f, 0.f, 0.f, 0.f};
#pragma unroll
  for (int i = 0; i < 8; ++i) c = MFMA(sel, as_frag(w[i]), c);
  u32x4 w4;
#pragma unroll
  for (int k = 0; k < 4; ++k) {
    const float a = c[k];
    w4[k] = __float_as_uint(a + __int_as_float(__builtin_amdgcn_update_dpp(0, __float_as_int(a), 0x128  , 0xf, 0xf, false)));
  }
  unsigned so = WS_CS + (unsigned)(((mat * 16 + jc) * 16 + g) * 768);
  OPAQUE_S(so);
  if ((lane & 40) == 0) st16_sc1(rs, (unsigned)(hw * 64 + (lane & 7) * 8 + (lane >> 4) * 4) * 4, so, w4);
}
__device__ __forceinline__ void job_pack(rsrc_t rs, u32x4 w, int i, int mat, int g, int jc, int hw, int lane) {
  st16_sc1(rs, (lane >> 3) * 128 + (((lane & 7) ^ ((((lane >> 4) & 1) | ((i & 1) << 1)) << 1)) * 16),
           WS_WP + (unsigned)(((mat * 16 + jc) * 16 + g) * 24576 + hw * 8192 + i * 1024), w);
}

__global__ __launch_bounds__(512, 2) void mega_kernel(Params p) {
  extern __shared__ __attribute__((aligned(16))) char smem[];
  const int tid = threadIdx.x;
  const int wave0 = __builtin_amdgcn_readfirstlane(tid >> 6);
  const int xx = blockIdx.x & 7, rr = blockIdx.x >> 3;
  const int g0 = (rr >> 2) * 2 + (xx & 1), j0 = (xx >> 1) * 4 + (rr & 3);
  const unsigned lds00 = (unsigned)(size_t)smem;
#define LOCALS0 int g = g0, j = j0, l = l0, wave = wave0; unsigned lds0 = lds00;                       \
  OPAQUE_S(g); OPAQUE_S(j); OPAQUE_S(l); OPAQUE_S(wave); OPAQUE_S(lds0);                               \
  const int hw = wave & 3, wm = wave & 1, h = (wave >> 1) & 1;                                         \
  const unsigned myrep = (unsigned)((j >> 2) * 2 + (g & 1)) * REP_STRIDE;                              \
  (void)hw; (void)wm; (void)h; (void)myrep;                                                            \
  int lane = tid; OPAQUE_V(lane); lane &= 63
#define LOCALS LOCALS0; const bool isb = l & 1, lead = (wave == 4); const int half = wave >> 2; (void)isb; (void)lead; (void)half
  const rsrc_t rs = mk_rsrc(p.ws, WS_SPAN);
  const bool cw = wave0 < 4;
  if (wave0 == 2) touch(mk_rsrc(p.ws, NREP * REP_STRIDE), (blockIdx.x * 7 + ((tid & 63) < 7 ? (tid & 63) : 0)) * 128, lds00 + L_SINK + 512);
  if (wave0 == 3) {
    const char* pc;
    unsigned len;
    asm volatile("s_getpc_b64 %0\n.Lpf_base:\n\ts_mov_b32 %1, .Lpf_end-.Lpf_base" : "=s"(pc), "=s"(len));
    const unsigned off = ((unsigned)rr * 16 + (tid & 15)) * 128;
    touch(mk_rsrc(pc, len), ((int)len > 0 && off + 4 <= len) ? off : 0u, lds00 + L_SINK);
  }
  u32x2 epoch = {0u, 0u};
  bool dead = false;
  { const int lane = tid & 63, wave = wave0; (void)lane; (void)wave; STAMP_INIT; STAMP(0); }

#pragma unroll 1
  for (int l0 = 0; l0 < 4; ++l0) {
    unsigned ready = ~0u;
    u32x4 xr[2][2];
    auto attention = [&](auto half_c, auto isb_c, const u32x4 (&qk)[4]) {
      LOCALS0;
      constexpr int half = decltype(half_c)::value;
      constexpr bool isb = decltype(isb_c)::value;
      const bool lead = half && wave == 4;
      const int fr = lane & 15, fq = lane >> 4;
      const unsigned otile = ob_off(g, j, l);
      const f32x4 zero = {0.f, 0.f, 0.f, 0.f};
      u32x2 clf;
      const unsigned clo = myrep + ((lane & 16) ? fl_line(FL_CLC + l, j) : fl_line(FL_CLR + l, g)) + fr * 8;
      auto clcheck = [&]() {
        if constexpr (half) {
          OPAQUE_V(clf);
          if (__builtin_expect(!__all(is_set(clf, epoch)), 0)) poll_all(rs, clo, epoch, dead);
        }
      };
      auto tail = [&](auto& wst) {
        constexpr int P0 = isb ? 5 : 3, N1 = isb ? PB1 : PA1, N2 = isb ? PB2 : PA2, N3 = (isb ? 12 : 20) - N1 - N2;
        const rsrc_t rw = mk_rsrc(isb ? p.woB : p.woA, 1024 * 2048);
        const unsigned vw = (unsigned)((hw * 8 + (lane >> 3)) * 2048) + (((lane & 7) ^ ((((lane >> 4) & 1) | (hw & 2)) << 1)) * 16) + j * 128;
        auto park = [&](int a, int n) {
#pragma unroll
          for (int i = a; i < a + n; ++i) wst[i] = __builtin_amdgcn_raw_buffer_load_b128(rw, vw, wo_src(P0 + (i >> 2), j) + (i & 3) * (32 * 2048), 0);
          SCHED;
        };
        auto stO = [&](int i) { dma4<4096>(rw, vw, wo_src(i, j), 32 * 2048, lds0 + wo_slot(i) + hw * 1024); };
        if (__builtin_expect(ready != ~0u, 0))
          poll_all(rs, myrep + ((l == 0 && fq) ? fl_line(FL_CS, JOBC(j, fq - 1)) : fl_line(FL_X + l, g)) + fr * 8, epoch, dead);
        f32x4 js[4], jc[4];
#pragma unroll
        for (int i = 0; i < 4; ++i) asm volatile("" : "=v"(jc[i]));
        if (l < 2) {
#pragma unroll
          for (int i = 0; i < 4; ++i) jc[i] = ld4f_sc1(rs, fq * 768 + hw * 192 + (fr < 12 ? fr : 0) * 16, WS_CS + (unsigned)(((isb ? 16 : 0) + j) * 16 + i * 4) * 768);
        }
#pragma unroll
        for (int i = 0; i < 4; ++i) js[i] = ld4f_sc1(rs, fq * 1024 + hw * 256 + fr * 16, st_off(g, l) + i * 4096);
        SCHED;
        if (isb) stO(4);
        WAITV(10 + (isb ? 8 : 4));
        BAR;
        if (l == SL) STAMP5(24);
        WAITV(0);
        park(0, N1);
#pragma unroll
        for (int i = 0; i < 4; ++i) { OPAQUE_V(js[i]); OPAQUE_V(jc[i]); }
        if (l < 2) {
          const f32x4 s = (jc[0] + jc[1]) + (jc[2] + jc[3]);
          float a = s[0], b = s[1], c = s[2], d = s[3];
          red2_add(a, b); red2_add(c, d);
          if (lane < 12) lds_st<f32x4>(lds0 + L_CS + (isb ? 768 : 0) + hw * 192 + lane * 16, f32x4{a, b, c, d});
        }
        {
          const f32x4 s = (js[0] + js[1]) + (js[2] + js[3]);
          float a = s[0], b = s[1], c = s[2], d = s[3];
          red2_add(a, b); red2_add(c, d);
          const float m0 = a * (1.0f / 1024), m1 = c * (1.0f / 1024);
          const float r0 = __builtin_amdgcn_rsqf(fmaxf(__builtin_fmaf(b, 1.0f / 1024, -m0 * m0), 0.f) + 1e-5f);
          const float r1 = __builtin_amdgcn_rsqf(fmaxf(__builtin_fmaf(d, 1.0f / 1024, -m1 * m1), 0.f) + 1e-5f);
          if (lane < 16) {
            lds_st<f32x2>(lds0 + L_MEAN + 64 + hw * 128 + fr * 8, f32x2{-m0, -m1});
            lds_st<f32x2>(lds0 + L_RSTD + 64 + hw * 128 + fr * 8, f32x2{r0, r1});
          }
        }
        SCHED;
        stO(0);
        WAITV(4 + N1);
        WAITL0;
        BAR;
        if (l == SL) STAMP5(25);
        if (lead) set_flags(rs, fl_line(FL_CLR + l, g), j, fl_line(FL_CLC + l, j), g, epoch, lane);
        stO(1);
        park(N1, N2);
        BAR;
        if (l == SL) STAMP5(26);
        stO(2);
        if (isb) stO(3);
        park(N1 + N2, N3);
        ld8_asm(clf, rs, clo);
        __builtin_amdgcn_s_setprio(0);
        if (l == SL) STAMP5(27);
        BAR;
        if (l == SL) STAMP5(28);
      };
      if constexpr (!isb) {
        u32x4 wst[20];
        u32x4 vb[8];
        if constexpr (half) tail(wst);
        if constexpr (!half) {
#pragma unroll
          for (int i = 0; i < 8; ++i) asm volatile("" : "=v"(vb[i]));
          if (l == 0) {
            const bool wj = hw < 3;
            const char* bp = (const char*)(wj ? p.wqkvB : p.woB);
            int j2 = j0;
            OPAQUE_S(j2);
            unsigned rs_ = wj ? 6144 : 2048, sz = rs_ * 1024, sb = g * 64 * rs_ + (wj ? hw * 2048 + JOBC(j2, hw) * 128 : (j2 ^ 4) * 128), ss = rs_ * 8;
            OPAQUE_S(bp); OPAQUE_S(rs_); OPAQUE_S(sz); OPAQUE_S(sb); OPAQUE_S(ss);
            const rsrc_t rb = mk_rsrc(bp, sz);
#pragma unroll
            for (int i = 0; i < 8; ++i) { if (wj) vb[i] = __builtin_amdgcn_raw_buffer_load_b128(rb, (lane >> 3) * rs_ + (lane & 7) * 16, sb + i * ss, 2); else vb[i] = __builtin_amdgcn_raw_buffer_load_b128(rb, (lane >> 3) * rs_ + (lane & 7) * 16, sb + i * ss, 0); }
          }
          SCHED;
        }
        STAMP(3 + l * 8);
        u32x4 qin[2];
#pragma unroll
        for (int i = 0; i < 2; ++i) qin[i] = half ? lds_ld<u32x4>(lds0 + SC_QF + hw * 4096 + (2 + i) * 1024 + lane * 16) : qk[i];
        float cq[2];
#pragma unroll
        for (int i = 0; i < 2; ++i) cq[i] = lds_ld<float>(lds0 + L_RSTD + (16 + wm * 64 + (half * 2 + i) * 16 + fr) * 4) * 1.4426950408889634f;
        const bf16x8 ones = {0x3f80, 0x3f80, 0x3f80, 0x3f80, 0x3f80, 0x3f80, 0x3f80, 0x3f80};
        const unsigned wa = lds0 + L_WPHI + h * 2048 + (fq * 4 + (fr >> 2)) * 64 + (lane & 3) * 8;
        const bf16x8 w0 = tr2(wa, wa + 1024);
        const bf16x8 w1 = tr2(wa + 32, wa + 32 + 1024);
#pragma unroll
        for (int i = 0; i < 2; ++i) {
          const int m = half * 2 + i;
          float ep[8], en[8];
          hedgehog_exp(w0, w1, half ? lds_ld<u32x4>(lds0 + SC_KF + hw * 4096 + m * 1024 + lane * 16) : qk[2 + i], cq[i], ep, en);
          const float sp = MFMA(ones, as_frag(pack8(ep)), zero)[0], sn = MFMA(ones, as_frag(pack8(en)), zero)[0];
          const u32x4 pp = pack8(ep, __builtin_amdgcn_rcpf(sp)), pn = pack8(en, __builtin_amdgcn_rcpf(sn));
          const unsigned a = lds0 + SC_PHIK + (h * 128 + wm * 64 + m * 16 + fr) * 160 + fq * 8;
          lds_st<u32x2>(a, u32x2{pp[0], pp[1]});
          lds_st<u32x2>(a + 32, u32x2{pp[2], pp[3]});
          lds_st<u32x2>(a + 64, u32x2{pn[0], pn[1]});
          lds_st<u32x2>(a + 96, u32x2{pn[2], pn[3]});
        }
        WAITL0;
        BAR;
        STAMP(4 + l * 8);
        if constexpr (half) WAITV(0);
        clcheck();
        const int h2 = wave >> 2, dt = (wave >> 1) & 1, s = wave & 1;
        const unsigned kvb = WS_KVP + (unsigned)j * KVP_J + (unsigned)(l >> 1) * KVP_L + (unsigned)wave * 1024;
        const unsigned kzb = WS_KVP + (unsigned)j * KVP_J + (unsigned)(l >> 1) * KVP_L + 8192 + h2 * 256;
        {
          f32x4 c0 = zero, c1 = zero, z0 = zero, z1 = zero;
          const int trow = h2 * 128 + fq * 4 + (fr >> 2);
          const unsigned va = lds0 + SC_VIM + trow * 80 + dt * 32 + (lane & 3) * 8;
          const unsigned pa = lds0 + SC_PHIK + trow * 160 + s * 64 + (lane & 3) * 8;
#pragma unroll
          for (int ks = 0; ks < 4; ++ks) {
            const bf16x8 v = tr2(va + ks * 32 * 80, va + (ks * 32 + 16) * 80);
            const bf16x8 a0 = tr2(pa + ks * 32 * 160, pa + (ks * 32 + 16) * 160);
            const bf16x8 a1 = tr2(pa + 32 + ks * 32 * 160, pa + 32 + (ks * 32 + 16) * 160);
            c0 = MFMA(a0, v, c0);
            c1 = MFMA(a1, v, c1);
            z0 = MFMA(a0, ones, z0);
            z1 = MFMA(a1, ones, z1);
          }
          st16_sc1(rs, lane * 16, kvb + (unsigned)g * KVP_G, nosent(u32x4{pk2(c0[0], c0[1]), pk2(c0[2], c0[3]), pk2(c1[0], c1[1]), pk2(c1[2], c1[3])}));
          if (dt == 0 && fr < 2) st16_sc1(rs, fq * 16 + fr * 64, kzb + (unsigned)g * KVP_G + s * 128, nosent(__builtin_bit_cast(u32x4, fr ? z1 : z0)));
        }
        SCHED;
        if constexpr (!half) {
#pragma unroll
          for (int i = 0; i < 8; ++i) OPAQUE_V(vb[i]);
          if (l == 0 && hw < 3) {
            job_colsum(rs, vb, 1, g, JOBC(j, hw), hw, lane);
#pragma unroll
            for (int i = 0; i < 8; ++i) job_pack(rs, vb[i], i, 1, g, JOBC(j, hw), hw, lane);
          }
        }
        CLOBBER;
        BAR;
        STAMP(5 + l * 8);
        u32x4 qp[2], qn[2];
        float sqp[2], sqn[2];
        auto phiq = [&](int i) {
          float ep[8], en[8];
          hedgehog_exp(w0, w1, qin[i], cq[i], ep, en);
          qp[i] = pack8(ep); qn[i] = pack8(en);
          sqp[i] = __builtin_amdgcn_rcpf(MFMA(ones, as_frag(qp[i]), zero)[0]);
          sqn[i] = __builtin_amdgcn_rcpf(MFMA(ones, as_frag(qn[i]), zero)[0]);
          OPAQUE_V(qp[i]); OPAQUE_V(qn[i]); OPAQUE_V(sqp[i]); OPAQUE_V(sqn[i]);
        };
        phiq(0); phiq(1);
        STAMP(6 + l * 8);
        {
          u32x4 v[16], zr[4];
          const bool doz = (wave & 3) == 0;
          SCHED;
#pragma unroll
          for (int i = 0; i < 4; ++i) zr[i] = u32x4{0u, 0u, 0u, 0u};
          if (doz) {
#pragma unroll
            for (int i = 0; i < 4; ++i) zr[i] = ld16_sc1(rs, (lane & 15) * 16 + (lane >> 4) * (4 * KVP_G), kzb + i * KVP_G);
          }
#pragma unroll
          for (int gg = 0; gg < 16; ++gg) {
            v[gg] = ld16_sc1(rs, lane * 16, kvb + gg * KVP_G);
            SCHED;
            if constexpr (half) {
#pragma unroll
              for (int i = (gg * 5) / 4; i < ((gg + 1) * 5) / 4; ++i)
                lds_st<u32x4>(lds0 + wo_slot(3 + (i >> 2)) + ((i & 3) * 4 + hw) * 1024 + lane * 16, wst[i]);
              SCHED;
            }
          }
          SCHED;
          SCHED;
          const unsigned one = (fq == (fr >> 2)) ? (0x3f80u << (16 * (fr & 1))) : 0u;
          const unsigned sa0 = (fr & 2) ? 0u : one, sa1 = (fr & 2) ? one : 0u;
          const bf16x8 sel0 = as_frag(u32x4{sa0, sa1, 0u, 0u}), sel1 = as_frag(u32x4{0u, 0u, sa0, sa1});
          f32x4 d0 = zero, d1 = zero;
          unsigned mall = umax4v(umax4v(umax4v(umax4v(0u, zr[0]), zr[1]), zr[2]), zr[3]);
#pragma unroll
          for (int gg = 0; gg < 16; ++gg) {
            d0 = MFMA(sel0, as_frag(v[gg]), d0); d1 = MFMA(sel1, as_frag(v[gg]), d1);
            mall = umax4v(mall, v[gg]);
            SGB(0x008, 1); SGB(0x002, 1); SGB(0x008, 1); SGB(0x002, 1);
          }
          SCHED;
          if (__builtin_expect(__any(mall == SENT), 0)) {
            for (unsigned spins = 0; !dead; ++spins) {
              if (spins > SPIN_CAP) { dead = true; break; }
              CLOBBER;
              bool again = false;
#pragma unroll
              for (int gg = 0; gg < 16; ++gg)
                if (__any(umax4v(0u, v[gg]) == SENT)) { v[gg] = ld16_sc1(rs, lane * 16, kvb + gg * KVP_G); again = true; }
              if (__any(umax4v(umax4v(umax4v(umax4v(0u, zr[0]), zr[1]), zr[2]), zr[3]) == SENT)) {
#pragma unroll
                for (int i = 0; i < 4; ++i) zr[i] = ld16_sc1(rs, (lane & 15) * 16 + (lane >> 4) * (4 * KVP_G), kzb + i * KVP_G);
                again = true;
              }
              if (!again) break;
              WAITV(0);
            }
            d0 = zero; d1 = zero;
#pragma unroll
            for (int gg = 0; gg < 16; ++gg) { d0 = MFMA(sel0, as_frag(v[gg]), d0); d1 = MFMA(sel1, as_frag(v[gg]), d1); }
#pragma unroll
            for (int i = 0; i < 4; ++i) OPAQUE_V(zr[i]);
          }
          f32x4 z[4];
#pragma unroll
          for (int i = 0; i < 4; ++i) z[i] = __builtin_bit_cast(f32x4, zr[i]);
          lds_st<u32x4>(lds0 + SC_KVF + wave * 1024 + lane * 16, u32x4{pk2(d0[0], d0[1]), pk2(d0[2], d0[3]), pk2(d1[0], d1[1]), pk2(d1[2], d1[3])});
          if (doz) {
            f32x4 zs = (z[0] + z[1]) + (z[2] + z[3]);
#pragma unroll
            for (int e = 0; e < 4; ++e) zs[e] = red_add4(zs[e]);
            if (lane < 16) {
              const int ft = lane >> 2;
              lds_st<u32x2>(lds0 + SC_ZF + ((h2 * 2 + (ft >> 1)) * 4 + (lane & 3)) * 16 + (ft & 1) * 8, u32x2{pk2(zs[0], zs[1]), pk2(zs[2], zs[3])});
            }
          }
          WAITV(0);
          WAITL0;
        }
        BAR;
        if (l == 0 && lead) set_flags(rs, fl_line(FL_PK, j), g, fl_line(FL_PK, j), g, epoch, lane);
        STAMP(7 + l * 8);
        bf16x8 kf[2][2], zf[2];
#pragma unroll
        for (int d = 0; d < 2; ++d)
#pragma unroll
          for (int sg = 0; sg < 2; ++sg) kf[d][sg] = lds_ld<bf16x8>(lds0 + SC_KVF + (h * 4 + d * 2 + sg) * 1024 + lane * 16);
#pragma unroll
        for (int sg = 0; sg < 2; ++sg) zf[sg] = lds_ld<bf16x8>(lds0 + SC_ZF + ((h * 2 + sg) * 4 + fq) * 16);
#pragma unroll
        for (int i = 0; i < 2; ++i) {
          const float dp = MFMA(zf[0], as_frag(qp[i]), zero)[0], dn = MFMA(zf[1], as_frag(qn[i]), zero)[0];
          const f32x4 p0 = MFMA(kf[0][0], as_frag(qp[i]), zero), n0 = MFMA(kf[0][1], as_frag(qn[i]), zero);
          const f32x4 p1 = MFMA(kf[1][0], as_frag(qp[i]), zero), n1 = MFMA(kf[1][1], as_frag(qn[i]), zero);
          const float inv = __builtin_amdgcn_rcpf(__builtin_fmaf(dp, sqp[i], __builtin_fmaf(dn, sqn[i], 1e-6f)));
          const float fa = sqp[i] * inv, fb = sqn[i] * inv;
          const f32x4 o0 = p0 * fa + n0 * fb, o1 = p1 * fa + n1 * fb;
          const u32x2 X = {pk2(o0[0], o0[1]), pk2(o0[2], o0[3])}, Y = {pk2(o1[0], o1[1]), pk2(o1[2], o1[3])};
          store_o(rs, otile, wm * 4 + half * 2 + i, h, lane, X, Y);
        }
        STAMP(8 + l * 8);
        BAR;
      } else {
        u32x4 wst[12];
        if constexpr (half) tail(wst);
        STAMP(3 + l * 8);
        const unsigned kim = lds0 + SC_IMG + h * (2 * 144 * 80), vim = kim + 144 * 80;
        const float scale = 0.17677669529663687f, L2E = 1.4426950408889634f;
        bf16x8 qf2[2], kf3[3];
        s16x4 vh[3][2];
        u32x4 hk, hv;
        const bool halo = !half && wm == 0 && g > 0;
        const unsigned hso = WS_KVP + (unsigned)j * KVP_J + (unsigned)(g - 1) * KVP_G + (unsigned)(l >> 1) * KVP_L + KVP_HALO + h * 2048;
#pragma unroll
        for (int i = 0; i < 3; ++i) {
          kf3[i] = (half || i == 0) ? lds_ld<bf16x8>(kim + (wm * 64 + (half * 2 + i) * 16 + fr) * 80 + fq * 16) : as_frag(qk[1 + i]);
          const unsigned va = vim + (wm * 64 + (half * 2 + i) * 16 + fq * 4 + (fr >> 2)) * 80 + (lane & 3) * 8;
          vh[i][0] = __builtin_amdgcn_ds_read_tr16_b64_v4i16((lds_s16x4_ptr)(va));
          vh[i][1] = __builtin_amdgcn_ds_read_tr16_b64_v4i16((lds_s16x4_ptr)(va + 32));
        }
#pragma unroll
        for (int i = 0; i < 2; ++i) qf2[i] = half ? lds_ld<bf16x8>(lds0 + SC_QF + hw * 4096 + (2 + i) * 1024 + lane * 16) : as_frag(qk[i]);
        const int par = fr & 1;
        float rk[3][2], rq[2];
#pragma unroll
        for (int i = 0; i < 3; ++i)
#pragma unroll
          for (int e = 0; e < 2; ++e) rk[i][e] = lds_ld<float>(lds0 + L_RSTD + (wm * 64 + (half * 2 + i) * 16 + fq * 4 + par + 2 * e) * 4);
#pragma unroll
        for (int i = 0; i < 2; ++i) rq[i] = lds_ld<float>(lds0 + L_RSTD + (16 + wm * 64 + (half * 2 + i) * 16 + fr) * 4) * (scale * L2E);
        if constexpr (!half) {
          asm volatile("" : "=v"(hk)); asm volatile("" : "=v"(hv));
          if (halo) { hk = ld16_sc1(rs, lane * 16, hso); hv = ld16_sc1(rs, lane * 16, hso + 1024); }
          SCHED;
        }
#pragma unroll
        for (int ii = 0; ii < 2; ++ii) {
          const int i = half ? ii : 1 - ii;
          if (!half && i == 0) {
            SCHED;
            OPAQUE_V(hk); OPAQUE_V(hv);
            if (wm == 0) {
              kf3[0] = bf16x8{0, 0, 0, 0, 0, 0, 0, 0}; vh[0][0] = s16x4{0, 0, 0, 0}; vh[0][1] = s16x4{0, 0, 0, 0};
              if (g > 0) {
                for (unsigned spins = 0; !dead && __any(umax4v(umax4v(0u, hk), hv) == SENT); ++spins) {
                  if (spins > SPIN_CAP) { dead = true; break; }
                  CLOBBER;
                  hk = ld16_sc1(rs, lane * 16, hso); hv = ld16_sc1(rs, lane * 16, hso + 1024);
                  OPAQUE_V(hk); OPAQUE_V(hv);
                }
                kf3[0] = as_frag(hk);
                lds_st<u32x4>(lds0 + SC_HV + h * 1280 + fr * 80 + fq * 16, hv);
                WAITL0;
                const unsigned va = lds0 + SC_HV + h * 1280 + (fq * 4 + (fr >> 2)) * 80 + (lane & 3) * 8;
                vh[0][0] = __builtin_amdgcn_ds_read_tr16_b64_v4i16((lds_s16x4_ptr)(va));
                vh[0][1] = __builtin_amdgcn_ds_read_tr16_b64_v4i16((lds_s16x4_ptr)(va + 32));
              }
            }
          }
          const int m = half * 2 + i;
          const int r0 = wm * 64 + m * 16;
          const bf16x8 qf = qf2[i], k0 = kf3[i], k1 = kf3[i + 1];
          const bf16x8 v0 = __builtin_shufflevector(vh[i][0], vh[i + 1][0], 0, 1, 2, 3, 4, 5, 6, 7);
          const bf16x8 v1 = __builtin_shufflevector(vh[i][1], vh[i + 1][1], 0, 1, 2, 3, 4, 5, 6, 7);
          f32x4 s0 = MFMA(k0, qf, zero);
          f32x4 s1 = MFMA(k1, qf, zero);
          const bool has_prev = (g > 0) || (r0 > 0);
          float x[4] = {par ? s0[1] : s0[0], par ? s0[3] : s0[2], par ? s1[1] : s1[0], par ? s1[3] : s1[2]};
#pragma unroll
          for (int e = 0; e < 4; ++e) {
            const int delta = fr + 16 - (e >> 1) * 16 - fq * 4 - par - (e & 1) * 2;
            const bool ok = (delta >= 0) && (delta <= 14) && ((e >= 2) || has_prev);
            x[e] = ok ? x[e] * rk[i + (e >> 1)][e & 1] : -3.0e38f;
          }
          const float mx = red_max4(fmaxf(fmaxf(x[0], x[1]), fmaxf(x[2], x[3])));
          const float mxl = mx * -rq[i];
          float ex[4];
#pragma unroll
          for (int e = 0; e < 4; ++e) ex[e] = __builtin_amdgcn_exp2f(__builtin_fmaf(x[e], rq[i], mxl));
          const float inv = __builtin_amdgcn_rcpf(red_add4((ex[0] + ex[1]) + (ex[2] + ex[3])));
          u32x4 pf;
#pragma unroll
          for (int e = 0; e < 4; ++e) pf[e] = pk2(ex[e] * inv * rk[i + (e >> 1)][e & 1], 0.f) << (16 * par);
          f32x4 o0 = MFMA(v0, as_frag(pf), zero);
          f32x4 o1 = MFMA(v1, as_frag(pf), zero);
          if (half && i == 0) {
            WAITV(0);
#pragma unroll
            for (int k = 0; k < 12; ++k) OPAQUE_V(wst[k]);
            OPAQUE_V(clf);
          }
          store_o(rs, otile, wm * 4 + m, h, lane, u32x2{pk2(o0[0], o0[1]), pk2(o0[2], o0[3])}, u32x2{pk2(o1[0], o1[1]), pk2(o1[2], o1[3])});
        }
        clcheck();
        STAMP(8 + l * 8);
        BAR;
        if constexpr (half) {
#pragma unroll
          for (int k = 0; k < 12; ++k) lds_st<u32x4>(lds0 + wo_slot(5) + (k * 4 + hw) * 1024 + lane * 16, wst[k]);
          WAITL0;
          if (lane == 0) __hip_atomic_fetch_add((LAS unsigned*)(lds0 + L_CNT), 1u, __ATOMIC_RELAXED, __HIP_MEMORY_SCOPE_WORKGROUP);
        }
      }
    };
    if (cw) {
      if (l0 == 0) {
        LOCALS;
        const int jc = wave < 3 ? JOBC(j, wave) : j ^ 4;
        u32x4 v[2][8];
        if (wave < 3) {
          const rsrc_t ra = mk_rsrc(p.wqkvA, 1024 * 6144);
#pragma unroll
          for (int i = 0; i < 8; ++i)
            v[0][i] = __builtin_amdgcn_raw_buffer_load_b128(ra, (lane >> 3) * 6144 + (lane & 7) * 16, (g * 64 + i * 8) * 6144 + wave * 2048 + jc * 128, 0);
          SCHED;
          if ((jc & 7) == 3) touch(ra, ((g ^ 1) * 64 + lane) * 6144 + wave * 2048 + jc * 128, lds0 + L_SINK + 256);
          if (wave == 0) touch(mk_rsrc(p.woA, 1024 * 2048), (g * 64 + lane) * 2048 + (j ^ 5) * 128, lds0 + L_SINK + 256);
          job_colsum(rs, v[0], 0, g, jc, wave, lane);
        } else {
          const int fr = lane & 15, fq = lane >> 4;
          const rsrc_t rx = mk_rsrc(p.x, 2048 * 2048);
#pragma unroll
          for (int i = 0; i < 16; ++i)
            v[i & 1][i >> 1] = __builtin_amdgcn_raw_buffer_load_b128(rx, fr * 2048 + fq * 16, (g * 128 + (i >> 1) * 16) * 2048 + jc * 128 + (i & 1) * 64, 0);
          SCHED;
          const f32x4 zero = {0.f, 0.f, 0.f, 0.f};
          const bf16x8 ones = {0x3f80, 0x3f80, 0x3f80, 0x3f80, 0x3f80, 0x3f80, 0x3f80, 0x3f80};
          float sm[8], q[8];
#pragma unroll
          for (int m = 0; m < 8; ++m) {
            const f32x4 a = MFMA(ones, as_frag(v[1][m]), MFMA(ones, as_frag(v[0][m]), zero));
            const f32x4 b = MFMA(as_frag(v[1][m]), as_frag(v[1][m]), MFMA(as_frag(v[0][m]), as_frag(v[0][m]), zero));
            sm[m] = a[0];
            q[m] = (fr & 2) ? ((fr & 1) ? b[3] : b[2]) : ((fr & 1) ? b[1] : b[0]);
          }
          if (fq == (fr >> 2)) {
#pragma unroll
            for (int m = 0; m < 8; ++m) st8_sc1(rs, (m * 16 + fr) * 8, st_off(g, 0) + jc * 1024, u32x2{__float_as_uint(sm[m]), __float_as_uint(q[m])});
          }
        }
      }
      LOCALS;
      qkv_loop(isb, lds0, wm, h, lane, wave * 256 + (40 + l * 2) * 4, l == 0,
               [&](u32x4 kk, u32x4 vv) {
                 LOCALS;
                 const unsigned so = WS_KVP + (unsigned)j * KVP_J + (unsigned)g * KVP_G + (unsigned)(l >> 1) * KVP_L + KVP_HALO + h * 2048;
                 st16_sc1(rs, lane * 16, so, nosent(kk));
                 st16_sc1(rs, lane * 16, so + 1024, nosent(vv));
               },
               [&](auto isb_c, const u32x4 (&qk)[4]) {
                 STAMP(2 + l * 8);
                 attention(std::integral_constant<int, 0>{}, isb_c, qk);
               });
#pragma unroll
      for (int i = 0; i < 4; ++i) asm volatile("" : "=v"(xr[i >> 1][i & 1]));
    } else {
      __builtin_amdgcn_s_setprio(3);
      LOCALS;
      const unsigned lin = lane * 16, vlin = lin + hw * 1024;
      const int jr = JROT(j);
      u32x4 sent4 = {SENT, SENT, SENT, SENT};
      OPAQUE_V(sent4);
      const unsigned cb0 = ob_off(g, j, l), cb2 = xb_off(g, l) + j * 16384 - 8 * 4096;
      const unsigned cb1 = WS_KVP + (unsigned)j * KVP_J + (unsigned)g * KVP_G + (unsigned)(l >> 1) * KVP_L - 4 * 4096;
      int nis = 0, sA[16], sW[16];
      auto need = [&](int t) { WAITV(nis - (sA[t] > sW[t] ? sA[t] : sW[t])); };
      auto ldres = [&](int slot) {
        const int fr = lane & 15, fq = lane >> 4, ch = (fq & 1) * 2 + (fq >> 1);
#pragma unroll
        for (int i = 0; i < 4; ++i)
          xr[i >> 1][i & 1] = lds_ld<u32x4>(lds0 + L_R2 + slot * 18432 + (hw * 32 + ((i >> 1) ^ 1) * 16 + fr) * 128 + (((ch + 4 * (i & 1)) ^ ((fr >> 1) & 7)) * 16));
      };
      auto aslot = [&](int t) { return lds0 + L_R2 + (t % 3) * 18432 + hw * 1024; };
      auto wslot = [&](int t) { return lds0 + L_R1 + ((t + 3) & 3) * 24576 + hw * 1024; };
      if (l == 0) {
        const rsrc_t rx = mk_rsrc(p.x, 2048 * 2048), ra = mk_rsrc(p.wqkvA, 1024 * 6144);
        const unsigned va = (hw * 8 + (lane >> 3)) * 2048 + (((lane & 7) ^ (((hw & 1) * 4 + (lane >> 4)) & 7)) * 16);
        const unsigned vq = (hw * 8 + (lane >> 3)) * 6144 + (((lane & 7) ^ ((((lane >> 4) & 1) | ((hw & 1) << 1)) << 1)) * 16);
        auto A0 = [&](int t) { dma4<4096>(rx, va, g * (128 * 2048) + ((jr + t) & 15) * 128, 32 * 2048, aslot(t)); nis += 4; sA[t] = nis; };
        auto W0 = [&](int t) {
          const unsigned so = ((jr + t) & 15) * (64 * 6144) + j * 128;
          dma3<8192>(ra, vq, so, 2048, wslot(t));
          dma3<8192>(ra, vq, so + 32 * 6144, 2048, wslot(t) + 4096);
          nis += 6; sW[t] = nis;
        };
        if (SL == 0) STAMP5(5);
        W0(0); A0(0);
        STAMP(48);
        W0(1); A0(1);
        u32x2 gw2, fl;
        int sJ = 0;
        const int tg = (g - jr) & 15;
        auto packA = [&](int t) {
          if ((tg | 1) == t) {
            const unsigned src = lds0 + L_R1 + ((tg + 3) & 3) * 24576 + vlin;
            u32x4 w[6];
#pragma unroll
            for (int ii = 0; ii < 6; ++ii) w[ii] = lds_ld<u32x4>(src + ii * 4096);
#pragma unroll
            for (int ii = 0; ii < 6; ++ii) st16_sc1(rs, vlin, WS_WP + (unsigned)((j * 16 + g) * 24576 + ii * 4096), w[ii]);
          }
        };
        if (lane < 16) lds_st<float>(lds0 + L_RSTD + lane * 4, 1.0f);
        lds_st<unsigned>(lds0 + L_CNT, 0u);
#pragma unroll
        for (int i = 0; i < 4; ++i) asm volatile("" : "=v"(xr[i >> 1][i & 1]));
        if (SL == 0) STAMP5(7);
        need(0);
        if (SL == 0) STAMP5(8);
#pragma unroll
        for (int t = 0; t <= 13; ++t) {
          BAR;
          if (SL == 0) STAMP5(10 + t);
          if (t == 0) W0(2);
          A0(t + 2);
          if (t == 13) break;
          W0(t + 3);
          if (t == 0) {
            dma1(mk_rsrc(p.wphi, 32 * 2048), vlin, j * 4096, lds0 + L_WPHI + hw * 1024);
            ld8_asm(gw2, rs, WS_EPOCH + 0u * lin);
            nis += 2; sJ = nis;
          }
          if (t < 12) st16_sc1(rs, vlin, (t < 4 ? cb0 : (t < 8 ? cb1 : cb2)) + (unsigned)t * 4096, sent4);
          if (t == 3) {
            WAITV(nis - sJ);
            OPAQUE_V(gw2);
            const u64 gword = ((u64)(unsigned)UNI(gw2[1]) << 32) | (unsigned)UNI(gw2[0]);
            u64 z = gword + 0x9e3779b97f4a7c15ull;
            z = (z ^ (z >> 30)) * 0xbf58476d1ce4e5b9ull;
            z = (z ^ (z >> 27)) * 0x94d049bb133111ebull;
            z = z ^ (z >> 31);
            z = (z == gword) ? ~z : z;
            epoch = u32x2{(unsigned)z, (unsigned)(z >> 32)};
          }
          if (t == DRAIN_T + 1 && lead) set_flags(rs, fl_line(FL_X, g), j, fl_line(FL_CS, j), g, epoch, lane);
          if (t == 11) { ld8_asm(fl, rs, myrep + (lane >= 16 ? fl_line(FL_CS, JOBC(j, (lane >> 4) - 1)) : fl_line(FL_X, g)) + (lane & 15) * 8); nis += 1; sJ = nis; }
          if (t < 4 && (j & 3) == t) { ldres(t % 3); WAITL0; }
          need(t + 1);
          if (t == 12) {
            WAITV(nis - sJ);
            OPAQUE_V(fl);
            ready = __all(is_set(fl, epoch)) ? ~0u : 0u;
          }
          if (!(t & 1)) packA(t + 1);
        }
        if (tg >= 14) { WAITV(4); packA(15); }
      } else {
        const unsigned sa = xb_off(g, l - 1), sw = WS_WP + (unsigned)((isb ? 16 : 0) + j) * (16 * 24576);
        const unsigned flo = myrep + fl_line(FL_X + l, g) + (lane & 15) * 8;
        const int dj = (j - jr) & 15;
        auto kseq = [&](int t) { return (jr + t - ((t <= dj) ? 1 : 0)) & 15; };
        auto ldA = [&](int k, unsigned d) { dma4<4096>(rs, vlin, sa + (unsigned)k * 16384, 4096, d); };
        auto ldW = [&](int k, unsigned d) {
          dma3<4096>(rs, vlin, sw + (unsigned)k * 24576, 4096, d);
          dma3<4096>(rs, vlin, sw + (unsigned)k * 24576 + 12288, 4096, d + 12288);
        };
        auto A1 = [&](int t) { ldA(kseq(t), aslot(t)); nis += 4; sA[t] = nis; };
        auto W1 = [&](int t) { ldW(kseq(t), wslot(t)); nis += 6; sW[t] = nis; };
        auto badA = [&](int t) {
          u32x4 v[4];
#pragma unroll
          for (int ii = 0; ii < 4; ++ii) v[ii] = lds_ld<u32x4>(aslot(t) + lin + ii * 4096);
          SCHED;
          return __any(umax4v(umax4v(umax4v(umax4v(0u, v[0]), v[1]), v[2]), v[3]) == SENT);
        };
        auto val = [&](int t) {
          CLOBBER;
          if (__builtin_expect(badA(t), 0)) {
            for (unsigned spins = 0; !dead; ++spins) {
              if (spins > SPIN_CAP) { dead = true; break; }
              WAITV(0);
              ldA(kseq(t), aslot(t));
              ldA(kseq(t + 1), aslot(t + 1));
              WAITV(0);
              if (!badA(t)) break;
              __builtin_amdgcn_s_sleep(1);
            }
          }
        };
        const unsigned cmask = 0xfu | (isb ? 0u : 0xf0u) | (l == 3 ? 0u : 0xf00u);
        auto clean = [&](int t) {
          if ((cmask >> t) & 1) st16_sc1(rs, vlin, cb0 + (t >= 4 ? cb1 - cb0 : 0u) + (t >= 8 ? cb2 - cb1 : 0u) + (unsigned)t * 4096, sent4);
        };
        sA[0] = sW[0] = 0;
        if (l == SL) STAMP5(5);
        WAITV(0);
        W1(1);
        STAMP(48 + l * 2);
        A1(1); A1(2);
        WAITL0;
        BAR;
        if (l == SL) STAMP5(10);
        if (lead) set_flags(rs, fl_line(FL_X + l, g), j, fl_line(FL_X + l, g), j, epoch, lane);
        ldres(0);
        W1(2);
        W1(3);
        clean(0);
        need(1);
        val(1);
        WAITL0;
#pragma unroll
        for (int t = 1; t <= NVAL; ++t) {
          BAR;
          if (l == SL) STAMP5(10 + t);
          if (t == NVAL) {
            const unsigned nd = 1u << kseq(t + 2);
            if (__builtin_expect((ready & nd) == 0, 0)) ready = poll_need(rs, flo, epoch, ready, nd, dead);
          }
          if (t == NVAL - 1) {
            u32x2 fl;
            ld8_asm(fl, rs, flo);
            A1(t + 2); W1(t + 3);
            WAITV(10);
            OPAQUE_V(fl);
            ready = (unsigned)__ballot(is_set(fl, epoch));
          } else {
            A1(t + 2); W1(t + 3);
          }
          clean(t);
          need(t + 1);
          val(t + 1);
        }
        int ka = kseq(NVAL + 3);
        unsigned da = aslot(NVAL + 3), dw = wslot(NVAL + 4);
#pragma unroll 1
        for (int t = NVAL + 1; t <= 13; ++t) {
          BAR;
          if (l == SL) STAMP5(10 + t);
          const unsigned nd = 1u << ka;
          if (__builtin_expect((ready & nd) == 0, 0)) ready = poll_need(rs, flo, epoch, ready, nd, dead);
          ldA(ka, da);
          if (t == 13) break;
          ka = kseq(t + 3);
          ldW(ka, dw);
          da = (da == aslot(2)) ? aslot(0) : da + 18432;
          dw = (dw == wslot(0)) ? wslot(1) : dw + 24576;
          clean(t);
          WAITV(16);
        }
      }
      const u32x4 qk[4] = {};
      if (isb) attention(std::integral_constant<int, 1>{}, std::true_type{}, qk);
      else attention(std::integral_constant<int, 1>{}, std::false_type{}, qk);
    }
    {
      LOCALS;
      const int fr = lane & 15, fq = lane >> 4;
      const int kh = half;
      const unsigned v0 = lane * 16 + hw * 4096 + kh * 2048, v1 = lane * 16 + hw * 4096 + (kh ^ 1) * 2048;
      const int r0 = (JROT(j) >> 1) + 2;
      const unsigned ob0 = ob_off(g, kh, l);
      const unsigned pko = myrep + fl_line(FL_PK, JOBC(j, fq - 1)) + fr * 8;
      u32x4 R[8][4];
      u32x2 pkf;
      auto issue_ = [&](int i) {
        const unsigned so = ob0 + (unsigned)((r0 + i) & 7) * 131072;
        R[i][0] = ld16_sc1(rs, v0, so); R[i][1] = ld16_sc1(rs, v1, so);
        R[i][2] = ld16_sc1(rs, v0 + 1024, so); R[i][3] = ld16_sc1(rs, v1 + 1024, so);
      };
      auto issue = [&](int i) { issue_(i); SCHED; };
      constexpr int LA = LOOKAHEAD;
      STAMP(41 + l * 2);
      __builtin_amdgcn_s_setprio(3);
      SCHED;
      issue(0);
      __builtin_amdgcn_s_setprio(2);
#pragma unroll
      for (int i = 1; i < LA; ++i) issue(i);
      auto vmax = [&](int i) { return umax4v(umax4v(umax4v(umax4v(0u, R[i][0]), R[i][1]), R[i][2]), R[i][3]); };
      auto bad = [&](int i) { return __any(umax4v(umax4v(umax4v(umax4v(0u, R[i][0]), R[i][1]), R[i][2]), R[i][3]) == SENT); };
      auto poll = [&](int i, int nfl) {
        for (unsigned spins = 0; !dead; ++spins) {
          if (spins > SPIN_CAP) { dead = true; break; }
          __builtin_amdgcn_s_sleep(2);
          CLOBBER;
          issue(i);
#pragma unroll
          for (int k = i + 1; k < i + nfl && k < 8; ++k)
            if (bad(k)) issue(k);
          WAITV(0);
#pragma unroll
          for (int k = i + 1; k < i + nfl && k < 8; ++k)
#pragma unroll
            for (int f = 0; f < 4; ++f) OPAQUE_V(R[k][f]);
          if (!bad(i)) break;
        }
      };
      unsigned bb[4];
      {
        const int kr = (fq & 1) * 16 + (fq >> 1) * 4 + (fr >> 2);
        const int gs = ((kr >> 1) & 1) | (((kr >> 4) & 1) << 1);
#pragma unroll
        for (int c = 0; c < 4; ++c) bb[c] = lds0 + kh * 8192 + kr * 128 + ((c ^ gs) * 32) + (lane & 3) * 8;
      }
      bf16x8 bf[2][4];
      auto rdB = [&](int i, int hh) {
        if (hh == 0) {
#pragma unroll
          for (int c = 0; c < 4; ++c) { bb[c] += wo_slot(i) - (i ? wo_slot(i - 1) : 0u); asm("" : "+v"(bb[c])); }
        }
#pragma unroll
        for (int c = 0; c < 4; ++c) bf[hh][c] = tr2(bb[c] + hh * 4096, bb[c] + hh * 4096 + 1024);
      };
      f32x4 acc[2][4];
#pragma unroll
      for (int i = 0; i < 8; ++i) acc[i >> 2][i & 3] = f32x4{0.f, 0.f, 0.f, 0.f};
      auto accinit = [&]() {
#pragma unroll
        for (int m = 0; m < 2; ++m)
#pragma unroll
          for (int pp = 0; pp < 2; ++pp) {
            auto r0_ = __builtin_amdgcn_permlane16_swap(xr[m][pp][0], xr[m][pp][2], false, false);
            auto r1_ = __builtin_amdgcn_permlane16_swap(xr[m][pp][1], xr[m][pp][3], false, false);
            acc[m][2 * pp] = f32x4{lo16(r0_[0]), hi16(r0_[0]), lo16(r1_[0]), hi16(r1_[0])};
            acc[m][2 * pp + 1] = f32x4{lo16(r0_[1]), hi16(r0_[1]), lo16(r1_[1]), hi16(r1_[1])};
          }
      };
      auto mms = [&](int i, int hh) {
#pragma unroll
        for (int n = 0; n < 4; ++n)
#pragma unroll
          for (int m = 0; m < 2; ++m) acc[m][n] = MFMA(bf[hh][n], as_frag(R[i][hh * 2 + m]), acc[m][n]);
      };
      if (half) accinit();
      const char* obase = (l == 3) ? (const char*)p.out : (const char*)p.ws;
      unsigned osize = (l == 3) ? 2048 * 2048 : WS_SPAN;
      unsigned sq = WS_WP + (unsigned)(((isb ? 0 : 16) + j) * 16 + j) * 24576 + kh * 12288;
      unsigned dst = lds0 + L_R1 + 3 * 24576 + kh * 12288 + hw * 1024, vq = hw * 1024 + lane * 16;
      OPAQUE_S(obase); OPAQUE_S(osize); OPAQUE_S(sq); OPAQUE_S(dst); OPAQUE_V(vq);
      const rsrc_t ro = mk_rsrc(obase, osize);
      unsigned so[2];
      unsigned xw = lds0 + (wave < 6 ? SC_XCH + wave * 4096 : L_R1 + wave * 4096) + lane * 16;
      unsigned xa = lds0 + ((wave ^ 4) < 6 ? SC_XCH + (wave ^ 4) * 4096 : L_R1 + (wave ^ 4) * 4096) + lane * 16;
      unsigned sb0 = (l == 3) ? g * (128 * 2048) + j * 128 : xb_off(g, l) + j * 16384;
      {
        const int row = (hw * 2 + kh) * 16 + fr, ch = (fq & 1) * 2 + (fq >> 1);
        const unsigned sw = (l == 3) ? 0u : (unsigned)((row >> 1) & 7), rb = (unsigned)row << ((l == 3) ? 11 : 7);
#pragma unroll
        for (int pp = 0; pp < 2; ++pp) { so[pp] = rb + (((ch + 4 * pp) ^ sw) * 16); OPAQUE_V(so[pp]); }
        OPAQUE_V(xa); OPAQUE_V(xw); OPAQUE_S(sb0);
      }
      SCHED;
      rdB(0, 0);
      unsigned mx = vmax(0);
#pragma unroll
      for (int i = 0; i < 8; ++i) {
        SCHED;
        if (__builtin_expect(__any(mx == SENT), 0)) poll(i, LA);
        if (i == 2) { pkf = ld8_sc1(rs, pko, 0); SCHED; }
        if (i == 4) {
          for (unsigned spins = 0; spins < SPIN_CAP; ++spins) {
            CLOBBER;
            if (UNI(lds_ld<unsigned>(lds0 + L_CNT)) >= 2u * (l + 1) - (isb ? 0u : 2u)) break;
          }
          CLOBBER;
        }
        if (i == 7) {
          OPAQUE_V(pkf);
          if (l == 0 && lead && !__all(is_set(pkf, epoch))) poll_all(rs, pko, epoch, dead);
          BAR;
        }
        if (i == 7 && l < 3) dma3<4096>(rs, vq, sq, 4096, dst);
        if (i == 0) STAMP(49 + l * 2);
        if (i == 2) __builtin_amdgcn_s_setprio(1);
        if (i == 5) __builtin_amdgcn_s_setprio(0);
        SCHED;
        rdB(i, 1);
        if (i + LA < 8) issue_(i + LA);
        mms(i, 0);
#pragma unroll
        for (int k = 0; k < 4; ++k) { SGB(0x008, 1); SGB(0x100, 1); SGB(0x020, 1); SGB(0x008, 1); SGB(0x100, 1); }
        SCHED;
        if (i < 7) { rdB(i + 1, 0); mx = vmax(i + 1); }
        mms(i, 1);
#pragma unroll
        for (int k = 0; k < 4; ++k) { SGB(0x008, 1); SGB(0x100, 2); }
#pragma unroll
        for (int k = 4; k < 8; ++k) { SGB(0x008, 1); SGB(0x002, 8 / (8 - 4)); }
      }
      SCHED;
      if (l == SL - 1) STAMP5(3);
      STAMP(34 + l);
#pragma unroll
      for (int n = 0; n < 4; ++n) lds_st<f32x4>(xw + n * 1024, acc[1][n]);
      WAITL0;
      BAR;
      SCHED;
      if (l == SL - 1) STAMP5(4);
      f32x4 pa[4];
#pragma unroll
      for (int n = 0; n < 4; ++n) pa[n] = lds_ld<f32x4>(xa + n * 1024);
      SCHED;
      u32x4 o[2];
#pragma unroll
      for (int pp = 0; pp < 2; ++pp) {
        const f32x4 a = acc[0][2 * pp] + pa[2 * pp], b = acc[0][2 * pp + 1] + pa[2 * pp + 1];
        auto r0_ = __builtin_amdgcn_permlane16_swap(pk2(a[0], a[1]), pk2(b[0], b[1]), false, false);
        auto r1_ = __builtin_amdgcn_permlane16_swap(pk2(a[2], a[3]), pk2(b[2], b[3]), false, false);
        o[pp] = nosent(u32x4{r0_[0], r1_[0], r0_[1], r1_[1]});
        st16_sc1(ro, so[pp], sb0, o[pp]);
      }
      if (l < 3) {
        lds_st<u32x4>(lds0 + L_R2 + so[0], o[0]);
        lds_st<u32x4>(lds0 + L_R2 + so[1], o[1]);
        const f32x4 zero = {0.f, 0.f, 0.f, 0.f};
        const bf16x8 ones = {0x3f80, 0x3f80, 0x3f80, 0x3f80, 0x3f80, 0x3f80, 0x3f80, 0x3f80};
        const f32x4 a = MFMA(ones, as_frag(o[1]), MFMA(ones, as_frag(o[0]), zero));
        const f32x4 b = MFMA(as_frag(o[1]), as_frag(o[1]), MFMA(as_frag(o[0]), as_frag(o[0]), zero));
        const float q = (fr & 2) ? ((fr & 1) ? b[3] : b[2]) : ((fr & 1) ? b[1] : b[0]);
        if (fq == (fr >> 2)) st8_sc1(rs, ((hw * 2 + kh) * 16 + fr) * 8, st_off(g, l + 1) + j * 1024, u32x2{__float_as_uint(a[0]), __float_as_uint(q)});
      } else if (lead && blockIdx.x == 0 && lane == 0) {
        st8_sc1(rs, WS_EPOCH, 0, epoch);
      }
    }
    { const int lane = tid & 63, wave = wave0; (void)lane; (void)wave; if (cw) STAMP(9 + l0 * 8); }
  }
  { const int lane = tid & 63, wave = wave0; (void)lane; (void)wave; STAMP_DUMP; }
  asm volatile(".Lpf_end:");
}

extern "C" void kernel_launch(void* const* d_in, const int* in_sizes, int n_in, void* d_out, int out_size,
                              void* d_ws, size_t ws_size, hipStream_t stream) {
  Params p;
  p.x = (const u16*)d_in[0];
  p.wqkvA = (const u16*)d_in[1];
  p.wphi = (const u16*)d_in[2];
  p.woA = (const u16*)d_in[3];
  p.wqkvB = (const u16*)d_in[5];
  p.woB = (const u16*)d_in[6];
  p.out = (u16*)d_out;
  p.ws = (char*)d_ws;
  (void)hipFuncSetAttribute((const void*)mega_kernel, hipFuncAttributeMaxDynamicSharedMemorySize, L_TOTAL);
  mega_kernel<<<256, 512, L_TOTAL, stream>>>(p);
}
```

```cpp
#include <hip/hip_runtime.h>
#include <stdint.h>
#include <type_traits>
constexpr int LOOKAHEAD = 2;
constexpr int NVAL = 4;
#define XD 0
#define WFIRST 1
#define WOT 5
#define POL0 0
constexpr int PA0 = 2, PA1 = 6, PA2 = 6, PB0 = 2, PB1 = 2, PB2 = 6;

typedef unsigned short u16;
typedef unsigned long long u64;
typedef __attribute__((ext_vector_type(8))) short bf16x8;
typedef __attribute__((ext_vector_type(4))) short s16x4;
typedef __attribute__((ext_vector_type(4))) float f32x4;
typedef __attribute__((ext_vector_type(2))) float f32x2;
typedef __attribute__((ext_vector_type(4))) unsigned u32x4;
typedef __attribute__((ext_vector_type(2))) unsigned u32x2;
typedef __attribute__((ext_vector_type(2))) __bf16 bf16x2_t;
#define LAS __attribute__((address_space(3)))
typedef LAS s16x4* lds_s16x4_ptr;

constexpr unsigned NREP = 8, REP_STRIDE = 17 * 2048;
enum { FL_X = 0, FL_CS = 4, FL_CLR = 5, FL_CLC = 9, FL_PK = 13 };
__device__ __forceinline__ unsigned fl_line(int set, int a) { return (unsigned)(set * 16 + a) * 128; }
constexpr unsigned WS_EPOCH = 512u << 10;
constexpr unsigned WS_CS = 1u << 20;
constexpr unsigned WS_KVP = 4u << 20;
constexpr unsigned KVP_J = 512u << 10, KVP_G = 32u << 10, KVP_L = 16u << 10, KVP_HALO = 12u << 10;
constexpr unsigned SENT = 0xffffffffu;
constexpr unsigned WS_G = 16u << 20, G_STRIDE = 2u << 20;
__device__ __forceinline__ unsigned xb_off(int g, int l) { return WS_G + (unsigned)g * G_STRIDE + (unsigned)l * (256u << 10); }
__device__ __forceinline__ unsigned ob_off(int g, int j, int l) { return WS_G + (unsigned)g * G_STRIDE + (3u << 18) + (unsigned)j * (64u << 10) + (unsigned)l * (16u << 10); }
__device__ __forceinline__ unsigned st_off(int g, int l) { return WS_G + (unsigned)g * G_STRIDE + (7u << 18) + (unsigned)l * (16u << 10); }
constexpr unsigned WS_WP = 48u << 20;
constexpr unsigned WS_SPAN = 64u << 20;

constexpr unsigned L_R1 = 0;
constexpr unsigned L_R2 = 98304, XS = 16384;
constexpr unsigned L_MISC = 147456;
constexpr unsigned L_MEAN = L_MISC;
constexpr unsigned L_RSTD = L_MISC + 576;
constexpr unsigned L_CS = L_MISC + 1152;
constexpr unsigned L_WPHI = L_MISC + 2688;
constexpr unsigned L_SINK = L_MISC + 6912;
constexpr unsigned L_EP = L_MISC + 6792;
constexpr unsigned L_CNT = L_MISC + 6784;
constexpr unsigned L_TOTAL = 163840;

#define JROT(j) ((j) & ~3)
#define JOBC(j, w) (((j) & ~3) + (((j) + (w) + 1) & 3))

struct Params {
  const u16* x;
  const u16* wqkvA;
  const u16* wphi;
  const u16* woA;
  const u16* wqkvB;
  const u16* woB;
  u16* out;
  char* ws;
};

#define SCHED __builtin_amdgcn_sched_barrier(0)
#define BAR __builtin_amdgcn_s_barrier()
#define SGB(m, n) __builtin_amdgcn_sched_group_barrier(m, n, 0)
#define WAITV(n) asm volatile("s_waitcnt vmcnt(%0)" ::"n"(n) : "memory")
#define WAITL0 asm volatile("s_waitcnt lgkmcnt(0)" ::: "memory")

#define TRC(slot) do {} while (0)
#define TRR(slot) do {} while (0)
#define STAMP_INIT do {} while (0)
#define STAMP_DUMP do {} while (0)

__device__ __forceinline__ unsigned pk2(float a, float b) {
  f32x2 v = {a, b};
  return __builtin_bit_cast(unsigned, __builtin_convertvector(v, bf16x2_t));
}
__device__ __forceinline__ float lo16(unsigned u) { return __uint_as_float(u << 16); }
__device__ __forceinline__ float hi16(unsigned u) { return __uint_as_float(u & 0xffff0000u); }

typedef __amdgpu_buffer_rsrc_t rsrc_t;
__device__ __forceinline__ rsrc_t mk_rsrc(const void* p, unsigned bytes) { return __builtin_amdgcn_make_buffer_rsrc((void*)p, 0, bytes, 0x00020000); }
#define UNI(x) __builtin_amdgcn_readfirstlane(x)
#define DMA_HEAD(P) "s_mov_b32 m0, %4\n\ts_mov_b32 %0, %3\n\ts_nop 2\n\tbuffer_load_dwordx4 %1, %2, %0 offen " P " lds\n\t"
#define DMA_NEXT(P) "s_add_u32 m0, m0, %6\n\ts_add_u32 %0, %0, %5\n\tbuffer_load_dwordx4 %1, %2, %0 offen " P " lds\n\t"
#define DMA_OPS : "=&s"(t) : "v"(voff), "s"(r), "s"(UNI(soff)), "s"(UNI(lds)), "s"(UNI(sstep)), "i"(LSTEP) : "memory", "scc", "m0"
template <int LSTEP, int POL = 1> __device__ __forceinline__ void dma4(rsrc_t r, unsigned voff, unsigned soff, unsigned sstep, unsigned lds) {
  unsigned t;
  if constexpr (POL == 1) asm volatile(DMA_HEAD("sc1") DMA_NEXT("sc1") DMA_NEXT("sc1") DMA_NEXT("sc1") DMA_OPS);
  else if constexpr (POL == 2) asm volatile(DMA_HEAD("nt") DMA_NEXT("nt") DMA_NEXT("nt") DMA_NEXT("nt") DMA_OPS);
  else asm volatile(DMA_HEAD("") DMA_NEXT("") DMA_NEXT("") DMA_NEXT("") DMA_OPS);
}
template <int LSTEP, int POL = 1> __device__ __forceinline__ void dma3(rsrc_t r, unsigned voff, unsigned soff, unsigned sstep, unsigned lds) {
  unsigned t;
  if constexpr (POL == 1) asm volatile(DMA_HEAD("sc1") DMA_NEXT("sc1") DMA_NEXT("sc1") DMA_OPS);
  else if constexpr (POL == 2) asm volatile(DMA_HEAD("nt") DMA_NEXT("nt") DMA_NEXT("nt") DMA_OPS);
  else asm volatile(DMA_HEAD("") DMA_NEXT("") DMA_NEXT("") DMA_OPS);
}
__device__ __forceinline__ void dma1(rsrc_t r, unsigned voff, unsigned soff, unsigned lds) {
  asm volatile("s_mov_b32 m0, %3\n\ts_nop 3\n\tbuffer_load_dwordx4 %0, %1, %2 offen sc1 lds" ::"v"(voff), "s"(r), "s"(UNI(soff)), "s"(UNI(lds)) : "memory", "m0");
}
__device__ __forceinline__ void touch(rsrc_t r, unsigned voff, unsigned lds) {
  asm volatile("s_mov_b32 m0, %2\n\ts_nop 3\n\tbuffer_load_dword %0, %1, 0 offen lds" ::"v"(voff), "s"(r), "s"(UNI(lds)) : "memory", "m0");
}
__device__ __forceinline__ void ld8_asm(u32x2& d, rsrc_t r, unsigned voff) {
  asm volatile("s_nop 4\n\tbuffer_load_dwordx2 %0, %1, %2, 0 offen sc1" : "=v"(d) : "v"(voff), "s"(r) : "memory");
}

template <typename T> __device__ __forceinline__ T lds_ld(unsigned a) { return *(LAS T*)(a); }
template <typename T> __device__ __forceinline__ void lds_st(unsigned a, T v) { *(LAS T*)(a) = v; }
__device__ __forceinline__ bf16x8 tr2(unsigned a0, unsigned a1) {
  s16x4 v0 = __builtin_amdgcn_ds_read_tr16_b64_v4i16((lds_s16x4_ptr)(a0));
  s16x4 v1 = __builtin_amdgcn_ds_read_tr16_b64_v4i16((lds_s16x4_ptr)(a1));
  return __builtin_shufflevector(v0, v1, 0, 1, 2, 3, 4, 5, 6, 7);
}
__device__ __forceinline__ bf16x8 as_frag(u32x4 v) { return __builtin_bit_cast(bf16x8, v); }
#define MFMA(a, b, c) __builtin_amdgcn_mfma_f32_16x16x32_bf16(a, b, c, 0, 0, 0)

__device__ __forceinline__ float vmax2(float a, float b) { float d; asm("v_max_f32 %0, %1, %2" : "=v"(d) : "v"(a), "v"(b)); return d; }
#define SWAP16(a, b) __builtin_amdgcn_permlane16_swap(__float_as_uint(a), __float_as_uint(b), false, false)
#define SWAP32(a, b) __builtin_amdgcn_permlane32_swap(__float_as_uint(a), __float_as_uint(b), false, false)
__device__ __forceinline__ void red2_max(float& a, float& b) {
  auto r = SWAP16(a, b);
  float x = vmax2(__uint_as_float(r[0]), __uint_as_float(r[1]));
  r = SWAP32(x, x);
  x = vmax2(__uint_as_float(r[0]), __uint_as_float(r[1]));
  r = SWAP16(x, x);
  a = __uint_as_float(r[0]); b = __uint_as_float(r[1]);
}
__device__ __forceinline__ void red2_add(float& a, float& b) {
  auto r = SWAP16(a, b);
  float x = __uint_as_float(r[0]) + __uint_as_float(r[1]);
  r = SWAP32(x, x);
  x = __uint_as_float(r[0]) + __uint_as_float(r[1]);
  r = SWAP16(x, x);
  a = __uint_as_float(r[0]); b = __uint_as_float(r[1]);
}
__device__ __forceinline__ float red_max4(float x) {
  auto r = __builtin_amdgcn_permlane16_swap(__float_as_uint(x), __float_as_uint(x), false, false);
  x = vmax2(__uint_as_float(r[0]), __uint_as_float(r[1]));
  r = __builtin_amdgcn_permlane32_swap(__float_as_uint(x), __float_as_uint(x), false, false);
  return vmax2(__uint_as_float(r[0]), __uint_as_float(r[1]));
}
__device__ __forceinline__ float red_add4(float x) {
  auto r = __builtin_amdgcn_permlane16_swap(__float_as_uint(x), __float_as_uint(x), false, false);
  x = __uint_as_float(r[0]) + __uint_as_float(r[1]);
  r = __builtin_amdgcn_permlane32_swap(__float_as_uint(x), __float_as_uint(x), false, false);
  return __uint_as_float(r[0]) + __uint_as_float(r[1]);
}

__device__ __forceinline__ void st16_sc1(rsrc_t r, unsigned voff, unsigned soff, u32x4 v) {
  __builtin_amdgcn_raw_buffer_store_b128(v, r, voff, soff, 16);
  asm volatile("s_nop 1" ::"v"(v) : "memory");
}
__device__ __forceinline__ void st8_sc1(rsrc_t r, unsigned voff, unsigned soff, u32x2 v) { __builtin_amdgcn_raw_buffer_store_b64(v, r, voff, soff, 16); }
__device__ __forceinline__ u32x4 ld16_sc1(rsrc_t r, unsigned voff, unsigned soff) { return __builtin_amdgcn_raw_buffer_load_b128(r, voff, soff, 16); }
__device__ __forceinline__ u32x2 ld8_sc1(rsrc_t r, unsigned voff, unsigned soff) { return __builtin_amdgcn_raw_buffer_load_b64(r, voff, soff, 16); }
__device__ __forceinline__ f32x4 ld4f_sc1(rsrc_t r, unsigned voff, unsigned soff) { return __builtin_bit_cast(f32x4, __builtin_amdgcn_raw_buffer_load_b128(r, voff, soff, 16)); }
#define OPAQUE_V(x) asm volatile("" : "+v"(x))
#define OPAQUE_S(x) asm volatile("" : "+s"(x))

#define CLOBBER asm volatile("" ::: "memory")
constexpr unsigned SPIN_CAP = 1u << 17;
__device__ __forceinline__ bool is_set(u32x2 v, u32x2 epoch) { return v[0] == epoch[0] && v[1] == epoch[1]; }
__device__ __forceinline__ void set_flags(rsrc_t rs, unsigned lineA, int colA, unsigned lineB, int colB, u32x2 epoch, int lane) {
  const unsigned off = (lane & 7) * REP_STRIDE + ((lane & 8) ? lineB + colB * 8 : lineA + colA * 8);
  if (lane < 16) st8_sc1(rs, off, 0, epoch);
}
__device__ __forceinline__ unsigned poll_need(rsrc_t rs, unsigned voff, u32x2 epoch, unsigned ready, unsigned need, bool& dead) {
  for (unsigned spins = 0; !dead; ++spins) {
    CLOBBER;
    ready |= (unsigned)__ballot(is_set(ld8_sc1(rs, voff, 0), epoch));
    if ((ready & need) == need) break;
    if (spins > SPIN_CAP) dead = true;
    __builtin_amdgcn_s_sleep(1);
  }
  CLOBBER;
  return dead ? ~0u : ready;
}

__device__ __forceinline__ void poll_all(rsrc_t rs, unsigned voff, u32x2 epoch, bool& dead) {
  for (unsigned spins = 0; !dead; ++spins) {
    CLOBBER;
    if (__all(is_set(ld8_sc1(rs, voff, 0), epoch))) break;
    if (spins > SPIN_CAP) dead = true;
    __builtin_amdgcn_s_sleep(1);
  }
  CLOBBER;
}

constexpr unsigned sc_qf(bool isb) { return L_R1 + (isb ? 32768 : 16384); }
constexpr unsigned SC_KF = L_R1 + 24576;
constexpr unsigned SC_VIM = L_R1 + 32768;
constexpr unsigned SC_PHIK = L_R1 + 53248;
constexpr unsigned SC_KVF = L_R1 + 65536;
constexpr unsigned SC_ZF = L_R1 + 65536 + 8192;
constexpr unsigned SC_IMG = L_R1 + 40960;
constexpr unsigned SC_HV = L_R1 + 87040;
constexpr unsigned SC_XCH = L_R2 + XS;
constexpr unsigned wo_slot(int s) {
  return s < 2 ? L_R2 + (unsigned)(s + 1) * XS : (s < 6 ? L_R1 + (unsigned)(s - 2) * 16384 : (s == 6 ? L_R1 + 81920 : L_R2));
}
__device__ __forceinline__ unsigned wo_src(int s, int j) { return (unsigned)(((JROT(j) >> 1) + 2 + s) & 7) * (128 * 2048); }
__device__ __forceinline__ unsigned umax3(unsigned a, unsigned b, unsigned c) { return max(max(a, b), c); }
__device__ __forceinline__ unsigned umax4v(unsigned m, const u32x4& v) { return umax3(umax3(m, v[0], v[1]), v[2], v[3]); }
__device__ __forceinline__ u32x4 nosent(u32x4 v) { return u32x4{min(v[0], SENT - 1), min(v[1], SENT - 1), min(v[2], SENT - 1), min(v[3], SENT - 1)}; }

template <typename F, typename G>
__device__ __forceinline__ void qkv_loop(bool isb, unsigned lds0, int wm, int h, int lane, bool first, int& trv, F publish, G rest) {
  f32x4 acc[4][6];
  const int fr = lane & 15, fq = lane >> 4;
  unsigned ab[2], bb[2];
  {
    const int r = wm * 64 + fr;
#pragma unroll
    for (int ks = 0; ks < 2; ++ks) ab[ks] = lds0 + L_R2 + r * 128 + (((ks * 4 + fq) ^ ((fr >> 1) & 7)) * 16);
    const int kr = fq * 8 + (fr >> 2);
    const int gs = ((kr >> 1) & 1) | (((kr >> 3) & 1) << 1);
#pragma unroll
    for (int c = 0; c < 2; ++c) bb[c] = lds0 + L_R1 + kr * 128 + (((h * 2 + c) ^ gs) * 32) + (lane & 3) * 8;
  }
#pragma unroll
  for (int m = 0; m < 4; ++m)
#pragma unroll
    for (int n = 0; n < 6; ++n) acc[m][n] = f32x4{0.f, 0.f, 0.f, 0.f};
  bf16x8 af[2][4], bf[2][6];
  auto rds = [&](int ks, int p) {
#pragma unroll
    for (int m = 0; m < 4; ++m) af[p][m] = lds_ld<bf16x8>(ab[ks] + m * 2048);
#pragma unroll
    for (int n = 0; n < 6; ++n) {
      const unsigned a = bb[n & 1] + (n >> 1) * 8192 + ks * 4096;
      bf[p][n] = tr2(a, a + 512);
    }
  };
  auto mms = [&](int p) {
#pragma unroll
    for (int n = 0; n < 6; ++n)
#pragma unroll
      for (int mm = 0; mm < 4; ++mm) { const int m = (n & 1) ? 3 - mm : mm; acc[m][n] = MFMA(bf[p][n], af[p][m], acc[m][n]); }
  };
  auto ilv2 = [&]() {
#pragma unroll
    for (int i = 0; i < 16; ++i) { SGB(0x008, 1); SGB(0x100, 1); }
#pragma unroll
    for (int i = 0; i < 4; ++i) { SGB(0x008, 1); SGB(0x002, 1); }
    SGB(0x008, 4);
  };
  auto ilv3 = [&]() {
#pragma unroll
    for (int i = 0; i < 16; ++i) { SGB(0x008, 1); SGB(0x100, 1); }
#pragma unroll
    for (int i = 0; i < 7; ++i) { SGB(0x008, 1); SGB(0x004, 2); }
    SGB(0x008, 1);
  };
  if (!first) { WAITV(6); WAITL0; }
  TRC(1); BAR;
  int tslot = 2;
  rds(0, 0);
  int sa = 0, sb = 0;
  int da = XS, db = 24576;
  auto step = [&]() {
    ab[0] += da; ab[1] += da;
    bb[0] += db; bb[1] += db;
  };
  auto ring = [&]() {
    sa = (sa == 2) ? 0 : sa + 1;
    sb = (sb == 3) ? 0 : sb + 1;
    da = (sa == 2) ? -2 * (int)XS : (int)XS;
    db = (sb == 3) ? -3 * 24576 : 24576;
  };
#pragma unroll 1
  for (int t = 14; t > 0; --t) {
    SCHED;
    rds(1, 1); mms(0);
    step();
    ilv2();
    SCHED;
    __builtin_amdgcn_s_waitcnt(0x0070);
    TRC(tslot); tslot++; BAR;
    SCHED;
    rds(0, 0);
    ring();
    mms(1); ilv3();
  }
  SCHED;
  f32x4 cs[6];
  float nmean[4], rstd[4];
  auto fold = [&](int n) {
#pragma unroll
    for (int mm = 0; mm < 4; ++mm) {
      const int m = (n & 1) ? 3 - mm : mm;
#pragma unroll
      for (int e = 0; e < 4; ++e) { float t = __builtin_fmaf(nmean[m], cs[n][e], acc[m][n][e]); asm("" : "+v"(t)); acc[m][n][e] = t; }
    }
  };
  {
    const unsigned pm = lds0 + L_MEAN + (16 + wm * 64 + fr) * 4, pc = lds0 + L_CS + ((isb ? 192 : 0) + h * 32 + fq * 4) * 4;
#pragma unroll
    for (int m = 0; m < 4; ++m) nmean[m] = lds_ld<float>(pm + m * 64);
#pragma unroll
    for (int n = 0; n < 6; ++n) cs[n] = lds_ld<f32x4>(pc + ((n >> 1) * 64 + (n & 1) * 16) * 4);
    rds(1, 1); mms(0);
    step();
    fold(0); fold(1);
    SGB(0x002, 6);
#pragma unroll
    for (int i = 0; i < 8; ++i) { SGB(0x008, 1); SGB(0x100, 2); }
#pragma unroll
    for (int i = 0; i < 10; ++i) { SGB(0x008, 1); SGB(0x100, 1); SGB(0x002, 2); }
#pragma unroll
    for (int i = 0; i < 6; ++i) { SGB(0x008, 1); SGB(0x002, 3); }
    SCHED;
    __builtin_amdgcn_s_waitcnt(0xC07F);
    TRC(16); BAR;
    SCHED;
    rds(0, 0);
    fold(5); mms(1); fold(2);
#pragma unroll
    for (int i = 0; i < 16; ++i) { SGB(0x008, 1); SGB(0x100, 1); SGB(0x002, 1); }
#pragma unroll
    for (int i = 0; i < 8; ++i) { SGB(0x008, 1); SGB(0x002, 2); }
    SCHED;
    rds(1, 1);
#pragma unroll
    for (int m = 0; m < 4; ++m) rstd[m] = lds_ld<float>(pm + (L_RSTD - L_MEAN) + m * 64);
    fold(4); mms(0); fold(3);
#pragma unroll
    for (int i = 0; i < 16; ++i) { SGB(0x008, 1); SGB(0x100, 1); SGB(0x002, 1); }
#pragma unroll
    for (int i = 0; i < 4; ++i) { SGB(0x008, 1); SGB(0x100, 1); SGB(0x002, 2); }
#pragma unroll
    for (int i = 0; i < 4; ++i) { SGB(0x008, 1); SGB(0x002, 2); }
  }
  SCHED;
  __builtin_amdgcn_s_waitcnt(0xC07F);
  TRC(17); BAR;
  SCHED;
  auto pack = [&](const f32x4& a, const f32x4& b) { return u32x4{pk2(a[0], a[1]), pk2(a[2], a[3]), pk2(b[0], b[1]), pk2(b[2], b[3])}; };
  auto mm6 = [&](int m) {
#pragma unroll
    for (int nn = 0; nn < 6; ++nn) { const int n = (nn + 4) % 6; acc[m][n] = MFMA(bf[1][n], af[1][m], acc[m][n]); }
  };
  auto ilv4 = [&]() {
#pragma unroll
    for (int i = 0; i < 3; ++i) { SGB(0x008, 1); SGB(0x002, 3); SGB(0x008, 1); SGB(0x002, 3); SGB(0x200, 1); }
  };
  auto fin = [&](auto isb_c) {
    constexpr bool ISB = decltype(isb_c)::value;
    u32x4 qk[4];
    int ln = lane; OPAQUE_V(ln);
    const int fr2 = ln & 15, fq2 = ln >> 4, hw = h * 2 + wm;
    constexpr unsigned d1s = ISB ? 16 * 80 : 1024, d2s = 16 * 80;
    const unsigned d1 = ISB ? lds0 + SC_IMG + h * (2 * 144 * 80) + (16 + wm * 64 + fr2) * 80 + fq2 * 16 : lds0 + SC_KF + hw * 2048 - 2048 + ln * 16;
    const unsigned d2 = ISB ? d1 + 144 * 80 : lds0 + SC_VIM + (h * 128 + wm * 64 + fr2) * 80 + fq2 * 16;
    const unsigned d0 = lds0 + sc_qf(ISB) + hw * 2048 - 2048 + ln * 16;
    auto out3 = [&](int m) {
      if (ISB) lds_st<u32x4>(d2 + m * d2s, pack(acc[m][4], acc[m][5]));
      else lds_st<u32x4>(d2 + m * d2s, pack(acc[m][4] * rstd[m], acc[m][5] * rstd[m]));
      if (m < 2) qk[m] = pack(acc[m][0], acc[m][1]);
      else lds_st<u32x4>(d0 + m * 1024, pack(acc[m][0], acc[m][1]));
      const u32x4 kk = pack(acc[m][2], acc[m][3]);
      if (m < 2) qk[2 + m] = kk;
      if (m >= (ISB ? 1 : 2)) lds_st<u32x4>(d1 + m * d1s, kk);
    };
    mm6(3);
#pragma unroll
    for (int i = 0; i < 6; ++i) { SGB(0x008, 1); SGB(0x002, 4); }
    SCHED;
    mm6(2);
    out3(3);
    ilv4();
    SCHED;
    if (ISB && wm) publish(pack(acc[3][2] * rstd[3], acc[3][3] * rstd[3]), pack(acc[3][4] * rstd[3], acc[3][5] * rstd[3]));
    SCHED;
    mm6(0);
    out3(2);
    ilv4();
    SCHED;
    if (!ISB) {
      WAITL0;
      TRC(18); BAR;
      SCHED;
    }
    mm6(1);
    out3(0);
    ilv4();
    SCHED;
    out3(1);
    SCHED;
    if (ISB) {
      WAITL0;
      TRC(18); BAR;
    }
    TRC(39); TRR(60);
    rest(isb_c, qk);
  };
  if (isb) fin(std::true_type{}); else fin(std::false_type{});
}

__device__ __forceinline__ void hedgehog_exp(const bf16x8& w0, const bf16x8& w1, u32x4 xin, float c, float (&ep)[8], float (&en)[8]) {
  const f32x4 zero = {0.f, 0.f, 0.f, 0.f};
  const f32x4 r0 = MFMA(w0, as_frag(xin), zero);
  const f32x4 r1 = MFMA(w1, as_frag(xin), zero);
  const float x[8] = {r0[0], r0[1], r0[2], r0[3], r1[0], r1[1], r1[2], r1[3]};
  float mx = fmaxf(fmaxf(fmaxf(fmaxf(x[0], x[1]), x[2]), fmaxf(fmaxf(x[3], x[4]), x[5])), fmaxf(x[6], x[7]));
  float nm = -fminf(fminf(fminf(fminf(x[0], x[1]), x[2]), fminf(fminf(x[3], x[4]), x[5])), fminf(x[6], x[7]));
  red2_max(mx, nm);
  const float mxl = mx * -c, mnl = nm * -c;
#pragma unroll
  for (int i = 0; i < 8; ++i) {
    ep[i] = __builtin_amdgcn_exp2f(__builtin_fmaf(x[i], c, mxl));
    en[i] = __builtin_amdgcn_exp2f(__builtin_fmaf(x[i], -c, mnl));
  }
}
__device__ __forceinline__ u32x4 pack8(const float (&e)[8], float f) {
  return u32x4{pk2(e[0] * f, e[1] * f), pk2(e[2] * f, e[3] * f), pk2(e[4] * f, e[5] * f), pk2(e[6] * f, e[7] * f)};
}
__device__ __forceinline__ u32x4 pack8(const float (&e)[8]) { return u32x4{pk2(e[0], e[1]), pk2(e[2], e[3]), pk2(e[4], e[5]), pk2(e[6], e[7])}; }

__device__ __forceinline__ void store_o(rsrc_t rs, unsigned tile_off, int mt, int h, int lane, u32x2 X, u32x2 Y) {
  st16_sc1(rs, lane * 16, tile_off + (unsigned)(mt * 2 + h) * 1024, nosent(u32x4{X[0], X[1], Y[0], Y[1]}));
}

__device__ __forceinline__ void job_colsum(rsrc_t rs, const u32x4 (&w)[8], int mat, int g, int jc, int hw, int lane) {
  const int fr = lane & 15;
  const unsigned one = (fr < 8) ? (0x3f80u << (16 * (fr & 1))) : 0u;
  const int e = (fr >> 1) & 3;
  const bf16x8 sel = as_frag(u32x4{e == 0 ? one : 0u, e == 1 ? one : 0u, e == 2 ? one : 0u, e == 3 ? one : 0u});
  f32x4 c = {0.f, 0.f, 0.f, 0.f};
#pragma unroll
  for (int i = 0; i < 8; ++i) c = MFMA(sel, as_frag(w[i]), c);
  u32x4 w4;
#pragma unroll
  for (int k = 0; k < 4; ++k) {
    const float a = c[k];
    w4[k] = __float_as_uint(a + __int_as_float(__builtin_amdgcn_update_dpp(0, __float_as_int(a), 0x128  , 0xf, 0xf, false)));
  }
  unsigned so = WS_CS + (unsigned)(((mat * 16 + jc) * 16 + g) * 768);
  OPAQUE_S(so);
  if ((lane & 40) == 0) st16_sc1(rs, (unsigned)(hw * 64 + (lane & 7) * 8 + (lane >> 4) * 4) * 4, so, w4);
}
__device__ __forceinline__ unsigned img_off(int i, int lane) { return (lane >> 3) * 128 + (((lane & 7) ^ ((((lane >> 4) & 1) | ((i & 1) << 1)) << 1)) * 16); }
__device__ __forceinline__ void job_pack(rsrc_t rs, u32x4 w, int i, int mat, int g, int jc, int hw, int lane) {
  st16_sc1(rs, img_off(i, lane),
           WS_WP + (unsigned)(((mat * 16 + jc) * 16 + g) * 24576 + hw * 8192 + i * 1024), w);
}

__global__ __launch_bounds__(512, 2) void mega_kernel(Params p) {
  extern __shared__ __attribute__((aligned(16))) char smem[];
  const int tid = threadIdx.x;
  const int wave0 = __builtin_amdgcn_readfirstlane(tid >> 6);
  const int xx = blockIdx.x & 7, rr = blockIdx.x >> 3;
  const int g0 = (rr >> 2) * 2 + (xx & 1), j0 = (xx >> 1) * 4 + (rr & 3);
  const unsigned lds00 = (unsigned)(size_t)smem;
#define LOCALS0 int g = g0, j = j0, l = l0, wave = wave0; unsigned lds0 = lds00;                       \
  OPAQUE_S(g); OPAQUE_S(j); OPAQUE_S(l); OPAQUE_S(wave); OPAQUE_S(lds0);                               \
  const int hw = wave & 3, wm = wave & 1, h = (wave >> 1) & 1;                                         \
  const unsigned myrep = (unsigned)((j >> 2) * 2 + (g & 1)) * REP_STRIDE;                              \
  (void)hw; (void)wm; (void)h; (void)myrep;                                                            \
  int lane = tid; OPAQUE_V(lane); lane &= 63
#define LOCALS LOCALS0; const bool isb = l & 1, lead = (wave == 4); const int half = wave >> 2; (void)isb; (void)lead; (void)half
  const rsrc_t rs = mk_rsrc(p.ws, WS_SPAN);
  const bool cw = wave0 < 4;
  if (wave0 == 2) touch(mk_rsrc(p.ws, NREP * REP_STRIDE), min(blockIdx.x * 9 + ((tid & 63) < 9 ? (tid & 63) : 0), NREP * REP_STRIDE / 128 - 1) * 128, lds00 + L_SINK + 512);
  if (wave0 == 3) {
    const char* pc;
    unsigned len;
    asm volatile("s_getpc_b64 %0\n.Lpf_base:\n\ts_mov_b32 %1, .Lpf_end-.Lpf_base" : "=s"(pc), "=s"(len));
    const unsigned off = ((unsigned)rr * 16 + (tid & 15)) * 128;
    touch(mk_rsrc(pc, len), ((int)len > 0 && off + 4 <= len) ? off : 0u, lds00 + L_SINK);
  }
  int trv = 0, trA = 0, trB = 0, trC = 0;
  u32x2 epoch = {0u, 0u};
  bool dead = false;
  STAMP_INIT;

#pragma unroll 1
  for (int l0 = 0; l0 < 4; ++l0) {
    TRC(0); TRR(62);
    unsigned ready = ~0u;
    u32x4 xr[2][2];
    const char* a15p = p.ws;
    unsigned a15v = 0, a15s = 0, a15d = 0;
    auto attention = [&](auto half_c, auto isb_c, const u32x4 (&qk)[4]) {
      LOCALS0;
      constexpr int half = decltype(half_c)::value;
      constexpr bool isb = decltype(isb_c)::value;
      const bool lead = half && wave == 4;
      const int fr = lane & 15, fq = lane >> 4;
      const unsigned otile = ob_off(g, j, l);
      const f32x4 zero = {0.f, 0.f, 0.f, 0.f};
      u32x2 clf;
      const unsigned clo = myrep + ((lane & 16) ? fl_line(FL_CLC + l, j) : fl_line(FL_CLR + l, g)) + fr * 8;
      auto clcheck = [&]() {
        if constexpr (half) {
          OPAQUE_V(clf);
          if (__builtin_expect(!__all(is_set(clf, epoch)), 0)) poll_all(rs, clo, epoch, dead);
        }
      };
      auto tail = [&](auto& wst) {
        constexpr int P0 = isb ? 4 : 3, N0 = isb ? PB0 : PA0, N1 = isb ? PB1 : PA1, N2 = isb ? PB2 : PA2, N3 = (isb ? 12 : 16) - N0 - N1 - N2;
        const rsrc_t rw = mk_rsrc(isb ? p.woB : p.woA, 1024 * 2048);
        const unsigned vw = (unsigned)((hw * 8 + (lane >> 3)) * 2048) + (((lane & 7) ^ ((((lane >> 4) & 1) | (hw & 2)) << 1)) * 16) + j * 128;
        auto park = [&](int a, int n) {
#pragma unroll
          for (int i = a; i < a + n; ++i) wst[i] = __builtin_amdgcn_raw_buffer_load_b128(rw, vw, wo_src(P0 + (i >> 2), j) + (i & 3) * (32 * 2048), 0);
          SCHED;
        };
        auto stO = [&](int i) { dma4<4096>(rw, vw, wo_src(i, j), 32 * 2048, lds0 + wo_slot(i) + hw * 1024); };
        if (__builtin_expect(ready != ~0u, 0))
          poll_all(rs, myrep + ((l == 0 && fq) ? fl_line(FL_CS, JOBC(j, fq - 1)) : fl_line(FL_X + l, g)) + fr * 8, epoch, dead);
        f32x4 js[4], jc[4];
#pragma unroll
        for (int i = 0; i < 4; ++i) asm volatile("" : "=v"(jc[i]));
        if (l < 2) {
#pragma unroll
          for (int i = 0; i < 4; ++i) jc[i] = ld4f_sc1(rs, fq * 768 + hw * 192 + (fr < 12 ? fr : 0) * 16, WS_CS + (unsigned)(((isb ? 16 : 0) + j) * 16 + i * 4) * 768);
        }
#pragma unroll
        for (int i = 0; i < 4; ++i) js[i] = ld4f_sc1(rs, fq * 1024 + hw * 256 + fr * 16, st_off(g, l) + i * 4096);
        SCHED;
        WAITV(16 + 4);
        TRC(14); BAR;
        WAITV(0);
        TRC(41);
#pragma unroll
        for (int i = 0; i < 4; ++i) { OPAQUE_V(js[i]); OPAQUE_V(jc[i]); }
        if (l < 2) {
          const f32x4 s = (jc[0] + jc[1]) + (jc[2] + jc[3]);
          float a = s[0], b = s[1], c = s[2], d = s[3];
          red2_add(a, b); red2_add(c, d);
          if (lane < 12) lds_st<f32x4>(lds0 + L_CS + (isb ? 768 : 0) + hw * 192 + lane * 16, f32x4{a, b, c, d});
        }
        {
          const f32x4 s = (js[0] + js[1]) + (js[2] + js[3]);
          float a = s[0], b = s[1], c = s[2], d = s[3];
          red2_add(a, b); red2_add(c, d);
          const float m0 = a * (1.0f / 1024), m1 = c * (1.0f / 1024);
          const float r0 = __builtin_amdgcn_rsqf(fmaxf(__builtin_fmaf(b, 1.0f / 1024, -m0 * m0), 0.f) + 1e-5f);
          const float r1 = __builtin_amdgcn_rsqf(fmaxf(__builtin_fmaf(d, 1.0f / 1024, -m1 * m1), 0.f) + 1e-5f);
          if (lane < 16) {
            lds_st<f32x2>(lds0 + L_MEAN + 64 + hw * 128 + fr * 8, f32x2{-m0, -m1});
            lds_st<f32x2>(lds0 + L_RSTD + 64 + hw * 128 + fr * 8, f32x2{r0, r1});
          }
        }
        SCHED;
        TRC(42);
        dma4<4096>(mk_rsrc(a15p, l == 0 ? 2048 * 2048 : WS_SPAN), a15v, a15s, a15d, lds0 + L_R2 + hw * 1024);
        stO(2);
        park(0, N0);
        WAITL0;
        TRC(15); BAR;
        if (lead) set_flags(rs, fl_line(FL_CLR + l, g), j, fl_line(FL_CLC + l, j), g, epoch, lane);
        stO(0);
        if (isb) stO(3);
        park(N0, N1);
        WAITV(8 + N0 + N1 + (isb ? 4 : 0));
        TRC(16); BAR;
        stO(1);
        park(N0 + N1, N2);
        TRC(17); BAR;
        stO(7);
        park(N0 + N1 + N2, N3);
        ld8_asm(clf, rs, clo);
        __builtin_amdgcn_s_setprio(0);
        TRC(18); BAR;
      };
      if constexpr (!isb) {
        u32x4 wst[16];
        u32x4 vb[8];
        if constexpr (half) tail(wst);
        if constexpr (!half) {
#pragma unroll
          for (int i = 0; i < 8; ++i) asm volatile("" : "=v"(vb[i]));
          if (l == 0) {
            const bool wj = hw != 0;
            const char* bp = (const char*)(wj ? p.wqkvB : p.woB);
            int j2 = j0;
            OPAQUE_S(j2);
            unsigned rs_ = wj ? 6144 : 2048, sz = rs_ * 1024, sb = g * 64 * rs_ + (wj ? (hw - 1) * 2048 + JOBC(j2, hw - 1) * 128 : (j2 ^ 4) * 128), ss = rs_ * 8;
            OPAQUE_S(bp); OPAQUE_S(rs_); OPAQUE_S(sz); OPAQUE_S(sb); OPAQUE_S(ss);
            const rsrc_t rb = mk_rsrc(bp, sz);
#pragma unroll
            for (int i = 0; i < 8; ++i) {
              if (wj) vb[i] = __builtin_amdgcn_raw_buffer_load_b128(rb, (lane >> 3) * rs_ + (lane & 7) * 16, sb + i * ss, 2);
              else vb[i] = __builtin_amdgcn_raw_buffer_load_b128(rb, (lane >> 3) * rs_ + (lane & 7) * 16, sb + i * ss, 0);
            }
          }
          SCHED;
        }
        u32x4 qin[2];
#pragma unroll
        for (int i = 0; i < 2; ++i) qin[i] = half ? lds_ld<u32x4>(lds0 + sc_qf(false) + hw * 2048 + i * 1024 + lane * 16) : qk[i];
        float cq[2];
#pragma unroll
        for (int i = 0; i < 2; ++i) cq[i] = lds_ld<float>(lds0 + L_RSTD + (16 + wm * 64 + (half * 2 + i) * 16 + fr) * 4) * 1.4426950408889634f;
        const bf16x8 ones = {0x3f80, 0x3f80, 0x3f80, 0x3f80, 0x3f80, 0x3f80, 0x3f80, 0x3f80};
        const unsigned wa = lds0 + L_WPHI + h * 2048 + (fq * 4 + (fr >> 2)) * 64 + (lane & 3) * 8;
        const bf16x8 w0 = tr2(wa, wa + 1024);
        const bf16x8 w1 = tr2(wa + 32, wa + 32 + 1024);
#pragma unroll
        for (int i = 0; i < 2; ++i) {
          const int m = half * 2 + i;
          float ep[8], en[8];
          hedgehog_exp(w0, w1, half ? lds_ld<u32x4>(lds0 + SC_KF + hw * 2048 + i * 1024 + lane * 16) : qk[2 + i], cq[i], ep, en);
          const float sp = MFMA(ones, as_frag(pack8(ep)), zero)[0], sn = MFMA(ones, as_frag(pack8(en)), zero)[0];
          const u32x4 pp = pack8(ep, __builtin_amdgcn_rcpf(sp)), pn = pack8(en, __builtin_amdgcn_rcpf(sn));
          const unsigned a = lds0 + SC_PHIK + (h * 128 + wm * 64 + m * 16 + fr) * 160 + fq * 8;
          lds_st<u32x2>(a, u32x2{pp[0], pp[1]});
          lds_st<u32x2>(a + 32, u32x2{pp[2], pp[3]});
          lds_st<u32x2>(a + 64, u32x2{pn[0], pn[1]});
          lds_st<u32x2>(a + 96, u32x2{pn[2], pn[3]});
        }
        WAITL0;
        TRC(19); BAR;
        if constexpr (half) WAITV(0);
        clcheck();
        const int h2 = wave >> 2, dt = (wave >> 1) & 1, s = wave & 1;
        const unsigned kvb = WS_KVP + (unsigned)j * KVP_J + (unsigned)(l >> 1) * KVP_L + (unsigned)wave * 1024;
        const unsigned kzb = WS_KVP + (unsigned)j * KVP_J + (unsigned)(l >> 1) * KVP_L + 8192 + h2 * 256;
        {
          f32x4 c0 = zero, c1 = zero, z0 = zero, z1 = zero;
          const int trow = h2 * 128 + fq * 4 + (fr >> 2);
          const unsigned va = lds0 + SC_VIM + trow * 80 + dt * 32 + (lane & 3) * 8;
          const unsigned pa = lds0 + SC_PHIK + trow * 160 + s * 64 + (lane & 3) * 8;
#pragma unroll
          for (int ks = 0; ks < 4; ++ks) {
            const bf16x8 v = tr2(va + ks * 32 * 80, va + (ks * 32 + 16) * 80);
            const bf16x8 a0 = tr2(pa + ks * 32 * 160, pa + (ks * 32 + 16) * 160);
            const bf16x8 a1 = tr2(pa + 32 + ks * 32 * 160, pa + 32 + (ks * 32 + 16) * 160);
            c0 = MFMA(a0, v, c0);
            c1 = MFMA(a1, v, c1);
            z0 = MFMA(a0, ones, z0);
            z1 = MFMA(a1, ones, z1);
          }
          st16_sc1(rs, lane * 16, kvb + (unsigned)g * KVP_G, nosent(u32x4{pk2(c0[0], c0[1]), pk2(c0[2], c0[3]), pk2(c1[0], c1[1]), pk2(c1[2], c1[3])}));
          if (dt == 0 && fr < 2) st16_sc1(rs, fq * 16 + fr * 64, kzb + (unsigned)g * KVP_G + s * 128, nosent(__builtin_bit_cast(u32x4, fr ? z1 : z0)));
        }
        CLOBBER;
        TRC(20); BAR;
        u32x4 qp[2], qn[2];
        float sqp[2], sqn[2];
        auto phiq = [&](int i) {
          float ep[8], en[8];
          hedgehog_exp(w0, w1, qin[i], cq[i], ep, en);
          qp[i] = pack8(ep); qn[i] = pack8(en);
          sqp[i] = __builtin_amdgcn_rcpf(MFMA(ones, as_frag(qp[i]), zero)[0]);
          sqn[i] = __builtin_amdgcn_rcpf(MFMA(ones, as_frag(qn[i]), zero)[0]);
          OPAQUE_V(qp[i]); OPAQUE_V(qn[i]); OPAQUE_V(sqp[i]); OPAQUE_V(sqn[i]);
        };
        phiq(0); phiq(1);
        TRC(21);
        {
          u32x4 v[16], zr[4];
          const bool doz = (wave & 3) == 0;
          SCHED;
#pragma unroll
          for (int i = 0; i < 4; ++i) zr[i] = u32x4{0u, 0u, 0u, 0u};
          if (doz) {
#pragma unroll
            for (int i = 0; i < 4; ++i) zr[i] = ld16_sc1(rs, (lane & 15) * 16 + (lane >> 4) * (4 * KVP_G), kzb + i * KVP_G);
          }
#pragma unroll
          for (int gg = 0; gg < 16; ++gg) {
            v[gg] = ld16_sc1(rs, lane * 16, kvb + gg * KVP_G);
            SCHED;
            if constexpr (half) {
              lds_st<u32x4>(lds0 + wo_slot(3 + (gg >> 2)) + ((gg & 3) * 4 + hw) * 1024 + lane * 16, wst[gg]);
              SCHED;
            }
          }
          TRC(22);
          const unsigned one = (fq == (fr >> 2)) ? (0x3f80u << (16 * (fr & 1))) : 0u;
          const unsigned sa0 = (fr & 2) ? 0u : one, sa1 = (fr & 2) ? one : 0u;
          const bf16x8 sel0 = as_frag(u32x4{sa0, sa1, 0u, 0u}), sel1 = as_frag(u32x4{0u, 0u, sa0, sa1});
          f32x4 d0 = zero, d1 = zero;
          unsigned mall = umax4v(umax4v(umax4v(umax4v(0u, zr[0]), zr[1]), zr[2]), zr[3]);
#pragma unroll
          for (int gg = 0; gg < 16; ++gg) {
            d0 = MFMA(sel0, as_frag(v[gg]), d0); d1 = MFMA(sel1, as_frag(v[gg]), d1);
            mall = umax4v(mall, v[gg]);
            SGB(0x008, 1); SGB(0x002, 1); SGB(0x008, 1); SGB(0x002, 1);
          }
          SCHED;
          TRC(23);
          if (__builtin_expect(__any(mall == SENT), 0)) {
            for (unsigned spins = 0; !dead; ++spins) {
              if (spins > SPIN_CAP) { dead = true; break; }
              CLOBBER;
              bool again = false;
#pragma unroll
              for (int gg = 0; gg < 16; ++gg)
                if (__any(umax4v(0u, v[gg]) == SENT)) { v[gg] = ld16_sc1(rs, lane * 16, kvb + gg * KVP_G); again = true; }
              if (__any(umax4v(umax4v(umax4v(umax4v(0u, zr[0]), zr[1]), zr[2]), zr[3]) == SENT)) {
#pragma unroll
                for (int i = 0; i < 4; ++i) zr[i] = ld16_sc1(rs, (lane & 15) * 16 + (lane >> 4) * (4 * KVP_G), kzb + i * KVP_G);
                again = true;
              }
              if (!again) break;
              WAITV(0);
            }
            d0 = zero; d1 = zero;
#pragma unroll
            for (int gg = 0; gg < 16; ++gg) { d0 = MFMA(sel0, as_frag(v[gg]), d0); d1 = MFMA(sel1, as_frag(v[gg]), d1); }
#pragma unroll
            for (int i = 0; i < 4; ++i) OPAQUE_V(zr[i]);
          }
          f32x4 z[4];
#pragma unroll
          for (int i = 0; i < 4; ++i) z[i] = __builtin_bit_cast(f32x4, zr[i]);
          lds_st<u32x4>(lds0 + SC_KVF + wave * 1024 + lane * 16, u32x4{pk2(d0[0], d0[1]), pk2(d0[2], d0[3]), pk2(d1[0], d1[1]), pk2(d1[2], d1[3])});
          if (doz) {
            f32x4 zs = (z[0] + z[1]) + (z[2] + z[3]);
#pragma unroll
            for (int e = 0; e < 4; ++e) zs[e] = red_add4(zs[e]);
            if (lane < 16) {
              const int ft = lane >> 2;
              lds_st<u32x2>(lds0 + SC_ZF + ((h2 * 2 + (ft >> 1)) * 4 + (lane & 3)) * 16 + (ft & 1) * 8, u32x2{pk2(zs[0], zs[1]), pk2(zs[2], zs[3])});
            }
          }
          SCHED;
          if constexpr (!half) {
#pragma unroll
            for (int i = 0; i < 8; ++i) OPAQUE_V(vb[i]);
            if (l == 0 && hw) {
              job_colsum(rs, vb, 1, g, JOBC(j, hw - 1), hw - 1, lane);
#pragma unroll
              for (int i = 0; i < 8; ++i) job_pack(rs, vb[i], i, 1, g, JOBC(j, hw - 1), hw - 1, lane);
            }
          }
          WAITL0;
        }
        TRC(24); BAR;
        bf16x8 kf[2][2], zf[2];
#pragma unroll
        for (int d = 0; d < 2; ++d)
#pragma unroll
          for (int sg = 0; sg < 2; ++sg) kf[d][sg] = lds_ld<bf16x8>(lds0 + SC_KVF + (h * 4 + d * 2 + sg) * 1024 + lane * 16);
#pragma unroll
        for (int sg = 0; sg < 2; ++sg) zf[sg] = lds_ld<bf16x8>(lds0 + SC_ZF + ((h * 2 + sg) * 4 + fq) * 16);
#pragma unroll
        for (int i = 0; i < 2; ++i) {
          const float dp = MFMA(zf[0], as_frag(qp[i]), zero)[0], dn = MFMA(zf[1], as_frag(qn[i]), zero)[0];
          const f32x4 p0 = MFMA(kf[0][0], as_frag(qp[i]), zero), n0 = MFMA(kf[0][1], as_frag(qn[i]), zero);
          const f32x4 p1 = MFMA(kf[1][0], as_frag(qp[i]), zero), n1 = MFMA(kf[1][1], as_frag(qn[i]), zero);
          const float inv = __builtin_amdgcn_rcpf(__builtin_fmaf(dp, sqp[i], __builtin_fmaf(dn, sqn[i], 1e-6f)));
          const float fa = sqp[i] * inv, fb = sqn[i] * inv;
          const f32x4 o0 = p0 * fa + n0 * fb, o1 = p1 * fa + n1 * fb;
          const u32x2 X = {pk2(o0[0], o0[1]), pk2(o0[2], o0[3])}, Y = {pk2(o1[0], o1[1]), pk2(o1[2], o1[3])};
          store_o(rs, otile, wm * 4 + half * 2 + i, h, lane, X, Y);
        }
        TRC(25); BAR;
      } else {
        u32x4 wst[12];
        if constexpr (half) tail(wst);
        const unsigned kim = lds0 + SC_IMG + h * (2 * 144 * 80), vim = kim + 144 * 80;
        const float scale = 0.17677669529663687f, L2E = 1.4426950408889634f;
        bf16x8 qf2[2], kf3[3];
        s16x4 vh[3][2];
        u32x4 hk, hv;
        const bool halo = !half && wm == 0 && g > 0;
        const unsigned hso = WS_KVP + (unsigned)j * KVP_J + (unsigned)(g - 1) * KVP_G + (unsigned)(l >> 1) * KVP_L + KVP_HALO + h * 2048;
#pragma unroll
        for (int i = 0; i < 3; ++i) {
          kf3[i] = (half || i == 0) ? lds_ld<bf16x8>(kim + (wm * 64 + (half * 2 + i) * 16 + fr) * 80 + fq * 16) : as_frag(qk[1 + i]);
          const unsigned va = vim + (wm * 64 + (half * 2 + i) * 16 + fq * 4 + (fr >> 2)) * 80 + (lane & 3) * 8;
          vh[i][0] = __builtin_amdgcn_ds_read_tr16_b64_v4i16((lds_s16x4_ptr)(va));
          vh[i][1] = __builtin_amdgcn_ds_read_tr16_b64_v4i16((lds_s16x4_ptr)(va + 32));
        }
#pragma unroll
        for (int i = 0; i < 2; ++i) qf2[i] = half ? lds_ld<bf16x8>(lds0 + sc_qf(true) + hw * 2048 + i * 1024 + lane * 16) : as_frag(qk[i]);
        const int par = fr & 1;
        float rk[3][2], rq[2];
#pragma unroll
        for (int i = 0; i < 3; ++i)
#pragma unroll
          for (int e = 0; e < 2; ++e) rk[i][e] = lds_ld<float>(lds0 + L_RSTD + (wm * 64 + (half * 2 + i) * 16 + fq * 4 + par + 2 * e) * 4);
#pragma unroll
        for (int i = 0; i < 2; ++i) rq[i] = lds_ld<float>(lds0 + L_RSTD + (16 + wm * 64 + (half * 2 + i) * 16 + fr) * 4) * (scale * L2E);
        if constexpr (!half) {
          asm volatile("" : "=v"(hk)); asm volatile("" : "=v"(hv));
          if (halo) { hk = ld16_sc1(rs, lane * 16, hso); hv = ld16_sc1(rs, lane * 16, hso + 1024); }
          SCHED;
        }
#pragma unroll
        for (int ii = 0; ii < 2; ++ii) {
          const int i = half ? ii : 1 - ii;
          if (!half && i == 0) {
            SCHED;
            OPAQUE_V(hk); OPAQUE_V(hv);
            if (wm == 0) {
              kf3[0] = bf16x8{0, 0, 0, 0, 0, 0, 0, 0}; vh[0][0] = s16x4{0, 0, 0, 0}; vh[0][1] = s16x4{0, 0, 0, 0};
              if (g > 0) {
                for (unsigned spins = 0; !dead && __any(umax4v(umax4v(0u, hk), hv) == SENT); ++spins) {
                  if (spins > SPIN_CAP) { dead = true; break; }
                  CLOBBER;
                  hk = ld16_sc1(rs, lane * 16, hso); hv = ld16_sc1(rs, lane * 16, hso + 1024);
                  OPAQUE_V(hk); OPAQUE_V(hv);
                }
                kf3[0] = as_frag(hk);
                lds_st<u32x4>(lds0 + SC_HV + h * 1280 + fr * 80 + fq * 16, hv);
                WAITL0;
                const unsigned va = lds0 + SC_HV + h * 1280 + (fq * 4 + (fr >> 2)) * 80 + (lane & 3) * 8;
                vh[0][0] = __builtin_amdgcn_ds_read_tr16_b64_v4i16((lds_s16x4_ptr)(va));
                vh[0][1] = __builtin_amdgcn_ds_read_tr16_b64_v4i16((lds_s16x4_ptr)(va + 32));
              }
            }
          }
          const int m = half * 2 + i;
          const int r0 = wm * 64 + m * 16;
          const bf16x8 qf = qf2[i], k0 = kf3[i], k1 = kf3[i + 1];
          const bf16x8 v0 = __builtin_shufflevector(vh[i][0], vh[i + 1][0], 0, 1, 2, 3, 4, 5, 6, 7);
          const bf16x8 v1 = __builtin_shufflevector(vh[i][1], vh[i + 1][1], 0, 1, 2, 3, 4, 5, 6, 7);
          f32x4 s0 = MFMA(k0, qf, zero);
          f32x4 s1 = MFMA(k1, qf, zero);
          const bool has_prev = (g > 0) || (r0 > 0);
          float x[4] = {par ? s0[1] : s0[0], par ? s0[3] : s0[2], par ? s1[1] : s1[0], par ? s1[3] : s1[2]};
#pragma unroll
          for (int e = 0; e < 4; ++e) {
            const int delta = fr + 16 - (e >> 1) * 16 - fq * 4 - par - (e & 1) * 2;
            const bool ok = (delta >= 0) && (delta <= 14) && ((e >= 2) || has_prev);
            x[e] = ok ? x[e] * rk[i + (e >> 1)][e & 1] : -3.0e38f;
          }
          const float mx = red_max4(fmaxf(fmaxf(x[0], x[1]), fmaxf(x[2], x[3])));
          const float mxl = mx * -rq[i];
          float ex[4];
#pragma unroll
          for (int e = 0; e < 4; ++e) ex[e] = __builtin_amdgcn_exp2f(__builtin_fmaf(x[e], rq[i], mxl));
          const float inv = __builtin_amdgcn_rcpf(red_add4((ex[0] + ex[1]) + (ex[2] + ex[3])));
          u32x4 pf;
#pragma unroll
          for (int e = 0; e < 4; ++e) pf[e] = pk2(ex[e] * inv * rk[i + (e >> 1)][e & 1], 0.f) << (16 * par);
          f32x4 o0 = MFMA(v0, as_frag(pf), zero);
          f32x4 o1 = MFMA(v1, as_frag(pf), zero);
          if (half && i == 0) {
            WAITV(0);
#pragma unroll
            for (int k = 0; k < 12; ++k) OPAQUE_V(wst[k]);
            OPAQUE_V(clf);
          }
          store_o(rs, otile, wm * 4 + m, h, lane, u32x2{pk2(o0[0], o0[1]), pk2(o0[2], o0[3])}, u32x2{pk2(o1[0], o1[1]), pk2(o1[2], o1[3])});
        }
        clcheck();
        TRC(25); BAR;
        if constexpr (half) {
#pragma unroll
          for (int k = 0; k < 12; ++k) lds_st<u32x4>(lds0 + wo_slot(4 + (k >> 2)) + ((k & 3) * 4 + hw) * 1024 + lane * 16, wst[k]);
          WAITL0;
          if (lane == 0) __hip_atomic_fetch_add((LAS unsigned*)(lds0 + L_CNT), 1u, __ATOMIC_RELAXED, __HIP_MEMORY_SCOPE_WORKGROUP);
        }
      }
    };
    if (cw) {
      if (l0 == 0) {
        LOCALS;
        u32x4 v[2][8];
        if (wave < 3) {
          const int jc = JOBC(j, wave), kt = (JROT(j) + 8 * (g & 1) + (g >> 1)) & 15;
          const rsrc_t ra = mk_rsrc(p.wqkvA, 1024 * 6144);
#pragma unroll
          for (int i = 0; i < 8; ++i)
            v[0][i] = __builtin_amdgcn_raw_buffer_load_b128(ra, (lane >> 3) * 6144 + (lane & 7) * 16, (kt * 64 + i * 8) * 6144 + wave * 2048 + jc * 128, 0);
          SCHED;
          if ((jc & 7) == 3) touch(ra, ((kt ^ 8) * 64 + lane) * 6144 + wave * 2048 + jc * 128, lds0 + L_SINK + 256);
          TRC(44); BAR; SCHED;
          job_colsum(rs, v[0], 0, g, jc, wave, lane);
        } else {
          const int fr = lane & 15, fq = lane >> 4;
          const rsrc_t rx = mk_rsrc(p.x, 2048 * 2048);
#pragma unroll
          for (int i = 0; i < 16; ++i)
            v[i & 1][i >> 1] = __builtin_amdgcn_raw_buffer_load_b128(rx, fr * 2048 + fq * 16, (g * 128 + (i >> 1) * 16) * 2048 + ((j + 8 * (g & 1)) & 15) * 128 + (i & 1) * 64, 0);
          SCHED;
          TRC(44); BAR; SCHED;
          const f32x4 zero = {0.f, 0.f, 0.f, 0.f};
          const bf16x8 ones = {0x3f80, 0x3f80, 0x3f80, 0x3f80, 0x3f80, 0x3f80, 0x3f80, 0x3f80};
          float sm[8], q[8];
#pragma unroll
          for (int m = 0; m < 8; ++m) {
            const f32x4 a = MFMA(ones, as_frag(v[1][m]), MFMA(ones, as_frag(v[0][m]), zero));
            const f32x4 b = MFMA(as_frag(v[1][m]), as_frag(v[1][m]), MFMA(as_frag(v[0][m]), as_frag(v[0][m]), zero));
            sm[m] = a[0];
            q[m] = (fr & 2) ? ((fr & 1) ? b[3] : b[2]) : ((fr & 1) ? b[1] : b[0]);
          }
          if (fq == (fr >> 2)) {
#pragma unroll
            for (int m = 0; m < 8; ++m) st8_sc1(rs, (m * 16 + fr) * 8, st_off(g, 0) + j * 1024, u32x2{__float_as_uint(sm[m]), __float_as_uint(q[m])});
          }
        }
      }
      LOCALS;
      qkv_loop(isb, lds0, wm, h, lane, l == 0, trv,
               [&](u32x4 kk, u32x4 vv) {
                 LOCALS;
                 const unsigned so = WS_KVP + (unsigned)j * KVP_J + (unsigned)g * KVP_G + (unsigned)(l >> 1) * KVP_L + KVP_HALO + h * 2048;
                 st16_sc1(rs, lane * 16, so, nosent(kk));
                 st16_sc1(rs, lane * 16, so + 1024, nosent(vv));
               },
               [&](auto isb_c, const u32x4 (&qk)[4]) {
                 attention(std::integral_constant<int, 0>{}, isb_c, qk);
               });
#pragma unroll
      for (int i = 0; i < 4; ++i) asm volatile("" : "=v"(xr[i >> 1][i & 1]));
    } else {
      __builtin_amdgcn_s_setprio(3);
      LOCALS;
      const unsigned lin = lane * 16, vlin = lin + hw * 1024;
      const int jr = JROT(j);
      u32x4 sent4 = {SENT, SENT, SENT, SENT};
      OPAQUE_V(sent4);
      const unsigned cb0 = ob_off(g, j, l), cb2 = xb_off(g, l) + j * 16384 - 8 * 4096;
      const unsigned cb1 = WS_KVP + (unsigned)j * KVP_J + (unsigned)g * KVP_G + (unsigned)(l >> 1) * KVP_L - 4 * 4096;
      int nis = 0, sA[16], sW[16];
      auto need = [&](int t) { WAITV(nis - (sA[t] > sW[t] ? sA[t] : sW[t])); };
      auto ldres = [&](int slot) {
        const int fr = lane & 15, fq = lane >> 4, ch = (fq & 1) * 2 + (fq >> 1);
#pragma unroll
        for (int i = 0; i < 4; ++i)
          xr[i >> 1][i & 1] = lds_ld<u32x4>(lds0 + L_R2 + slot * XS + (hw * 32 + ((i >> 1) ^ 1) * 16 + fr) * 128 + (((ch + 4 * (i & 1)) ^ ((fr >> 1) & 7)) * 16));
      };
      auto aslot = [&](int t) { return lds0 + L_R2 + (t % 3) * XS + hw * 1024; };
      auto wslot = [&](int t) { return lds0 + L_R1 + (t & 3) * 24576 + hw * 1024; };
      if (l == 0) {
        const rsrc_t rx = mk_rsrc(p.x, 2048 * 2048), ra = mk_rsrc(p.wqkvA, 1024 * 6144);
        const unsigned va = (hw * 8 + (lane >> 3)) * 2048 + (((lane & 7) ^ (((hw & 1) * 4 + (lane >> 4)) & 7)) * 16);
        const unsigned vq = (hw * 8 + (lane >> 3)) * 6144 + (((lane & 7) ^ ((((lane >> 4) & 1) | ((hw & 1) << 1)) << 1)) * 16);
        const int k0 = jr + 8 * (g & 1);
        auto A0 = [&](int t, unsigned d) { dma4<4096, POL0>(rx, va, g * (128 * 2048) + ((k0 + t) & 15) * 128, 32 * 2048, d); };
        auto W0 = [&](int t, unsigned d) {
          const unsigned so = ((k0 + t) & 15) * (64 * 6144) + j * 128;
          dma3<8192, POL0>(ra, vq, so, 2048, d);
          dma3<8192, POL0>(ra, vq, so + 32 * 6144, 2048, d + 4096);
        };
        u32x2 gw2, fl;
        ld8_asm(gw2, rs, WS_EPOCH + 0u * lin);
        TRC(44); BAR;
        W0(0, wslot(0)); A0(0, aslot(0));
        W0(1, wslot(1)); A0(1, aslot(1));
        if (lane < 16) lds_st<float>(lds0 + L_RSTD + lane * 4, 1.0f);
        lds_st<unsigned>(lds0 + L_CNT, 0u);
#pragma unroll
        for (int i = 0; i < 4; ++i) asm volatile("" : "=v"(xr[i >> 1][i & 1]));
        WAITV(10);
        OPAQUE_V(gw2);
        TRC(1); BAR;
        W0(2, wslot(2));
        dma1(mk_rsrc(p.wphi, 32 * 2048), vlin, j * 4096, lds0 + L_WPHI + hw * 1024);
        {
          const u64 gword = ((u64)(unsigned)UNI(gw2[1]) << 32) | (unsigned)UNI(gw2[0]);
          u64 z = gword + 0x9e3779b97f4a7c15ull;
          z = (z ^ (z >> 30)) * 0xbf58476d1ce4e5b9ull;
          z = (z ^ (z >> 27)) * 0x94d049bb133111ebull;
          z = z ^ (z >> 31);
          z = (z == gword) ? ~z : z;
          epoch = u32x2{(unsigned)z, (unsigned)(z >> 32)};
          lds_st<u32x2>(lds0 + L_EP, epoch);
        }
        const int tc = (g >> 1) + 3, tr = (j & 3) + 8 * (g & 1);
        unsigned da = aslot(2), dw = wslot(3), ca = 0;
        auto jobs = [&](int t) {
          if (t == tc) {
            const unsigned src = lds0 + L_R1 + (t & 3) * 24576 + vlin;
            u32x4 w[6];
#pragma unroll
            for (int ii = 0; ii < 6; ++ii) w[ii] = lds_ld<u32x4>(src + ii * 4096);
#pragma unroll
            for (int ii = 0; ii < 6; ++ii) st16_sc1(rs, vlin, WS_WP + (unsigned)((j * 16 + ((k0 + t) & 15)) * 24576 + ii * 4096), w[ii]);
          }
          if (t == WOT && hw == 3) touch(mk_rsrc(p.woA, 1024 * 2048), (g * 64 + lane) * 2048 + (j ^ 5) * 128, lds0 + L_SINK + 256);
          if (t == tr) ldres(ca);
          WAITL0;
          ca = (ca == 2) ? 0 : ca + 1;
        };
        auto issue = [&](int t) {
          A0(t + 2, da);
          W0(t + 3, dw);
          da = (da == aslot(2)) ? aslot(0) : da + XS;
          dw = (dw == wslot(3)) ? wslot(0) : dw + 24576;
        };
        auto clean0 = [&](int t) { st16_sc1(rs, vlin, cb0 + (t >= 4 ? cb1 - cb0 : 0u) + (t >= 8 ? cb2 - cb1 : 0u) + (unsigned)t * 4096, sent4); };
#pragma unroll 1
        for (int t = 0; t <= 10; ++t) {
          if (t == 1 && lead) set_flags(rs, fl_line(FL_X, g), j, fl_line(FL_CS, j), g, epoch, lane);
          issue(t);
          clean0(t);
          jobs(t);
          if ((unsigned)(t - tc) < 2u) WAITV(23);
          else WAITV(17);
          TRC(2 + t); BAR;
        }
        ld8_asm(fl, rs, myrep + (lane >= 16 ? fl_line(FL_CS, JOBC(j, (lane >> 4) - 1)) : fl_line(FL_X, g)) + (lane & 15) * 8);
        issue(11);
        clean0(11);
        WAITV(11);
        OPAQUE_V(fl);
        ready = __all(is_set(fl, epoch)) ? ~0u : 0u;
        if (tr == 11) { ldres(ca); WAITL0; }
        TRC(13); BAR;
        issue(12);
        a15p = (const char*)p.x; a15v = va; a15s = g * (128 * 2048) + ((k0 + 15) & 15) * 128; a15d = 32 * 2048;
      } else {
        const unsigned sa = xb_off(g, l - 1), sw = WS_WP + (unsigned)((isb ? 16 : 0) + j) * (16 * 24576);
        const unsigned flo = myrep + fl_line(FL_X + l, g) + (lane & 15) * 8;
        const int dj = (j - jr) & 15;
        auto kseq = [&](int t) { return (jr + t - ((t <= dj) ? 1 : 0)) & 15; };
        auto ldA = [&](int k, unsigned d) { dma4<4096>(rs, vlin, sa + (unsigned)k * 16384, 4096, d); };
        auto ldW = [&](int k, unsigned d) {
          dma3<4096>(rs, vlin, sw + (unsigned)k * 24576, 4096, d);
          dma3<4096>(rs, vlin, sw + (unsigned)k * 24576 + 12288, 4096, d + 12288);
        };
        auto A1 = [&](int t) { ldA(kseq(t), aslot(t)); nis += 4; sA[t] = nis; };
        auto W1 = [&](int t) { ldW(kseq(t), wslot(t)); nis += 6; sW[t] = nis; };
        auto badA = [&](int t) {
          u32x4 v[4];
#pragma unroll
          for (int ii = 0; ii < 4; ++ii) v[ii] = lds_ld<u32x4>(aslot(t) + lin + ii * 4096);
          SCHED;
          return __any(umax4v(umax4v(umax4v(umax4v(0u, v[0]), v[1]), v[2]), v[3]) == SENT);
        };
        auto val = [&](int t) {
          CLOBBER;
          if (__builtin_expect(badA(t), 0)) {
            for (unsigned spins = 0; !dead; ++spins) {
              if (spins > SPIN_CAP) { dead = true; break; }
              WAITV(0);
              ldA(kseq(t), aslot(t));
              ldA(kseq(t + 1), aslot(t + 1));
              WAITV(0);
              if (!badA(t)) break;
              __builtin_amdgcn_s_sleep(1);
            }
          }
        };
        const unsigned cmask = 0xfu | (isb ? 0u : 0xf0u) | (l == 3 ? 0u : 0xf00u);
        auto clean = [&](int t) {
          if ((cmask >> t) & 1) st16_sc1(rs, vlin, cb0 + (t >= 4 ? cb1 - cb0 : 0u) + (t >= 8 ? cb2 - cb1 : 0u) + (unsigned)t * 4096, sent4);
        };
        sA[0] = sW[0] = sW[1] = 0;
        WAITV(6);
        WAITL0;
        TRC(1); BAR;
        if (WFIRST) W1(2);
        if (XD) __builtin_amdgcn_s_sleep(XD);
        A1(1); A1(2);
        TRC(40);
        if (!WFIRST) W1(2);
        W1(3);
        ldres(0);
        clean(0);
        need(1);
        val(1);
        WAITL0;
#pragma unroll
        for (int t = 1; t <= NVAL; ++t) {
          TRC(1 + t); BAR;
          if (t == 1 && lead) set_flags(rs, fl_line(FL_X + l, g), j, fl_line(FL_X + l, g), j, epoch, lane);
          if (t == NVAL) {
            const unsigned nd = 1u << kseq(t + 2);
            if (__builtin_expect((ready & nd) == 0, 0)) ready = poll_need(rs, flo, epoch, ready, nd, dead);
          }
          if (t == NVAL - 1) {
            u32x2 fl;
            ld8_asm(fl, rs, flo);
            A1(t + 2); W1(t + 3);
            WAITV(10);
            OPAQUE_V(fl);
            ready = (unsigned)__ballot(is_set(fl, epoch));
          } else {
            A1(t + 2); W1(t + 3);
          }
          clean(t);
          need(t + 1);
          val(t + 1);
        }
        int ka = kseq(NVAL + 3);
        unsigned da = aslot(NVAL + 3), dw = wslot(NVAL + 4);
#pragma unroll 1
        for (int t = NVAL + 1; t <= 12; ++t) {
          TRC(1 + t); BAR;
          const unsigned nd = 1u << ka;
          if (__builtin_expect((ready & nd) == 0, 0)) ready = poll_need(rs, flo, epoch, ready, nd, dead);
          ldA(ka, da);
          ka = kseq(t + 3);
          ldW(ka, dw);
          if (t == 12) break;
          da = (da == aslot(2)) ? aslot(0) : da + XS;
          dw = (dw == wslot(3)) ? wslot(0) : dw + 24576;
          clean(t);
          WAITV(16);
        }
        a15v = vlin; a15s = sa + (unsigned)ka * 16384; a15d = 4096;
      }
      const u32x4 qk[4] = {};
      if (isb) attention(std::integral_constant<int, 1>{}, std::true_type{}, qk);
      else attention(std::integral_constant<int, 1>{}, std::false_type{}, qk);
    }
    {
      LOCALS;
      const int fr = lane & 15, fq = lane >> 4;
      const int kh = half;
      const unsigned v0 = lane * 16 + hw * 4096 + kh * 2048, v1 = lane * 16 + hw * 4096 + (kh ^ 1) * 2048;
      const int r0 = (JROT(j) >> 1) + 2;
      const unsigned ob0 = ob_off(g, kh, l);
      const unsigned pko = myrep + fl_line(FL_PK + (fq == 3 ? 0 : fq), j) + fr * 8;
      u32x4 R[8][4];
      u32x2 pkf;
      auto issue_ = [&](int i) {
        const unsigned so = ob0 + (unsigned)((r0 + i) & 7) * 131072;
        R[i][0] = ld16_sc1(rs, v0, so); R[i][1] = ld16_sc1(rs, v1, so);
        R[i][2] = ld16_sc1(rs, v0 + 1024, so); R[i][3] = ld16_sc1(rs, v1 + 1024, so);
      };
      auto issue = [&](int i) { issue_(i); SCHED; };
      constexpr int LA = LOOKAHEAD;
      TRC(26); TRR(61);
      __builtin_amdgcn_s_setprio(3);
      SCHED;
      issue(0);
      __builtin_amdgcn_s_setprio(2);
#pragma unroll
      for (int i = 1; i < LA; ++i) issue(i);
      auto vmax = [&](int i) { return umax4v(umax4v(umax4v(umax4v(0u, R[i][0]), R[i][1]), R[i][2]), R[i][3]); };
      auto bad = [&](int i) { return __any(umax4v(umax4v(umax4v(umax4v(0u, R[i][0]), R[i][1]), R[i][2]), R[i][3]) == SENT); };
      auto poll = [&](int i, int nfl) {
        for (unsigned spins = 0; !dead; ++spins) {
          if (spins > SPIN_CAP) { dead = true; break; }
          __builtin_amdgcn_s_sleep(2);
          CLOBBER;
          issue(i);
#pragma unroll
          for (int k = i + 1; k < i + nfl && k < 8; ++k)
            if (bad(k)) issue(k);
          WAITV(0);
#pragma unroll
          for (int k = i + 1; k < i + nfl && k < 8; ++k)
#pragma unroll
            for (int f = 0; f < 4; ++f) OPAQUE_V(R[k][f]);
          if (!bad(i)) break;
        }
      };
      unsigned bb[4];
      {
        const int kr = (fq & 1) * 16 + (fq >> 1) * 4 + (fr >> 2);
        const int gs = ((kr >> 1) & 1) | (((kr >> 4) & 1) << 1);
#pragma unroll
        for (int c = 0; c < 4; ++c) bb[c] = lds0 + kh * 8192 + kr * 128 + ((c ^ gs) * 32) + (lane & 3) * 8;
      }
      bf16x8 bf[2][4];
      auto rdB = [&](int i, int hh) {
        if (hh == 0) {
#pragma unroll
          for (int c = 0; c < 4; ++c) { bb[c] += wo_slot(i) - (i ? wo_slot(i - 1) : 0u); asm("" : "+v"(bb[c])); }
        }
#pragma unroll
        for (int c = 0; c < 4; ++c) bf[hh][c] = tr2(bb[c] + hh * 4096, bb[c] + hh * 4096 + 1024);
      };
      f32x4 acc[2][4];
#pragma unroll
      for (int i = 0; i < 8; ++i) acc[i >> 2][i & 3] = f32x4{0.f, 0.f, 0.f, 0.f};
      auto accinit = [&]() {
#pragma unroll
        for (int m = 0; m < 2; ++m)
#pragma unroll
          for (int pp = 0; pp < 2; ++pp) {
            auto r0_ = __builtin_amdgcn_permlane16_swap(xr[m][pp][0], xr[m][pp][2], false, false);
            auto r1_ = __builtin_amdgcn_permlane16_swap(xr[m][pp][1], xr[m][pp][3], false, false);
            acc[m][2 * pp] = f32x4{lo16(r0_[0]), hi16(r0_[0]), lo16(r1_[0]), hi16(r1_[0])};
            acc[m][2 * pp + 1] = f32x4{lo16(r0_[1]), hi16(r0_[1]), lo16(r1_[1]), hi16(r1_[1])};
          }
      };
      auto mms = [&](int i, int hh) {
#pragma unroll
        for (int n = 0; n < 4; ++n)
#pragma unroll
          for (int m = 0; m < 2; ++m) acc[m][n] = MFMA(bf[hh][n], as_frag(R[i][hh * 2 + m]), acc[m][n]);
      };
      if (half) accinit();
      const char* obase = (l == 3) ? (const char*)p.out : (const char*)p.ws;
      unsigned osize = (l == 3) ? 2048 * 2048 : WS_SPAN;
      unsigned sq = WS_WP + (unsigned)(((isb ? 0 : 16) + j) * 16 + j) * 24576 + kh * 12288;
      unsigned sq1 = WS_WP + (unsigned)(((isb ? 0 : 16) + j) * 16 + JROT(j) + ((1 >> (j & 3)) & 1)) * 24576 + kh * 12288;
      unsigned dst = lds0 + L_R1 + kh * 12288 + hw * 1024, vq = hw * 1024 + lane * 16;
      OPAQUE_S(obase); OPAQUE_S(osize); OPAQUE_S(sq); OPAQUE_S(sq1); OPAQUE_S(dst); OPAQUE_V(vq);
      const rsrc_t ro = mk_rsrc(obase, osize);
      unsigned so[2];
      unsigned xw = lds0 + SC_XCH + wave * 4096 + lane * 16;
      unsigned xa = lds0 + SC_XCH + (wave ^ 4) * 4096 + lane * 16;
      unsigned sb0 = (l == 3) ? g * (128 * 2048) + j * 128 : xb_off(g, l) + j * 16384;
      {
        const int row = (hw * 2 + kh) * 16 + fr, ch = (fq & 1) * 2 + (fq >> 1);
        const unsigned sw = (l == 3) ? 0u : (unsigned)((row >> 1) & 7), rb = (unsigned)row << ((l == 3) ? 11 : 7);
#pragma unroll
        for (int pp = 0; pp < 2; ++pp) { so[pp] = rb + (((ch + 4 * pp) ^ sw) * 16); OPAQUE_V(so[pp]); }
        OPAQUE_V(xa); OPAQUE_V(xw); OPAQUE_S(sb0);
      }
      SCHED;
      rdB(0, 0);
      unsigned mx = vmax(0);
#pragma unroll
      for (int i = 0; i < 8; ++i) {
        SCHED;
        if (__builtin_expect(__any(mx == SENT), 0)) poll(i, LA);
        TRC(27 + i);
        if (i == 1 && l == 0 && !half && hw) {
          const unsigned ln = fl_line(FL_PK + hw - 1, JOBC(j, hw - 1));
          set_flags(rs, ln, g, ln, g, lds_ld<u32x2>(lds0 + L_EP), lane);
        }
        if (i == 5) { pkf = ld8_sc1(rs, pko, 0); SCHED; }
        if (i == 3) {
          for (unsigned spins = 0; spins < SPIN_CAP; ++spins) {
            CLOBBER;
            if (UNI(lds_ld<unsigned>(lds0 + L_CNT)) >= 2u * (l + 1) - (isb ? 0u : 2u)) break;
          }
          CLOBBER;
        }
        if (i == 7) {
          OPAQUE_V(pkf);
          if (l == 0 && lead && !__all(is_set(pkf, epoch))) poll_all(rs, pko, epoch, dead);
          TRC(35); BAR;
        }
        if (i == 7 && l < 3) dma3<4096>(rs, vq, sq, 4096, dst);
        if (i == 2) __builtin_amdgcn_s_setprio(1);
        if (i == 5) __builtin_amdgcn_s_setprio(0);
        SCHED;
        rdB(i, 1);
        if (i + LA < 8) issue_(i + LA);
        mms(i, 0);
#pragma unroll
        for (int k = 0; k < 4; ++k) { SGB(0x008, 1); SGB(0x100, 1); SGB(0x020, 1); SGB(0x008, 1); SGB(0x100, 1); }
        SCHED;
        if (i == 7 && l < 3) { dma3<4096>(rs, vq, sq1, 4096, dst + 24576); SCHED; }
        if (i < 7) { rdB(i + 1, 0); mx = vmax(i + 1); }
        mms(i, 1);
#pragma unroll
        for (int k = 0; k < 4; ++k) { SGB(0x008, 1); SGB(0x100, 2); }
#pragma unroll
        for (int k = 4; k < 8; ++k) { SGB(0x008, 1); SGB(0x002, 8 / (8 - 4)); }
      }
      SCHED;
      TRC(36);
#pragma unroll
      for (int n = 0; n < 4; ++n) lds_st<f32x4>(xw + n * 1024, acc[1][n]);
      WAITL0;
      TRC(37); BAR;
      SCHED;
      f32x4 pa[4];
#pragma unroll
      for (int n = 0; n < 4; ++n) pa[n] = lds_ld<f32x4>(xa + n * 1024);
      SCHED;
      u32x4 o[2];
#pragma unroll
      for (int pp = 0; pp < 2; ++pp) {
        const f32x4 a = acc[0][2 * pp] + pa[2 * pp], b = acc[0][2 * pp + 1] + pa[2 * pp + 1];
        auto r0_ = __builtin_amdgcn_permlane16_swap(pk2(a[0], a[1]), pk2(b[0], b[1]), false, false);
        auto r1_ = __builtin_amdgcn_permlane16_swap(pk2(a[2], a[3]), pk2(b[2], b[3]), false, false);
        o[pp] = nosent(u32x4{r0_[0], r1_[0], r0_[1], r1_[1]});
        st16_sc1(ro, so[pp], sb0, o[pp]);
      }
      if (l < 3) {
        lds_st<u32x4>(lds0 + L_R2 + so[0], o[0]);
        lds_st<u32x4>(lds0 + L_R2 + so[1], o[1]);
        const f32x4 zero = {0.f, 0.f, 0.f, 0.f};
        const bf16x8 ones = {0x3f80, 0x3f80, 0x3f80, 0x3f80, 0x3f80, 0x3f80, 0x3f80, 0x3f80};
        const f32x4 a = MFMA(ones, as_frag(o[1]), MFMA(ones, as_frag(o[0]), zero));
        const f32x4 b = MFMA(as_frag(o[1]), as_frag(o[1]), MFMA(as_frag(o[0]), as_frag(o[0]), zero));
        const float q = (fr & 2) ? ((fr & 1) ? b[3] : b[2]) : ((fr & 1) ? b[1] : b[0]);
        if (fq == (fr >> 2)) st8_sc1(rs, ((hw * 2 + kh) * 16 + fr) * 8, st_off(g, l + 1) + j * 1024, u32x2{__float_as_uint(a[0]), __float_as_uint(q)});
      } else if (lead && blockIdx.x == 0 && lane == 0) {
        st8_sc1(rs, WS_EPOCH, 0, epoch);
      }
    }
    TRC(38); TRR(63);
  }
  STAMP_DUMP;
  asm volatile(".Lpf_end:");
}

extern "C" void kernel_launch(void* const* d_in, const int* in_sizes, int n_in, void* d_out, int out_size,
                              void* d_ws, size_t ws_size, hipStream_t stream) {
  Params p;
  p.x = (const u16*)d_in[0];
  p.wqkvA = (const u16*)d_in[1];
  p.wphi = (const u16*)d_in[2];
  p.woA = (const u16*)d_in[3];
  p.wqkvB = (const u16*)d_in[5];
  p.woB = (const u16*)d_in[6];
  p.out = (u16*)d_out;
  p.ws = (char*)d_ws;
  (void)hipFuncSetAttribute((const void*)mega_kernel, hipFuncAttributeMaxDynamicSharedMemorySize, L_TOTAL);
  mega_kernel<<<256, 512, L_TOTAL, stream>>>(p);
}
```

```cpp
#include <hip/hip_runtime.h>
#include <stdint.h>
#include <type_traits>
constexpr int LOOKAHEAD = 2;
constexpr int NVAL = 4;
#define XD 0
#define WFIRST 1
#define WT 1
#define LAW 2
#define POL0 0
constexpr int PA0 = LAW == 3 ? 2 : 0, PA1 = LAW == 3 ? 6 : 3, PA2 = LAW == 3 ? 6 : 7, PB0 = PA0, PB1 = LAW == 3 ? 6 : 3, PB2 = LAW == 3 ? 2 : 3;

typedef unsigned short u16;
typedef unsigned long long u64;
typedef __attribute__((ext_vector_type(8))) short bf16x8;
typedef __attribute__((ext_vector_type(4))) short s16x4;
typedef __attribute__((ext_vector_type(4))) float f32x4;
typedef __attribute__((ext_vector_type(2))) float f32x2;
typedef __attribute__((ext_vector_type(4))) unsigned u32x4;
typedef __attribute__((ext_vector_type(2))) unsigned u32x2;
typedef __attribute__((ext_vector_type(2))) __bf16 bf16x2_t;
#define LAS __attribute__((address_space(3)))
typedef LAS s16x4* lds_s16x4_ptr;

constexpr unsigned NREP = 8, REP_STRIDE = 18 * 2048;
enum { FL_X = 0, FL_CS = 4, FL_CLR = 5, FL_CLC = 9, FL_PK = 13, FL_WO = 16 };
__device__ __forceinline__ unsigned fl_line(int set, int a) { return (unsigned)(set * 16 + a) * 128; }
constexpr unsigned WS_EPOCH = 512u << 10;
constexpr unsigned WS_CS = 1u << 20;
constexpr unsigned WS_KVP = 4u << 20;
constexpr unsigned KVP_J = 512u << 10, KVP_G = 32u << 10, KVP_L = 16u << 10, KVP_HALO = 12u << 10;
constexpr unsigned SENT = 0xffffffffu;
constexpr unsigned WS_G = 16u << 20, G_STRIDE = 2u << 20;
__device__ __forceinline__ unsigned xb_off(int g, int l) { return WS_G + (unsigned)g * G_STRIDE + (unsigned)l * (256u << 10); }
__device__ __forceinline__ unsigned ob_off(int g, int j, int l) { return WS_G + (unsigned)g * G_STRIDE + (3u << 18) + (unsigned)j * (64u << 10) + (unsigned)l * (16u << 10); }
__device__ __forceinline__ unsigned st_off(int g, int l) { return WS_G + (unsigned)g * G_STRIDE + (7u << 18) + (unsigned)l * (16u << 10); }
constexpr unsigned WS_WP = 48u << 20;
constexpr unsigned WS_WO = 60u << 20;
constexpr unsigned WS_SPAN = 64u << 20;

constexpr unsigned L_R1 = 0;
constexpr unsigned L_R2 = 98304, XS = 16384;
constexpr unsigned L_MISC = 147456;
constexpr unsigned L_MEAN = L_MISC;
constexpr unsigned L_RSTD = L_MISC + 576;
constexpr unsigned L_CS = L_MISC + 1152;
constexpr unsigned L_WPHI = L_MISC + 2688;
constexpr unsigned L_SINK = L_MISC + 6912;
constexpr unsigned L_EP = L_MISC + 6792;
constexpr unsigned L_CNT = L_MISC + 6784;
constexpr unsigned L_TOTAL = 163840;

#define JROT(j) ((j) & ~3)
#define JOBC(j, w) (((j) & ~3) + (((j) + (w) + 1) & 3))

#define SLOWC(jc) (((((unsigned)(size_t)p.wqkvA >> 7) + (jc)) & 7) == 3)

struct Params {
  const u16* x;
  const u16* wqkvA;
  const u16* wphi;
  const u16* woA;
  const u16* wqkvB;
  const u16* woB;
  u16* out;
  char* ws;
};

#define SCHED __builtin_amdgcn_sched_barrier(0)
#define BAR __builtin_amdgcn_s_barrier()
#define SGB(m, n) __builtin_amdgcn_sched_group_barrier(m, n, 0)
#define WAITV(n) asm volatile("s_waitcnt vmcnt(%0)" ::"n"(n) : "memory")
#define WAITL0 asm volatile("s_waitcnt lgkmcnt(0)" ::: "memory")

#define TRQ(slot) do {} while (0)
#define TRC(slot) do {} while (0)
#define TRR(slot) do {} while (0)
#define STAMP_INIT do {} while (0)
#define STAMP_DUMP do {} while (0)

__device__ __forceinline__ unsigned pk2(float a, float b) {
  f32x2 v = {a, b};
  return __builtin_bit_cast(unsigned, __builtin_convertvector(v, bf16x2_t));
}
__device__ __forceinline__ float lo16(unsigned u) { return __uint_as_float(u << 16); }
__device__ __forceinline__ float hi16(unsigned u) { return __uint_as_float(u & 0xffff0000u); }

template <int A, int B, typename F> __device__ __forceinline__ void static_for(F f) {
  if constexpr (A < B) { f(std::integral_constant<int, A>{}); static_for<A + 1, B>(f); }
}
typedef __amdgpu_buffer_rsrc_t rsrc_t;
__device__ __forceinline__ rsrc_t mk_rsrc(const void* p, unsigned bytes) { return __builtin_amdgcn_make_buffer_rsrc((void*)p, 0, bytes, 0x00020000); }
#define UNI(x) __builtin_amdgcn_readfirstlane(x)
#define DMA_HEAD(P) "s_mov_b32 m0, %4\n\ts_mov_b32 %0, %3\n\ts_nop 2\n\tbuffer_load_dwordx4 %1, %2, %0 offen " P " lds\n\t"
#define DMA_NEXT(P) "s_add_u32 m0, m0, %6\n\ts_add_u32 %0, %0, %5\n\tbuffer_load_dwordx4 %1, %2, %0 offen " P " lds\n\t"
#define DMA_OPS : "=&s"(t) : "v"(voff), "s"(r), "s"(UNI(soff)), "s"(UNI(lds)), "s"(UNI(sstep)), "i"(LSTEP) : "memory", "scc", "m0"
template <int LSTEP, int POL = 1> __device__ __forceinline__ void dma4(rsrc_t r, unsigned voff, unsigned soff, unsigned sstep, unsigned lds) {
  unsigned t;
  if constexpr (POL == 1) asm volatile(DMA_HEAD("sc1") DMA_NEXT("sc1") DMA_NEXT("sc1") DMA_NEXT("sc1") DMA_OPS);
  else if constexpr (POL == 2) asm volatile(DMA_HEAD("nt") DMA_NEXT("nt") DMA_NEXT("nt") DMA_NEXT("nt") DMA_OPS);
  else asm volatile(DMA_HEAD("") DMA_NEXT("") DMA_NEXT("") DMA_NEXT("") DMA_OPS);
}
template <int LSTEP, int POL = 1> __device__ __forceinline__ void dma3(rsrc_t r, unsigned voff, unsigned soff, unsigned sstep, unsigned lds) {
  unsigned t;
  if constexpr (POL == 1) asm volatile(DMA_HEAD("sc1") DMA_NEXT("sc1") DMA_NEXT("sc1") DMA_OPS);
  else if constexpr (POL == 2) asm volatile(DMA_HEAD("nt") DMA_NEXT("nt") DMA_NEXT("nt") DMA_OPS);
  else asm volatile(DMA_HEAD("") DMA_NEXT("") DMA_NEXT("") DMA_OPS);
}
template <int LSTEP, int N> __device__ __forceinline__ void bar_dma4(rsrc_t r, unsigned voff, unsigned soff, unsigned sstep, unsigned lds) {
  unsigned t;
  asm volatile("s_mov_b32 m0, %4\n\ts_mov_b32 %0, %3\n\ts_nop 0\n\ts_waitcnt vmcnt(%7) lgkmcnt(0)\n\ts_barrier\n\t"
               "buffer_load_dwordx4 %1, %2, %0 offen sc1 lds\n\t" DMA_NEXT("sc1") DMA_NEXT("sc1") DMA_NEXT("sc1")
               : "=&s"(t) : "v"(voff), "s"(r), "s"(UNI(soff)), "s"(UNI(lds)), "s"(UNI(sstep)), "i"(LSTEP), "n"(N) : "memory", "scc", "m0");
}
__device__ __forceinline__ void dma1(rsrc_t r, unsigned voff, unsigned soff, unsigned lds) {
  asm volatile("s_mov_b32 m0, %3\n\ts_nop 3\n\tbuffer_load_dwordx4 %0, %1, %2 offen sc1 lds" ::"v"(voff), "s"(r), "s"(UNI(soff)), "s"(UNI(lds)) : "memory", "m0");
}
__device__ __forceinline__ void touch(rsrc_t r, unsigned voff, unsigned lds) {
  asm volatile("s_mov_b32 m0, %2\n\ts_nop 3\n\tbuffer_load_dword %0, %1, 0 offen lds" ::"v"(voff), "s"(r), "s"(UNI(lds)) : "memory", "m0");
}
__device__ __forceinline__ void ld8_asm(u32x2& d, rsrc_t r, unsigned voff) {
  asm volatile("s_nop 4\n\tbuffer_load_dwordx2 %0, %1, %2, 0 offen sc1" : "=v"(d) : "v"(voff), "s"(r) : "memory");
}

__device__ __forceinline__ void ld16nt_asm(u32x4& d, rsrc_t r, unsigned voff, unsigned soff) {
  asm volatile("s_nop 4\n\tbuffer_load_dwordx4 %0, %1, %2, %3 offen nt" : "=v"(d) : "v"(voff), "s"(r), "s"(UNI(soff)) : "memory");
}
__device__ __forceinline__ void ld16_asm(f32x4& d, rsrc_t r, unsigned voff, unsigned soff) {
  asm volatile("s_nop 4\n\tbuffer_load_dwordx4 %0, %1, %2, %3 offen sc1" : "=v"(d) : "v"(voff), "s"(r), "s"(UNI(soff)) : "memory");
}

template <typename T> __device__ __forceinline__ T lds_ld(unsigned a) { return *(LAS T*)(a); }
template <typename T> __device__ __forceinline__ void lds_st(unsigned a, T v) { *(LAS T*)(a) = v; }
__device__ __forceinline__ bf16x8 tr2(unsigned a0, unsigned a1) {
  s16x4 v0 = __builtin_amdgcn_ds_read_tr16_b64_v4i16((lds_s16x4_ptr)(a0));
  s16x4 v1 = __builtin_amdgcn_ds_read_tr16_b64_v4i16((lds_s16x4_ptr)(a1));
  return __builtin_shufflevector(v0, v1, 0, 1, 2, 3, 4, 5, 6, 7);
}
__device__ __forceinline__ bf16x8 as_frag(u32x4 v) { return __builtin_bit_cast(bf16x8, v); }
#define MFMA(a, b, c) __builtin_amdgcn_mfma_f32_16x16x32_bf16(a, b, c, 0, 0, 0)

__device__ __forceinline__ float vmax2(float a, float b) { float d; asm("v_max_f32 %0, %1, %2" : "=v"(d) : "v"(a), "v"(b)); return d; }
#define SWAP16(a, b) __builtin_amdgcn_permlane16_swap(__float_as_uint(a), __float_as_uint(b), false, false)
#define SWAP32(a, b) __builtin_amdgcn_permlane32_swap(__float_as_uint(a), __float_as_uint(b), false, false)
__device__ __forceinline__ void red2_max(float& a, float& b) {
  auto r = SWAP16(a, b);
  float x = vmax2(__uint_as_float(r[0]), __uint_as_float(r[1]));
  r = SWAP32(x, x);
  x = vmax2(__uint_as_float(r[0]), __uint_as_float(r[1]));
  r = SWAP16(x, x);
  a = __uint_as_float(r[0]); b = __uint_as_float(r[1]);
}
__device__ __forceinline__ void red2_add(float& a, float& b) {
  auto r = SWAP16(a, b);
  float x = __uint_as_float(r[0]) + __uint_as_float(r[1]);
  r = SWAP32(x, x);
  x = __uint_as_float(r[0]) + __uint_as_float(r[1]);
  r = SWAP16(x, x);
  a = __uint_as_float(r[0]); b = __uint_as_float(r[1]);
}
__device__ __forceinline__ float red_max4(float x) {
  auto r = __builtin_amdgcn_permlane16_swap(__float_as_uint(x), __float_as_uint(x), false, false);
  x = vmax2(__uint_as_float(r[0]), __uint_as_float(r[1]));
  r = __builtin_amdgcn_permlane32_swap(__float_as_uint(x), __float_as_uint(x), false, false);
  return vmax2(__uint_as_float(r[0]), __uint_as_float(r[1]));
}
__device__ __forceinline__ float red_add4(float x) {
  auto r = __builtin_amdgcn_permlane16_swap(__float_as_uint(x), __float_as_uint(x), false, false);
  x = __uint_as_float(r[0]) + __uint_as_float(r[1]);
  r = __builtin_amdgcn_permlane32_swap(__float_as_uint(x), __float_as_uint(x), false, false);
  return __uint_as_float(r[0]) + __uint_as_float(r[1]);
}

__device__ __forceinline__ void st16_sc1(rsrc_t r, unsigned voff, unsigned soff, u32x4 v) {
  __builtin_amdgcn_raw_buffer_store_b128(v, r, voff, soff, 16);
  asm volatile("s_nop 1" ::"v"(v) : "memory");
}
__device__ __forceinline__ void st8_sc1(rsrc_t r, unsigned voff, unsigned soff, u32x2 v) { __builtin_amdgcn_raw_buffer_store_b64(v, r, voff, soff, 16); }
__device__ __forceinline__ u32x4 ld16_sc1(rsrc_t r, unsigned voff, unsigned soff) { return __builtin_amdgcn_raw_buffer_load_b128(r, voff, soff, 16); }
__device__ __forceinline__ u32x2 ld8_sc1(rsrc_t r, unsigned voff, unsigned soff) { return __builtin_amdgcn_raw_buffer_load_b64(r, voff, soff, 16); }
__device__ __forceinline__ f32x4 ld4f_sc1(rsrc_t r, unsigned voff, unsigned soff) { return __builtin_bit_cast(f32x4, __builtin_amdgcn_raw_buffer_load_b128(r, voff, soff, 16)); }
#define OPAQUE_V(x) asm volatile("" : "+v"(x))
#define OPAQUE_S(x) asm volatile("" : "+s"(x))

#define CLOBBER asm volatile("" ::: "memory")
constexpr unsigned SPIN_CAP = 1u << 17;
__device__ __forceinline__ bool is_set(u32x2 v, u32x2 epoch) { return v[0] == epoch[0] && v[1] == epoch[1]; }
__device__ __forceinline__ void set_flags(rsrc_t rs, unsigned lineA, int colA, unsigned lineB, int colB, u32x2 epoch, int lane) {
  const unsigned off = (lane & 7) * REP_STRIDE + ((lane & 8) ? lineB + colB * 8 : lineA + colA * 8);
  if (lane < 16) st8_sc1(rs, off, 0, epoch);
}
__device__ __forceinline__ unsigned poll_need(rsrc_t rs, unsigned voff, u32x2 epoch, unsigned ready, unsigned need, bool& dead) {
  for (unsigned spins = 0; !dead; ++spins) {
    CLOBBER;
    ready |= (unsigned)__ballot(is_set(ld8_sc1(rs, voff, 0), epoch));
    if ((ready & need) == need) break;
    if (spins > SPIN_CAP) dead = true;
    __builtin_amdgcn_s_sleep(1);
  }
  CLOBBER;
  return dead ? ~0u : ready;
}

__device__ __forceinline__ void poll_all(rsrc_t rs, unsigned voff, u32x2 epoch, bool& dead) {
  for (unsigned spins = 0; !dead; ++spins) {
    CLOBBER;
    if (__all(is_set(ld8_sc1(rs, voff, 0), epoch))) break;
    if (spins > SPIN_CAP) dead = true;
    __builtin_amdgcn_s_sleep(1);
  }
  CLOBBER;
}

constexpr unsigned sc_qf(bool isb) { return L_R1 + (isb ? 32768 : 16384); }
constexpr unsigned SC_KF = L_R1 + 24576;
constexpr unsigned SC_VIM = L_R1 + 32768;
constexpr unsigned SC_PHIK = L_R1 + 53248;
constexpr unsigned SC_KVF = L_R1 + 65536;
constexpr unsigned SC_ZF = L_R1 + 65536 + 8192;
constexpr unsigned SC_IMG = L_R1 + 40960;
constexpr unsigned SC_HV = L_R1 + 87040;
constexpr unsigned SC_XCH = L_R2 + XS;
constexpr unsigned wo_slot(int s) {
  return s < 2 ? L_R2 + (unsigned)(s + 1) * XS : (s < 6 ? L_R1 + (unsigned)(s - 2) * 16384 : (s == 6 ? L_R1 + 81920 : L_R2));
}
__device__ __forceinline__ unsigned wo_src(int s, int j) { return (unsigned)(((JROT(j) >> 1) + 2 + s) & 7) * 16384; }
__device__ __forceinline__ unsigned wo_swz(int i, int lane) { return (unsigned)((lane >> 3) * 2048) + (((lane & 7) ^ ((((lane >> 4) & 1) | (i & 2)) << 1)) * 16); }
__device__ __forceinline__ unsigned umax3(unsigned a, unsigned b, unsigned c) { return max(max(a, b), c); }
__device__ __forceinline__ unsigned umax4v(unsigned m, const u32x4& v) { return umax3(umax3(m, v[0], v[1]), v[2], v[3]); }
__device__ __forceinline__ u32x4 nosent(u32x4 v) { return u32x4{min(v[0], SENT - 1), min(v[1], SENT - 1), min(v[2], SENT - 1), min(v[3], SENT - 1)}; }

template <typename F, typename G>
__device__ __forceinline__ void qkv_loop(bool isb, unsigned lds0, int wm, int h, int lane, bool first, int& trv, F publish, G rest) {
  f32x4 acc[4][6];
  const int fr = lane & 15, fq = lane >> 4;
  unsigned ab[2], bb[2];
  {
    const int r = wm * 64 + fr;
#pragma unroll
    for (int ks = 0; ks < 2; ++ks) ab[ks] = lds0 + L_R2 + r * 128 + (((ks * 4 + fq) ^ ((fr >> 1) & 7)) * 16);
    const int kr = fq * 8 + (fr >> 2);
    const int gs = ((kr >> 1) & 1) | (((kr >> 3) & 1) << 1);
#pragma unroll
    for (int c = 0; c < 2; ++c) bb[c] = lds0 + L_R1 + kr * 128 + (((h * 2 + c) ^ gs) * 32) + (lane & 3) * 8;
  }
#pragma unroll
  for (int m = 0; m < 4; ++m)
#pragma unroll
    for (int n = 0; n < 6; ++n) acc[m][n] = f32x4{0.f, 0.f, 0.f, 0.f};
  bf16x8 af[2][4], bf[2][6];
  auto rds = [&](int ks, int p) {
#pragma unroll
    for (int m = 0; m < 4; ++m) af[p][m] = lds_ld<bf16x8>(ab[ks] + m * 2048);
#pragma unroll
    for (int n = 0; n < 6; ++n) {
      const unsigned a = bb[n & 1] + (n >> 1) * 8192 + ks * 4096;
      bf[p][n] = tr2(a, a + 512);
    }
  };
  auto mms = [&](int p) {
#pragma unroll
    for (int n = 0; n < 6; ++n)
#pragma unroll
      for (int mm = 0; mm < 4; ++mm) { const int m = (n & 1) ? 3 - mm : mm; acc[m][n] = MFMA(bf[p][n], af[p][m], acc[m][n]); }
  };
  auto ilv2 = [&]() {
#pragma unroll
    for (int i = 0; i < 16; ++i) { SGB(0x008, 1); SGB(0x100, 1); }
#pragma unroll
    for (int i = 0; i < 4; ++i) { SGB(0x008, 1); SGB(0x002, 1); }
    SGB(0x008, 4);
  };
  auto ilv3 = [&]() {
#pragma unroll
    for (int i = 0; i < 16; ++i) { SGB(0x008, 1); SGB(0x100, 1); }
#pragma unroll
    for (int i = 0; i < 7; ++i) { SGB(0x008, 1); SGB(0x004, 2); }
    SGB(0x008, 1);
  };
  if (!first) { WAITV(6); WAITL0; }
  TRQ(1); BAR;
  int tslot = 2;
  rds(0, 0);
  int sa = 0, sb = 0;
  int da = XS, db = 24576;
  auto step = [&]() {
    ab[0] += da; ab[1] += da;
    bb[0] += db; bb[1] += db;
  };
  auto ring = [&]() {
    sa = (sa == 2) ? 0 : sa + 1;
    sb = (sb == 3) ? 0 : sb + 1;
    da = (sa == 2) ? -2 * (int)XS : (int)XS;
    db = (sb == 3) ? -3 * 24576 : 24576;
  };
#pragma unroll 1
  for (int t = 14; t > 0; --t) {
    SCHED;
    rds(1, 1); mms(0);
    step();
    ilv2();
    SCHED;
    __builtin_amdgcn_s_waitcnt(0x0070);
    TRQ(tslot); tslot++; BAR;
    SCHED;
    rds(0, 0);
    ring();
    mms(1); ilv3();
  }
  SCHED;
  f32x4 cs[6];
  float nmean[4], rstd[4];
  auto fold = [&](int n) {
#pragma unroll
    for (int mm = 0; mm < 4; ++mm) {
      const int m = (n & 1) ? 3 - mm : mm;
#pragma unroll
      for (int e = 0; e < 4; ++e) { float t = __builtin_fmaf(nmean[m], cs[n][e], acc[m][n][e]); asm("" : "+v"(t)); acc[m][n][e] = t; }
    }
  };
  {
    const unsigned pm = lds0 + L_MEAN + (16 + wm * 64 + fr) * 4, pc = lds0 + L_CS + (h * 32 + fq * 4) * 4;
#pragma unroll
    for (int m = 0; m < 4; ++m) nmean[m] = lds_ld<float>(pm + m * 64);
#pragma unroll
    for (int n = 0; n < 6; ++n) cs[n] = lds_ld<f32x4>(pc + ((n >> 1) * 64 + (n & 1) * 16) * 4);
    rds(1, 1); mms(0);
    step();
    fold(0); fold(1);
    SGB(0x002, 6);
#pragma unroll
    for (int i = 0; i < 8; ++i) { SGB(0x008, 1); SGB(0x100, 2); }
#pragma unroll
    for (int i = 0; i < 10; ++i) { SGB(0x008, 1); SGB(0x100, 1); SGB(0x002, 2); }
#pragma unroll
    for (int i = 0; i < 6; ++i) { SGB(0x008, 1); SGB(0x002, 3); }
    SCHED;
    __builtin_amdgcn_s_waitcnt(0xC07F);
    TRQ(16); BAR;
    SCHED;
    rds(0, 0);
    fold(5); mms(1); fold(2);
#pragma unroll
    for (int i = 0; i < 16; ++i) { SGB(0x008, 1); SGB(0x100, 1); SGB(0x002, 1); }
#pragma unroll
    for (int i = 0; i < 8; ++i) { SGB(0x008, 1); SGB(0x002, 2); }
    SCHED;
    rds(1, 1);
#pragma unroll
    for (int m = 0; m < 4; ++m) rstd[m] = lds_ld<float>(pm + (L_RSTD - L_MEAN) + m * 64);
    fold(4); mms(0); fold(3);
#pragma unroll
    for (int i = 0; i < 16; ++i) { SGB(0x008, 1); SGB(0x100, 1); SGB(0x002, 1); }
#pragma unroll
    for (int i = 0; i < 4; ++i) { SGB(0x008, 1); SGB(0x100, 1); SGB(0x002, 2); }
#pragma unroll
    for (int i = 0; i < 4; ++i) { SGB(0x008, 1); SGB(0x002, 2); }
  }
  SCHED;
  __builtin_amdgcn_s_waitcnt(0xC07F);
  TRQ(17); BAR;
  SCHED;
  auto pack = [&](const f32x4& a, const f32x4& b) { return u32x4{pk2(a[0], a[1]), pk2(a[2], a[3]), pk2(b[0], b[1]), pk2(b[2], b[3])}; };
  auto mm6 = [&](int m) {
#pragma unroll
    for (int nn = 0; nn < 6; ++nn) { const int n = (nn + 4) % 6; acc[m][n] = MFMA(bf[1][n], af[1][m], acc[m][n]); }
  };
  auto ilv4 = [&]() {
#pragma unroll
    for (int i = 0; i < 3; ++i) { SGB(0x008, 1); SGB(0x002, 3); SGB(0x008, 1); SGB(0x002, 3); SGB(0x200, 1); }
  };
  auto fin = [&](auto isb_c) {
    constexpr bool ISB = decltype(isb_c)::value;
    u32x4 qk[4];
    int ln = lane; OPAQUE_V(ln);
    const int fr2 = ln & 15, fq2 = ln >> 4, hw = h * 2 + wm;
    constexpr unsigned d1s = ISB ? 16 * 80 : 1024, d2s = 16 * 80;
    const unsigned d1 = ISB ? lds0 + SC_IMG + h * (2 * 144 * 80) + (16 + wm * 64 + fr2) * 80 + fq2 * 16 : lds0 + SC_KF + hw * 2048 - 2048 + ln * 16;
    const unsigned d2 = ISB ? d1 + 144 * 80 : lds0 + SC_VIM + (h * 128 + wm * 64 + fr2) * 80 + fq2 * 16;
    const unsigned d0 = lds0 + sc_qf(ISB) + hw * 2048 - 2048 + ln * 16;
    auto out3 = [&](int m) {
      if (ISB) lds_st<u32x4>(d2 + m * d2s, pack(acc[m][4], acc[m][5]));
      else lds_st<u32x4>(d2 + m * d2s, pack(acc[m][4] * rstd[m], acc[m][5] * rstd[m]));
      if (m < 2) qk[m] = pack(acc[m][0], acc[m][1]);
      else lds_st<u32x4>(d0 + m * 1024, pack(acc[m][0], acc[m][1]));
      const u32x4 kk = pack(acc[m][2], acc[m][3]);
      if (m < 2) qk[2 + m] = kk;
      if (m >= (ISB ? 1 : 2)) lds_st<u32x4>(d1 + m * d1s, kk);
    };
    mm6(3);
#pragma unroll
    for (int i = 0; i < 6; ++i) { SGB(0x008, 1); SGB(0x002, 4); }
    SCHED;
    mm6(2);
    out3(3);
    ilv4();
    SCHED;
    if (ISB && wm) publish(pack(acc[3][2] * rstd[3], acc[3][3] * rstd[3]), pack(acc[3][4] * rstd[3], acc[3][5] * rstd[3]));
    SCHED;
    mm6(0);
    out3(2);
    ilv4();
    SCHED;
    if (!ISB) {
      WAITL0;
      TRQ(18); BAR;
      SCHED;
    }
    mm6(1);
    out3(0);
    ilv4();
    SCHED;
    out3(1);
    SCHED;
    if (ISB) {
      WAITL0;
      TRQ(18); BAR;
    }
    TRQ(39); TRR(60);
    rest(isb_c, qk);
  };
  if (isb) fin(std::true_type{}); else fin(std::false_type{});
}

__device__ __forceinline__ void hedgehog_exp(const bf16x8& w0, const bf16x8& w1, u32x4 xin, float c, float (&ep)[8], float (&en)[8]) {
  const f32x4 zero = {0.f, 0.f, 0.f, 0.f};
  const f32x4 r0 = MFMA(w0, as_frag(xin), zero);
  const f32x4 r1 = MFMA(w1, as_frag(xin), zero);
  const float x[8] = {r0[0], r0[1], r0[2], r0[3], r1[0], r1[1], r1[2], r1[3]};
  float mx = fmaxf(fmaxf(fmaxf(fmaxf(x[0], x[1]), x[2]), fmaxf(fmaxf(x[3], x[4]), x[5])), fmaxf(x[6], x[7]));
  float nm = -fminf(fminf(fminf(fminf(x[0], x[1]), x[2]), fminf(fminf(x[3], x[4]), x[5])), fminf(x[6], x[7]));
  red2_max(mx, nm);
  const float mxl = mx * -c, mnl = nm * -c;
#pragma unroll
  for (int i = 0; i < 8; ++i) {
    ep[i] = __builtin_amdgcn_exp2f(__builtin_fmaf(x[i], c, mxl));
    en[i] = __builtin_amdgcn_exp2f(__builtin_fmaf(x[i], -c, mnl));
  }
}
__device__ __forceinline__ u32x4 pack8(const float (&e)[8], float f) {
  return u32x4{pk2(e[0] * f, e[1] * f), pk2(e[2] * f, e[3] * f), pk2(e[4] * f, e[5] * f), pk2(e[6] * f, e[7] * f)};
}
__device__ __forceinline__ u32x4 pack8(const float (&e)[8]) { return u32x4{pk2(e[0], e[1]), pk2(e[2], e[3]), pk2(e[4], e[5]), pk2(e[6], e[7])}; }

__device__ __forceinline__ void store_o(rsrc_t rs, unsigned tile_off, int mt, int h, int lane, u32x2 X, u32x2 Y) {
  st16_sc1(rs, lane * 16, tile_off + (unsigned)(mt * 2 + h) * 1024, nosent(u32x4{X[0], X[1], Y[0], Y[1]}));
}

__device__ __forceinline__ void job_colsum(rsrc_t rs, const u32x4 (&w)[8], int mat, int g, int jc, int hw, int lane) {
  const int fr = lane & 15;
  const unsigned one = (fr < 8) ? (0x3f80u << (16 * (fr & 1))) : 0u;
  const int e = (fr >> 1) & 3;
  const bf16x8 sel = as_frag(u32x4{e == 0 ? one : 0u, e == 1 ? one : 0u, e == 2 ? one : 0u, e == 3 ? one : 0u});
  f32x4 c = {0.f, 0.f, 0.f, 0.f};
#pragma unroll
  for (int i = 0; i < 8; ++i) c = MFMA(sel, as_frag(w[i]), c);
  u32x4 w4;
#pragma unroll
  for (int k = 0; k < 4; ++k) {
    const float a = c[k];
    w4[k] = __float_as_uint(a + __int_as_float(__builtin_amdgcn_update_dpp(0, __float_as_int(a), 0x128  , 0xf, 0xf, false)));
  }
  unsigned so = WS_CS + (unsigned)(((mat * 16 + jc) * 16 + g) * 768);
  OPAQUE_S(so);
  if ((lane & 40) == 0) st16_sc1(rs, (unsigned)(hw * 64 + (lane & 7) * 8 + (lane >> 4) * 4) * 4, so, w4);
}
__device__ __forceinline__ unsigned img_off(int i, int lane) { return (lane >> 3) * 128 + (((lane & 7) ^ ((((lane >> 4) & 1) | ((i & 1) << 1)) << 1)) * 16); }
__device__ __forceinline__ void job_pack(rsrc_t rs, u32x4 w, int i, int mat, int g, int jc, int hw, int lane) {
  st16_sc1(rs, img_off(i, lane),
           WS_WP + (unsigned)(((mat * 16 + jc) * 16 + g) * 24576 + hw * 8192 + i * 1024), w);
}

__global__ __launch_bounds__(512, 2) void mega_kernel(Params p) {
  extern __shared__ __attribute__((aligned(16))) char smem[];
  const int tid = threadIdx.x;
  const int wave0 = __builtin_amdgcn_readfirstlane(tid >> 6);
  const int xx = blockIdx.x & 7, rr = blockIdx.x >> 3;
  const int g0 = (rr >> 2) * 2 + (xx & 1), j0 = (xx >> 1) * 4 + (rr & 3);
  const unsigned lds00 = (unsigned)(size_t)smem;
#define LOCALS0 int g = g0, j = j0, l = l0, wave = wave0; unsigned lds0 = lds00;                       \
  OPAQUE_S(g); OPAQUE_S(j); OPAQUE_S(l); OPAQUE_S(wave); OPAQUE_S(lds0);                               \
  const int hw = wave & 3, wm = wave & 1, h = (wave >> 1) & 1;                                         \
  const unsigned myrep = (unsigned)((j >> 2) * 2 + (g & 1)) * REP_STRIDE;                              \
  (void)hw; (void)wm; (void)h; (void)myrep;                                                            \
  int lane = tid; OPAQUE_V(lane); lane &= 63
#define LOCALS LOCALS0; const bool isb = l & 1, lead = (wave == 4); const int half = wave >> 2; (void)isb; (void)lead; (void)half
  const rsrc_t rs = mk_rsrc(p.ws, WS_SPAN);
  const bool cw = wave0 < 4;
  if (wave0 == 2) touch(mk_rsrc(p.ws, NREP * REP_STRIDE), min(blockIdx.x * 9 + ((tid & 63) < 9 ? (tid & 63) : 0), NREP * REP_STRIDE / 128 - 1) * 128, lds00 + L_SINK + 512);
  if (wave0 == 3) {
    const char* pc;
    unsigned len;
    asm volatile("s_getpc_b64 %0\n.Lpf_base:\n\ts_mov_b32 %1, .Lpf_end-.Lpf_base" : "=s"(pc), "=s"(len));
    const unsigned off = ((unsigned)rr * 16 + (tid & 15)) * 128;
    touch(mk_rsrc(pc, len), ((int)len > 0 && off + 4 <= len) ? off : 0u, lds00 + L_SINK);
  }
  int trv = 0, trA = 0, trB = 0, trC = 0;
  u32x2 epoch = {0u, 0u};
  bool dead = false;
  STAMP_INIT;

#pragma unroll 1
  for (int l0 = 0; l0 < 4; ++l0) {
    TRC(0); TRR(62);
    u32x4 xr[2][2];

    auto attention = [&](auto half_c, auto isb_c, const u32x4 (&qk)[4]) {
      LOCALS0;
      constexpr int half = decltype(half_c)::value;
      constexpr bool isb = decltype(isb_c)::value;
      const bool lead = half && wave == 4;
      const int fr = lane & 15, fq = lane >> 4;
      const unsigned otile = ob_off(g, j, l);
      const f32x4 zero = {0.f, 0.f, 0.f, 0.f};
      u32x2 clf;
      const unsigned clo = myrep + ((lane & 16) ? fl_line(FL_CLC + l, j) : fl_line(FL_CLR + l, g)) + fr * 8;
      auto clcheck = [&]() {
        if constexpr (half) {
          OPAQUE_V(clf);
          if (__builtin_expect(!__all(is_set(clf, epoch)), 0)) poll_all(rs, clo, epoch, dead);
        }
      };
      auto tail = [&](auto& wst) {
        constexpr int P0 = isb ? 4 : 3, N0 = isb ? PB0 : PA0, N1 = isb ? PB1 : PA1, N2 = isb ? PB2 : PA2, N3 = (isb ? 12 : 16) - N0 - N1 - N2;
        const unsigned vw = lane * 16 + hw * 1024, wb = WS_WO + (unsigned)((isb ? 16 : 0) + j) * 131072;
        auto park = [&](int a, int n) {
#pragma unroll
          for (int i = a; i < a + n; ++i) wst[i] = ld16_sc1(rs, vw, wb + wo_src(P0 + (i >> 2), j) + (i & 3) * 4096);
          SCHED;
        };
        auto stO = [&](int i) { dma4<4096>(rs, vw, wb + wo_src(i, j), 4096, lds0 + wo_slot(i) + hw * 1024); };
        auto barO = [&](auto n_c, int i) { bar_dma4<4096, decltype(n_c)::value>(rs, vw, wb + wo_src(i, j), 4096, lds0 + wo_slot(i) + hw * 1024); };
        if (LAW == 3) { stO(2); park(0, N0); }
        TRC(15);
        barO(std::integral_constant<int, LAW == 3 ? 14 + N0 : 10>{}, 0);
        if (lead) set_flags(rs, fl_line(FL_CLR + l, g), j, fl_line(FL_CLC + l, j), g, epoch, lane);
        if (LAW == 2) stO(2);
        park(N0, N1);
        TRC(16);
        barO(std::integral_constant<int, 8 + N0 + N1>{}, 1);
        if (isb) stO(3);
        park(N0 + N1, N2);
        TRC(17);
        barO(std::integral_constant<int, 63>{}, 7);
        park(N0 + N1 + N2, N3);
        ld8_asm(clf, rs, clo);
        __builtin_amdgcn_s_setprio(0);
        TRC(18); BAR;
      };
      if constexpr (!isb) {
        u32x4 wst[16];
        u32x4 vb[8];
        if constexpr (half) tail(wst);
        if constexpr (!half) {
#pragma unroll
          for (int i = 0; i < 8; ++i) asm volatile("" : "=v"(vb[i]));
          if (l == 0) {
            const bool wj = hw != 0;
            const char* bp = (const char*)(wj ? p.wqkvB : p.woB);
            int j2 = j0;
            OPAQUE_S(j2);
            unsigned rs_ = wj ? 6144 : 2048, sz = rs_ * 1024, ss = rs_ * 8, s2 = wj ? 16 * 6144 : 128;
            unsigned sb = g * 64 * rs_ + (wj ? (hw - 1) * 2048 + JOBC(j2, hw - 1) * 128 : (j2 & 3) * (16 * 2048) + JROT(j2) * 128);
            OPAQUE_S(bp); OPAQUE_S(rs_); OPAQUE_S(sz); OPAQUE_S(sb); OPAQUE_S(ss); OPAQUE_S(s2);
            const rsrc_t rb = mk_rsrc(bp, sz);
#pragma unroll
            for (int i = 0; i < 8; ++i) vb[i] = __builtin_amdgcn_raw_buffer_load_b128(rb, (lane >> 3) * rs_ + (lane & 7) * 16, sb + (i & 1) * ss + (i >> 1) * s2, 2);
          }
          SCHED;
        }
        u32x4 qin[2];
#pragma unroll
        for (int i = 0; i < 2; ++i) qin[i] = half ? lds_ld<u32x4>(lds0 + sc_qf(false) + hw * 2048 + i * 1024 + lane * 16) : qk[i];
        float cq[2];
#pragma unroll
        for (int i = 0; i < 2; ++i) cq[i] = lds_ld<float>(lds0 + L_RSTD + (16 + wm * 64 + (half * 2 + i) * 16 + fr) * 4) * 1.4426950408889634f;
        const bf16x8 ones = {0x3f80, 0x3f80, 0x3f80, 0x3f80, 0x3f80, 0x3f80, 0x3f80, 0x3f80};
        const unsigned wa = lds0 + L_WPHI + h * 2048 + (fq * 4 + (fr >> 2)) * 64 + (lane & 3) * 8;
        const bf16x8 w0 = tr2(wa, wa + 1024);
        const bf16x8 w1 = tr2(wa + 32, wa + 32 + 1024);
#pragma unroll
        for (int i = 0; i < 2; ++i) {
          const int m = half * 2 + i;
          float ep[8], en[8];
          hedgehog_exp(w0, w1, half ? lds_ld<u32x4>(lds0 + SC_KF + hw * 2048 + i * 1024 + lane * 16) : qk[2 + i], cq[i], ep, en);
          const float sp = MFMA(ones, as_frag(pack8(ep)), zero)[0], sn = MFMA(ones, as_frag(pack8(en)), zero)[0];
          const u32x4 pp = pack8(ep, __builtin_amdgcn_rcpf(sp)), pn = pack8(en, __builtin_amdgcn_rcpf(sn));
          const unsigned a = lds0 + SC_PHIK + (h * 128 + wm * 64 + m * 16 + fr) * 160 + fq * 8;
          lds_st<u32x2>(a, u32x2{pp[0], pp[1]});
          lds_st<u32x2>(a + 32, u32x2{pp[2], pp[3]});
          lds_st<u32x2>(a + 64, u32x2{pn[0], pn[1]});
          lds_st<u32x2>(a + 96, u32x2{pn[2], pn[3]});
        }
        WAITL0;
        TRC(19); BAR;
        if constexpr (half) WAITV(0);
        clcheck();
        const int h2 = wave >> 2, dt = (wave >> 1) & 1, s = wave & 1;
        const unsigned kvb = WS_KVP + (unsigned)j * KVP_J + (unsigned)(l >> 1) * KVP_L + (unsigned)wave * 1024;
        const unsigned kzb = WS_KVP + (unsigned)j * KVP_J + (unsigned)(l >> 1) * KVP_L + 8192 + h2 * 256;
        {
          f32x4 c0 = zero, c1 = zero, z0 = zero, z1 = zero;
          const int trow = h2 * 128 + fq * 4 + (fr >> 2);
          const unsigned va = lds0 + SC_VIM + trow * 80 + dt * 32 + (lane & 3) * 8;
          const unsigned pa = lds0 + SC_PHIK + trow * 160 + s * 64 + (lane & 3) * 8;
#pragma unroll
          for (int ks = 0; ks < 4; ++ks) {
            const bf16x8 v = tr2(va + ks * 32 * 80, va + (ks * 32 + 16) * 80);
            const bf16x8 a0 = tr2(pa + ks * 32 * 160, pa + (ks * 32 + 16) * 160);
            const bf16x8 a1 = tr2(pa + 32 + ks * 32 * 160, pa + 32 + (ks * 32 + 16) * 160);
            c0 = MFMA(a0, v, c0);
            c1 = MFMA(a1, v, c1);
            z0 = MFMA(a0, ones, z0);
            z1 = MFMA(a1, ones, z1);
          }
          st16_sc1(rs, lane * 16, kvb + (unsigned)g * KVP_G, nosent(u32x4{pk2(c0[0], c0[1]), pk2(c0[2], c0[3]), pk2(c1[0], c1[1]), pk2(c1[2], c1[3])}));
          if (dt == 0 && fr < 2) st16_sc1(rs, fq * 16 + fr * 64, kzb + (unsigned)g * KVP_G + s * 128, nosent(__builtin_bit_cast(u32x4, fr ? z1 : z0)));
        }
        CLOBBER;
        TRC(20); BAR;
        u32x4 qp[2], qn[2];
        float sqp[2], sqn[2];
        auto phiq = [&](int i) {
          float ep[8], en[8];
          hedgehog_exp(w0, w1, qin[i], cq[i], ep, en);
          qp[i] = pack8(ep); qn[i] = pack8(en);
          sqp[i] = __builtin_amdgcn_rcpf(MFMA(ones, as_frag(qp[i]), zero)[0]);
          sqn[i] = __builtin_amdgcn_rcpf(MFMA(ones, as_frag(qn[i]), zero)[0]);
          OPAQUE_V(qp[i]); OPAQUE_V(qn[i]); OPAQUE_V(sqp[i]); OPAQUE_V(sqn[i]);
        };
        phiq(0); phiq(1);
        TRC(21);
        {
          u32x4 v[16], zr[4];
          const bool doz = (wave & 3) == 0;
          SCHED;
#pragma unroll
          for (int i = 0; i < 4; ++i) zr[i] = u32x4{0u, 0u, 0u, 0u};
          if (doz) {
#pragma unroll
            for (int i = 0; i < 4; ++i) zr[i] = ld16_sc1(rs, (lane & 15) * 16 + (lane >> 4) * (4 * KVP_G), kzb + i * KVP_G);
          }
#pragma unroll
          for (int gg = 0; gg < 16; ++gg) {
            v[gg] = ld16_sc1(rs, lane * 16, kvb + gg * KVP_G);
            SCHED;
            if constexpr (half) {
              lds_st<u32x4>(lds0 + wo_slot(3 + (gg >> 2)) + ((gg & 3) * 4 + hw) * 1024 + lane * 16, wst[gg]);
              SCHED;
            }
          }
          TRC(22);
          const unsigned one = (fq == (fr >> 2)) ? (0x3f80u << (16 * (fr & 1))) : 0u;
          const unsigned sa0 = (fr & 2) ? 0u : one, sa1 = (fr & 2) ? one : 0u;
          const bf16x8 sel0 = as_frag(u32x4{sa0, sa1, 0u, 0u}), sel1 = as_frag(u32x4{0u, 0u, sa0, sa1});
          f32x4 d0 = zero, d1 = zero;
          unsigned mall = umax4v(umax4v(umax4v(umax4v(0u, zr[0]), zr[1]), zr[2]), zr[3]);
#pragma unroll
          for (int gg = 0; gg < 16; ++gg) {
            d0 = MFMA(sel0, as_frag(v[gg]), d0); d1 = MFMA(sel1, as_frag(v[gg]), d1);
            mall = umax4v(mall, v[gg]);
            SGB(0x008, 1); SGB(0x002, 1); SGB(0x008, 1); SGB(0x002, 1);
          }
          SCHED;
          TRC(23);
          if (__builtin_expect(__any(mall == SENT), 0)) {
            for (unsigned spins = 0; !dead; ++spins) {
              if (spins > SPIN_CAP) { dead = true; break; }
              CLOBBER;
              bool again = false;
#pragma unroll
              for (int gg = 0; gg < 16; ++gg)
                if (__any(umax4v(0u, v[gg]) == SENT)) { v[gg] = ld16_sc1(rs, lane * 16, kvb + gg * KVP_G); again = true; }
              if (__any(umax4v(umax4v(umax4v(umax4v(0u, zr[0]), zr[1]), zr[2]), zr[3]) == SENT)) {
#pragma unroll
                for (int i = 0; i < 4; ++i) zr[i] = ld16_sc1(rs, (lane & 15) * 16 + (lane >> 4) * (4 * KVP_G), kzb + i * KVP_G);
                again = true;
              }
              if (!again) break;
              WAITV(0);
            }
            d0 = zero; d1 = zero;
#pragma unroll
            for (int gg = 0; gg < 16; ++gg) { d0 = MFMA(sel0, as_frag(v[gg]), d0); d1 = MFMA(sel1, as_frag(v[gg]), d1); }
#pragma unroll
            for (int i = 0; i < 4; ++i) OPAQUE_V(zr[i]);
          }
          f32x4 z[4];
#pragma unroll
          for (int i = 0; i < 4; ++i) z[i] = __builtin_bit_cast(f32x4, zr[i]);
          lds_st<u32x4>(lds0 + SC_KVF + wave * 1024 + lane * 16, u32x4{pk2(d0[0], d0[1]), pk2(d0[2], d0[3]), pk2(d1[0], d1[1]), pk2(d1[2], d1[3])});
          if (doz) {
            f32x4 zs = (z[0] + z[1]) + (z[2] + z[3]);
#pragma unroll
            for (int e = 0; e < 4; ++e) zs[e] = red_add4(zs[e]);
            if (lane < 16) {
              const int ft = lane >> 2;
              lds_st<u32x2>(lds0 + SC_ZF + ((h2 * 2 + (ft >> 1)) * 4 + (lane & 3)) * 16 + (ft & 1) * 8, u32x2{pk2(zs[0], zs[1]), pk2(zs[2], zs[3])});
            }
          }
          SCHED;
          if constexpr (!half) {
#pragma unroll
            for (int i = 0; i < 8; ++i) OPAQUE_V(vb[i]);
            if (l == 0 && hw) {
              job_colsum(rs, vb, 1, g, JOBC(j, hw - 1), hw - 1, lane);
#pragma unroll
              for (int i = 0; i < 8; ++i) job_pack(rs, vb[i], i, 1, g, JOBC(j, hw - 1), hw - 1, lane);
            } else if (l == 0) {
#pragma unroll
              for (int i = 0; i < 8; ++i) st16_sc1(rs, img_off(j, lane), WS_WO + (unsigned)((16 + JROT(j) + (i >> 1)) * 131072 + g * 8192 + ((j & 3) * 2 + (i & 1)) * 1024), vb[i]);
            }
          }
          WAITL0;
        }
        TRC(24); BAR;
        bf16x8 kf[2][2], zf[2];
#pragma unroll
        for (int d = 0; d < 2; ++d)
#pragma unroll
          for (int sg = 0; sg < 2; ++sg) kf[d][sg] = lds_ld<bf16x8>(lds0 + SC_KVF + (h * 4 + d * 2 + sg) * 1024 + lane * 16);
#pragma unroll
        for (int sg = 0; sg < 2; ++sg) zf[sg] = lds_ld<bf16x8>(lds0 + SC_ZF + ((h * 2 + sg) * 4 + fq) * 16);
#pragma unroll
        for (int i = 0; i < 2; ++i) {
          const float dp = MFMA(zf[0], as_frag(qp[i]), zero)[0], dn = MFMA(zf[1], as_frag(qn[i]), zero)[0];
          const f32x4 p0 = MFMA(kf[0][0], as_frag(qp[i]), zero), n0 = MFMA(kf[0][1], as_frag(qn[i]), zero);
          const f32x4 p1 = MFMA(kf[1][0], as_frag(qp[i]), zero), n1 = MFMA(kf[1][1], as_frag(qn[i]), zero);
          const float inv = __builtin_amdgcn_rcpf(__builtin_fmaf(dp, sqp[i], __builtin_fmaf(dn, sqn[i], 1e-6f)));
          const float fa = sqp[i] * inv, fb = sqn[i] * inv;
          const f32x4 o0 = p0 * fa + n0 * fb, o1 = p1 * fa + n1 * fb;
          const u32x2 X = {pk2(o0[0], o0[1]), pk2(o0[2], o0[3])}, Y = {pk2(o1[0], o1[1]), pk2(o1[2], o1[3])};
          store_o(rs, otile, wm * 4 + half * 2 + i, h, lane, X, Y);
        }
        TRC(25); BAR;
      } else {
        u32x4 wst[12];
        if constexpr (half) tail(wst);
        const unsigned kim = lds0 + SC_IMG + h * (2 * 144 * 80), vim = kim + 144 * 80;
        const float scale = 0.17677669529663687f, L2E = 1.4426950408889634f;
        bf16x8 qf2[2], kf3[3];
        s16x4 vh[3][2];
        u32x4 hk, hv;
        const bool halo = !half && wm == 0 && g > 0;
        const unsigned hso = WS_KVP + (unsigned)j * KVP_J + (unsigned)(g - 1) * KVP_G + (unsigned)(l >> 1) * KVP_L + KVP_HALO + h * 2048;
#pragma unroll
        for (int i = 0; i < 3; ++i) {
          kf3[i] = (half || i == 0) ? lds_ld<bf16x8>(kim + (wm * 64 + (half * 2 + i) * 16 + fr) * 80 + fq * 16) : as_frag(qk[1 + i]);
          const unsigned va = vim + (wm * 64 + (half * 2 + i) * 16 + fq * 4 + (fr >> 2)) * 80 + (lane & 3) * 8;
          vh[i][0] = __builtin_amdgcn_ds_read_tr16_b64_v4i16((lds_s16x4_ptr)(va));
          vh[i][1] = __builtin_amdgcn_ds_read_tr16_b64_v4i16((lds_s16x4_ptr)(va + 32));
        }
#pragma unroll
        for (int i = 0; i < 2; ++i) qf2[i] = half ? lds_ld<bf16x8>(lds0 + sc_qf(true) + hw * 2048 + i * 1024 + lane * 16) : as_frag(qk[i]);
        const int par = fr & 1;
        float rk[3][2], rq[2];
#pragma unroll
        for (int i = 0; i < 3; ++i)
#pragma unroll
          for (int e = 0; e < 2; ++e) rk[i][e] = lds_ld<float>(lds0 + L_RSTD + (wm * 64 + (half * 2 + i) * 16 + fq * 4 + par + 2 * e) * 4);
#pragma unroll
        for (int i = 0; i < 2; ++i) rq[i] = lds_ld<float>(lds0 + L_RSTD + (16 + wm * 64 + (half * 2 + i) * 16 + fr) * 4) * (scale * L2E);
        if constexpr (!half) {
          asm volatile("" : "=v"(hk)); asm volatile("" : "=v"(hv));
          if (halo) { hk = ld16_sc1(rs, lane * 16, hso); hv = ld16_sc1(rs, lane * 16, hso + 1024); }
          SCHED;
        }
#pragma unroll
        for (int ii = 0; ii < 2; ++ii) {
          const int i = half ? ii : 1 - ii;
          if (!half && i == 0) {
            SCHED;
            OPAQUE_V(hk); OPAQUE_V(hv);
            if (wm == 0) {
              kf3[0] = bf16x8{0, 0, 0, 0, 0, 0, 0, 0}; vh[0][0] = s16x4{0, 0, 0, 0}; vh[0][1] = s16x4{0, 0, 0, 0};
              if (g > 0) {
                for (unsigned spins = 0; !dead && __any(umax4v(umax4v(0u, hk), hv) == SENT); ++spins) {
                  if (spins > SPIN_CAP) { dead = true; break; }
                  CLOBBER;
                  hk = ld16_sc1(rs, lane * 16, hso); hv = ld16_sc1(rs, lane * 16, hso + 1024);
                  OPAQUE_V(hk); OPAQUE_V(hv);
                }
                kf3[0] = as_frag(hk);
                lds_st<u32x4>(lds0 + SC_HV + h * 1280 + fr * 80 + fq * 16, hv);
                WAITL0;
                const unsigned va = lds0 + SC_HV + h * 1280 + (fq * 4 + (fr >> 2)) * 80 + (lane & 3) * 8;
                vh[0][0] = __builtin_amdgcn_ds_read_tr16_b64_v4i16((lds_s16x4_ptr)(va));
                vh[0][1] = __builtin_amdgcn_ds_read_tr16_b64_v4i16((lds_s16x4_ptr)(va + 32));
              }
            }
          }
          const int m = half * 2 + i;
          const int r0 = wm * 64 + m * 16;
          const bf16x8 qf = qf2[i], k0 = kf3[i], k1 = kf3[i + 1];
          const bf16x8 v0 = __builtin_shufflevector(vh[i][0], vh[i + 1][0], 0, 1, 2, 3, 4, 5, 6, 7);
          const bf16x8 v1 = __builtin_shufflevector(vh[i][1], vh[i + 1][1], 0, 1, 2, 3, 4, 5, 6, 7);
          f32x4 s0 = MFMA(k0, qf, zero);
          f32x4 s1 = MFMA(k1, qf, zero);
          const bool has_prev = (g > 0) || (r0 > 0);
          float x[4] = {par ? s0[1] : s0[0], par ? s0[3] : s0[2], par ? s1[1] : s1[0], par ? s1[3] : s1[2]};
#pragma unroll
          for (int e = 0; e < 4; ++e) {
            const int delta = fr + 16 - (e >> 1) * 16 - fq * 4 - par - (e & 1) * 2;
            const bool ok = (delta >= 0) && (delta <= 14) && ((e >= 2) || has_prev);
            x[e] = ok ? x[e] * rk[i + (e >> 1)][e & 1] : -3.0e38f;
          }
          const float mx = red_max4(fmaxf(fmaxf(x[0], x[1]), fmaxf(x[2], x[3])));
          const float mxl = mx * -rq[i];
          float ex[4];
#pragma unroll
          for (int e = 0; e < 4; ++e) ex[e] = __builtin_amdgcn_exp2f(__builtin_fmaf(x[e], rq[i], mxl));
          const float inv = __builtin_amdgcn_rcpf(red_add4((ex[0] + ex[1]) + (ex[2] + ex[3])));
          u32x4 pf;
#pragma unroll
          for (int e = 0; e < 4; ++e) pf[e] = pk2(ex[e] * inv * rk[i + (e >> 1)][e & 1], 0.f) << (16 * par);
          f32x4 o0 = MFMA(v0, as_frag(pf), zero);
          f32x4 o1 = MFMA(v1, as_frag(pf), zero);
          if (half && i == 0) {
            WAITV(0);
#pragma unroll
            for (int k = 0; k < 12; ++k) OPAQUE_V(wst[k]);
            OPAQUE_V(clf);
          }
          store_o(rs, otile, wm * 4 + m, h, lane, u32x2{pk2(o0[0], o0[1]), pk2(o0[2], o0[3])}, u32x2{pk2(o1[0], o1[1]), pk2(o1[2], o1[3])});
        }
        clcheck();
        TRC(25); BAR;
        if constexpr (half) {
#pragma unroll
          for (int k = 0; k < 12; ++k) lds_st<u32x4>(lds0 + wo_slot(4 + (k >> 2)) + ((k & 3) * 4 + hw) * 1024 + lane * 16, wst[k]);
          WAITL0;
          if (lane == 0) __hip_atomic_fetch_add((LAS unsigned*)(lds0 + L_CNT), 1u, __ATOMIC_RELAXED, __HIP_MEMORY_SCOPE_WORKGROUP);
        }
      }
    };
    if (cw) {
      if (l0 == 0) {
        LOCALS;
        u32x4 v[2][8];
        if (wave < 3) {
          const int jc = JOBC(j, wave), kt = (JROT(j) + 8 * (g & 1) + (g >> 1)) & 15;
          const rsrc_t ra = mk_rsrc(p.wqkvA, 1024 * 6144);
#pragma unroll
          for (int i = 0; i < 8; ++i)
            v[0][i] = __builtin_amdgcn_raw_buffer_load_b128(ra, (lane >> 3) * 6144 + (lane & 7) * 16, (kt * 64 + i * 8) * 6144 + wave * 2048 + jc * 128, 0);
          SCHED;
          TRC(44); BAR; SCHED;
          job_colsum(rs, v[0], 0, g, jc, wave, lane);
          if (SLOWC(jc)) {
#pragma unroll
            for (int i = 0; i < 8; ++i) job_pack(rs, v[0][i], i, 0, kt, jc, wave, lane);
          }
        } else {
          const int fr = lane & 15, fq = lane >> 4;
          const rsrc_t rx = mk_rsrc(p.x, 2048 * 2048);
#pragma unroll
          for (int i = 0; i < 16; ++i)
            v[i & 1][i >> 1] = __builtin_amdgcn_raw_buffer_load_b128(rx, fr * 2048 + fq * 16, (g * 128 + (j & 3) * 32 + ((i >> 1) & 1) * 16) * 2048 + ((JROT(j) + 8 * (g & 1)) & 15) * 128 + (i >> 2) * 128 + (i & 1) * 64, 0);
          SCHED;
          TRC(44); BAR; SCHED;
          const f32x4 zero = {0.f, 0.f, 0.f, 0.f};
          const bf16x8 ones = {0x3f80, 0x3f80, 0x3f80, 0x3f80, 0x3f80, 0x3f80, 0x3f80, 0x3f80};
          float sm[8], q[8];
#pragma unroll
          for (int m = 0; m < 8; ++m) {
            const f32x4 a = MFMA(ones, as_frag(v[1][m]), MFMA(ones, as_frag(v[0][m]), zero));
            const f32x4 b = MFMA(as_frag(v[1][m]), as_frag(v[1][m]), MFMA(as_frag(v[0][m]), as_frag(v[0][m]), zero));
            sm[m] = a[0];
            q[m] = (fr & 2) ? ((fr & 1) ? b[3] : b[2]) : ((fr & 1) ? b[1] : b[0]);
          }
          if (fq == (fr >> 2)) {
#pragma unroll
            for (int m = 0; m < 8; ++m) st8_sc1(rs, ((m & 1) * 16 + fr) * 8 + (m >> 1) * 1024, st_off(g, 0) + JROT(j) * 1024 + (j & 3) * 256, u32x2{__float_as_uint(sm[m]), __float_as_uint(q[m])});
          }
        }
      }
      LOCALS;
      qkv_loop(isb, lds0, wm, h, lane, l == 0, trv,
               [&](u32x4 kk, u32x4 vv) {
                 LOCALS;
                 const unsigned so = WS_KVP + (unsigned)j * KVP_J + (unsigned)g * KVP_G + (unsigned)(l >> 1) * KVP_L + KVP_HALO + h * 2048;
                 st16_sc1(rs, lane * 16, so, nosent(kk));
                 st16_sc1(rs, lane * 16, so + 1024, nosent(vv));
               },
               [&](auto isb_c, const u32x4 (&qk)[4]) {
                 attention(std::integral_constant<int, 0>{}, isb_c, qk);
               });
#pragma unroll
      for (int i = 0; i < 4; ++i) asm volatile("" : "=v"(xr[i >> 1][i & 1]));
    } else {
      __builtin_amdgcn_s_setprio(3);
      LOCALS;
      const unsigned lin = lane * 16, vlin = lin + hw * 1024;
      const int jr = JROT(j);
      u32x4 sent4 = {SENT, SENT, SENT, SENT};
      OPAQUE_V(sent4);
      const unsigned cb0 = ob_off(g, j, l), cb2 = xb_off(g, l) + j * 16384 - 8 * 4096;
      const unsigned cb1 = WS_KVP + (unsigned)j * KVP_J + (unsigned)g * KVP_G + (unsigned)(l >> 1) * KVP_L - 4 * 4096;
      auto ldres = [&](int slot) {
        const int fr = lane & 15, fq = lane >> 4, ch = (fq & 1) * 2 + (fq >> 1);
#pragma unroll
        for (int i = 0; i < 4; ++i)
          xr[i >> 1][i & 1] = lds_ld<u32x4>(lds0 + L_R2 + slot * XS + (hw * 32 + ((i >> 1) ^ 1) * 16 + fr) * 128 + (((ch + 4 * (i & 1)) ^ ((fr >> 1) & 7)) * 16));
      };
      auto aslot = [&](int t) { return lds0 + L_R2 + (t % 3) * XS + hw * 1024; };
      auto wslot = [&](int t) { return lds0 + L_R1 + (t & 3) * 24576 + hw * 1024; };
      const bool l0f = l == 0;
      const char* pA = l0f ? (const char*)p.x : p.ws;
      const char* pW = l0f ? (const char*)p.wqkvA : p.ws;
      unsigned zA = l0f ? 2048 * 2048 : WS_SPAN, zW = l0f ? 1024 * 6144 : WS_SPAN;
      unsigned bA = l0f ? g * (128 * 2048) : xb_off(g, l - 1), kA = l0f ? 128 : 16384, pcA = l0f ? 32 * 2048 : 4096;
      unsigned bW = l0f ? j * 128 : WS_WP + (unsigned)((isb ? 16 : 0) + j) * (16 * 24576), kW = l0f ? 64 * 6144 : 24576;
      unsigned pcW = l0f ? 2048 : 8192, hW = l0f ? 32 * 6144 : 4096;
      int kb = jr + (l0f ? 8 * (g & 1) : 0);
      OPAQUE_S(pA); OPAQUE_S(pW); OPAQUE_S(zA); OPAQUE_S(zW); OPAQUE_S(bA); OPAQUE_S(kA); OPAQUE_S(pcA);
      OPAQUE_S(bW); OPAQUE_S(kW); OPAQUE_S(pcW); OPAQUE_S(hW); OPAQUE_S(kb);
      const rsrc_t rA = mk_rsrc(pA, zA);
      unsigned vA = vlin, vW = vlin;
      if (l0f) {
        vA = (hw * 8 + (lane >> 3)) * 2048 + (((lane & 7) ^ (((hw & 1) * 4 + (lane >> 4)) & 7)) * 16);
        vW = (hw * 8 + (lane >> 3)) * 6144 + (((lane & 7) ^ ((((lane >> 4) & 1) | ((hw & 1) << 1)) << 1)) * 16);
      }
      int dj = l0f ? -1 : (j & 3);
      OPAQUE_S(dj);
      auto kseq = [&](int t) { return (kb + t - ((t < 4 && t <= dj) ? 1 : 0)) & 15; };
      auto offA = [&](int t) { return bA + (unsigned)kseq(t) * kA; };
      auto offW = [&](int t) { return bW + (unsigned)kseq(t) * kW; };
      auto ldA = [&](int t) { dma4<4096>(rA, vA, offA(t), pcA, aslot(t)); };
      auto ldWh = [&](int t, unsigned o, int hf) { dma3<8192>(mk_rsrc(pW, zW), vW, o + (hf ? hW : 0u), pcW, wslot(t) + hf * 4096); };
      const bool slowj = l0f && SLOWC(j);
      auto packedW = [&]() {
        pW = p.ws; zW = WS_SPAN; bW = WS_WP + (unsigned)j * (16 * 24576); kW = 24576; pcW = 8192; hW = 4096; vW = vlin;
      };
      auto ldW = [&](int t) { const unsigned o = offW(t); ldWh(t, o, 0); ldWh(t, o, 1); };
      const unsigned cmask = 0xfu | (isb ? 0u : 0xf0u) | (l == 3 ? 0u : 0xf00u);
      auto clean = [&](int t) {
        if (t < 4 || (t < 12 && ((cmask >> t) & 1))) st16_sc1(rs, vlin, (t < 4 ? cb0 : (t < 8 ? cb1 : cb2)) + (unsigned)t * 4096, sent4);
      };
      const unsigned flo = myrep + ((l0f && lane >= 16) ? fl_line(FL_CS, JOBC(j, (lane >> 4) - 1)) : fl_line(FL_X + l, g)) + (lane & 15) * 8;
      unsigned ready = ~0u;
      int tc = -2, tr = 0;
      u32x4 cv[4];
#pragma unroll
      for (int ii = 0; ii < 4; ++ii) asm volatile("" : "=v"(cv[ii]));
      auto chkrd = [&](int t) {
#pragma unroll
        for (int ii = 0; ii < 4; ++ii) cv[ii] = lds_ld<u32x4>(aslot(t) + lin + ii * 4096);
      };
      auto chkbad = [&]() { return __any(max(umax4v(umax4v(0u, cv[0]), cv[1]), umax4v(umax4v(0u, cv[2]), cv[3])) == SENT) && !l0f; };
      auto refetch = [&](int t) __attribute__((always_inline)) {
        for (unsigned spins = 0; !dead; ++spins) {
          if (spins > SPIN_CAP) { dead = true; break; }
          WAITV(0);
          ldA(t);
          ldA(t + 1);
          WAITV(0);
          chkrd(t);
          if (!chkbad()) break;
          __builtin_amdgcn_s_sleep(1);
        }
      };
      if (l0f) {
        u32x2 gw2;
        ld8_asm(gw2, rs, WS_EPOCH + 0u * lin);
        TRC(44); BAR;
        ldW(0); ldA(0);
        ldW(1);
        dma1(mk_rsrc(p.wphi, 32 * 2048), vlin, j * 4096, lds0 + L_WPHI + hw * 1024);
        if (lane < 16) lds_st<float>(lds0 + L_RSTD + lane * 4, 1.0f);
        lds_st<unsigned>(lds0 + L_CNT, 0u);
        WAITV(17);
        OPAQUE_V(gw2);
        const u64 gword = ((u64)(unsigned)UNI(gw2[1]) << 32) | (unsigned)UNI(gw2[0]);
        u64 z = gword + 0x9e3779b97f4a7c15ull;
        z = (z ^ (z >> 30)) * 0xbf58476d1ce4e5b9ull;
        z = (z ^ (z >> 27)) * 0x94d049bb133111ebull;
        z = z ^ (z >> 31);
        z = (z == gword) ? ~z : z;
        epoch = u32x2{(unsigned)z, (unsigned)(z >> 32)};
        lds_st<u32x2>(lds0 + L_EP, epoch);
        tc = slowj ? -2 : (g >> 1) + 2; tr = (j & 3) + 8 * (g & 1);
      }
#pragma unroll
      for (int i = 0; i < 4; ++i) asm volatile("" : "=v"(xr[i >> 1][i & 1]));
      unsigned oA = offA(3), oW = offW(2);
      OPAQUE_S(oA); OPAQUE_S(oW);
      WAITV(6);
      WAITL0;
      TRC(1); BAR;
      if (WFIRST) { ldWh(2, oW, 0); ldWh(2, oW, 1); }
      if (XD) __builtin_amdgcn_s_sleep(XD);
      ldA(1); ldA(2);
      TRC(40);
      if (!WFIRST) { ldWh(2, oW, 0); ldWh(2, oW, 1); }
      if (LAW == 3) ldW(3);
      if (tr == 0) ldres(0);
      clean(0);
      WAITV((WFIRST ? 5 : 11) + (LAW == 3 ? 6 : 0));
      chkrd(1);
      if (__builtin_expect(chkbad(), 0)) refetch(1);
      const int fr = lane & 15, fq = lane >> 4;
      u32x2 fl;
      f32x4 js[4], jc[4];
      float ra[4], rb[4];
      u32x4 wo2[2];
      asm volatile("" : "=v"(wo2[0])); asm volatile("" : "=v"(wo2[1]));
      const unsigned fwo = myrep + fl_line(FL_WO + (isb ? 1 : 0), jr + (lane >> 4)) + (lane & 15) * 8;
      static_for<1, 14>([&](auto t_c) __attribute__((always_inline)) {
        constexpr int t = decltype(t_c)::value;
        constexpr auto rest = [](int s) {
          return s == 0 ? (WFIRST ? 0 : 6) + (LAW == 3 ? 6 : 0) + 1
                        : (s + LAW <= 15 ? 6 : 0) + (s < 4) + (s == NVAL - 2 || s == 5 || s == 11) + ((s == 7 || s == 8) ? 4 : 0);
        };
        constexpr int NW = t == 1 ? 63 : 4 + rest(t - 1) + ((LAW == 3 && (t > 2 || WFIRST)) ? rest(t - 2) : 0);
        TRC(1 + t);
        if (t >= NVAL && t <= 6 && __builtin_expect((ready & (1u << kseq(t + 2))) == 0, 0)) {
          WAITV(NW); WAITL0; BAR;
          ready = poll_need(rs, flo, epoch, ready, 1u << kseq(t + 2), dead);
          dma4<4096>(rA, vA, oA, pcA, aslot(t + 2));
        } else if (t == 7 && __builtin_expect(ready != ~0u, 0)) {
          WAITV(NW); WAITL0; BAR;
          poll_all(rs, flo, epoch, dead);
          dma4<4096>(rA, vA, oA, pcA, aslot(t + 2));
        } else {
          bar_dma4<4096, NW>(rA, vA, oA, pcA, aslot(t + 2));
        }
        if (t == 1 && lead) set_flags(rs, fl_line(FL_X + l, g), j, l0f ? fl_line(FL_CS, j) : fl_line(FL_X + l, g), l0f ? g : j, epoch, lane);
        if (t == 7 && slowj) packedW();
        if (t + LAW <= 15) oW = offW(t + LAW);
        if (t < 13) oA = offA(t + 3);
        OPAQUE_S(oA); OPAQUE_S(oW);
        clean(t);
        if (t <= NVAL && !l0f) { WAITV(rest(t - 1) + 4 + (t < 4)); chkrd(t + 1); }
        if (t == WT + 3 && l0f) {
          OPAQUE_V(wo2[0]); OPAQUE_V(wo2[1]);
#pragma unroll
          for (int i = 0; i < 2; ++i) st16_sc1(rs, lin, WS_WO + (unsigned)((jr + hw) * 131072 + g * 8192 + ((j & 3) * 2 + i) * 1024), wo2[i]);
        }
        if (t == WT + 6 && l0f && lead) set_flags(rs, fl_line(FL_WO, j), g, fl_line(FL_WO, j), g, epoch, lane);
        if (t >= 2 && t <= 9 && (t & 1) == (LAW == 2) && (tc | 1) == (t | 1)) {
          const unsigned src = lds0 + L_R1 + (tc & 3) * 24576 + vlin;
          u32x4 w[6];
#pragma unroll
          for (int ii = 0; ii < 6; ++ii) w[ii] = lds_ld<u32x4>(src + ii * 4096);
#pragma unroll
          for (int ii = 0; ii < 6; ++ii) st16_sc1(rs, vlin, WS_WP + (unsigned)((j * 16 + ((kb + tc) & 15)) * 24576 + ii * 4096), w[ii]);
        }
        if ((t < 4 || (t >= 8 && t < 12)) && tr == t) ldres(t % 3);
        if (t == 7) {
#pragma unroll
          for (int i = 0; i < 4; ++i) ld16_asm(jc[i], rs, fq * 768 + hw * 192 + (fr < 12 ? fr : 0) * 16, WS_CS + (unsigned)(((isb ? 16 : 0) + j) * 16 + i * 4) * 768);
        }
        if (t == 8) {
#pragma unroll
          for (int i = 0; i < 4; ++i) ld16_asm(js[i], rs, fq * 1024 + hw * 256 + fr * 16, st_off(g, l) + i * 4096);
        }
        SCHED;
        if (t + LAW <= 15) ldWh(t + LAW, oW, 0);
        SCHED;
        bool bad = false;
        if (t <= NVAL) bad = chkbad();
        if (t == 10) {
#pragma unroll
          for (int i = 0; i < 4; ++i) OPAQUE_V(jc[i]);
          const f32x4 s = (jc[0] + jc[1]) + (jc[2] + jc[3]);
#pragma unroll
          for (int i = 0; i < 4; ++i) { ra[i] = s[i]; OPAQUE_V(ra[i]); }
        }
        if (t == 11) {
#pragma unroll
          for (int i = 0; i < 4; ++i) OPAQUE_V(js[i]);
          const f32x4 s = (js[0] + js[1]) + (js[2] + js[3]);
#pragma unroll
          for (int i = 0; i < 4; ++i) { rb[i] = s[i]; OPAQUE_V(rb[i]); }
        }
        if (t == 12) {
          red2_add(ra[0], ra[1]); red2_add(ra[2], ra[3]); red2_add(rb[0], rb[1]); red2_add(rb[2], rb[3]);
          if (lane < 12) lds_st<f32x4>(lds0 + L_CS + hw * 192 + lane * 16, f32x4{ra[0], ra[1], ra[2], ra[3]});
#pragma unroll
          for (int i = 0; i < 4; ++i) OPAQUE_V(rb[i]);
        }
        if (t == 13) {
          const float m0 = rb[0] * (1.0f / 1024), m1 = rb[2] * (1.0f / 1024);
          const float r0 = __builtin_amdgcn_rsqf(fmaxf(__builtin_fmaf(rb[1], 1.0f / 1024, -m0 * m0), 0.f) + 1e-5f);
          const float r1 = __builtin_amdgcn_rsqf(fmaxf(__builtin_fmaf(rb[3], 1.0f / 1024, -m1 * m1), 0.f) + 1e-5f);
          if (lane < 16) {
            lds_st<f32x2>(lds0 + L_MEAN + 64 + hw * 128 + fr * 8, f32x2{-m0, -m1});
            lds_st<f32x2>(lds0 + L_RSTD + 64 + hw * 128 + fr * 8, f32x2{r0, r1});
          }
        }
        SCHED;
        if (t + LAW <= 15) ldWh(t + LAW, oW, 1);
        if (t == NVAL - 1 || t == 6) {
          WAITV(10);
          OPAQUE_V(fl);
          if (t == 6) ready = __all(is_set(fl, epoch)) ? ~0u : 0u;
          else {
            ready = l0f ? ~0u : (unsigned)__ballot(is_set(fl, epoch));
            if (slowj && __all(is_set(fl, epoch))) packedW();
          }
        }
        if (t == 12) { WAITV(10); OPAQUE_V(fl); }
        if (t == 13 && __builtin_expect(!__all(is_set(fl, epoch)), 0)) poll_all(rs, fwo, epoch, dead);
        if (t == NVAL - 2 || t == 5) ld8_asm(fl, rs, flo);
        if (t == 11) ld8_asm(fl, rs, fwo);
        if (t == WT && l0f) {
#pragma unroll
          for (int i = 0; i < 2; ++i) ld16nt_asm(wo2[i], mk_rsrc(p.woA, 1024 * 2048), wo_swz(j * 2, lane), (g * 64 + (j & 3) * 16 + i * 8) * 2048 + (jr + hw) * 128);
        }
        if constexpr (t <= NVAL) { if (__builtin_expect(bad, 0)) refetch(t + 1); }
      });
      const u32x4 qk[4] = {};
      if (isb) attention(std::integral_constant<int, 1>{}, std::true_type{}, qk);
      else attention(std::integral_constant<int, 1>{}, std::false_type{}, qk);
    }
    {
      LOCALS;
      const int fr = lane & 15, fq = lane >> 4;
      const int kh = half;
      const unsigned v0 = lane * 16 + hw * 4096 + kh * 2048, v1 = lane * 16 + hw * 4096 + (kh ^ 1) * 2048;
      const int r0 = (JROT(j) >> 1) + 2;
      const unsigned ob0 = ob_off(g, kh, l);
      const unsigned pko = myrep + fl_line(FL_PK + (fq == 3 ? 0 : fq), j) + fr * 8;
      u32x4 R[8][4];
      u32x2 pkf;
      auto issue_ = [&](int i) {
        const unsigned so = ob0 + (unsigned)((r0 + i) & 7) * 131072;
        R[i][0] = ld16_sc1(rs, v0, so); R[i][1] = ld16_sc1(rs, v1, so);
        R[i][2] = ld16_sc1(rs, v0 + 1024, so); R[i][3] = ld16_sc1(rs, v1 + 1024, so);
      };
      auto issue = [&](int i) { issue_(i); SCHED; };
      constexpr int LA = LOOKAHEAD;
      TRC(26); TRR(61);
      __builtin_amdgcn_s_setprio(3);
      SCHED;
      issue(0);
      __builtin_amdgcn_s_setprio(2);
#pragma unroll
      for (int i = 1; i < LA; ++i) issue(i);
      auto vmax = [&](int i) { return umax4v(umax4v(umax4v(umax4v(0u, R[i][0]), R[i][1]), R[i][2]), R[i][3]); };
      auto bad = [&](int i) { return __any(umax4v(umax4v(umax4v(umax4v(0u, R[i][0]), R[i][1]), R[i][2]), R[i][3]) == SENT); };
      auto poll = [&](int i, int nfl) {
        for (unsigned spins = 0; !dead; ++spins) {
          if (spins > SPIN_CAP) { dead = true; break; }
          __builtin_amdgcn_s_sleep(2);
          CLOBBER;
          issue(i);
#pragma unroll
          for (int k = i + 1; k < i + nfl && k < 8; ++k)
            if (bad(k)) issue(k);
          WAITV(0);
#pragma unroll
          for (int k = i + 1; k < i + nfl && k < 8; ++k)
#pragma unroll
            for (int f = 0; f < 4; ++f) OPAQUE_V(R[k][f]);
          if (!bad(i)) break;
        }
      };
      unsigned bb[4];
      {
        const int kr = (fq & 1) * 16 + (fq >> 1) * 4 + (fr >> 2);
        const int gs = ((kr >> 1) & 1) | (((kr >> 4) & 1) << 1);
#pragma unroll
        for (int c = 0; c < 4; ++c) bb[c] = lds0 + kh * 8192 + kr * 128 + ((c ^ gs) * 32) + (lane & 3) * 8;
      }
      bf16x8 bf[2][4];
      auto rdB = [&](int i, int hh) {
        if (hh == 0) {
#pragma unroll
          for (int c = 0; c < 4; ++c) { bb[c] += wo_slot(i) - (i ? wo_slot(i - 1) : 0u); asm("" : "+v"(bb[c])); }
        }
#pragma unroll
        for (int c = 0; c < 4; ++c) bf[hh][c] = tr2(bb[c] + hh * 4096, bb[c] + hh * 4096 + 1024);
      };
      f32x4 acc[2][4];
#pragma unroll
      for (int i = 0; i < 8; ++i) acc[i >> 2][i & 3] = f32x4{0.f, 0.f, 0.f, 0.f};
      auto accinit = [&]() {
#pragma unroll
        for (int m = 0; m < 2; ++m)
#pragma unroll
          for (int pp = 0; pp < 2; ++pp) {
            auto r0_ = __builtin_amdgcn_permlane16_swap(xr[m][pp][0], xr[m][pp][2], false, false);
            auto r1_ = __builtin_amdgcn_permlane16_swap(xr[m][pp][1], xr[m][pp][3], false, false);
            acc[m][2 * pp] = f32x4{lo16(r0_[0]), hi16(r0_[0]), lo16(r1_[0]), hi16(r1_[0])};
            acc[m][2 * pp + 1] = f32x4{lo16(r0_[1]), hi16(r0_[1]), lo16(r1_[1]), hi16(r1_[1])};
          }
      };
      auto mms = [&](int i, int hh) {
#pragma unroll
        for (int n = 0; n < 4; ++n)
#pragma unroll
          for (int m = 0; m < 2; ++m) acc[m][n] = MFMA(bf[hh][n], as_frag(R[i][hh * 2 + m]), acc[m][n]);
      };
      if (half) accinit();
      const char* obase = (l == 3) ? (const char*)p.out : (const char*)p.ws;
      unsigned osize = (l == 3) ? 2048 * 2048 : WS_SPAN;
      unsigned sq = WS_WP + (unsigned)(((isb ? 0 : 16) + j) * 16 + j) * 24576 + kh * 12288;
      unsigned sq1 = WS_WP + (unsigned)(((isb ? 0 : 16) + j) * 16 + JROT(j) + ((1 >> (j & 3)) & 1)) * 24576 + kh * 12288;
      unsigned dst = lds0 + L_R1 + kh * 12288 + hw * 1024, vq = hw * 1024 + lane * 16;
      OPAQUE_S(obase); OPAQUE_S(osize); OPAQUE_S(sq); OPAQUE_S(sq1); OPAQUE_S(dst); OPAQUE_V(vq);
      const rsrc_t ro = mk_rsrc(obase, osize);
      unsigned so[2];
      unsigned xw = lds0 + SC_XCH + wave * 4096 + lane * 16;
      unsigned xa = lds0 + SC_XCH + (wave ^ 4) * 4096 + lane * 16;
      unsigned sb0 = (l == 3) ? g * (128 * 2048) + j * 128 : xb_off(g, l) + j * 16384;
      {
        const int row = (hw * 2 + kh) * 16 + fr, ch = (fq & 1) * 2 + (fq >> 1);
        const unsigned sw = (l == 3) ? 0u : (unsigned)((row >> 1) & 7), rb = (unsigned)row << ((l == 3) ? 11 : 7);
#pragma unroll
        for (int pp = 0; pp < 2; ++pp) { so[pp] = rb + (((ch + 4 * pp) ^ sw) * 16); OPAQUE_V(so[pp]); }
        OPAQUE_V(xa); OPAQUE_V(xw); OPAQUE_S(sb0);
      }
      SCHED;
      rdB(0, 0);
      unsigned mx = vmax(0);
#pragma unroll
      for (int i = 0; i < 8; ++i) {
        SCHED;
        if (__builtin_expect(__any(mx == SENT), 0)) poll(i, LA);
        if (i == 0) TRC(27); else TRQ(27 + i);
        if (i == 1 && l == 0 && !half) {
          const unsigned ln = hw ? fl_line(FL_PK + hw - 1, JOBC(j, hw - 1)) : fl_line(FL_WO + 1, j);
          set_flags(rs, ln, g, ln, g, lds_ld<u32x2>(lds0 + L_EP), lane);
        }
        if (i == 5) { pkf = ld8_sc1(rs, pko, 0); SCHED; }
        if (i == 3) {
          for (unsigned spins = 0; spins < SPIN_CAP; ++spins) {
            CLOBBER;
            if (UNI(lds_ld<unsigned>(lds0 + L_CNT)) >= 2u * (l + 1) - (isb ? 0u : 2u)) break;
          }
          CLOBBER;
        }
        if (i == 7) {
          OPAQUE_V(pkf);
          if (l == 0 && lead && !__all(is_set(pkf, epoch))) poll_all(rs, pko, epoch, dead);
          TRC(35); BAR;
        }
        if (i == 7 && l < 3) dma3<4096>(rs, vq, sq, 4096, dst);
        if (i == 2) __builtin_amdgcn_s_setprio(1);
        if (i == 5) __builtin_amdgcn_s_setprio(0);
        SCHED;
        rdB(i, 1);
        if (i + LA < 8) issue_(i + LA);
        mms(i, 0);
#pragma unroll
        for (int k = 0; k < 4; ++k) { SGB(0x008, 1); SGB(0x100, 1); SGB(0x020, 1); SGB(0x008, 1); SGB(0x100, 1); }
        SCHED;
        if (i == 7 && l < 3) { dma3<4096>(rs, vq, sq1, 4096, dst + 24576); SCHED; }
        if (i < 7) { rdB(i + 1, 0); mx = vmax(i + 1); }
        mms(i, 1);
#pragma unroll
        for (int k = 0; k < 4; ++k) { SGB(0x008, 1); SGB(0x100, 2); }
#pragma unroll
        for (int k = 4; k < 8; ++k) { SGB(0x008, 1); SGB(0x002, 8 / (8 - 4)); }
      }
      SCHED;
      TRC(36);
#pragma unroll
      for (int n = 0; n < 4; ++n) lds_st<f32x4>(xw + n * 1024, acc[1][n]);
      WAITL0;
      TRC(37); BAR;
      SCHED;
      f32x4 pa[4];
#pragma unroll
      for (int n = 0; n < 4; ++n) pa[n] = lds_ld<f32x4>(xa + n * 1024);
      SCHED;
      u32x4 o[2];
#pragma unroll
      for (int pp = 0; pp < 2; ++pp) {
        const f32x4 a = acc[0][2 * pp] + pa[2 * pp], b = acc[0][2 * pp + 1] + pa[2 * pp + 1];
        auto r0_ = __builtin_amdgcn_permlane16_swap(pk2(a[0], a[1]), pk2(b[0], b[1]), false, false);
        auto r1_ = __builtin_amdgcn_permlane16_swap(pk2(a[2], a[3]), pk2(b[2], b[3]), false, false);
        o[pp] = nosent(u32x4{r0_[0], r1_[0], r0_[1], r1_[1]});
        st16_sc1(ro, so[pp], sb0, o[pp]);
      }
      if (l < 3) {
        lds_st<u32x4>(lds0 + L_R2 + so[0], o[0]);
        lds_st<u32x4>(lds0 + L_R2 + so[1], o[1]);
        const f32x4 zero = {0.f, 0.f, 0.f, 0.f};
        const bf16x8 ones = {0x3f80, 0x3f80, 0x3f80, 0x3f80, 0x3f80, 0x3f80, 0x3f80, 0x3f80};
        const f32x4 a = MFMA(ones, as_frag(o[1]), MFMA(ones, as_frag(o[0]), zero));
        const f32x4 b = MFMA(as_frag(o[1]), as_frag(o[1]), MFMA(as_frag(o[0]), as_frag(o[0]), zero));
        const float q = (fr & 2) ? ((fr & 1) ? b[3] : b[2]) : ((fr & 1) ? b[1] : b[0]);
        if (fq == (fr >> 2)) st8_sc1(rs, ((hw * 2 + kh) * 16 + fr) * 8, st_off(g, l + 1) + j * 1024, u32x2{__float_as_uint(a[0]), __float_as_uint(q)});
      } else if (lead && blockIdx.x == 0 && lane == 0) {
        st8_sc1(rs, WS_EPOCH, 0, epoch);
      }
    }
    TRC(38); TRR(63);
  }
  STAMP_DUMP;
  asm volatile(".Lpf_end:");
}

extern "C" void kernel_launch(void* const* d_in, const int* in_sizes, int n_in, void* d_out, int out_size,
                              void* d_ws, size_t ws_size, hipStream_t stream) {
  Params p;
  p.x = (const u16*)d_in[0];
  p.wqkvA = (const u16*)d_in[1];
  p.wphi = (const u16*)d_in[2];
  p.woA = (const u16*)d_in[3];
  p.wqkvB = (const u16*)d_in[5];
  p.woB = (const u16*)d_in[6];
  p.out = (u16*)d_out;
  p.ws = (char*)d_ws;
  (void)hipFuncSetAttribute((const void*)mega_kernel, hipFuncAttributeMaxDynamicSharedMemorySize, L_TOTAL);
  mega_kernel<<<256, 512, L_TOTAL, stream>>>(p);
}
```

```cpp
#include <hip/hip_runtime.h>
#include <stdint.h>
#include <type_traits>
constexpr int LOOKAHEAD = 2;
constexpr int NVAL = 4;
#define XD 0
#define WFIRST 1
#define WT 1
#define LAW 2
#define POL0 0
#define RX 8
#define H1D 2
#define H2D 0

typedef unsigned short u16;
typedef unsigned long long u64;
typedef __attribute__((ext_vector_type(8))) short bf16x8;
typedef __attribute__((ext_vector_type(4))) short s16x4;
typedef __attribute__((ext_vector_type(4))) float f32x4;
typedef __attribute__((ext_vector_type(2))) float f32x2;
typedef __attribute__((ext_vector_type(4))) unsigned u32x4;
typedef __attribute__((ext_vector_type(2))) unsigned u32x2;
typedef __attribute__((ext_vector_type(2))) __bf16 bf16x2_t;
#define LAS __attribute__((address_space(3)))
typedef LAS s16x4* lds_s16x4_ptr;

constexpr unsigned NREP = 8, REP_STRIDE = 17 * 2048;
enum { FL_X = 0, FL_CS = 4, FL_CLR = 5, FL_CLC = 9, FL_PK = 13, FL_WO = 14 };
__device__ __forceinline__ unsigned fl_line(int set, int a) { return (unsigned)(set * 16 + a) * 128; }
constexpr unsigned WS_EPOCH = 512u << 10;
constexpr unsigned WS_CS = 1u << 20;
constexpr unsigned WS_KVP = 4u << 20;
constexpr unsigned KVP_J = 512u << 10, KVP_G = 32u << 10, KVP_L = 16u << 10, KVP_Z = 8192, KVP_SUM = 8704, KVP_HALO = 12u << 10;
constexpr unsigned SENT = 0xffffffffu;
constexpr unsigned WS_G = 16u << 20, G_STRIDE = 2u << 20;
__device__ __forceinline__ unsigned xb_off(int g, int l) { return WS_G + (unsigned)g * G_STRIDE + (unsigned)l * (256u << 10); }
__device__ __forceinline__ unsigned ob_off(int g, int j, int l) { return WS_G + (unsigned)g * G_STRIDE + (3u << 18) + (unsigned)j * (64u << 10) + (unsigned)l * (16u << 10); }
__device__ __forceinline__ unsigned st_off(int g, int l) { return WS_G + (unsigned)g * G_STRIDE + (7u << 18) + (unsigned)l * (16u << 10); }
constexpr unsigned WS_WP = 48u << 20;
constexpr unsigned WS_WO = 60u << 20;
constexpr unsigned WS_SPAN = 64u << 20;

constexpr unsigned L_R1 = 0;
constexpr unsigned L_R2 = 98304, XS = 16384;
constexpr unsigned L_MISC = 147456;
constexpr unsigned L_MEAN = L_MISC;
constexpr unsigned L_RSTD = L_MISC + 576;
constexpr unsigned L_CS = L_MISC + 1152;
constexpr unsigned L_WPHI = L_MISC + 2688;
constexpr unsigned L_SINK = L_MISC + 6912;
constexpr unsigned L_ZF = L_MISC + 1920;
constexpr unsigned L_PREV = L_MISC + 8192;
constexpr unsigned L_TOTAL = 163840;

#define JROT(j) ((j) & ~3)
#define JOBC(j, w) (((j) & ~3) + (((j) + (w) + 1) & 3))

#define SLOWC(jc) (((((unsigned)(size_t)p.wqkvA >> 7) + (jc)) & 7) == 3)

struct Params {
  const u16* x;
  const u16* wqkvA;
  const u16* wphi;
  const u16* woA;
  const u16* wqkvB;
  const u16* woB;
  u16* out;
  char* ws;
};

#define SCHED __builtin_amdgcn_sched_barrier(0)
#define BAR __builtin_amdgcn_s_barrier()
#define SGB(m, n) __builtin_amdgcn_sched_group_barrier(m, n, 0)
#define WAITV(n) asm volatile("s_waitcnt vmcnt(%0)" ::"n"(n) : "memory")
#define WAITL0 asm volatile("s_waitcnt lgkmcnt(0)" ::: "memory")

#define TRV(slot, val) do {} while (0)
#define TRQ(slot) do {} while (0)
#define TRC(slot) do {} while (0)
#define TRR(slot) do {} while (0)
#define STAMP_INIT do {} while (0)
#define STAMP_DUMP do {} while (0)

__device__ __forceinline__ unsigned pk2(float a, float b) {
  f32x2 v = {a, b};
  return __builtin_bit_cast(unsigned, __builtin_convertvector(v, bf16x2_t));
}
__device__ __forceinline__ float lo16(unsigned u) { return __uint_as_float(u << 16); }
__device__ __forceinline__ float hi16(unsigned u) { return __uint_as_float(u & 0xffff0000u); }

template <int A, int B, typename F> __device__ __forceinline__ void static_for(F f) {
  if constexpr (A < B) { f(std::integral_constant<int, A>{}); static_for<A + 1, B>(f); }
}
typedef __amdgpu_buffer_rsrc_t rsrc_t;
__device__ __forceinline__ rsrc_t mk_rsrc(const void* p, unsigned bytes) { return __builtin_amdgcn_make_buffer_rsrc((void*)p, 0, bytes, 0x00020000); }
#define UNI(x) __builtin_amdgcn_readfirstlane(x)
#define DMA_HEAD(P) "s_mov_b32 m0, %4\n\ts_mov_b32 %0, %3\n\ts_nop 2\n\tbuffer_load_dwordx4 %1, %2, %0 offen " P " lds\n\t"
#define DMA_NEXT(P) "s_add_u32 m0, m0, %6\n\ts_add_u32 %0, %0, %5\n\tbuffer_load_dwordx4 %1, %2, %0 offen " P " lds\n\t"
#define DMA_OPS : "=&s"(t) : "v"(voff), "s"(r), "s"(UNI(soff)), "s"(UNI(lds)), "s"(UNI(sstep)), "i"(LSTEP) : "memory", "scc", "m0"
template <int LSTEP, int POL = 1> __device__ __forceinline__ void dma4(rsrc_t r, unsigned voff, unsigned soff, unsigned sstep, unsigned lds) {
  unsigned t;
  if constexpr (POL == 1) asm volatile(DMA_HEAD("sc1") DMA_NEXT("sc1") DMA_NEXT("sc1") DMA_NEXT("sc1") DMA_OPS);
  else if constexpr (POL == 2) asm volatile(DMA_HEAD("nt") DMA_NEXT("nt") DMA_NEXT("nt") DMA_NEXT("nt") DMA_OPS);
  else asm volatile(DMA_HEAD("") DMA_NEXT("") DMA_NEXT("") DMA_NEXT("") DMA_OPS);
}
template <int LSTEP, int POL = 1> __device__ __forceinline__ void dma3(rsrc_t r, unsigned voff, unsigned soff, unsigned sstep, unsigned lds) {
  unsigned t;
  if constexpr (POL == 1) asm volatile(DMA_HEAD("sc1") DMA_NEXT("sc1") DMA_NEXT("sc1") DMA_OPS);
  else if constexpr (POL == 2) asm volatile(DMA_HEAD("nt") DMA_NEXT("nt") DMA_NEXT("nt") DMA_OPS);
  else asm volatile(DMA_HEAD("") DMA_NEXT("") DMA_NEXT("") DMA_OPS);
}
template <int LSTEP, int N> __device__ __forceinline__ void bar_dma4(rsrc_t r, unsigned voff, unsigned soff, unsigned sstep, unsigned lds) {
  unsigned t;
  asm volatile("s_mov_b32 m0, %4\n\ts_mov_b32 %0, %3\n\ts_nop 0\n\ts_waitcnt vmcnt(%7) lgkmcnt(0)\n\ts_barrier\n\t"
               "buffer_load_dwordx4 %1, %2, %0 offen sc1 lds\n\t" DMA_NEXT("sc1") DMA_NEXT("sc1") DMA_NEXT("sc1")
               : "=&s"(t) : "v"(voff), "s"(r), "s"(UNI(soff)), "s"(UNI(lds)), "s"(UNI(sstep)), "i"(LSTEP), "n"(N) : "memory", "scc", "m0");
}
__device__ __forceinline__ void dma1(rsrc_t r, unsigned voff, unsigned soff, unsigned lds) {
  asm volatile("s_mov_b32 m0, %3\n\ts_nop 3\n\tbuffer_load_dwordx4 %0, %1, %2 offen sc1 lds" ::"v"(voff), "s"(r), "s"(UNI(soff)), "s"(UNI(lds)) : "memory", "m0");
}
__device__ __forceinline__ void touch(rsrc_t r, unsigned voff, unsigned lds) {
  asm volatile("s_mov_b32 m0, %2\n\ts_nop 3\n\tbuffer_load_dword %0, %1, 0 offen lds" ::"v"(voff), "s"(r), "s"(UNI(lds)) : "memory", "m0");
}
__device__ __forceinline__ void ld8_asm(u32x2& d, rsrc_t r, unsigned voff) {
  asm volatile("s_nop 4\n\tbuffer_load_dwordx2 %0, %1, %2, 0 offen sc1" : "=v"(d) : "v"(voff), "s"(r) : "memory");
}

__device__ __forceinline__ void ld16nt_asm(u32x4& d, rsrc_t r, unsigned voff, unsigned soff) {
  asm volatile("s_nop 4\n\tbuffer_load_dwordx4 %0, %1, %2, %3 offen nt" : "=v"(d) : "v"(voff), "s"(r), "s"(UNI(soff)) : "memory");
}
__device__ __forceinline__ void ld16_asm(f32x4& d, rsrc_t r, unsigned voff, unsigned soff) {
  asm volatile("s_nop 4\n\tbuffer_load_dwordx4 %0, %1, %2, %3 offen sc1" : "=v"(d) : "v"(voff), "s"(r), "s"(UNI(soff)) : "memory");
}

template <typename T> __device__ __forceinline__ T lds_ld(unsigned a) { return *(LAS T*)(a); }
template <typename T> __device__ __forceinline__ void lds_st(unsigned a, T v) { *(LAS T*)(a) = v; }
__device__ __forceinline__ bf16x8 tr2(unsigned a0, unsigned a1) {
  s16x4 v0 = __builtin_amdgcn_ds_read_tr16_b64_v4i16((lds_s16x4_ptr)(a0));
  s16x4 v1 = __builtin_amdgcn_ds_read_tr16_b64_v4i16((lds_s16x4_ptr)(a1));
  return __builtin_shufflevector(v0, v1, 0, 1, 2, 3, 4, 5, 6, 7);
}
__device__ __forceinline__ bf16x8 as_frag(u32x4 v) { return __builtin_bit_cast(bf16x8, v); }
#define MFMA(a, b, c) __builtin_amdgcn_mfma_f32_16x16x32_bf16(a, b, c, 0, 0, 0)

__device__ __forceinline__ float vmax2(float a, float b) { float inf = __builtin_inff(); asm("" : "+s"(inf)); return __builtin_amdgcn_fmed3f(a, b, inf); }
#define SWAP16(a, b) __builtin_amdgcn_permlane16_swap(__float_as_uint(a), __float_as_uint(b), false, false)
#define SWAP32(a, b) __builtin_amdgcn_permlane32_swap(__float_as_uint(a), __float_as_uint(b), false, false)
__device__ __forceinline__ void red2_max(float& a, float& b) {
  auto r = SWAP16(a, b);
  float x = vmax2(__uint_as_float(r[0]), __uint_as_float(r[1]));
  r = SWAP32(x, x);
  x = vmax2(__uint_as_float(r[0]), __uint_as_float(r[1]));
  r = SWAP16(x, x);
  a = __uint_as_float(r[0]); b = __uint_as_float(r[1]);
}
__device__ __forceinline__ void red2_add(float& a, float& b) {
  auto r = SWAP16(a, b);
  float x = __uint_as_float(r[0]) + __uint_as_float(r[1]);
  r = SWAP32(x, x);
  x = __uint_as_float(r[0]) + __uint_as_float(r[1]);
  r = SWAP16(x, x);
  a = __uint_as_float(r[0]); b = __uint_as_float(r[1]);
}
template <bool MAX, typename M> __device__ __forceinline__ void red4(float& a, float& b, float& c, float& d, M mid) {
  auto op = [](unsigned x, unsigned y) { return MAX ? vmax2(__uint_as_float(x), __uint_as_float(y)) : __uint_as_float(x) + __uint_as_float(y); };
  auto r = SWAP16(a, b), q = SWAP16(c, d);
  const float x = op(r[0], r[1]), y = op(q[0], q[1]);
  r = SWAP32(x, y);
  const float w = mid(op(r[0], r[1]));
  r = SWAP32(w, w);
  q = __builtin_amdgcn_permlane16_swap(r[1], r[1], false, false);
  r = __builtin_amdgcn_permlane16_swap(r[0], r[0], false, false);
  a = __uint_as_float(r[0]); b = __uint_as_float(r[1]); c = __uint_as_float(q[0]); d = __uint_as_float(q[1]);
}
__device__ __forceinline__ float red_max4(float x) {
  auto r = __builtin_amdgcn_permlane16_swap(__float_as_uint(x), __float_as_uint(x), false, false);
  x = vmax2(__uint_as_float(r[0]), __uint_as_float(r[1]));
  r = __builtin_amdgcn_permlane32_swap(__float_as_uint(x), __float_as_uint(x), false, false);
  return vmax2(__uint_as_float(r[0]), __uint_as_float(r[1]));
}
__device__ __forceinline__ float red_add4(float x) {
  auto r = __builtin_amdgcn_permlane16_swap(__float_as_uint(x), __float_as_uint(x), false, false);
  x = __uint_as_float(r[0]) + __uint_as_float(r[1]);
  r = __builtin_amdgcn_permlane32_swap(__float_as_uint(x), __float_as_uint(x), false, false);
  return __uint_as_float(r[0]) + __uint_as_float(r[1]);
}

__device__ __forceinline__ void st16_sc1(rsrc_t r, unsigned voff, unsigned soff, u32x4 v) {
  __builtin_amdgcn_raw_buffer_store_b128(v, r, voff, soff, 16);
  asm volatile("s_nop 1" ::"v"(v) : "memory");
}
__device__ __forceinline__ void st8_sc1(rsrc_t r, unsigned voff, unsigned soff, u32x2 v) { __builtin_amdgcn_raw_buffer_store_b64(v, r, voff, soff, 16); }
__device__ __forceinline__ u32x4 ld16_sc1(rsrc_t r, unsigned voff, unsigned soff) { return __builtin_amdgcn_raw_buffer_load_b128(r, voff, soff, 16); }
__device__ __forceinline__ u32x2 ld8_sc1(rsrc_t r, unsigned voff, unsigned soff) { return __builtin_amdgcn_raw_buffer_load_b64(r, voff, soff, 16); }
__device__ __forceinline__ f32x4 ld4f_sc1(rsrc_t r, unsigned voff, unsigned soff) { return __builtin_bit_cast(f32x4, __builtin_amdgcn_raw_buffer_load_b128(r, voff, soff, 16)); }
#define OPAQUE_V(x) asm volatile("" : "+v"(x))
#define OPAQUE_S(x) asm volatile("" : "+s"(x))

#define CLOBBER asm volatile("" ::: "memory")
constexpr unsigned SPIN_CAP = 1u << 17;
__device__ __forceinline__ bool is_set(u32x2 v, u32x2 epoch) { return v[0] == epoch[0] && v[1] == epoch[1]; }
__device__ __forceinline__ void set_flags(rsrc_t rs, unsigned lineA, int colA, unsigned lineB, int colB, u32x2 epoch, int lane) {
  const unsigned off = (lane & 7) * REP_STRIDE + ((lane & 8) ? lineB + colB * 8 : lineA + colA * 8);
  if (lane < 16) st8_sc1(rs, off, 0, epoch);
}
__device__ __forceinline__ unsigned poll_need(rsrc_t rs, unsigned voff, u32x2 epoch, unsigned ready, unsigned need, bool& dead) {
  for (unsigned spins = 0; !dead; ++spins) {
    CLOBBER;
    ready |= (unsigned)__ballot(is_set(ld8_sc1(rs, voff, 0), epoch));
    if ((ready & need) == need) break;
    if (spins > SPIN_CAP) dead = true;
    __builtin_amdgcn_s_sleep(1);
  }
  CLOBBER;
  return dead ? ~0u : ready;
}

__device__ __forceinline__ void poll_all(rsrc_t rs, unsigned voff, u32x2 epoch, bool& dead) {
  for (unsigned spins = 0; !dead; ++spins) {
    CLOBBER;
    if (__all(is_set(ld8_sc1(rs, voff, 0), epoch))) break;
    if (spins > SPIN_CAP) dead = true;
    __builtin_amdgcn_s_sleep(1);
  }
  CLOBBER;
}

constexpr unsigned SC_VF = L_R1 + 32768;
constexpr unsigned SC_PHIK = L_R1 + 49152;
constexpr unsigned SC_XCH = L_R2 + XS;
constexpr unsigned wo_slot(int s) {
  return s < 2 ? L_R2 + (unsigned)(s + 1) * XS : (s < 6 ? L_R1 + (unsigned)(s - 2) * 16384 : (s == 6 ? L_R1 + 81920 : L_R2));
}
__device__ __forceinline__ unsigned wo_src(int s, int j) { return (unsigned)(((JROT(j) >> 1) + 2 + s) & 7) * 16384; }
__device__ __forceinline__ unsigned wo_swz(int lane) { return (unsigned)(((lane & 7) ^ ((lane >> 3) & 6)) * 16); }
__device__ __forceinline__ unsigned umax3(unsigned a, unsigned b, unsigned c) { return max(max(a, b), c); }
__device__ __forceinline__ unsigned umax4v(unsigned m, const u32x4& v) { return umax3(umax3(m, v[0], v[1]), v[2], v[3]); }
__device__ __forceinline__ u32x4 nosent(u32x4 v) { return u32x4{min(v[0], SENT - 1), min(v[1], SENT - 1), min(v[2], SENT - 1), min(v[3], SENT - 1)}; }

template <typename P, typename F>
__device__ __forceinline__ void qkv_loop(int isb, int wm, int h, int lane, bool first, int& trv, P pre, F fin) {
  constexpr unsigned lds0 = 0;
  f32x4 acc[4][6];
  const int fr = lane & 15, fq = lane >> 4;
  unsigned ab[2], bb[2];
  {
    const int r = wm * 64 + fr;
#pragma unroll
    for (int ks = 0; ks < 2; ++ks) ab[ks] = lds0 + L_R2 + r * 128 + (((ks * 4 + fq) ^ ((fr >> 1) & 7)) * 16);
    const int kr = fq * 8 + (fr >> 2);
    const int gs = ((kr >> 1) & 1) | (((kr >> 3) & 1) << 1);
#pragma unroll
    for (int c = 0; c < 2; ++c) bb[c] = lds0 + L_R1 + kr * 128 + (((h * 2 + c) ^ gs) * 32) + (lane & 3) * 8;
  }
#pragma unroll
  for (int m = 0; m < 4; ++m)
#pragma unroll
    for (int n = 0; n < 6; ++n) acc[m][n] = f32x4{0.f, 0.f, 0.f, 0.f};
  bf16x8 af[2][4], bf[2][6];
  auto rds = [&](int ks, int p) {
#pragma unroll
    for (int m = 0; m < 4; ++m) af[p][m] = lds_ld<bf16x8>(ab[ks] + m * 2048);
#pragma unroll
    for (int n = 0; n < 6; ++n) {
      const unsigned a = bb[n & 1] + (n >> 1) * 8192 + ks * 4096;
      bf[p][n] = tr2(a, a + 512);
    }
  };
  auto mms = [&](int p) {
#pragma unroll
    for (int n = 0; n < 6; ++n)
#pragma unroll
      for (int mm = 0; mm < 4; ++mm) { const int m = (n & 1) ? 3 - mm : mm; acc[m][n] = n < 4 ? MFMA(bf[p][n], af[p][m], acc[m][n]) : MFMA(af[p][m], bf[p][n], acc[m][n]); }
  };
  auto ilv2 = [&]() {
#pragma unroll
    for (int i = 0; i < 16; ++i) { SGB(0x008, 1); SGB(0x100, 1); }
#pragma unroll
    for (int i = 0; i < 4; ++i) { SGB(0x008, 1); SGB(0x002, 1); }
    SGB(0x008, 4);
  };
  auto ilv3 = [&]() {
#pragma unroll
    for (int i = 0; i < 16; ++i) { SGB(0x008, 1); SGB(0x100, 1); }
#pragma unroll
    for (int i = 0; i < 7; ++i) { SGB(0x008, 1); SGB(0x004, 2); }
    SGB(0x008, 1);
  };
  if (!first) { WAITV(6); WAITL0; }
  TRQ(1); BAR;
  int tslot = 2;
  rds(0, 0);
  int sa = 0, sb = 0;
  int da = XS, db = 24576;
  auto step = [&]() {
    ab[0] += da; ab[1] += da;
    bb[0] += db; bb[1] += db;
  };
  auto ring = [&]() {
    sa = (sa == 2) ? 0 : sa + 1;
    sb = (sb == 3) ? 0 : sb + 1;
    da = (sa == 2) ? -2 * (int)XS : (int)XS;
    db = (sb == 3) ? -3 * 24576 : 24576;
  };
#pragma unroll 1
  for (int t = 14; t > 0; --t) {
    SCHED;
    rds(1, 1); mms(0);
    step();
    ilv2();
    SCHED;
    __builtin_amdgcn_s_waitcnt(0x0070);
    TRQ(tslot); tslot++; BAR;
    SCHED;
    rds(0, 0);
    ring();
    mms(1); ilv3();
  }
  SCHED;
  f32x4 cs[4], nm4[4];
  float nmean[4], csv[2];
  auto fold = [&](int n) {
#pragma unroll
    for (int mm = 0; mm < 4; ++mm) {
      const int m = (n & 1) ? 3 - mm : mm;
#pragma unroll
      for (int e = 0; e < 4; ++e) {
        float t = n < 4 ? __builtin_fmaf(nmean[m], cs[n & 3][e], acc[m][n][e]) : __builtin_fmaf(nm4[m][e], csv[n & 1], acc[m][n][e]);
        asm("" : "+v"(t));
        acc[m][n][e] = t;
      }
    }
  };
  {
    unsigned pm = lds0 + L_MEAN + (16 + wm * 64 + fr) * 4, pc = lds0 + L_CS + (h * 32 + fq * 4) * 4;
    unsigned pm4 = lds0 + L_MEAN + (16 + wm * 64 + fq * 4) * 4, pcv = lds0 + L_CS + (128 + h * 32 + fr) * 4;
    asm("" : "+v"(pm)); asm("" : "+v"(pc)); asm("" : "+v"(pm4)); asm("" : "+v"(pcv));
#pragma unroll
    for (int m = 0; m < 4; ++m) nmean[m] = lds_ld<float>(pm + m * 64);
#pragma unroll
    for (int n = 0; n < 4; ++n) cs[n] = lds_ld<f32x4>(pc + ((n >> 1) * 64 + (n & 1) * 16) * 4);
#pragma unroll
    for (int m = 0; m < 4; ++m) nm4[m] = lds_ld<f32x4>(pm4 + m * 64);
#pragma unroll
    for (int n = 0; n < 2; ++n) csv[n] = lds_ld<float>(pcv + n * 64);
    rds(1, 1); mms(0);
    step();
    fold(0); fold(1);
    SGB(0x002, 8);
#pragma unroll
    for (int i = 0; i < 8; ++i) { SGB(0x008, 1); SGB(0x100, 2); }
#pragma unroll
    for (int i = 0; i < 11; ++i) { SGB(0x008, 1); SGB(0x100, 1); SGB(0x002, 2); }
#pragma unroll
    for (int i = 0; i < 5; ++i) { SGB(0x008, 1); SGB(0x002, 3); }
    SCHED;
    __builtin_amdgcn_s_waitcnt(0xC07F);
    TRQ(16); BAR;
    SCHED;
    rds(0, 0);
    fold(5); mms(1); fold(2);
#pragma unroll
    for (int i = 0; i < 16; ++i) { SGB(0x008, 1); SGB(0x100, 1); SGB(0x002, 1); }
#pragma unroll
    for (int i = 0; i < 8; ++i) { SGB(0x008, 1); SGB(0x002, 2); }
    SCHED;
    pre(0);
    SCHED;
    rds(1, 1);
    fold(4); mms(0); fold(3);
    pre(1);
#pragma unroll
    for (int i = 0; i < 16; ++i) { SGB(0x008, 1); SGB(0x100, 1); SGB(0x002, 1); SGB(0x004, 1); }
#pragma unroll
    for (int i = 0; i < 8; ++i) { SGB(0x008, 1); SGB(0x002, 3); SGB(0x004, 1); }
  }
  SCHED;
  __builtin_amdgcn_s_waitcnt(0xC07F);
  TRQ(17); BAR;
  SCHED;
  auto mm6 = [&](int m, int n0 = 0, int n1 = 6) {
#pragma unroll
    for (int nn = n0; nn < n1; ++nn) { const int n = (nn + 2) % 6; acc[m][n] = n < 4 ? MFMA(bf[1][n], af[1][m], acc[m][n]) : MFMA(af[1][m], bf[1][n], acc[m][n]); }
  };
  if (!isb) {
    fin(std::false_type{}, acc, mm6);
#pragma unroll
    for (int i = 0; i < 24; ++i) asm volatile("" : "=v"(acc[i / 6][i % 6]));
#pragma unroll
    for (int i = 0; i < 6; ++i) asm volatile("" : "=v"(bf[1][i]));
#pragma unroll
    for (int i = 0; i < 4; ++i) asm volatile("" : "=v"(af[1][i]));
  }
  OPAQUE_S(isb);
  if (isb) fin(std::true_type{}, acc, mm6);
}
__device__ __forceinline__ u32x4 pack(const f32x4& a, const f32x4& b) { return u32x4{pk2(a[0], a[1]), pk2(a[2], a[3]), pk2(b[0], b[1]), pk2(b[2], b[3])}; }

template <int N> __device__ __forceinline__ void hedgehog(const f32x4 (&r)[4][2], const float (&c)[4], float (&ep)[4][8], float (&en)[4][8]) {
  float mx[4], nm[4];
#pragma unroll
  for (int m = 0; m < N; ++m) {
    const f32x4 a = r[m][0], b = r[m][1];
    mx[m] = vmax2(fmaxf(fmaxf(a[3], b[0]), b[1]), fmaxf(fmaxf(fmaxf(fmaxf(a[0], a[1]), a[2]), b[2]), b[3]));
    nm[m] = vmax2(fmaxf(fmaxf(-a[3], -b[0]), -b[1]), fmaxf(fmaxf(fmaxf(fmaxf(-a[0], -a[1]), -a[2]), -b[2]), -b[3]));
  }
  SCHED;
  red4<true>(mx[0], mx[1], nm[0], nm[1], [](float x) { return x; });
  if (N == 4) red4<true>(mx[2], mx[3], nm[2], nm[3], [](float x) { return x; });
#pragma unroll
  for (int m = 0; m < N; ++m) { mx[m] *= -c[m]; nm[m] *= -c[m]; }
  SCHED;
#pragma unroll
  for (int m = 0; m < N; ++m) {
#pragma unroll
    for (int i = 0; i < 8; ++i) { ep[m][i] = __builtin_fmaf(r[m][i >> 2][i & 3], c[m], mx[m]); en[m][i] = __builtin_fmaf(r[m][i >> 2][i & 3], -c[m], nm[m]); }
    SCHED;
#pragma unroll
    for (int i = 0; i < 8; ++i) { ep[m][i] = __builtin_amdgcn_exp2f(ep[m][i]); en[m][i] = __builtin_amdgcn_exp2f(en[m][i]); }
    SCHED;
  }
}
__device__ __forceinline__ u32x4 pack8(const float (&e)[8], float f) {
  return u32x4{pk2(e[0] * f, e[1] * f), pk2(e[2] * f, e[3] * f), pk2(e[4] * f, e[5] * f), pk2(e[6] * f, e[7] * f)};
}
__device__ __forceinline__ u32x4 pack8(const float (&e)[8]) { return u32x4{pk2(e[0], e[1]), pk2(e[2], e[3]), pk2(e[4], e[5]), pk2(e[6], e[7])}; }

__device__ __forceinline__ void phik2(const f32x4 (&r)[4][2], const float (&c)[4], unsigned pw) {
  const f32x4 zero = {0.f, 0.f, 0.f, 0.f};
  const bf16x8 ones = {0x3f80, 0x3f80, 0x3f80, 0x3f80, 0x3f80, 0x3f80, 0x3f80, 0x3f80};
  float ep[4][8], en[4][8];
  f32x4 sp[2], sn[2];
  hedgehog<2>(r, c, ep, en);
#pragma unroll
  for (int m = 0; m < 2; ++m) { sp[m] = MFMA(ones, as_frag(pack8(ep[m])), zero); sn[m] = MFMA(ones, as_frag(pack8(en[m])), zero); }
#pragma unroll
  for (int m = 0; m < 2; ++m) { asm("" : "+v"(sp[m])); asm("" : "+v"(sn[m])); }
  SCHED;
#pragma unroll
  for (int m = 0; m < 2; ++m) {
    const float fp = __builtin_amdgcn_rcpf(sp[m][0]), fn = __builtin_amdgcn_rcpf(sn[m][0]);
#pragma unroll
    for (int i = 0; i < 8; ++i) { ep[m][i] *= fp; en[m][i] *= fn; }
    SCHED;
    const u32x4 pp = pack8(ep[m]), pn = pack8(en[m]);
    const unsigned d = pw + m * (16 * 160);
    lds_st<u32x2>(d, u32x2{pp[0], pp[1]});
    lds_st<u32x2>(d + 32, u32x2{pp[2], pp[3]});
    lds_st<u32x2>(d + 64, u32x2{pn[0], pn[1]});
    lds_st<u32x2>(d + 96, u32x2{pn[2], pn[3]});
    SCHED;
  }
}
__device__ __forceinline__ bf16x8 rowsum_sel(int lane) {
  const int fr = lane & 15;
  const unsigned one = (fr < 8) ? (0x3f80u << (16 * (fr & 1))) : 0u;
  const int e = (fr >> 1) & 3;
  return as_frag(u32x4{e == 0 ? one : 0u, e == 1 ? one : 0u, e == 2 ? one : 0u, e == 3 ? one : 0u});
}
__device__ __forceinline__ f32x4 rowsum_fin(f32x4 c) {
#pragma unroll
  for (int k = 0; k < 4; ++k) {
    const float a = c[k];
    c[k] = a + __int_as_float(__builtin_amdgcn_update_dpp(0, __float_as_int(a), 0x128  , 0xf, 0xf, false));
  }
  return c;
}
__device__ __forceinline__ void job_colsum(rsrc_t rs, const u32x4 (&w)[8], int mat, int g, int jc, int hw, int lane) {
  const bf16x8 sel = rowsum_sel(lane);
  f32x4 c = {0.f, 0.f, 0.f, 0.f};
#pragma unroll
  for (int i = 0; i < 8; ++i) c = MFMA(sel, as_frag(w[i]), c);
  const u32x4 w4 = __builtin_bit_cast(u32x4, rowsum_fin(c));
  unsigned so = WS_CS + (unsigned)(((mat * 16 + jc) * 16 + g) * 768);
  OPAQUE_S(so);
  if ((lane & 40) == 0) st16_sc1(rs, (unsigned)(hw * 64 + (lane & 7) * 8 + (lane >> 4) * 4) * 4, so, w4);
}
__device__ __forceinline__ unsigned img_off(int i, int lane) { return (lane >> 3) * 128 + (((lane & 7) ^ ((((lane >> 4) & 1) | ((i & 1) << 1)) << 1)) * 16); }
__device__ __forceinline__ void job_pack(rsrc_t rs, u32x4 w, int i, int mat, int g, int jc, int hw, int lane) {
  st16_sc1(rs, img_off(i, lane),
           WS_WP + (unsigned)(((mat * 16 + jc) * 16 + g) * 24576 + hw * 8192 + i * 1024), w);
}

__global__ __launch_bounds__(512, 2) void mega_kernel(Params p) {
  extern __shared__ __attribute__((aligned(16))) char smem[];
  const int tid = threadIdx.x;
  const int wave0 = __builtin_amdgcn_readfirstlane(tid >> 6);
  const int xx = blockIdx.x & 7, rr = blockIdx.x >> 3;
  const int g0 = (rr >> 2) * 2 + (xx & 1), j0 = (xx >> 1) * 4 + (rr & 3);
  constexpr unsigned lds0 = 0, lds00 = 0;
  if ((unsigned)(size_t)(LAS char*)smem != 0u) return;
#define LOCALS0 int g = g0, j = j0, l = l0, wave = wave0;                                              \
  OPAQUE_S(g); OPAQUE_S(j); OPAQUE_S(l); OPAQUE_S(wave);                                               \
  const int hw = wave & 3, wm = wave & 1, h = (wave >> 1) & 1;                                         \
  const unsigned myrep = (unsigned)((j >> 2) * 2 + (g & 1)) * REP_STRIDE;                              \
  (void)hw; (void)wm; (void)h; (void)myrep;                                                            \
  int lane = tid; OPAQUE_V(lane); lane &= 63
#define LOCALS LOCALS0; const bool isb = l & 1, lead = (wave == 4); const int half = wave >> 2; (void)isb; (void)lead; (void)half
  const rsrc_t rs = mk_rsrc(p.ws, WS_SPAN);
  const bool cw = wave0 < 4;
  if (wave0 == 2) touch(mk_rsrc(p.ws, NREP * REP_STRIDE), min(blockIdx.x * 9 + ((tid & 63) < 9 ? (tid & 63) : 0), NREP * REP_STRIDE / 128 - 1) * 128, lds00 + L_SINK + 512);
  if (wave0 == 3) {
    const char* pc;
    unsigned len;
    asm volatile("s_getpc_b64 %0\n.Lpf_base:\n\ts_mov_b32 %1, .Lpf_end-.Lpf_base" : "=s"(pc), "=s"(len));
    const unsigned off = ((unsigned)rr * 16 + (tid & 15)) * 128;
    touch(mk_rsrc(pc, len), ((int)len > 0 && off + 4 <= len) ? off : 0u, lds00 + L_SINK);
  }
  int trv = 0, trA = 0, trB = 0, trC = 0;
  u32x2 epoch = {0u, 0u};
  bool dead = false;
  STAMP_INIT;

#pragma unroll 1
  for (int l0 = 0; l0 < 4; ++l0) {
    TRC(0); TRR(62);

    f32x4 acc[2][4];
    auto kvpart = [&](unsigned va, unsigned pa, unsigned kvb, int lane, int wave) {
      const f32x4 zero = {0.f, 0.f, 0.f, 0.f};
      const bf16x8 ones = {0x3f80, 0x3f80, 0x3f80, 0x3f80, 0x3f80, 0x3f80, 0x3f80, 0x3f80};
      f32x4 c0 = zero, c1 = zero, z0 = zero, z1 = zero;
#pragma unroll
      for (int ks = 0; ks < 4; ++ks) {
        const bf16x8 v = lds_ld<bf16x8>(va + ks * 2048);
        const bf16x8 a0 = tr2(pa + ks * 32 * 160, pa + (ks * 32 + 16) * 160);
        const bf16x8 a1 = tr2(pa + 32 + ks * 32 * 160, pa + 32 + (ks * 32 + 16) * 160);
        c0 = MFMA(a0, v, c0);
        c1 = MFMA(a1, v, c1);
        z0 = MFMA(a0, ones, z0);
        z1 = MFMA(a1, ones, z1);
      }
      st16_sc1(rs, lane * 16, kvb + (unsigned)wave * 1024, nosent(pack(c0, c1)));
      if (!(wave & 2) && (lane & 15) == 0) st16_sc1(rs, lane, kvb + KVP_Z + (wave >> 2) * 128 + (wave & 1) * 64, nosent(pack(z0, z1)));
    };
#define KV_ADDR                                                                                                            \
    unsigned va = lds0 + SC_VF + (wave >> 2) * 8192 + ((wave >> 1) & 1) * 1024 + lane * 16;                                \
    unsigned pa = lds0 + SC_PHIK + ((wave >> 2) * 128 + fq * 4 + (fr >> 2)) * 160 + (wave & 1) * 64 + (lane & 3) * 8;      \
    unsigned kvj = WS_KVP + (unsigned)j * KVP_J + (unsigned)(l >> 1) * KVP_L;                                              \
    OPAQUE_V(va); OPAQUE_V(pa); OPAQUE_S(kvj)
    if (cw) {
      if (l0 == 0) {
        LOCALS;
        u32x4 v[2][8];
        if (wave < 3) {
          const int jc = JOBC(j, wave), kt = (JROT(j) + 8 * (g & 1) + (g >> 1)) & 15;
          const rsrc_t ra = mk_rsrc(p.wqkvA, 1024 * 6144);
#pragma unroll
          for (int i = 0; i < 8; ++i)
            v[0][i] = __builtin_amdgcn_raw_buffer_load_b128(ra, (lane >> 3) * 6144 + (lane & 7) * 16, (kt * 64 + i * 8) * 6144 + wave * 2048 + jc * 128, 0);
          SCHED;
          TRC(44); BAR; SCHED;
          job_colsum(rs, v[0], 0, g, jc, wave, lane);
          if (SLOWC(jc)) {
#pragma unroll
            for (int i = 0; i < 8; ++i) job_pack(rs, v[0][i], i, 0, kt, jc, wave, lane);
          }
        } else {
          const int fr = lane & 15, fq = lane >> 4;
          const rsrc_t rx = mk_rsrc(p.x, 2048 * 2048);
#pragma unroll
          for (int i = 0; i < 16; ++i)
            v[i & 1][i >> 1] = __builtin_amdgcn_raw_buffer_load_b128(rx, fr * 2048 + fq * 16, (g * 128 + (j & 3) * 32 + ((i >> 1) & 1) * 16) * 2048 + ((JROT(j) + 8 * (g & 1)) & 15) * 128 + (i >> 2) * 128 + (i & 1) * 64, 0);
          SCHED;
          TRC(44); BAR; SCHED;
          const f32x4 zero = {0.f, 0.f, 0.f, 0.f};
          const bf16x8 ones = {0x3f80, 0x3f80, 0x3f80, 0x3f80, 0x3f80, 0x3f80, 0x3f80, 0x3f80};
          float sm[8], q[8];
#pragma unroll
          for (int m = 0; m < 8; ++m) {
            const f32x4 a = MFMA(ones, as_frag(v[1][m]), MFMA(ones, as_frag(v[0][m]), zero));
            const f32x4 b = MFMA(as_frag(v[1][m]), as_frag(v[1][m]), MFMA(as_frag(v[0][m]), as_frag(v[0][m]), zero));
            sm[m] = a[0];
            q[m] = (fr & 2) ? ((fr & 1) ? b[3] : b[2]) : ((fr & 1) ? b[1] : b[0]);
          }
          if (fq == (fr >> 2)) {
#pragma unroll
            for (int m = 0; m < 8; ++m) st8_sc1(rs, ((m & 1) * 16 + fr) * 8 + (m >> 1) * 1024, st_off(g, 0) + JROT(j) * 1024 + (j & 3) * 256, u32x2{__float_as_uint(sm[m]), __float_as_uint(q[m])});
          }
        }
      }
      LOCALS;
      unsigned otile, kvo, l16, pr1, pr4;
      int g2, j2, l2, ln2;
      qkv_loop(l & 1, wm, h, lane, l == 0, trv, [&](int st) {
        if (st == 0) {
          g2 = g0; j2 = j0; l2 = l0; ln2 = tid;
          OPAQUE_S(g2); OPAQUE_S(j2); OPAQUE_S(l2); OPAQUE_V(ln2);
          return;
        }
        otile = ob_off(g2, j2, l2) + (unsigned)(wm * 8 + h) * 1024;
        kvo = WS_KVP + (unsigned)j2 * KVP_J + (unsigned)g2 * KVP_G + (unsigned)(l2 >> 1) * KVP_L;
        l16 = (ln2 & 63) * 16;
        pr1 = lds0 + L_RSTD + (16 + wm * 64 + (ln2 & 15)) * 4;
        pr4 = lds0 + L_RSTD + wm * 256 + (ln2 & 48);
        asm("" : "+s"(otile)); asm("" : "+s"(kvo)); asm("" : "+v"(l16)); asm("" : "+v"(pr1)); asm("" : "+v"(pr4));
      }, [&](auto isb_c, f32x4 (&a)[4][6], auto& mm6) {
        const f32x4 zero = {0.f, 0.f, 0.f, 0.f};
        constexpr float L2E = 1.4426950408889634f;
        float rq[4];
#define ilv6(nv) do { _Pragma("unroll") for (int i_ = 0; i_ < 6; ++i_) { SGB(0x008, 1); SGB(0x100, 2); SGB(0x002, nv); } SCHED; } while (0)
        if constexpr (decltype(isb_c)::value) {
          u32x4 kf[5], qf[4];
          u32x2 vh[5][2];
          float rk[5][2];
          auto pk3 = [&](int m) {
            kf[m + 1] = pack(a[m][2], a[m][3]);
#pragma unroll
            for (int n = 0; n < 2; ++n) vh[m + 1][n] = u32x2{pk2(a[m][4 + n][0], a[m][4 + n][1]), pk2(a[m][4 + n][2], a[m][4 + n][3])};
            qf[m] = pack(a[m][0], a[m][1]);
          };
          constexpr auto window = [](int e) {
            u64 mk = 0;
            for (int ln = 0; ln < 64; ++ln) {
              const int delta = (ln & 14) + 16 - (e >> 1) * 16 - (ln >> 4) * 4 - (e & 1) * 2;
              if (delta >= 0 && delta <= 14) mk |= 1ull << ln;
            }
            return mk;
          };
          const bool par = __builtin_amdgcn_inverse_ballot_w64(0xaaaaaaaaaaaaaaaaull);
          f32x4 s0[4], s1[4];
          float x[4][4], mx[4], sm[4];
          u32x4 pf[4];
          unsigned psel0, psel1;
          auto sc = [&](int m) { s0[m] = MFMA(as_frag(kf[m]), as_frag(qf[m]), zero); s1[m] = MFMA(as_frag(kf[m + 1]), as_frag(qf[m]), zero); };
          auto st1 = [&](int m, u64 prev) {
            const float u0 = s0[m][0], u1 = s0[m][1], u2 = s0[m][2], u3 = s0[m][3], t0 = s1[m][0], t1 = s1[m][1], t2 = s1[m][2], t3 = s1[m][3];
            x[m][0] = par ? u1 : u0; x[m][1] = par ? u3 : u2; x[m][2] = par ? t1 : t0; x[m][3] = par ? t3 : t2;
            static_for<0, 4>([&](auto e_c) __attribute__((always_inline)) {
              constexpr int e = decltype(e_c)::value;
              constexpr u64 mk = window(e);
              x[m][e] = __builtin_amdgcn_inverse_ballot_w64(e < 2 ? mk & prev : mk) ? x[m][e] * rk[m + (e >> 1)][e & 1] : -3.0e38f;
            });
            mx[m] = fmaxf(fmaxf(fmaxf(x[m][0], x[m][1]), x[m][2]), x[m][3]);
          };
          auto st2 = [&](int m) {
            const float mxl = mx[m] * -rq[m];
#pragma unroll
            for (int e = 0; e < 4; ++e) x[m][e] = __builtin_fmaf(x[m][e], rq[m], mxl);
          };
          auto st3 = [&](int m) {
#pragma unroll
            for (int e = 0; e < 4; ++e) x[m][e] = __builtin_amdgcn_exp2f(x[m][e]);
          };
          auto st4 = [&](int m) {
            sm[m] = (x[m][0] + x[m][1]) + (x[m][2] + x[m][3]);
#pragma unroll
            for (int e = 0; e < 4; ++e) x[m][e] *= rk[m + (e >> 1)][e & 1];
          };
          auto st5 = [&](int m) {
            const unsigned p0 = pk2(x[m][0] * sm[m], x[m][1] * sm[m]), p1 = pk2(x[m][2] * sm[m], x[m][3] * sm[m]);
            pf[m] = u32x4{__builtin_amdgcn_perm(0u, p0, psel0), __builtin_amdgcn_perm(0u, p0, psel1), __builtin_amdgcn_perm(0u, p1, psel0), __builtin_amdgcn_perm(0u, p1, psel1)};
          };
          auto st6 = [&](int m) {
            s0[m] = MFMA(as_frag(u32x4{vh[m][0][0], vh[m][0][1], vh[m + 1][0][0], vh[m + 1][0][1]}), as_frag(pf[m]), zero);
            s1[m] = MFMA(as_frag(u32x4{vh[m][1][0], vh[m][1][1], vh[m + 1][1][0], vh[m + 1][1][1]}), as_frag(pf[m]), zero);
          };
          auto st7 = [&](int m) { st16_sc1(rs, l16, otile + m * 2048, nosent(pack(s0[m], s1[m]))); };
          mm6(3);
#pragma unroll
          for (int m = 0; m < 4; ++m) rq[m] = lds_ld<float>(pr1 + m * 64);
          const f32x4 rs3 = lds_ld<f32x4>(pr4 + 256);
          unsigned prk = pr4 + (par ? 4u : 0u), pv_ = l16 + lds0 + L_PREV + h * 2048;
          psel0 = par ? 0x01000c0cu : 0x0c0c0100u; psel1 = par ? 0x03020c0cu : 0x0c0c0302u;
          asm("" : "+v"(prk)); asm("" : "+v"(pv_));
#pragma unroll
          for (int i = 0; i < 5; ++i)
#pragma unroll
            for (int e = 0; e < 2; ++e) rk[i][e] = (i & 3) ? lds_ld<float>(prk + i * 64 + e * 8) : 1.0f;
          const unsigned hso = kvo + KVP_HALO + h * 2048, hsi = hso - (g > 0 ? KVP_G : 0u);
          ilv6(2);
          mm6(2);
          kf[4] = pack(a[3][2] * rq[3], a[3][3] * rq[3]);
          u32x4 v3 = pack(a[3][4] * rs3, a[3][5] * rs3);
          vh[4][0] = u32x2{v3[0], v3[1]}; vh[4][1] = u32x2{v3[2], v3[3]};
          ilv6(3);
          OPAQUE_V(kf[4]); OPAQUE_V(v3);
          if (wm) {
            st16_sc1(rs, l16, hso, nosent(kf[4]));
            st16_sc1(rs, l16, hso + 1024, nosent(v3));
          } else {
            lds_st<u32x4>(pv_, kf[4]);
            lds_st<u32x4>(pv_ + 1024, v3);
          }
          SCHED;
          mm6(1);
          qf[3] = pack(a[3][0], a[3][1]);
          pk3(2);
          ilv6(3);
          mm6(0);
          pk3(1);
#pragma unroll
          for (int m = 0; m < 4; ++m) rq[m] *= 0.17677669529663687f * L2E;
          ilv6(3);
          u32x4 hk[2], hv[2];
          const bool halo = wm == 0 && g > 0;
          hk[0] = ld16_sc1(rs, l16, hsi); hv[0] = ld16_sc1(rs, l16, hsi + 1024);
          SCHED;
          sc(3); sc(2);
          pk3(0);
          sc(1);
          SCHED;
          st1(3, ~0ull); st1(2, ~0ull); st1(1, ~0ull);
          SCHED;
          { float d = mx[1]; red4<true>(mx[3], mx[2], mx[1], d, [](float v) { return v; }); }
          st2(3); st2(2); st2(1);
          SCHED;
          st3(3); st3(2); st3(1);
          SCHED;
          CLOBBER;
          hk[1] = ld16_sc1(rs, l16, hsi); hv[1] = ld16_sc1(rs, l16, hsi + 1024);
          CLOBBER;
          SCHED;
          st4(3); st4(2); st4(1);
          { float d = sm[1]; red4<false>(sm[3], sm[2], sm[1], d, [](float v) { return __builtin_amdgcn_rcpf(v); }); }
          SCHED;
          st5(3); st5(2); st5(1);
          SCHED;
          st6(3); st6(2); st6(1);
          WAITL0;
          TRR(60);
          TRC(18); BAR;
          u32x4 pk = lds_ld<u32x4>(pv_), pv = lds_ld<u32x4>(pv_ + 1024);
          st7(3); st7(2); st7(1);
          SCHED;
          OPAQUE_V(qf[0]); OPAQUE_V(rq[0]);
          TRV(55, 0); TRC(21);
          if (halo) {
            pk = hk[0]; pv = hv[0];
            if (__builtin_expect(__any(umax4v(umax4v(0u, pk), pv) == SENT), 0)) {
              pk = hk[1]; pv = hv[1];
              TRV(55, 1);
              for (unsigned spins = 0; !dead && __any(umax4v(umax4v(0u, pk), pv) == SENT); ++spins) {
                if (spins > SPIN_CAP) { dead = true; break; }
                CLOBBER;
                TRV(55, spins + 2);
                pk = ld16_sc1(rs, l16, hsi); pv = ld16_sc1(rs, l16, hsi + 1024);
                OPAQUE_V(pk); OPAQUE_V(pv);
              }
            }
          }
          kf[0] = pk; vh[0][0] = u32x2{pv[0], pv[1]}; vh[0][1] = u32x2{pv[2], pv[3]};
          sc(0);
          st1(0, (wm || g > 0) ? ~0ull : 0ull);
          mx[0] = red_max4(mx[0]);
          st2(0); st3(0); st4(0);
          sm[0] = __builtin_amdgcn_rcpf(red_add4(sm[0]));
          st5(0); st6(0); st7(0);
        } else {
          LOCALS;
          const int fr = lane & 15, fq = lane >> 4;
          f32x4 rs4[4], r[4][2];
          const bf16x8 ones = {0x3f80, 0x3f80, 0x3f80, 0x3f80, 0x3f80, 0x3f80, 0x3f80, 0x3f80};
          auto proj = [&](int m, int n, const bf16x8& w0, const bf16x8& w1) {
            const u32x4 f = pack(a[m][n], a[m][n + 1]);
            r[m][0] = MFMA(w0, as_frag(f), zero); r[m][1] = MFMA(w1, as_frag(f), zero);
          };
          mm6(3, 0, 2); mm6(2, 0, 2); mm6(1, 0, 2); mm6(0, 0, 2);
          unsigned wa = lds0 + L_WPHI + h * 2048 + (fq * 4 + (fr >> 2)) * 64 + (lane & 3) * 8, kw = lds0 + L_PREV + hw * 2048 + l16;
          asm("" : "+v"(wa)); asm("" : "+v"(kw));
#pragma unroll
          for (int m = 0; m < 4; ++m) rq[m] = lds_ld<float>(pr1 + m * 64);
#pragma unroll
          for (int m = 0; m < 4; ++m) rs4[m] = lds_ld<f32x4>(pr4 + 64 + m * 64);
          const bf16x8 w0 = tr2(wa, wa + 1024);
          const bf16x8 w1 = tr2(wa + 32, wa + 32 + 1024);
#pragma unroll
          for (int i = 0; i < 8; ++i) { SGB(0x008, 1); SGB(0x100, 2); SGB(0x002, 2); }
          SCHED;
          mm6(3, 2, 4); mm6(2, 2, 4);
          lds_st<u32x4>(kw + 1024, pack(a[3][2], a[3][3]));
          lds_st<u32x4>(kw, pack(a[2][2], a[2][3]));
#pragma unroll
          for (int i = 0; i < 2; ++i) { SGB(0x008, 1); SGB(0x002, 2); SGB(0x008, 1); SGB(0x002, 2); SGB(0x200, 1); }
          SCHED;
          mm6(1, 2, 4); mm6(0, 2, 4);
          proj(0, 2, w0, w1); proj(1, 2, w0, w1);
#pragma unroll
          for (int i = 0; i < 4; ++i) { SGB(0x008, 1); SGB(0x002, 2); }
          SCHED;
          WAITL0;
          TRC(18); BAR;
          SCHED;
          unsigned pw = lds0 + SC_PHIK + (h * 128 + wm * 64 + fr) * 160 + fq * 8, vw = lds0 + SC_VF + h * 8192 + wm * 4096 + l16;
          asm("" : "+v"(pw)); asm("" : "+v"(vw));
#pragma unroll
          for (int m = 0; m < 4; ++m) mm6(m, 4, 6);
#pragma unroll
          for (int m = 0; m < 4; ++m) rq[m] *= L2E;
#pragma unroll
          for (int i = 0; i < 4; ++i)
            lds_st<u32x4>(vw + i * 1024, pack(a[i & 2][4 + (i & 1)] * rs4[i & 2], a[(i & 2) + 1][4 + (i & 1)] * rs4[(i & 2) + 1]));
#pragma unroll
          for (int i = 0; i < 8; ++i) { SGB(0x008, 1); SGB(0x002, 5); }
          SCHED;
          phik2(r, rq, pw);
          KV_ADDR;
          WAITL0;
          TRR(60);
          TRC(19); BAR;
          kvpart(va, pa, kvo, lane, wave);
          CLOBBER;
          TRC(20); BAR;
          u32x4 qp[4], qn[4];
          float sqp[4], sqn[4];
#pragma unroll
          for (int m = 0; m < 4; ++m) proj(m, 0, w0, w1);
          SCHED;
          float ep[4][8], en[4][8];
          hedgehog<4>(r, rq, ep, en);
#pragma unroll
          for (int m = 0; m < 4; ++m) {
            qp[m] = pack8(ep[m]); qn[m] = pack8(en[m]);
            sqp[m] = MFMA(ones, as_frag(qp[m]), zero)[0];
            sqn[m] = MFMA(ones, as_frag(qn[m]), zero)[0];
          }
          SCHED;
#pragma unroll
          for (int m = 0; m < 4; ++m) { sqp[m] = __builtin_amdgcn_rcpf(sqp[m]); sqn[m] = __builtin_amdgcn_rcpf(sqn[m]); }
#pragma unroll
          for (int m = 0; m < 4; ++m) { OPAQUE_V(qp[m]); OPAQUE_V(qn[m]); OPAQUE_V(sqp[m]); OPAQUE_V(sqn[m]); }
          unsigned ko = (lane >> 5) * KVP_G + (lane & 31) * 16, ks = kvj + (unsigned)((j ^ RX) - j) * KVP_J + KVP_SUM + h * (8 * KVP_G), za = lds0 + L_ZF + (h * 8 + fq) * 16;
          OPAQUE_V(ko); OPAQUE_S(ks); OPAQUE_V(za);
          TRV(51, 0);
          TRC(24); BAR;
          if (H2D) __builtin_amdgcn_s_sleep(H2D);
          u32x4 kv[4];
#pragma unroll
          for (int i = 0; i < 4; ++i) kv[i] = ld16_sc1(rs, ko, ks + i * (2 * KVP_G));
          SCHED;
          {
            const bf16x8 zf0 = lds_ld<bf16x8>(za), zf1 = lds_ld<bf16x8>(za + 64);
            float dp[4], dn[4];
#pragma unroll
            for (int m = 0; m < 4; ++m) { dp[m] = MFMA(zf0, as_frag(qp[m]), zero)[0]; dn[m] = MFMA(zf1, as_frag(qn[m]), zero)[0]; }
            SCHED;
#pragma unroll
            for (int m = 0; m < 4; ++m) {
              const float inv = __builtin_amdgcn_rcpf(__builtin_fmaf(dp[m], sqp[m], __builtin_fmaf(dn[m], sqn[m], 1e-6f)));
              qp[m] = pack8(ep[m], sqp[m] * inv); qn[m] = pack8(en[m], sqn[m] * inv);
            }
#pragma unroll
            for (int m = 0; m < 4; ++m) { OPAQUE_V(qp[m]); OPAQUE_V(qn[m]); }
          }
          SCHED;
          for (unsigned spins = 0; !dead && __any(max(umax4v(umax4v(0u, kv[0]), kv[1]), umax4v(umax4v(0u, kv[2]), kv[3])) == SENT); ++spins) {
            if (spins > SPIN_CAP) { dead = true; break; }
            CLOBBER;
            TRV(51, spins + 1);
#pragma unroll
            for (int i = 0; i < 4; ++i) kv[i] = ld16_sc1(rs, ko, ks + i * (2 * KVP_G));
          }
          TRC(23);
          f32x4 o[4][2];
#pragma unroll
          for (int m = 0; m < 4; ++m)
#pragma unroll
            for (int i = 0; i < 2; ++i) o[m][i] = MFMA(as_frag(kv[2 * i + 1]), as_frag(qn[m]), MFMA(as_frag(kv[2 * i]), as_frag(qp[m]), zero));
#pragma unroll
          for (int m = 0; m < 4; ++m) st16_sc1(rs, l16, otile + m * 2048, nosent(pack(o[m][0], o[m][1])));
        }
      });
#pragma unroll
      for (int i = 0; i < 8; ++i) asm volatile("" : "=v"(acc[i >> 2][i & 3]));
      TRC(25); BAR;
    } else {
      __builtin_amdgcn_s_setprio(3);
      if (l0) {
        WAITV(6);
        WAITL0;
        TRC(1); BAR;
      }
      LOCALS;
      u32x4 xr[2][2];
      const unsigned lin = lane * 16, vlin = lin + hw * 1024;
      const int jr = JROT(j);
      u32x4 sent4 = {SENT, SENT, SENT, SENT};
      OPAQUE_V(sent4);
      const unsigned cb0 = ob_off(g, j, l), cb2 = xb_off(g, l) + j * 16384 - 8 * 4096;
      const unsigned cb1 = WS_KVP + (unsigned)j * KVP_J + (unsigned)g * KVP_G + (unsigned)(l >> 1) * KVP_L - 4 * 4096;
      auto ldres = [&](int slot) {
        const int fr = lane & 15, fq = lane >> 4, ch = (fq & 1) * 2 + (fq >> 1);
#pragma unroll
        for (int i = 0; i < 4; ++i)
          xr[i >> 1][i & 1] = lds_ld<u32x4>(lds0 + L_R2 + slot * XS + (hw * 32 + ((i >> 1) ^ 1) * 16 + fr) * 128 + (((ch + 4 * (i & 1)) ^ ((fr >> 1) & 7)) * 16));
      };
      auto aslot = [&](int t) { return lds0 + L_R2 + (t % 3) * XS + hw * 1024; };
      auto wslot = [&](int t) { return lds0 + L_R1 + (t & 3) * 24576 + hw * 1024; };
      const bool l0f = l == 0;
      const char* pA = l0f ? (const char*)p.x : p.ws;
      const char* pW = l0f ? (const char*)p.wqkvA : p.ws;
      unsigned zA = l0f ? 2048 * 2048 : WS_SPAN, zW = l0f ? 1024 * 6144 : WS_SPAN;
      unsigned bA = l0f ? g * (128 * 2048) : xb_off(g, l - 1), kA = l0f ? 128 : 16384, pcA = l0f ? 32 * 2048 : 4096;
      unsigned bW = l0f ? j * 128 : WS_WP + (unsigned)((isb ? 16 : 0) + j) * (16 * 24576), kW = l0f ? 64 * 6144 : 24576;
      unsigned pcW = l0f ? 2048 : 8192, hW = l0f ? 32 * 6144 : 4096;
      int kb = jr + (l0f ? 8 * (g & 1) : 0);
      OPAQUE_S(pA); OPAQUE_S(pW); OPAQUE_S(zA); OPAQUE_S(zW); OPAQUE_S(bA); OPAQUE_S(kA); OPAQUE_S(pcA);
      OPAQUE_S(bW); OPAQUE_S(kW); OPAQUE_S(pcW); OPAQUE_S(hW); OPAQUE_S(kb);
      const rsrc_t rA = mk_rsrc(pA, zA);
      unsigned vA = vlin, vW = vlin;
      if (l0f) {
        vA = (hw * 8 + (lane >> 3)) * 2048 + (((lane & 7) ^ (((hw & 1) * 4 + (lane >> 4)) & 7)) * 16);
        vW = (hw * 8 + (lane >> 3)) * 6144 + (((lane & 7) ^ ((((lane >> 4) & 1) | ((hw & 1) << 1)) << 1)) * 16);
      }
      int dj = l0f ? -1 : (j & 3);
      OPAQUE_S(dj);
      auto kseq = [&](int t) { return (kb + t - ((t < 4 && t <= dj) ? 1 : 0)) & 15; };
      auto offA = [&](int t) { return bA + (unsigned)kseq(t) * kA; };
      auto offW = [&](int t) { return bW + (unsigned)kseq(t) * kW; };
      auto ldA = [&](int t) { dma4<4096>(rA, vA, offA(t), pcA, aslot(t)); };
      auto ldWh = [&](int t, unsigned o, int hf) { dma3<8192>(mk_rsrc(pW, zW), vW, o + (hf ? hW : 0u), pcW, wslot(t) + hf * 4096); };
      const bool slowj = l0f && SLOWC(j);
      auto packedW = [&]() {
        pW = p.ws; zW = WS_SPAN; bW = WS_WP + (unsigned)j * (16 * 24576); kW = 24576; pcW = 8192; hW = 4096; vW = vlin;
      };
      auto ldW = [&](int t) { const unsigned o = offW(t); ldWh(t, o, 0); ldWh(t, o, 1); };
      const unsigned cmask = 0xfu | (isb ? 0u : 0xf0u) | (l == 3 ? 0u : 0xf00u);
      auto clean = [&](int t) {
        if (t < 4 || (t < 12 && ((cmask >> t) & 1))) st16_sc1(rs, vlin, (t < 4 ? cb0 : (t < 8 ? cb1 : cb2)) + (unsigned)t * 4096, sent4);
      };
      const unsigned flo = myrep + ((l0f && lane >= 16) ? fl_line(FL_CS, JOBC(j, (lane >> 4) - 1)) : fl_line(FL_X + l, g)) + (lane & 15) * 8;
      unsigned ready = ~0u;
      int tc = -2, tr = 0;
      u32x4 cv[4];
#pragma unroll
      for (int ii = 0; ii < 4; ++ii) asm volatile("" : "=v"(cv[ii]));
      auto chkrd = [&](int t) {
#pragma unroll
        for (int ii = 0; ii < 4; ++ii) cv[ii] = lds_ld<u32x4>(aslot(t) + lin + ii * 4096);
      };
      auto chkbad = [&]() { return __any(max(umax4v(umax4v(0u, cv[0]), cv[1]), umax4v(umax4v(0u, cv[2]), cv[3])) == SENT) && !l0f; };
      auto refetch = [&](int t) __attribute__((always_inline)) {
        for (unsigned spins = 0; !dead; ++spins) {
          if (spins > SPIN_CAP) { dead = true; break; }
          WAITV(0);
          TRV(t == 1 ? 56 : 57, spins + 1);
          ldA(t);
          ldA(t + 1);
          WAITV(0);
          chkrd(t);
          if (!chkbad()) break;
          __builtin_amdgcn_s_sleep(1);
        }
      };
      if (l0f) {
        u32x2 gw2;
        ld8_asm(gw2, rs, WS_EPOCH + 0u * lin);
        TRC(44); BAR;
        ldW(0); ldA(0);
        ldW(1);
        dma1(mk_rsrc(p.wphi, 32 * 2048), vlin, j * 4096, lds0 + L_WPHI + hw * 1024);
        WAITV(17);
        OPAQUE_V(gw2);
        const u64 gword = ((u64)(unsigned)UNI(gw2[1]) << 32) | (unsigned)UNI(gw2[0]);
        u64 z = gword + 0x9e3779b97f4a7c15ull;
        z = (z ^ (z >> 30)) * 0xbf58476d1ce4e5b9ull;
        z = (z ^ (z >> 27)) * 0x94d049bb133111ebull;
        z = z ^ (z >> 31);
        z = (z == gword) ? ~z : z;
        epoch = u32x2{(unsigned)z, (unsigned)(z >> 32)};
        tc = slowj ? -2 : (g >> 1) + 2; tr = (j & 3) + 8 * (g & 1);
        WAITV(7);
        TRC(1); BAR;
      }
#pragma unroll
      for (int i = 0; i < 4; ++i) asm volatile("" : "=v"(xr[i >> 1][i & 1]));
      unsigned oA = offA(3), oW = offW(2);
      OPAQUE_S(oA); OPAQUE_S(oW);
      TRV(56, 0); TRV(57, 0);
      if (WFIRST) { ldWh(2, oW, 0); ldWh(2, oW, 1); }
      if (XD) __builtin_amdgcn_s_sleep(XD);
      ldA(1); ldA(2);
      TRC(40);
      if (!WFIRST) { ldWh(2, oW, 0); ldWh(2, oW, 1); }
      if (LAW == 3) ldW(3);
      if (tr == 0) ldres(0);
      clean(0);
      WAITV((WFIRST ? 5 : 11) + (LAW == 3 ? 6 : 0));
      chkrd(1);
      if (__builtin_expect(chkbad(), 0)) refetch(1);
      const int fr = lane & 15, fq = lane >> 4;
      u32x2 fl;
      f32x4 js[4], jc[4];
      float ra[4], rb[4];
      u32x4 wo2[2];
      asm volatile("" : "=v"(wo2[0])); asm volatile("" : "=v"(wo2[1]));
      const unsigned fwo = myrep + fl_line(FL_WO + (isb ? 1 : 0), jr + (lane >> 4)) + (lane & 15) * 8;
      static_for<1, 14>([&](auto t_c) __attribute__((always_inline)) {
        constexpr int t = decltype(t_c)::value;
        constexpr auto rest = [](int s) {
          return s == 0 ? (WFIRST ? 0 : 6) + (LAW == 3 ? 6 : 0) + 1
                        : (s + LAW <= 15 ? 6 : 0) + (s < 4) + (s == NVAL - 2 || s == 5 || s == 11) + ((s == 7 || s == 8) ? 4 : 0);
        };
        constexpr int NW = t == 1 ? 63 : 4 + rest(t - 1) + ((LAW == 3 && (t > 2 || WFIRST)) ? rest(t - 2) : 0);
        TRC(1 + t);
        if (t >= NVAL && t <= 6 && __builtin_expect((ready & (1u << kseq(t + 2))) == 0, 0)) {
          WAITV(NW); WAITL0; BAR;
          ready = poll_need(rs, flo, epoch, ready, 1u << kseq(t + 2), dead);
          dma4<4096>(rA, vA, oA, pcA, aslot(t + 2));
        } else if (t == 7 && __builtin_expect(ready != ~0u, 0)) {
          WAITV(NW); WAITL0; BAR;
          poll_all(rs, flo, epoch, dead);
          dma4<4096>(rA, vA, oA, pcA, aslot(t + 2));
        } else {
          bar_dma4<4096, NW>(rA, vA, oA, pcA, aslot(t + 2));
        }
        if (t == 1 && lead) set_flags(rs, fl_line(FL_X + l, g), j, l0f ? fl_line(FL_CS, j) : fl_line(FL_X + l, g), l0f ? g : j, epoch, lane);
        if (t == 7 && slowj) packedW();
        if (t + LAW <= 15) oW = offW(t + LAW);
        if (t < 13) oA = offA(t + 3);
        OPAQUE_S(oA); OPAQUE_S(oW);
        clean(t);
        if (t <= NVAL && !l0f) { WAITV(rest(t - 1) + 4 + (t < 4)); chkrd(t + 1); }
        if (t == WT + 3 && l0f) {
          OPAQUE_V(wo2[0]); OPAQUE_V(wo2[1]);
#pragma unroll
          for (int i = 0; i < 2; ++i) st16_sc1(rs, lin, WS_WO + (unsigned)((jr + hw) * 131072 + g * 8192 + ((j & 3) * 2 + i) * 1024), wo2[i]);
        }
        if (t == WT + 6 && l0f && lead) set_flags(rs, fl_line(FL_WO, j), g, fl_line(FL_WO, j), g, epoch, lane);
        if (t >= 2 && t <= 9 && (t & 1) == (LAW == 2) && (tc | 1) == (t | 1)) {
          const unsigned src = lds0 + L_R1 + (tc & 3) * 24576 + vlin;
          u32x4 w[6];
#pragma unroll
          for (int ii = 0; ii < 6; ++ii) w[ii] = lds_ld<u32x4>(src + ii * 4096);
#pragma unroll
          for (int ii = 0; ii < 6; ++ii) st16_sc1(rs, vlin, WS_WP + (unsigned)((j * 16 + ((kb + tc) & 15)) * 24576 + ii * 4096), w[ii]);
        }
        if ((t < 4 || (t >= 8 && t < 12)) && tr == t) ldres(t % 3);
        if (t == 7) {
#pragma unroll
          for (int i = 0; i < 4; ++i) ld16_asm(jc[i], rs, fq * 768 + hw * 192 + (fr < 12 ? fr : 0) * 16, WS_CS + (unsigned)(((isb ? 16 : 0) + j) * 16 + i * 4) * 768);
        }
        if (t == 8) {
#pragma unroll
          for (int i = 0; i < 4; ++i) ld16_asm(js[i], rs, fq * 1024 + hw * 256 + fr * 16, st_off(g, l) + i * 4096);
        }
        SCHED;
        if (t + LAW <= 15) ldWh(t + LAW, oW, 0);
        SCHED;
        bool bad = false;
        if (t <= NVAL) bad = chkbad();
        if (t == 10) {
#pragma unroll
          for (int i = 0; i < 4; ++i) OPAQUE_V(jc[i]);
          const f32x4 s = (jc[0] + jc[1]) + (jc[2] + jc[3]);
#pragma unroll
          for (int i = 0; i < 4; ++i) { ra[i] = s[i]; OPAQUE_V(ra[i]); }
        }
        if (t == 11) {
#pragma unroll
          for (int i = 0; i < 4; ++i) OPAQUE_V(js[i]);
          const f32x4 s = (js[0] + js[1]) + (js[2] + js[3]);
#pragma unroll
          for (int i = 0; i < 4; ++i) { rb[i] = s[i]; OPAQUE_V(rb[i]); }
        }
        if (t == 12) {
          red2_add(ra[0], ra[1]); red2_add(ra[2], ra[3]); red2_add(rb[0], rb[1]); red2_add(rb[2], rb[3]);
          if (lane < 12) lds_st<f32x4>(lds0 + L_CS + hw * 192 + lane * 16, f32x4{ra[0], ra[1], ra[2], ra[3]});
#pragma unroll
          for (int i = 0; i < 4; ++i) OPAQUE_V(rb[i]);
        }
        if (t == 13) {
          const float m0 = rb[0] * (1.0f / 1024), m1 = rb[2] * (1.0f / 1024);
          const float r0 = __builtin_amdgcn_rsqf(fmaxf(__builtin_fmaf(rb[1], 1.0f / 1024, -m0 * m0), 0.f) + 1e-5f);
          const float r1 = __builtin_amdgcn_rsqf(fmaxf(__builtin_fmaf(rb[3], 1.0f / 1024, -m1 * m1), 0.f) + 1e-5f);
          if (lane < 16) {
            lds_st<f32x2>(lds0 + L_MEAN + 64 + hw * 128 + fr * 8, f32x2{-m0, -m1});
            lds_st<f32x2>(lds0 + L_RSTD + 64 + hw * 128 + fr * 8, f32x2{r0, r1});
          }
        }
        SCHED;
        if (t + LAW <= 15) ldWh(t + LAW, oW, 1);
        if (t == NVAL - 1 || t == 6) {
          WAITV(10);
          OPAQUE_V(fl);
          if (t == 6) ready = __all(is_set(fl, epoch)) ? ~0u : 0u;
          else {
            ready = l0f ? ~0u : (unsigned)__ballot(is_set(fl, epoch));
            if (slowj && __all(is_set(fl, epoch))) packedW();
          }
        }
        if (t == 12) { WAITV(10); OPAQUE_V(fl); }
        if (t == 13 && __builtin_expect(!__all(is_set(fl, epoch)), 0)) poll_all(rs, fwo, epoch, dead);
        if (t == NVAL - 2 || t == 5) ld8_asm(fl, rs, flo);
        if (t == 11) ld8_asm(fl, rs, fwo);
        if (t == WT && l0f) {
#pragma unroll
          for (int i = 0; i < 2; ++i) ld16nt_asm(wo2[i], mk_rsrc(p.woA, 1024 * 2048), (lane >> 3) * 2048 + wo_swz(lane), (g * 64 + (j & 3) * 16 + i * 8) * 2048 + (jr + hw) * 128);
        }
        if constexpr (t <= NVAL) { if (__builtin_expect(bad, 0)) refetch(t + 1); }
      });
      const unsigned wb = WS_WO + (unsigned)((isb ? 16 : 0) + j) * 131072;
      auto stO = [&](int i) { dma4<4096>(rs, vlin, wb + wo_src(i, j), 4096, lds0 + wo_slot(i) + hw * 1024); };
      const unsigned clo = myrep + ((lane & 16) ? fl_line(FL_CLC + l, (lane & 32) ? j ^ RX : j) : fl_line(FL_CLR + l, g)) + fr * 8;
      auto clcheck = [&]() {
        OPAQUE_V(fl);
        TRV(58, 0);
        if (__builtin_expect(!__all(is_set(fl, epoch)), 0)) { TRV(58, 1); poll_all(rs, clo, epoch, dead); }
      };
      auto accinit = [&]() {
#pragma unroll
        for (int m = 0; m < 2; ++m)
#pragma unroll
          for (int pp = 0; pp < 2; ++pp) {
            auto r0_ = __builtin_amdgcn_permlane16_swap(xr[m][pp][0], xr[m][pp][2], false, false);
            auto r1_ = __builtin_amdgcn_permlane16_swap(xr[m][pp][1], xr[m][pp][3], false, false);
            acc[m][2 * pp] = f32x4{lo16(r0_[0]), hi16(r0_[0]), lo16(r1_[0]), hi16(r1_[0])};
            acc[m][2 * pp + 1] = f32x4{lo16(r0_[1]), hi16(r0_[1]), lo16(r1_[1]), hi16(r1_[1])};
          }
#pragma unroll
        for (int i = 0; i < 8; ++i) OPAQUE_V(acc[i >> 2][i & 3]);
      };
      WAITV(10);
      WAITL0;
      TRC(15); BAR;
      if (lead) set_flags(rs, fl_line(FL_CLR + l, g), j, fl_line(FL_CLC + l, j), g, epoch, lane);
      if (isb) {
        stO(0); stO(2); stO(3);
        WAITV(12);
        TRC(16); BAR;
        stO(1); stO(4); stO(5);
        TRC(17); BAR;
        stO(7); stO(6);
        __builtin_amdgcn_s_setprio(0);
        accinit();
        ld8_asm(fl, rs, clo);
        TRC(18); BAR;
        WAITV(0);
        clcheck();
      } else {
        u32x4 vb[8];
#pragma unroll
        for (int i = 0; i < 8; ++i) asm volatile("" : "=v"(vb[i]));
        stO(0); stO(2); stO(3);
        WAITV(12);
        TRC(16); BAR;
        stO(1);
        if (l0f) {
          const bool wj = hw != 0;
          const char* bp = (const char*)(wj ? p.wqkvB : p.woB);
          unsigned rs_ = wj ? 6144 : 2048, sz = rs_ * 1024, ss = rs_ * 8, s2 = wj ? 16 * 6144 : 128;
          unsigned sb = g * 64 * rs_ + (wj ? (hw - 1) * 2048 + JOBC(j, hw - 1) * 128 : (j & 3) * (16 * 2048) + jr * 128);
          OPAQUE_S(bp); OPAQUE_S(rs_); OPAQUE_S(sz); OPAQUE_S(sb); OPAQUE_S(ss); OPAQUE_S(s2);
          const rsrc_t rb = mk_rsrc(bp, sz);
#pragma unroll
          for (int i = 0; i < 8; ++i) ld16nt_asm(vb[i], rb, (lane >> 3) * rs_ + (lane & 7) * 16, sb + (i & 1) * ss + (i >> 1) * s2);
        }
        __builtin_amdgcn_s_setprio(0);
        KV_ADDR;
        bf16x8 sel = rowsum_sel(lane);
        unsigned h1 = (lane >> 3) * KVP_G + (lane & 7) * 16;
        unsigned pw = lds0 + SC_PHIK + (h * 128 + wm * 64 + 32 + fr) * 160 + fq * 8, kw = lds0 + L_PREV + hw * 2048 + lin;
        const unsigned wa = lds0 + L_WPHI + h * 2048 + (fq * 4 + (fr >> 2)) * 64 + (lane & 3) * 8;
        bf16x8 w0 = tr2(wa, wa + 1024), w1 = tr2(wa + 32, wa + 32 + 1024);
        float cq[4];
#pragma unroll
        for (int m = 0; m < 2; ++m) { cq[m] = lds_ld<float>(lds0 + L_RSTD + (16 + wm * 64 + 32 + m * 16 + fr) * 4) * 1.4426950408889634f; OPAQUE_V(cq[m]); }
        OPAQUE_V(sel); OPAQUE_V(h1); OPAQUE_V(pw); OPAQUE_V(kw); OPAQUE_V(w0); OPAQUE_V(w1);
        TRC(17); BAR;
        TRC(18); BAR;
        ld8_asm(fl, rs, clo);
        {
          const f32x4 zero = {0.f, 0.f, 0.f, 0.f};
          f32x4 r[4][2];
#pragma unroll
          for (int m = 0; m < 2; ++m) {
            const bf16x8 f = lds_ld<bf16x8>(kw + m * 1024);
            r[m][0] = MFMA(w0, f, zero); r[m][1] = MFMA(w1, f, zero);
          }
          phik2(r, cq, pw);
        }
        WAITV(0);
#pragma unroll
        for (int i = 0; i < 8; ++i) OPAQUE_V(vb[i]);
        clcheck();
        WAITL0;
        TRC(19); BAR;
        kvpart(va, pa, kvj + (unsigned)g * KVP_G, lane, wave);
        CLOBBER;
        TRC(20); BAR;
        if (H1D > 0) stO(4);
        if (H1D > 1) stO(5);
        const f32x4 zero = {0.f, 0.f, 0.f, 0.f};
        const unsigned s1 = kvj + (unsigned)((j ^ RX) - j) * KVP_J + g * 512 + hw * 128, s2 = kvj + KVP_Z + (hw & 1) * 128;
        u32x4 v[4];
#pragma unroll
        for (int i = 0; i < 4; ++i) v[i] = ld16_sc1(rs, h1, (i < 2 ? s1 : s2) + (i & 1) * (8 * KVP_G));
        TRC(21); TRV(50, 0);
        for (unsigned spins = 0; !dead && __any(umax4v(umax4v(0u, v[0]), v[1]) == SENT); ++spins) {
          if (spins > SPIN_CAP) { dead = true; break; }
          CLOBBER;
          TRV(50, spins + 1);
          v[0] = ld16_sc1(rs, h1, s1); v[1] = ld16_sc1(rs, h1, s1 + 8 * KVP_G);
        }
        TRC(22);
        const f32x4 c = rowsum_fin(MFMA(sel, as_frag(v[1]), MFMA(sel, as_frag(v[0]), zero)));
        if ((lane & 40) == 0)
          st8_sc1(rs, fr * 16 + fq * 8, kvj + (unsigned)g * KVP_G + KVP_SUM + hw * 128, u32x2{min(pk2(c[0], c[1]), SENT - 1), min(pk2(c[2], c[3]), SENT - 1)});
        for (unsigned spins = 0; !dead && __any(umax4v(umax4v(0u, v[2]), v[3]) == SENT); ++spins) {
          if (spins > SPIN_CAP) { dead = true; break; }
          CLOBBER;
          v[2] = ld16_sc1(rs, h1, s2); v[3] = ld16_sc1(rs, h1, s2 + 8 * KVP_G);
        }
        const f32x4 z = rowsum_fin(MFMA(sel, as_frag(v[3]), MFMA(sel, as_frag(v[2]), zero)));
        if ((lane & 40) == 0) lds_st<u32x2>(lds0 + L_ZF + (hw & 1) * 128 + fr * 16 + fq * 8, u32x2{pk2(z[0], z[1]), pk2(z[2], z[3])});
        WAITL0;
        TRC(24); BAR;
        accinit();
        if (H2D) __builtin_amdgcn_s_sleep(H2D);
        if (H1D < 1) stO(4);
        if (H1D < 2) stO(5);
        stO(6); stO(7);
        if (l0f) {
          if (hw) {
            job_colsum(rs, vb, 1, g, JOBC(j, hw - 1), hw - 1, lane);
#pragma unroll
            for (int i = 0; i < 8; ++i) job_pack(rs, vb[i], i, 1, g, JOBC(j, hw - 1), hw - 1, lane);
          } else {
#pragma unroll
            for (int i = 0; i < 8; ++i)
              st16_sc1(rs, (lane >> 3) * 128 + wo_swz(lane), WS_WO + (unsigned)((16 + jr + (i >> 1)) * 131072 + g * 8192 + ((j & 3) * 2 + (i & 1)) * 1024), vb[i]);
          }
        }
        WAITV(0);
      }
      TRC(25); BAR;
      if (l0f && lead) set_flags(rs, fl_line(FL_PK, j), g, fl_line(FL_WO + 1, j), g, epoch, lane);
    }
    {
      LOCALS;
      const int fr = lane & 15, fq = lane >> 4;
      const int kh = half;
      const unsigned v0 = lane * 16 + hw * 4096 + kh * 2048, v1 = lane * 16 + hw * 4096 + (kh ^ 1) * 2048;
      const int r0 = (JROT(j) >> 1) + 2;
      const unsigned ob0 = ob_off(g, kh, l);
      const unsigned pko = myrep + fl_line(FL_PK, JOBC(j, fq - 1)) + fr * 8;
      u32x4 R[8][4];
      u32x2 pkf;
      auto issue_ = [&](int i) {
        const unsigned so = ob0 + (unsigned)((r0 + i) & 7) * 131072;
        R[i][0] = ld16_sc1(rs, v0, so); R[i][1] = ld16_sc1(rs, v1, so);
        R[i][2] = ld16_sc1(rs, v0 + 1024, so); R[i][3] = ld16_sc1(rs, v1 + 1024, so);
      };
      auto issue = [&](int i) { issue_(i); SCHED; };
      constexpr int LA = LOOKAHEAD;
      TRV(52, 0); TRV(53, 0); TRV(54, 0);
      TRC(26); TRR(61);
      __builtin_amdgcn_s_setprio(3);
      SCHED;
      issue(0);
      __builtin_amdgcn_s_setprio(2);
      auto vmax = [&](int i) { return umax4v(umax4v(umax4v(umax4v(0u, R[i][0]), R[i][1]), R[i][2]), R[i][3]); };
      auto bad = [&](int i) { return __any(umax4v(umax4v(umax4v(umax4v(0u, R[i][0]), R[i][1]), R[i][2]), R[i][3]) == SENT); };
      auto poll = [&](int i, int nfl) {
        for (unsigned spins = 0; !dead; ++spins) {
          if (spins > SPIN_CAP) { dead = true; break; }
          __builtin_amdgcn_s_sleep(2);
          CLOBBER;
          TRV(52 + (i > 2 ? 2 : i), spins + 1);
          issue(i);
#pragma unroll
          for (int k = i + 1; k < i + nfl && k < 8; ++k)
            if (bad(k)) issue(k);
          WAITV(0);
#pragma unroll
          for (int k = i + 1; k < i + nfl && k < 8; ++k)
#pragma unroll
            for (int f = 0; f < 4; ++f) OPAQUE_V(R[k][f]);
          if (!bad(i)) break;
        }
      };
      unsigned bb[4];
      {
        const int kr = fq * 4 + (fr >> 2);
        const int gs = (kr >> 1) & 3;
#pragma unroll
        for (int c = 0; c < 4; ++c) bb[c] = lds0 + kh * 8192 + kr * 128 + ((c ^ gs) * 32) + (lane & 3) * 8;
      }
      bf16x8 bf[2][4];
      auto rdB = [&](int i, int hh) {
        if (hh == 0) {
#pragma unroll
          for (int c = 0; c < 4; ++c) { bb[c] += wo_slot(i) - (i ? wo_slot(i - 1) : 0u); asm("" : "+v"(bb[c])); }
        }
#pragma unroll
        for (int c = 0; c < 4; ++c) bf[hh][c] = tr2(bb[c] + hh * 4096, bb[c] + hh * 4096 + 2048);
      };
      auto mms = [&](int i, int hh) {
#pragma unroll
        for (int n = 0; n < 4; ++n)
#pragma unroll
          for (int m = 0; m < 2; ++m) acc[m][n] = MFMA(bf[hh][n], as_frag(R[i][hh * 2 + m]), acc[m][n]);
      };
      if (!half) {
#pragma unroll
        for (int i = 0; i < 8; ++i) acc[i >> 2][i & 3] = f32x4{0.f, 0.f, 0.f, 0.f};
      }
      const char* obase = (l == 3) ? (const char*)p.out : (const char*)p.ws;
      unsigned osize = (l == 3) ? 2048 * 2048 : WS_SPAN;
      unsigned sq = WS_WP + (unsigned)(((isb ? 0 : 16) + j) * 16 + j) * 24576 + kh * 12288;
      unsigned sq1 = WS_WP + (unsigned)(((isb ? 0 : 16) + j) * 16 + JROT(j) + ((1 >> (j & 3)) & 1)) * 24576 + kh * 12288;
      unsigned dst = lds0 + L_R1 + kh * 12288 + hw * 1024, vq = hw * 1024 + lane * 16;
      OPAQUE_S(obase); OPAQUE_S(osize); OPAQUE_S(sq); OPAQUE_S(sq1); OPAQUE_S(dst); OPAQUE_V(vq);
      const rsrc_t ro = mk_rsrc(obase, osize);
      unsigned so[2];
      unsigned xw = lds0 + SC_XCH + wave * 4096 + lane * 16;
      unsigned xa = lds0 + SC_XCH + (wave ^ 4) * 4096 + lane * 16;
      unsigned sb0 = (l == 3) ? g * (128 * 2048) + j * 128 : xb_off(g, l) + j * 16384;
      {
        const int row = (hw * 2 + kh) * 16 + fr, ch = (fq & 1) * 2 + (fq >> 1);
        const unsigned sw = (l == 3) ? 0u : (unsigned)((row >> 1) & 7), rb = (unsigned)row << ((l == 3) ? 11 : 7);
#pragma unroll
        for (int pp = 0; pp < 2; ++pp) { so[pp] = rb + (((ch + 4 * pp) ^ sw) * 16); OPAQUE_V(so[pp]); }
        OPAQUE_V(xa); OPAQUE_V(xw); OPAQUE_S(sb0);
      }
      SCHED;
#pragma unroll
      for (int i = 1; i < LA; ++i) issue(i);
      rdB(0, 0);
      unsigned mx = vmax(0);
#pragma unroll
      for (int i = 0; i < 8; ++i) {
        SCHED;
        if (__builtin_expect(__any(mx == SENT), 0)) poll(i, LA);
        if (i == 0) TRC(27); else TRQ(27 + i);
        if (i == 3) { pkf = ld8_sc1(rs, pko, 0); SCHED; }
        if (i == 7) {
          OPAQUE_V(pkf);
          if (l == 0 && lead && !__all(is_set(pkf, epoch))) poll_all(rs, pko, epoch, dead);
          TRC(35); BAR;
        }
        if (i == 7 && l < 3) dma3<4096>(rs, vq, sq, 4096, dst);
        if (i == 2) __builtin_amdgcn_s_setprio(1);
        if (i == 5) __builtin_amdgcn_s_setprio(0);
        SCHED;
        rdB(i, 1);
        if (i + LA < 8) issue_(i + LA);
        mms(i, 0);
#pragma unroll
        for (int k = 0; k < 4; ++k) { SGB(0x008, 1); SGB(0x100, 1); SGB(0x020, 1); SGB(0x008, 1); SGB(0x100, 1); }
        SCHED;
        if (i == 7 && l < 3) { dma3<4096>(rs, vq, sq1, 4096, dst + 24576); SCHED; }
        if (i < 7) { rdB(i + 1, 0); mx = vmax(i + 1); }
        mms(i, 1);
#pragma unroll
        for (int k = 0; k < 4; ++k) { SGB(0x008, 1); SGB(0x100, 2); }
#pragma unroll
        for (int k = 4; k < 8; ++k) { SGB(0x008, 1); SGB(0x002, 8 / (8 - 4)); }
      }
      SCHED;
      TRC(36);
#pragma unroll
      for (int n = 0; n < 4; ++n) lds_st<f32x4>(xw + n * 1024, acc[1][n]);
      WAITL0;
      TRC(37); BAR;
      SCHED;
      f32x4 pa[4];
#pragma unroll
      for (int n = 0; n < 4; ++n) pa[n] = lds_ld<f32x4>(xa + n * 1024);
      SCHED;
      u32x4 o[2];
#pragma unroll
      for (int pp = 0; pp < 2; ++pp) {
        const f32x4 a = acc[0][2 * pp] + pa[2 * pp], b = acc[0][2 * pp + 1] + pa[2 * pp + 1];
        auto r0_ = __builtin_amdgcn_permlane16_swap(pk2(a[0], a[1]), pk2(b[0], b[1]), false, false);
        auto r1_ = __builtin_amdgcn_permlane16_swap(pk2(a[2], a[3]), pk2(b[2], b[3]), false, false);
        o[pp] = nosent(u32x4{r0_[0], r1_[0], r0_[1], r1_[1]});
        st16_sc1(ro, so[pp], sb0, o[pp]);
      }
      if (l < 3) {
        lds_st<u32x4>(lds0 + L_R2 + so[0], o[0]);
        lds_st<u32x4>(lds0 + L_R2 + so[1], o[1]);
        const f32x4 zero = {0.f, 0.f, 0.f, 0.f};
        const bf16x8 ones = {0x3f80, 0x3f80, 0x3f80, 0x3f80, 0x3f80, 0x3f80, 0x3f80, 0x3f80};
        const f32x4 a = MFMA(ones, as_frag(o[1]), MFMA(ones, as_frag(o[0]), zero));
        const f32x4 b = MFMA(as_frag(o[1]), as_frag(o[1]), MFMA(as_frag(o[0]), as_frag(o[0]), zero));
        const float q = (fr & 2) ? ((fr & 1) ? b[3] : b[2]) : ((fr & 1) ? b[1] : b[0]);
        if (fq == (fr >> 2)) st8_sc1(rs, ((hw * 2 + kh) * 16 + fr) * 8, st_off(g, l + 1) + j * 1024, u32x2{__float_as_uint(a[0]), __float_as_uint(q)});
      } else if (lead && blockIdx.x == 0 && lane == 0) {
        st8_sc1(rs, WS_EPOCH, 0, epoch);
      }
    }
    TRC(38); TRR(63);
  }
  STAMP_DUMP;
  asm volatile(".Lpf_end:");
}

extern "C" void kernel_launch(void* const* d_in, const int* in_sizes, int n_in, void* d_out, int out_size,
                              void* d_ws, size_t ws_size, hipStream_t stream) {
  Params p;
  p.x = (const u16*)d_in[0];
  p.wqkvA = (const u16*)d_in[1];
  p.wphi = (const u16*)d_in[2];
  p.woA = (const u16*)d_in[3];
  p.wqkvB = (const u16*)d_in[5];
  p.woB = (const u16*)d_in[6];
  p.out = (u16*)d_out;
  p.ws = (char*)d_ws;
  (void)hipFuncSetAttribute((const void*)mega_kernel, hipFuncAttributeMaxDynamicSharedMemorySize, L_TOTAL);
  mega_kernel<<<256, 512, L_TOTAL, stream>>>(p);
}
```

```cpp
#include <hip/hip_runtime.h>
#include <stdint.h>
#include <type_traits>
constexpr int LOOKAHEAD = 2;
constexpr int NVAL = 4;
#define XD 0
#define WT 1
#define LAW 2
#define POL0 0
#define PKALL 0u
#define W2F 0
#define RX 8
#define H1D 2
#define H2D 5

typedef unsigned short u16;
typedef unsigned long long u64;
typedef __attribute__((ext_vector_type(8))) short bf16x8;
typedef __attribute__((ext_vector_type(4))) short s16x4;
typedef __attribute__((ext_vector_type(4))) float f32x4;
typedef __attribute__((ext_vector_type(2))) float f32x2;
typedef __attribute__((ext_vector_type(4))) unsigned u32x4;
typedef __attribute__((ext_vector_type(2))) unsigned u32x2;
typedef __attribute__((ext_vector_type(2))) __bf16 bf16x2_t;
#define LAS __attribute__((address_space(3)))
typedef LAS s16x4* lds_s16x4_ptr;

constexpr unsigned NREP = 8, REP_STRIDE = 17 * 2048;
enum { FL_X = 0, FL_CS = 4, FL_CLR = 5, FL_CLC = 9, FL_PK = 13, FL_WO = 14 };
__device__ __forceinline__ unsigned fl_line(int set, int a) { return (unsigned)(set * 16 + a) * 128; }
constexpr unsigned WS_EPOCH = 512u << 10;
constexpr unsigned WS_CS = 1u << 20;
constexpr unsigned WS_KVP = 4u << 20;
constexpr unsigned KVP_J = 512u << 10, KVP_G = 32u << 10, KVP_L = 16u << 10, KVP_Z = 8192, KVP_SUM = 8704, KVP_HALO = 12u << 10;
constexpr unsigned SENT = 0xffffffffu;
constexpr unsigned WS_G = 16u << 20, G_STRIDE = 2u << 20;
__device__ __forceinline__ unsigned xb_off(int g, int l) { return WS_G + (unsigned)g * G_STRIDE + (unsigned)l * (256u << 10); }
__device__ __forceinline__ unsigned ob_off(int g, int j, int l) { return WS_G + (unsigned)g * G_STRIDE + (3u << 18) + (unsigned)j * (64u << 10) + (unsigned)l * (16u << 10); }
__device__ __forceinline__ unsigned st_off(int g, int l) { return WS_G + (unsigned)g * G_STRIDE + (7u << 18) + (unsigned)l * (16u << 10); }
constexpr unsigned WS_WP = 48u << 20;
constexpr unsigned WS_WO = 60u << 20;
constexpr unsigned WS_SPAN = 64u << 20;

constexpr unsigned L_R1 = 0;
constexpr unsigned L_R2 = 98304, XS = 16384;
constexpr unsigned L_MISC = 147456;
constexpr unsigned L_MEAN = L_MISC;
constexpr unsigned L_RSTD = L_MISC + 576;
constexpr unsigned L_CS = L_MISC + 1152;
constexpr unsigned L_WPHI = L_MISC + 2688;
constexpr unsigned L_SINK = L_MISC + 6912;
constexpr unsigned L_ZF = L_MISC + 1920;
constexpr unsigned L_PREV = L_MISC + 8192;
constexpr unsigned L_TOTAL = 163840;

#define JROT(j) ((j) & ~3)
#define JOBC(j, w) (((j) & ~3) + (((j) + (w) + 1) & 3))

#define SLOWC(m, jc) ((slowm >> ((m) * 8 + ((jc) & 7))) & 1)

struct Params {
  const u16* x;
  const u16* wqkvA;
  const u16* wphi;
  const u16* woA;
  const u16* wqkvB;
  const u16* woB;
  u16* out;
  char* ws;
};

#define SCHED __builtin_amdgcn_sched_barrier(0)
#define BAR __builtin_amdgcn_s_barrier()
#define SGB(m, n) __builtin_amdgcn_sched_group_barrier(m, n, 0)
#define WAITV(n) asm volatile("s_waitcnt vmcnt(%0)" ::"n"(n) : "memory")
#define WAITL0 asm volatile("s_waitcnt lgkmcnt(0)" ::: "memory")

#define TRV(slot, val) do {} while (0)
#define TRQ(slot) do {} while (0)
#define TRC(slot) do {} while (0)
#define TRR(slot) do {} while (0)
#define STAMP_INIT do {} while (0)
#define STAMP_DUMP do {} while (0)

__device__ __forceinline__ unsigned pk2(float a, float b) {
  f32x2 v = {a, b};
  return __builtin_bit_cast(unsigned, __builtin_convertvector(v, bf16x2_t));
}
__device__ __forceinline__ float lo16(unsigned u) { return __uint_as_float(u << 16); }
__device__ __forceinline__ float hi16(unsigned u) { return __uint_as_float(u & 0xffff0000u); }

template <int A, int B, typename F> __device__ __forceinline__ void static_for(F f) {
  if constexpr (A < B) { f(std::integral_constant<int, A>{}); static_for<A + 1, B>(f); }
}
typedef __amdgpu_buffer_rsrc_t rsrc_t;
__device__ __forceinline__ rsrc_t mk_rsrc(const void* p, unsigned bytes) { return __builtin_amdgcn_make_buffer_rsrc((void*)p, 0, bytes, 0x00020000); }
#define UNI(x) __builtin_amdgcn_readfirstlane(x)
#define DMA_HEAD(P) "s_mov_b32 m0, %4\n\ts_mov_b32 %0, %3\n\ts_nop 2\n\tbuffer_load_dwordx4 %1, %2, %0 offen " P " lds\n\t"
#define DMA_NEXT(P) "s_add_u32 m0, m0, %6\n\ts_add_u32 %0, %0, %5\n\tbuffer_load_dwordx4 %1, %2, %0 offen " P " lds\n\t"
#define DMA_OPS : "=&s"(t) : "v"(voff), "s"(r), "s"(UNI(soff)), "s"(UNI(lds)), "s"(UNI(sstep)), "i"(LSTEP) : "memory", "scc", "m0"
template <int LSTEP, int POL = 1> __device__ __forceinline__ void dma4(rsrc_t r, unsigned voff, unsigned soff, unsigned sstep, unsigned lds) {
  unsigned t;
  if constexpr (POL == 1) asm volatile(DMA_HEAD("sc1") DMA_NEXT("sc1") DMA_NEXT("sc1") DMA_NEXT("sc1") DMA_OPS);
  else if constexpr (POL == 2) asm volatile(DMA_HEAD("nt") DMA_NEXT("nt") DMA_NEXT("nt") DMA_NEXT("nt") DMA_OPS);
  else asm volatile(DMA_HEAD("") DMA_NEXT("") DMA_NEXT("") DMA_NEXT("") DMA_OPS);
}
template <int LSTEP, int POL = 1> __device__ __forceinline__ void dma3(rsrc_t r, unsigned voff, unsigned soff, unsigned sstep, unsigned lds) {
  unsigned t;
  if constexpr (POL == 1) asm volatile(DMA_HEAD("sc1") DMA_NEXT("sc1") DMA_NEXT("sc1") DMA_OPS);
  else if constexpr (POL == 2) asm volatile(DMA_HEAD("nt") DMA_NEXT("nt") DMA_NEXT("nt") DMA_OPS);
  else asm volatile(DMA_HEAD("") DMA_NEXT("") DMA_NEXT("") DMA_OPS);
}
template <int LSTEP, int N> __device__ __forceinline__ void bar_dma4(rsrc_t r, unsigned voff, unsigned soff, unsigned sstep, unsigned lds) {
  unsigned t;
  asm volatile("s_mov_b32 m0, %4\n\ts_mov_b32 %0, %3\n\ts_nop 0\n\ts_waitcnt vmcnt(%7) lgkmcnt(0)\n\ts_barrier\n\t"
               "buffer_load_dwordx4 %1, %2, %0 offen sc1 lds\n\t" DMA_NEXT("sc1") DMA_NEXT("sc1") DMA_NEXT("sc1")
               : "=&s"(t) : "v"(voff), "s"(r), "s"(UNI(soff)), "s"(UNI(lds)), "s"(UNI(sstep)), "i"(LSTEP), "n"(N) : "memory", "scc", "m0");
}
__device__ __forceinline__ void dma1(rsrc_t r, unsigned voff, unsigned soff, unsigned lds) {
  asm volatile("s_mov_b32 m0, %3\n\ts_nop 3\n\tbuffer_load_dwordx4 %0, %1, %2 offen sc1 lds" ::"v"(voff), "s"(r), "s"(UNI(soff)), "s"(UNI(lds)) : "memory", "m0");
}
__device__ __forceinline__ void touch(rsrc_t r, unsigned voff, unsigned lds) {
  asm volatile("s_mov_b32 m0, %2\n\ts_nop 3\n\tbuffer_load_dword %0, %1, 0 offen lds" ::"v"(voff), "s"(r), "s"(UNI(lds)) : "memory", "m0");
}
__device__ __forceinline__ void ld8_asm(u32x2& d, rsrc_t r, unsigned voff) {
  asm volatile("s_nop 4\n\tbuffer_load_dwordx2 %0, %1, %2, 0 offen sc1" : "=v"(d) : "v"(voff), "s"(r) : "memory");
}

__device__ __forceinline__ void ld16in_asm(u32x4& d, rsrc_t r, unsigned voff, unsigned soff) {
  asm volatile("s_nop 4\n\tbuffer_load_dwordx4 %0, %1, %2, %3 offen" : "=v"(d) : "v"(voff), "s"(r), "s"(UNI(soff)) : "memory");
}
__device__ __forceinline__ void ld16_asm(f32x4& d, rsrc_t r, unsigned voff, unsigned soff) {
  asm volatile("s_nop 4\n\tbuffer_load_dwordx4 %0, %1, %2, %3 offen sc1" : "=v"(d) : "v"(voff), "s"(r), "s"(UNI(soff)) : "memory");
}

template <typename T> __device__ __forceinline__ T lds_ld(unsigned a) { return *(LAS T*)(a); }
template <typename T> __device__ __forceinline__ void lds_st(unsigned a, T v) { *(LAS T*)(a) = v; }
__device__ __forceinline__ bf16x8 tr2(unsigned a0, unsigned a1) {
  s16x4 v0 = __builtin_amdgcn_ds_read_tr16_b64_v4i16((lds_s16x4_ptr)(a0));
  s16x4 v1 = __builtin_amdgcn_ds_read_tr16_b64_v4i16((lds_s16x4_ptr)(a1));
  return __builtin_shufflevector(v0, v1, 0, 1, 2, 3, 4, 5, 6, 7);
}
__device__ __forceinline__ bf16x8 as_frag(u32x4 v) { return __builtin_bit_cast(bf16x8, v); }
#define MFMA(a, b, c) __builtin_amdgcn_mfma_f32_16x16x32_bf16(a, b, c, 0, 0, 0)

__device__ __forceinline__ float vmax2(float a, float b) { float inf = __builtin_inff(); asm("" : "+s"(inf)); return __builtin_amdgcn_fmed3f(a, b, inf); }
#define SWAP16(a, b) __builtin_amdgcn_permlane16_swap(__float_as_uint(a), __float_as_uint(b), false, false)
#define SWAP32(a, b) __builtin_amdgcn_permlane32_swap(__float_as_uint(a), __float_as_uint(b), false, false)
__device__ __forceinline__ void red2_max(float& a, float& b) {
  auto r = SWAP16(a, b);
  float x = vmax2(__uint_as_float(r[0]), __uint_as_float(r[1]));
  r = SWAP32(x, x);
  x = vmax2(__uint_as_float(r[0]), __uint_as_float(r[1]));
  r = SWAP16(x, x);
  a = __uint_as_float(r[0]); b = __uint_as_float(r[1]);
}
__device__ __forceinline__ void red2_add(float& a, float& b) {
  auto r = SWAP16(a, b);
  float x = __uint_as_float(r[0]) + __uint_as_float(r[1]);
  r = SWAP32(x, x);
  x = __uint_as_float(r[0]) + __uint_as_float(r[1]);
  r = SWAP16(x, x);
  a = __uint_as_float(r[0]); b = __uint_as_float(r[1]);
}
template <bool MAX, typename M> __device__ __forceinline__ void red4(float& a, float& b, float& c, float& d, M mid) {
  auto op = [](unsigned x, unsigned y) { return MAX ? vmax2(__uint_as_float(x), __uint_as_float(y)) : __uint_as_float(x) + __uint_as_float(y); };
  auto r = SWAP16(a, b), q = SWAP16(c, d);
  const float x = op(r[0], r[1]), y = op(q[0], q[1]);
  r = SWAP32(x, y);
  const float w = mid(op(r[0], r[1]));
  r = SWAP32(w, w);
  q = __builtin_amdgcn_permlane16_swap(r[1], r[1], false, false);
  r = __builtin_amdgcn_permlane16_swap(r[0], r[0], false, false);
  a = __uint_as_float(r[0]); b = __uint_as_float(r[1]); c = __uint_as_float(q[0]); d = __uint_as_float(q[1]);
}
__device__ __forceinline__ float red_max4(float x) {
  auto r = __builtin_amdgcn_permlane16_swap(__float_as_uint(x), __float_as_uint(x), false, false);
  x = vmax2(__uint_as_float(r[0]), __uint_as_float(r[1]));
  r = __builtin_amdgcn_permlane32_swap(__float_as_uint(x), __float_as_uint(x), false, false);
  return vmax2(__uint_as_float(r[0]), __uint_as_float(r[1]));
}
__device__ __forceinline__ float red_add4(float x) {
  auto r = __builtin_amdgcn_permlane16_swap(__float_as_uint(x), __float_as_uint(x), false, false);
  x = __uint_as_float(r[0]) + __uint_as_float(r[1]);
  r = __builtin_amdgcn_permlane32_swap(__float_as_uint(x), __float_as_uint(x), false, false);
  return __uint_as_float(r[0]) + __uint_as_float(r[1]);
}

__device__ __forceinline__ void st16_sc1(rsrc_t r, unsigned voff, unsigned soff, u32x4 v) {
  __builtin_amdgcn_raw_buffer_store_b128(v, r, voff, soff, 16);
  asm volatile("s_nop 1" ::"v"(v) : "memory");
}
__device__ __forceinline__ void st8_sc1(rsrc_t r, unsigned voff, unsigned soff, u32x2 v) { __builtin_amdgcn_raw_buffer_store_b64(v, r, voff, soff, 16); }
__device__ __forceinline__ u32x4 ld16_sc1(rsrc_t r, unsigned voff, unsigned soff) { return __builtin_amdgcn_raw_buffer_load_b128(r, voff, soff, 16); }
__device__ __forceinline__ u32x2 ld8_sc1(rsrc_t r, unsigned voff, unsigned soff) { return __builtin_amdgcn_raw_buffer_load_b64(r, voff, soff, 16); }
__device__ __forceinline__ f32x4 ld4f_sc1(rsrc_t r, unsigned voff, unsigned soff) { return __builtin_bit_cast(f32x4, __builtin_amdgcn_raw_buffer_load_b128(r, voff, soff, 16)); }
#define OPAQUE_V(x) asm volatile("" : "+v"(x))
#define OPAQUE_S(x) asm volatile("" : "+s"(x))

#define CLOBBER asm volatile("" ::: "memory")
constexpr unsigned SPIN_CAP = 1u << 17;
__device__ __forceinline__ bool is_set(u32x2 v, u32x2 epoch) { return v[0] == epoch[0] && v[1] == epoch[1]; }
__device__ __forceinline__ void set_flags(rsrc_t rs, unsigned lineA, int colA, unsigned lineB, int colB, u32x2 epoch, int lane) {
  const unsigned off = (lane & 7) * REP_STRIDE + ((lane & 8) ? lineB + colB * 8 : lineA + colA * 8);
  if (lane < 16) st8_sc1(rs, off, 0, epoch);
}
__device__ __forceinline__ unsigned poll_need(rsrc_t rs, unsigned voff, u32x2 epoch, unsigned ready, unsigned need, bool& dead) {
  for (unsigned spins = 0; !dead; ++spins) {
    CLOBBER;
    ready |= (unsigned)__ballot(is_set(ld8_sc1(rs, voff, 0), epoch));
    if ((ready & need) == need) break;
    if (spins > SPIN_CAP) dead = true;
    __builtin_amdgcn_s_sleep(1);
  }
  CLOBBER;
  return dead ? ~0u : ready;
}

__device__ __forceinline__ void poll_all(rsrc_t rs, unsigned voff, u32x2 epoch, bool& dead) {
  for (unsigned spins = 0; !dead; ++spins) {
    CLOBBER;
    if (__all(is_set(ld8_sc1(rs, voff, 0), epoch))) break;
    if (spins > SPIN_CAP) dead = true;
    __builtin_amdgcn_s_sleep(1);
  }
  CLOBBER;
}

constexpr unsigned SC_VF = L_R1 + 32768;
constexpr unsigned SC_PHIK = L_R1 + 49152;
constexpr unsigned SC_XCH = L_R2 + XS;
constexpr unsigned wo_slot(int s) {
  return s < 2 ? L_R2 + (unsigned)(s + 1) * XS : (s < 6 ? L_R1 + (unsigned)(s - 2) * 16384 : (s == 6 ? L_R1 + 81920 : L_R2));
}
__device__ __forceinline__ unsigned wo_src(int s, int j) { return (unsigned)(((JROT(j) >> 1) + 2 + s) & 7); }
__device__ __forceinline__ unsigned wo_swz(int lane) { return (unsigned)(((lane & 7) ^ ((lane >> 3) & 6)) * 16); }
__device__ __forceinline__ unsigned umax3(unsigned a, unsigned b, unsigned c) { return max(max(a, b), c); }
__device__ __forceinline__ unsigned umax4v(unsigned m, const u32x4& v) { return umax3(umax3(m, v[0], v[1]), v[2], v[3]); }
__device__ __forceinline__ u32x4 nosent(u32x4 v) { return u32x4{min(v[0], SENT - 1), min(v[1], SENT - 1), min(v[2], SENT - 1), min(v[3], SENT - 1)}; }

template <typename P, typename F>
__device__ __forceinline__ void qkv_loop(int isb, int wm, int h, int lane, bool first, int& trv, P pre, F fin) {
  constexpr unsigned lds0 = 0;
  f32x4 acc[4][6];
  const int fr = lane & 15, fq = lane >> 4;
  unsigned ab[2], bb[2];
  {
    const int r = wm * 64 + fr;
#pragma unroll
    for (int ks = 0; ks < 2; ++ks) ab[ks] = lds0 + L_R2 + r * 128 + (((ks * 4 + fq) ^ ((fr >> 1) & 7)) * 16);
    const int kr = fq * 8 + (fr >> 2);
    const int gs = ((kr >> 1) & 1) | (((kr >> 3) & 1) << 1);
#pragma unroll
    for (int c = 0; c < 2; ++c) bb[c] = lds0 + L_R1 + kr * 128 + (((h * 2 + c) ^ gs) * 32) + (lane & 3) * 8;
  }
#pragma unroll
  for (int m = 0; m < 4; ++m)
#pragma unroll
    for (int n = 0; n < 6; ++n) acc[m][n] = f32x4{0.f, 0.f, 0.f, 0.f};
  bf16x8 af[2][4], bf[2][6];
  auto rds = [&](int ks, int p) {
#pragma unroll
    for (int m = 0; m < 4; ++m) af[p][m] = lds_ld<bf16x8>(ab[ks] + m * 2048);
#pragma unroll
    for (int n = 0; n < 6; ++n) {
      const unsigned a = bb[n & 1] + (n >> 1) * 8192 + ks * 4096;
      bf[p][n] = tr2(a, a + 512);
    }
  };
  auto mms = [&](int p) {
#pragma unroll
    for (int n = 0; n < 6; ++n)
#pragma unroll
      for (int mm = 0; mm < 4; ++mm) { const int m = (n & 1) ? 3 - mm : mm; acc[m][n] = n < 4 ? MFMA(bf[p][n], af[p][m], acc[m][n]) : MFMA(af[p][m], bf[p][n], acc[m][n]); }
  };
  auto ilv2 = [&]() {
#pragma unroll
    for (int i = 0; i < 16; ++i) { SGB(0x008, 1); SGB(0x100, 1); }
#pragma unroll
    for (int i = 0; i < 4; ++i) { SGB(0x008, 1); SGB(0x002, 1); }
    SGB(0x008, 4);
  };
  auto ilv3 = [&]() {
#pragma unroll
    for (int i = 0; i < 16; ++i) { SGB(0x008, 1); SGB(0x100, 1); }
#pragma unroll
    for (int i = 0; i < 7; ++i) { SGB(0x008, 1); SGB(0x004, 2); }
    SGB(0x008, 1);
  };
  if (!first) { WAITV(3); WAITL0; }
  TRQ(1); BAR;
  int tslot = 2;
  rds(0, 0);
  int sa = 0, sb = 0;
  int da = XS, db = 24576;
  auto step = [&]() {
    ab[0] += da; ab[1] += da;
    bb[0] += db; bb[1] += db;
  };
  auto ring = [&]() {
    sa = (sa == 2) ? 0 : sa + 1;
    sb = (sb == 3) ? 0 : sb + 1;
    da = (sa == 2) ? -2 * (int)XS : (int)XS;
    db = (sb == 3) ? -3 * 24576 : 24576;
  };
#pragma unroll 1
  for (int t = 14; t > 0; --t) {
    SCHED;
    rds(1, 1); mms(0);
    step();
    ilv2();
    SCHED;
    __builtin_amdgcn_s_waitcnt(0x0070);
    TRQ(tslot); tslot++; BAR;
    SCHED;
    rds(0, 0);
    ring();
    mms(1); ilv3();
  }
  SCHED;
  f32x4 cs[4], nm4[4];
  float nmean[4], csv[2];
  auto fold = [&](int n) {
#pragma unroll
    for (int mm = 0; mm < 4; ++mm) {
      const int m = (n & 1) ? 3 - mm : mm;
#pragma unroll
      for (int e = 0; e < 4; ++e) {
        float t = n < 4 ? __builtin_fmaf(nmean[m], cs[n & 3][e], acc[m][n][e]) : __builtin_fmaf(nm4[m][e], csv[n & 1], acc[m][n][e]);
        asm("" : "+v"(t));
        acc[m][n][e] = t;
      }
    }
  };
  {
    unsigned pm = lds0 + L_MEAN + (16 + wm * 64 + fr) * 4, pc = lds0 + L_CS + (h * 32 + fq * 4) * 4;
    unsigned pm4 = lds0 + L_MEAN + (16 + wm * 64 + fq * 4) * 4, pcv = lds0 + L_CS + (128 + h * 32 + fr) * 4;
    asm("" : "+v"(pm)); asm("" : "+v"(pc)); asm("" : "+v"(pm4)); asm("" : "+v"(pcv));
#pragma unroll
    for (int m = 0; m < 4; ++m) nmean[m] = lds_ld<float>(pm + m * 64);
#pragma unroll
    for (int n = 0; n < 4; ++n) cs[n] = lds_ld<f32x4>(pc + ((n >> 1) * 64 + (n & 1) * 16) * 4);
#pragma unroll
    for (int m = 0; m < 4; ++m) nm4[m] = lds_ld<f32x4>(pm4 + m * 64);
#pragma unroll
    for (int n = 0; n < 2; ++n) csv[n] = lds_ld<float>(pcv + n * 64);
    rds(1, 1); mms(0);
    step();
    fold(0); fold(1);
    SGB(0x002, 8);
#pragma unroll
    for (int i = 0; i < 8; ++i) { SGB(0x008, 1); SGB(0x100, 2); }
#pragma unroll
    for (int i = 0; i < 11; ++i) { SGB(0x008, 1); SGB(0x100, 1); SGB(0x002, 2); }
#pragma unroll
    for (int i = 0; i < 5; ++i) { SGB(0x008, 1); SGB(0x002, 3); }
    SCHED;
    __builtin_amdgcn_s_waitcnt(0xC07F);
    TRQ(16); BAR;
    SCHED;
    rds(0, 0);
    fold(5); mms(1); fold(2);
#pragma unroll
    for (int i = 0; i < 16; ++i) { SGB(0x008, 1); SGB(0x100, 1); SGB(0x002, 1); }
#pragma unroll
    for (int i = 0; i < 8; ++i) { SGB(0x008, 1); SGB(0x002, 2); }
    SCHED;
    pre(0);
    SCHED;
    rds(1, 1);
    fold(4); mms(0); fold(3);
    pre(1);
#pragma unroll
    for (int i = 0; i < 16; ++i) { SGB(0x008, 1); SGB(0x100, 1); SGB(0x002, 1); SGB(0x004, 1); }
#pragma unroll
    for (int i = 0; i < 8; ++i) { SGB(0x008, 1); SGB(0x002, 3); SGB(0x004, 1); }
  }
  SCHED;
  __builtin_amdgcn_s_waitcnt(0xC07F);
  TRQ(17); BAR;
  SCHED;
  auto mm6 = [&](int m, int n0 = 0, int n1 = 6) {
#pragma unroll
    for (int nn = n0; nn < n1; ++nn) { const int n = (nn + 2) % 6; acc[m][n] = n < 4 ? MFMA(bf[1][n], af[1][m], acc[m][n]) : MFMA(af[1][m], bf[1][n], acc[m][n]); }
  };
  if (!isb) {
    fin(std::false_type{}, acc, mm6);
#pragma unroll
    for (int i = 0; i < 24; ++i) asm volatile("" : "=v"(acc[i / 6][i % 6]));
#pragma unroll
    for (int i = 0; i < 6; ++i) asm volatile("" : "=v"(bf[1][i]));
#pragma unroll
    for (int i = 0; i < 4; ++i) asm volatile("" : "=v"(af[1][i]));
  }
  OPAQUE_S(isb);
  if (isb) fin(std::true_type{}, acc, mm6);
}
__device__ __forceinline__ u32x4 pack(const f32x4& a, const f32x4& b) { return u32x4{pk2(a[0], a[1]), pk2(a[2], a[3]), pk2(b[0], b[1]), pk2(b[2], b[3])}; }

template <int N> __device__ __forceinline__ void hedgehog(const f32x4 (&r)[4][2], const float (&c)[4], float (&ep)[4][8], float (&en)[4][8]) {
  float mx[4], nm[4];
#pragma unroll
  for (int m = 0; m < N; ++m) {
    const f32x4 a = r[m][0], b = r[m][1];
    mx[m] = vmax2(fmaxf(fmaxf(a[3], b[0]), b[1]), fmaxf(fmaxf(fmaxf(fmaxf(a[0], a[1]), a[2]), b[2]), b[3]));
    nm[m] = vmax2(fmaxf(fmaxf(-a[3], -b[0]), -b[1]), fmaxf(fmaxf(fmaxf(fmaxf(-a[0], -a[1]), -a[2]), -b[2]), -b[3]));
  }
  SCHED;
  red4<true>(mx[0], mx[1], nm[0], nm[1], [](float x) { return x; });
  if (N == 4) red4<true>(mx[2], mx[3], nm[2], nm[3], [](float x) { return x; });
#pragma unroll
  for (int m = 0; m < N; ++m) { mx[m] *= -c[m]; nm[m] *= -c[m]; }
  SCHED;
#pragma unroll
  for (int m = 0; m < N; ++m) {
#pragma unroll
    for (int i = 0; i < 8; ++i) { ep[m][i] = __builtin_fmaf(r[m][i >> 2][i & 3], c[m], mx[m]); en[m][i] = __builtin_fmaf(r[m][i >> 2][i & 3], -c[m], nm[m]); }
    SCHED;
#pragma unroll
    for (int i = 0; i < 8; ++i) { ep[m][i] = __builtin_amdgcn_exp2f(ep[m][i]); en[m][i] = __builtin_amdgcn_exp2f(en[m][i]); }
    SCHED;
  }
}
__device__ __forceinline__ u32x4 pack8(const float (&e)[8], float f) {
  return u32x4{pk2(e[0] * f, e[1] * f), pk2(e[2] * f, e[3] * f), pk2(e[4] * f, e[5] * f), pk2(e[6] * f, e[7] * f)};
}
__device__ __forceinline__ u32x4 pack8(const float (&e)[8]) { return u32x4{pk2(e[0], e[1]), pk2(e[2], e[3]), pk2(e[4], e[5]), pk2(e[6], e[7])}; }

__device__ __forceinline__ void phik2(const f32x4 (&r)[4][2], const float (&c)[4], unsigned pw) {
  const f32x4 zero = {0.f, 0.f, 0.f, 0.f};
  const bf16x8 ones = {0x3f80, 0x3f80, 0x3f80, 0x3f80, 0x3f80, 0x3f80, 0x3f80, 0x3f80};
  float ep[4][8], en[4][8];
  f32x4 sp[2], sn[2];
  hedgehog<2>(r, c, ep, en);
#pragma unroll
  for (int m = 0; m < 2; ++m) { sp[m] = MFMA(ones, as_frag(pack8(ep[m])), zero); sn[m] = MFMA(ones, as_frag(pack8(en[m])), zero); }
#pragma unroll
  for (int m = 0; m < 2; ++m) { asm("" : "+v"(sp[m])); asm("" : "+v"(sn[m])); }
  SCHED;
#pragma unroll
  for (int m = 0; m < 2; ++m) {
    const float fp = __builtin_amdgcn_rcpf(sp[m][0]), fn = __builtin_amdgcn_rcpf(sn[m][0]);
#pragma unroll
    for (int i = 0; i < 8; ++i) { ep[m][i] *= fp; en[m][i] *= fn; }
    SCHED;
    const u32x4 pp = pack8(ep[m]), pn = pack8(en[m]);
    const unsigned d = pw + m * (16 * 160);
    lds_st<u32x2>(d, u32x2{pp[0], pp[1]});
    lds_st<u32x2>(d + 32, u32x2{pp[2], pp[3]});
    lds_st<u32x2>(d + 64, u32x2{pn[0], pn[1]});
    lds_st<u32x2>(d + 96, u32x2{pn[2], pn[3]});
    SCHED;
  }
}
__device__ __forceinline__ bf16x8 rowsum_sel(int lane) {
  const int fr = lane & 15;
  const unsigned one = (fr < 8) ? (0x3f80u << (16 * (fr & 1))) : 0u;
  const int e = (fr >> 1) & 3;
  return as_frag(u32x4{e == 0 ? one : 0u, e == 1 ? one : 0u, e == 2 ? one : 0u, e == 3 ? one : 0u});
}
__device__ __forceinline__ f32x4 rowsum_fin(f32x4 c) {
#pragma unroll
  for (int k = 0; k < 4; ++k) {
    const float a = c[k];
    c[k] = a + __int_as_float(__builtin_amdgcn_update_dpp(0, __float_as_int(a), 0x128  , 0xf, 0xf, false));
  }
  return c;
}
__device__ __forceinline__ void job_colsum(rsrc_t rs, const u32x4 (&w)[8], int mat, int g, int jc, int hw, int lane) {
  const bf16x8 sel = rowsum_sel(lane);
  f32x4 c = {0.f, 0.f, 0.f, 0.f};
#pragma unroll
  for (int i = 0; i < 8; ++i) c = MFMA(sel, as_frag(w[i]), c);
  const u32x4 w4 = __builtin_bit_cast(u32x4, rowsum_fin(c));
  unsigned so = WS_CS + (unsigned)(((mat * 16 + jc) * 16 + g) * 768);
  OPAQUE_S(so);
  if ((lane & 40) == 0) st16_sc1(rs, (unsigned)(hw * 64 + (lane & 7) * 8 + (lane >> 4) * 4) * 4, so, w4);
}
__device__ __forceinline__ unsigned img_off(int i, int lane) { return (lane >> 3) * 128 + (((lane & 7) ^ ((((lane >> 4) & 1) | ((i & 1) << 1)) << 1)) * 16); }
__device__ __forceinline__ void job_pack(rsrc_t rs, u32x4 w, int i, int mat, int g, int jc, int hw, int lane) {
  st16_sc1(rs, img_off(i, lane),
           WS_WP + (unsigned)(((mat * 16 + jc) * 16 + g) * 24576 + hw * 8192 + i * 1024), w);
}

__global__ __launch_bounds__(512, 2) void mega_kernel(Params p) {
  extern __shared__ __attribute__((aligned(16))) char smem[];
  const int tid = threadIdx.x;
  const int wave0 = __builtin_amdgcn_readfirstlane(tid >> 6);
  const int xx = blockIdx.x & 7, rr = blockIdx.x >> 3;
  const int g0 = (rr >> 2) * 2 + (xx & 1), j0 = (xx >> 1) * 4 + (rr & 3);
  constexpr unsigned lds0 = 0, lds00 = 0;
  if ((unsigned)(size_t)(LAS char*)smem != 0u) return;
#define LOCALS0 int g = g0, j = j0, l = l0, wave = wave0;                                              \
  OPAQUE_S(g); OPAQUE_S(j); OPAQUE_S(l); OPAQUE_S(wave);                                               \
  const int hw = wave & 3, wm = wave & 1, h = (wave >> 1) & 1;                                         \
  const unsigned myrep = (unsigned)((j >> 2) * 2 + (g & 1)) * REP_STRIDE;                              \
  (void)hw; (void)wm; (void)h; (void)myrep;                                                            \
  int lane = tid; OPAQUE_V(lane); lane &= 63
#define LOCALS LOCALS0; const bool isb = l & 1, lead = (wave == 4); const int half = wave >> 2; (void)isb; (void)lead; (void)half
  const rsrc_t rs = mk_rsrc(p.ws, WS_SPAN);
  const bool cw = wave0 < 4;
  auto slowbit = [](const void* q) { return 1u << ((3 - ((unsigned)(size_t)q >> 7)) & 7); };
  unsigned slowm = slowbit(p.wqkvA) | (slowbit(p.wqkvB) << 8) | (slowbit(p.woA) << 16) | (slowbit(p.woB) << 24);
  slowm |= PKALL;
  OPAQUE_S(slowm);
  if (wave0 == 2) touch(mk_rsrc(p.ws, NREP * REP_STRIDE), min(blockIdx.x * 9 + ((tid & 63) < 9 ? (tid & 63) : 0), NREP * REP_STRIDE / 128 - 1) * 128, lds00 + L_SINK + 512);
  if (wave0 == 3) {
    const char* pc;
    unsigned len;
    asm volatile("s_getpc_b64 %0\n.Lpf_base:\n\ts_mov_b32 %1, .Lpf_end-.Lpf_base" : "=s"(pc), "=s"(len));
    const unsigned off = ((unsigned)rr * 16 + (tid & 15)) * 128;
    touch(mk_rsrc(pc, len), ((int)len > 0 && off + 4 <= len) ? off : 0u, lds00 + L_SINK);
  }
  int trv = 0, trA = 0, trB = 0, trC = 0;
  u32x2 epoch = {0u, 0u};
  bool dead = false;
  STAMP_INIT;
  const char *pA = (const char*)p.x, *pW = (const char*)p.wqkvA;
  unsigned zA = 2048 * 2048, bA = g0 * (128 * 2048), kA = 128, pcA = 32 * 2048, vA;
  unsigned zW = 1024 * 6144, bW = j0 * 128, kW = 64 * 6144, pcW = 2048, hW = 32 * 6144, vW;
  int kb = JROT(j0) + 8 * (g0 & 1), dj = -1;
  {
    const int lane = tid & 63, hw = wave0 & 3;
    vA = (hw * 8 + (lane >> 3)) * 2048 + (((lane & 7) ^ (((hw & 1) * 4 + (lane >> 4)) & 7)) * 16);
    vW = (hw * 8 + (lane >> 3)) * 6144 + (((lane & 7) ^ ((((lane >> 4) & 1) | ((hw & 1) << 1)) << 1)) * 16);
  }

#pragma unroll 1
  for (int l0 = 0; l0 < 4; ++l0) {
    TRC(0); TRR(62);

    f32x4 acc[2][4];
    auto kvpart = [&](unsigned va, unsigned pa, unsigned kvb, int lane, int wave) {
      const f32x4 zero = {0.f, 0.f, 0.f, 0.f};
      const bf16x8 ones = {0x3f80, 0x3f80, 0x3f80, 0x3f80, 0x3f80, 0x3f80, 0x3f80, 0x3f80};
      f32x4 c0 = zero, c1 = zero, z0 = zero, z1 = zero;
#pragma unroll
      for (int ks = 0; ks < 4; ++ks) {
        const bf16x8 v = lds_ld<bf16x8>(va + ks * 2048);
        const bf16x8 a0 = tr2(pa + ks * 32 * 160, pa + (ks * 32 + 16) * 160);
        const bf16x8 a1 = tr2(pa + 32 + ks * 32 * 160, pa + 32 + (ks * 32 + 16) * 160);
        c0 = MFMA(a0, v, c0);
        c1 = MFMA(a1, v, c1);
        z0 = MFMA(a0, ones, z0);
        z1 = MFMA(a1, ones, z1);
      }
      st16_sc1(rs, lane * 16, kvb + (unsigned)wave * 1024, nosent(pack(c0, c1)));
      if (!(wave & 2) && (lane & 15) == 0) st16_sc1(rs, lane, kvb + KVP_Z + (wave >> 2) * 128 + (wave & 1) * 64, nosent(pack(z0, z1)));
    };
#define KV_ADDR                                                                                                            \
    unsigned va = lds0 + SC_VF + (wave >> 2) * 8192 + ((wave >> 1) & 1) * 1024 + lane * 16;                                \
    unsigned pa = lds0 + SC_PHIK + ((wave >> 2) * 128 + fq * 4 + (fr >> 2)) * 160 + (wave & 1) * 64 + (lane & 3) * 8;      \
    unsigned kvj = WS_KVP + (unsigned)j * KVP_J + (unsigned)(l >> 1) * KVP_L;                                              \
    OPAQUE_V(va); OPAQUE_V(pa); OPAQUE_S(kvj)
    if (cw) {
      if (l0 == 0) {
        LOCALS;
        u32x4 v[2][8];
        if (wave < 3) {
          const int jc = JOBC(j, wave), kt = (JROT(j) + 8 * (g & 1) + (g >> 1)) & 15;
          const rsrc_t ra = mk_rsrc(p.wqkvA, 1024 * 6144);
#pragma unroll
          for (int i = 0; i < 8; ++i)
            v[0][i] = __builtin_amdgcn_raw_buffer_load_b128(ra, (lane >> 3) * 6144 + (lane & 7) * 16, (kt * 64 + i * 8) * 6144 + wave * 2048 + jc * 128, 0);
          SCHED;
          TRC(44); BAR; SCHED;
          job_colsum(rs, v[0], 0, g, jc, wave, lane);
          if (SLOWC(0, jc)) {
#pragma unroll
            for (int i = 0; i < 8; ++i) job_pack(rs, v[0][i], i, 0, kt, jc, wave, lane);
          }
        } else {
          const int fr = lane & 15, fq = lane >> 4;
          const rsrc_t rx = mk_rsrc(p.x, 2048 * 2048);
#pragma unroll
          for (int i = 0; i < 16; ++i)
            v[i & 1][i >> 1] = __builtin_amdgcn_raw_buffer_load_b128(rx, fr * 2048 + fq * 16, (g * 128 + (j & 3) * 32 + ((i >> 1) & 1) * 16) * 2048 + ((JROT(j) + 8 * (g & 1)) & 15) * 128 + (i >> 2) * 128 + (i & 1) * 64, 0);
          SCHED;
          TRC(44); BAR; SCHED;
          const f32x4 zero = {0.f, 0.f, 0.f, 0.f};
          const bf16x8 ones = {0x3f80, 0x3f80, 0x3f80, 0x3f80, 0x3f80, 0x3f80, 0x3f80, 0x3f80};
          float sm[8], q[8];
#pragma unroll
          for (int m = 0; m < 8; ++m) {
            const f32x4 a = MFMA(ones, as_frag(v[1][m]), MFMA(ones, as_frag(v[0][m]), zero));
            const f32x4 b = MFMA(as_frag(v[1][m]), as_frag(v[1][m]), MFMA(as_frag(v[0][m]), as_frag(v[0][m]), zero));
            sm[m] = a[0];
            q[m] = (fr & 2) ? ((fr & 1) ? b[3] : b[2]) : ((fr & 1) ? b[1] : b[0]);
          }
          if (fq == (fr >> 2)) {
#pragma unroll
            for (int m = 0; m < 8; ++m) st8_sc1(rs, ((m & 1) * 16 + fr) * 8 + (m >> 1) * 1024, st_off(g, 0) + JROT(j) * 1024 + (j & 3) * 256, u32x2{__float_as_uint(sm[m]), __float_as_uint(q[m])});
          }
        }
      }
      LOCALS;
      unsigned otile, kvo, l16, pr1, pr4;
      int g2, j2, l2, ln2;
      qkv_loop(l & 1, wm, h, lane, l == 0, trv, [&](int st) {
        if (st == 0) {
          g2 = g0; j2 = j0; l2 = l0; ln2 = tid;
          OPAQUE_S(g2); OPAQUE_S(j2); OPAQUE_S(l2); OPAQUE_V(ln2);
          return;
        }
        otile = ob_off(g2, j2, l2) + (unsigned)(wm * 8 + h) * 1024;
        kvo = WS_KVP + (unsigned)j2 * KVP_J + (unsigned)g2 * KVP_G + (unsigned)(l2 >> 1) * KVP_L;
        l16 = (ln2 & 63) * 16;
        pr1 = lds0 + L_RSTD + (16 + wm * 64 + (ln2 & 15)) * 4;
        pr4 = lds0 + L_RSTD + wm * 256 + (ln2 & 48);
        asm("" : "+s"(otile)); asm("" : "+s"(kvo)); asm("" : "+v"(l16)); asm("" : "+v"(pr1)); asm("" : "+v"(pr4));
      }, [&](auto isb_c, f32x4 (&a)[4][6], auto& mm6) {
        const f32x4 zero = {0.f, 0.f, 0.f, 0.f};
        constexpr float L2E = 1.4426950408889634f;
        float rq[4];
#define ilv6(nv) do { _Pragma("unroll") for (int i_ = 0; i_ < 6; ++i_) { SGB(0x008, 1); SGB(0x100, 2); SGB(0x002, nv); } SCHED; } while (0)
        if constexpr (decltype(isb_c)::value) {
          u32x4 kf[5], qf[4];
          u32x2 vh[5][2];
          float rk[5][2];
          auto pk3 = [&](int m) {
            kf[m + 1] = pack(a[m][2], a[m][3]);
#pragma unroll
            for (int n = 0; n < 2; ++n) vh[m + 1][n] = u32x2{pk2(a[m][4 + n][0], a[m][4 + n][1]), pk2(a[m][4 + n][2], a[m][4 + n][3])};
            qf[m] = pack(a[m][0], a[m][1]);
          };
          constexpr auto window = [](int e) {
            u64 mk = 0;
            for (int ln = 0; ln < 64; ++ln) {
              const int delta = (ln & 14) + 16 - (e >> 1) * 16 - (ln >> 4) * 4 - (e & 1) * 2;
              if (delta >= 0 && delta <= 14) mk |= 1ull << ln;
            }
            return mk;
          };
          const bool par = __builtin_amdgcn_inverse_ballot_w64(0xaaaaaaaaaaaaaaaaull);
          f32x4 s0[4], s1[4];
          float x[4][4], mx[4], sm[4];
          u32x4 pf[4];
          unsigned psel0, psel1;
          auto sc = [&](int m) { s0[m] = MFMA(as_frag(kf[m]), as_frag(qf[m]), zero); s1[m] = MFMA(as_frag(kf[m + 1]), as_frag(qf[m]), zero); };
          auto st1 = [&](int m, u64 prev) {
            const float u0 = s0[m][0], u1 = s0[m][1], u2 = s0[m][2], u3 = s0[m][3], t0 = s1[m][0], t1 = s1[m][1], t2 = s1[m][2], t3 = s1[m][3];
            x[m][0] = par ? u1 : u0; x[m][1] = par ? u3 : u2; x[m][2] = par ? t1 : t0; x[m][3] = par ? t3 : t2;
            static_for<0, 4>([&](auto e_c) __attribute__((always_inline)) {
              constexpr int e = decltype(e_c)::value;
              constexpr u64 mk = window(e);
              x[m][e] = __builtin_amdgcn_inverse_ballot_w64(e < 2 ? mk & prev : mk) ? x[m][e] * rk[m + (e >> 1)][e & 1] : -3.0e38f;
            });
            mx[m] = fmaxf(fmaxf(fmaxf(x[m][0], x[m][1]), x[m][2]), x[m][3]);
          };
          auto st2 = [&](int m) {
            const float mxl = mx[m] * -rq[m];
#pragma unroll
            for (int e = 0; e < 4; ++e) x[m][e] = __builtin_fmaf(x[m][e], rq[m], mxl);
          };
          auto st3 = [&](int m) {
#pragma unroll
            for (int e = 0; e < 4; ++e) x[m][e] = __builtin_amdgcn_exp2f(x[m][e]);
          };
          auto st4 = [&](int m) {
            sm[m] = (x[m][0] + x[m][1]) + (x[m][2] + x[m][3]);
#pragma unroll
            for (int e = 0; e < 4; ++e) x[m][e] *= rk[m + (e >> 1)][e & 1];
          };
          auto st5 = [&](int m) {
            const unsigned p0 = pk2(x[m][0] * sm[m], x[m][1] * sm[m]), p1 = pk2(x[m][2] * sm[m], x[m][3] * sm[m]);
            pf[m] = u32x4{__builtin_amdgcn_perm(0u, p0, psel0), __builtin_amdgcn_perm(0u, p0, psel1), __builtin_amdgcn_perm(0u, p1, psel0), __builtin_amdgcn_perm(0u, p1, psel1)};
          };
          auto st6 = [&](int m) {
            s0[m] = MFMA(as_frag(u32x4{vh[m][0][0], vh[m][0][1], vh[m + 1][0][0], vh[m + 1][0][1]}), as_frag(pf[m]), zero);
            s1[m] = MFMA(as_frag(u32x4{vh[m][1][0], vh[m][1][1], vh[m + 1][1][0], vh[m + 1][1][1]}), as_frag(pf[m]), zero);
          };
          auto st7 = [&](int m) { st16_sc1(rs, l16, otile + m * 2048, nosent(pack(s0[m], s1[m]))); };
          mm6(3);
#pragma unroll
          for (int m = 0; m < 4; ++m) rq[m] = lds_ld<float>(pr1 + m * 64);
          const f32x4 rs3 = lds_ld<f32x4>(pr4 + 256);
          unsigned prk = pr4 + (par ? 4u : 0u), pv_ = l16 + lds0 + L_PREV + h * 2048;
          psel0 = par ? 0x01000c0cu : 0x0c0c0100u; psel1 = par ? 0x03020c0cu : 0x0c0c0302u;
          asm("" : "+v"(prk)); asm("" : "+v"(pv_));
#pragma unroll
          for (int i = 0; i < 5; ++i)
#pragma unroll
            for (int e = 0; e < 2; ++e) rk[i][e] = (i & 3) ? lds_ld<float>(prk + i * 64 + e * 8) : 1.0f;
          const unsigned hso = kvo + KVP_HALO + h * 2048, hsi = hso - (g > 0 ? KVP_G : 0u);
          ilv6(2);
          mm6(2);
          kf[4] = pack(a[3][2] * rq[3], a[3][3] * rq[3]);
          u32x4 v3 = pack(a[3][4] * rs3, a[3][5] * rs3);
          vh[4][0] = u32x2{v3[0], v3[1]}; vh[4][1] = u32x2{v3[2], v3[3]};
          ilv6(3);
          OPAQUE_V(kf[4]); OPAQUE_V(v3);
          if (wm) {
            st16_sc1(rs, l16, hso, nosent(kf[4]));
            st16_sc1(rs, l16, hso + 1024, nosent(v3));
          } else {
            lds_st<u32x4>(pv_, kf[4]);
            lds_st<u32x4>(pv_ + 1024, v3);
          }
          SCHED;
          mm6(1);
          qf[3] = pack(a[3][0], a[3][1]);
          pk3(2);
          ilv6(3);
          mm6(0);
          pk3(1);
#pragma unroll
          for (int m = 0; m < 4; ++m) rq[m] *= 0.17677669529663687f * L2E;
          ilv6(3);
          u32x4 hk[2], hv[2];
          const bool halo = wm == 0 && g > 0;
          hk[0] = ld16_sc1(rs, l16, hsi); hv[0] = ld16_sc1(rs, l16, hsi + 1024);
          SCHED;
          sc(3); sc(2);
          pk3(0);
          sc(1);
          SCHED;
          st1(3, ~0ull); st1(2, ~0ull); st1(1, ~0ull);
          SCHED;
          { float d = mx[1]; red4<true>(mx[3], mx[2], mx[1], d, [](float v) { return v; }); }
          st2(3); st2(2); st2(1);
          SCHED;
          st3(3); st3(2); st3(1);
          SCHED;
          CLOBBER;
          hk[1] = ld16_sc1(rs, l16, hsi); hv[1] = ld16_sc1(rs, l16, hsi + 1024);
          CLOBBER;
          SCHED;
          st4(3); st4(2); st4(1);
          { float d = sm[1]; red4<false>(sm[3], sm[2], sm[1], d, [](float v) { return __builtin_amdgcn_rcpf(v); }); }
          SCHED;
          st5(3); st5(2); st5(1);
          SCHED;
          st6(3); st6(2); st6(1);
          WAITL0;
          TRR(60);
          TRC(18); BAR;
          u32x4 pk = lds_ld<u32x4>(pv_), pv = lds_ld<u32x4>(pv_ + 1024);
          st7(3); st7(2); st7(1);
          SCHED;
          OPAQUE_V(qf[0]); OPAQUE_V(rq[0]);
          TRV(55, 0); TRC(21);
          if (halo) {
            pk = hk[0]; pv = hv[0];
            if (__builtin_expect(__any(umax4v(umax4v(0u, pk), pv) == SENT), 0)) {
              pk = hk[1]; pv = hv[1];
              TRV(55, 1);
              for (unsigned spins = 0; !dead && __any(umax4v(umax4v(0u, pk), pv) == SENT); ++spins) {
                if (spins > SPIN_CAP) { dead = true; break; }
                CLOBBER;
                TRV(55, spins + 2);
                pk = ld16_sc1(rs, l16, hsi); pv = ld16_sc1(rs, l16, hsi + 1024);
                OPAQUE_V(pk); OPAQUE_V(pv);
              }
            }
          }
          kf[0] = pk; vh[0][0] = u32x2{pv[0], pv[1]}; vh[0][1] = u32x2{pv[2], pv[3]};
          sc(0);
          st1(0, (wm || g > 0) ? ~0ull : 0ull);
          mx[0] = red_max4(mx[0]);
          st2(0); st3(0); st4(0);
          sm[0] = __builtin_amdgcn_rcpf(red_add4(sm[0]));
          st5(0); st6(0); st7(0);
        } else {
          LOCALS;
          const int fr = lane & 15, fq = lane >> 4;
          f32x4 rs4[4], r[4][2];
          const bf16x8 ones = {0x3f80, 0x3f80, 0x3f80, 0x3f80, 0x3f80, 0x3f80, 0x3f80, 0x3f80};
          auto proj = [&](int m, int n, const bf16x8& w0, const bf16x8& w1) {
            const u32x4 f = pack(a[m][n], a[m][n + 1]);
            r[m][0] = MFMA(w0, as_frag(f), zero); r[m][1] = MFMA(w1, as_frag(f), zero);
          };
          mm6(3, 0, 2); mm6(2, 0, 2); mm6(1, 0, 2); mm6(0, 0, 2);
          unsigned wa = lds0 + L_WPHI + h * 2048 + (fq * 4 + (fr >> 2)) * 64 + (lane & 3) * 8, kw = lds0 + L_PREV + hw * 2048 + l16;
          asm("" : "+v"(wa)); asm("" : "+v"(kw));
#pragma unroll
          for (int m = 0; m < 4; ++m) rq[m] = lds_ld<float>(pr1 + m * 64);
#pragma unroll
          for (int m = 0; m < 4; ++m) rs4[m] = lds_ld<f32x4>(pr4 + 64 + m * 64);
          const bf16x8 w0 = tr2(wa, wa + 1024);
          const bf16x8 w1 = tr2(wa + 32, wa + 32 + 1024);
#pragma unroll
          for (int i = 0; i < 8; ++i) { SGB(0x008, 1); SGB(0x100, 2); SGB(0x002, 2); }
          SCHED;
          mm6(3, 2, 4); mm6(2, 2, 4);
          lds_st<u32x4>(kw + 1024, pack(a[3][2], a[3][3]));
          lds_st<u32x4>(kw, pack(a[2][2], a[2][3]));
#pragma unroll
          for (int i = 0; i < 2; ++i) { SGB(0x008, 1); SGB(0x002, 2); SGB(0x008, 1); SGB(0x002, 2); SGB(0x200, 1); }
          SCHED;
          mm6(1, 2, 4); mm6(0, 2, 4);
          proj(0, 2, w0, w1); proj(1, 2, w0, w1);
#pragma unroll
          for (int i = 0; i < 4; ++i) { SGB(0x008, 1); SGB(0x002, 2); }
          SCHED;
          WAITL0;
          TRC(18); BAR;
          SCHED;
          unsigned pw = lds0 + SC_PHIK + (h * 128 + wm * 64 + fr) * 160 + fq * 8, vw = lds0 + SC_VF + h * 8192 + wm * 4096 + l16;
          asm("" : "+v"(pw)); asm("" : "+v"(vw));
#pragma unroll
          for (int m = 0; m < 4; ++m) mm6(m, 4, 6);
#pragma unroll
          for (int m = 0; m < 4; ++m) rq[m] *= L2E;
#pragma unroll
          for (int i = 0; i < 4; ++i)
            lds_st<u32x4>(vw + i * 1024, pack(a[i & 2][4 + (i & 1)] * rs4[i & 2], a[(i & 2) + 1][4 + (i & 1)] * rs4[(i & 2) + 1]));
#pragma unroll
          for (int i = 0; i < 8; ++i) { SGB(0x008, 1); SGB(0x002, 5); }
          SCHED;
          phik2(r, rq, pw);
          KV_ADDR;
          WAITL0;
          TRR(60);
          TRC(19); BAR;
          kvpart(va, pa, kvo, lane, wave);
          CLOBBER;
          TRC(20); BAR;
          u32x4 qp[4], qn[4];
          float sqp[4], sqn[4];
#pragma unroll
          for (int m = 0; m < 4; ++m) proj(m, 0, w0, w1);
          SCHED;
          float ep[4][8], en[4][8];
          hedgehog<4>(r, rq, ep, en);
          const unsigned zo = (lane >> 3) * KVP_G + (lane & 7) * 16, zs = kvj + KVP_Z + h * 128;
          u32x4 zv[2] = {ld16_sc1(rs, zo, zs), ld16_sc1(rs, zo, zs + 8 * KVP_G)};
          SCHED;
#pragma unroll
          for (int m = 0; m < 4; ++m) {
            qp[m] = pack8(ep[m]); qn[m] = pack8(en[m]);
            sqp[m] = MFMA(ones, as_frag(qp[m]), zero)[0];
            sqn[m] = MFMA(ones, as_frag(qn[m]), zero)[0];
          }
          SCHED;
#pragma unroll
          for (int m = 0; m < 4; ++m) { sqp[m] = __builtin_amdgcn_rcpf(sqp[m]); sqn[m] = __builtin_amdgcn_rcpf(sqn[m]); }
#pragma unroll
          for (int m = 0; m < 4; ++m) { OPAQUE_V(qp[m]); OPAQUE_V(qn[m]); OPAQUE_V(sqp[m]); OPAQUE_V(sqn[m]); }
          unsigned ko = (lane >> 5) * KVP_G + (lane & 31) * 16, ks = kvj + (unsigned)((j ^ RX) - j) * KVP_J + KVP_SUM + h * (8 * KVP_G);
          bf16x8 sel = rowsum_sel(lane);
          OPAQUE_V(ko); OPAQUE_S(ks); OPAQUE_V(sel);
          TRC(21); TRV(50, 0);
          for (unsigned spins = 0; !dead && __any(umax4v(umax4v(0u, zv[0]), zv[1]) == SENT); ++spins) {
            if (spins > SPIN_CAP) { dead = true; break; }
            CLOBBER;
            TRV(50, spins + 1);
            zv[0] = ld16_sc1(rs, zo, zs); zv[1] = ld16_sc1(rs, zo, zs + 8 * KVP_G);
          }
          TRC(22);
          {
            const f32x4 z = rowsum_fin(MFMA(sel, as_frag(zv[1]), MFMA(sel, as_frag(zv[0]), zero)));
            if ((lane & 40) == 0) lds_st<u32x2>(lds0 + L_ZF + hw * 128 + fr * 16 + fq * 8, u32x2{pk2(z[0], z[1]), pk2(z[2], z[3])});
            WAITL0;
            const bf16x8 zf0 = lds_ld<bf16x8>(lds0 + L_ZF + hw * 128 + fq * 16), zf1 = lds_ld<bf16x8>(lds0 + L_ZF + hw * 128 + 64 + fq * 16);
            float dp[4], dn[4];
#pragma unroll
            for (int m = 0; m < 4; ++m) { dp[m] = MFMA(zf0, as_frag(qp[m]), zero)[0]; dn[m] = MFMA(zf1, as_frag(qn[m]), zero)[0]; }
            SCHED;
#pragma unroll
            for (int m = 0; m < 4; ++m) {
              const float inv = __builtin_amdgcn_rcpf(__builtin_fmaf(dp[m], sqp[m], __builtin_fmaf(dn[m], sqn[m], 1e-6f)));
              sqp[m] *= inv; sqn[m] *= inv;
              OPAQUE_V(sqp[m]); OPAQUE_V(sqn[m]);
            }
          }
          TRV(51, 0);
          TRC(43); BAR;
          if (H2D) __builtin_amdgcn_s_sleep(H2D);
          u32x4 kv[4];
#pragma unroll
          for (int i = 0; i < 4; ++i) kv[i] = ld16_sc1(rs, ko, ks + i * (2 * KVP_G));
          SCHED;
#pragma unroll
          for (int m = 0; m < 4; ++m) { qp[m] = pack8(ep[m], sqp[m]); qn[m] = pack8(en[m], sqn[m]); }
#pragma unroll
          for (int m = 0; m < 4; ++m) { OPAQUE_V(qp[m]); OPAQUE_V(qn[m]); }
          SCHED;
          for (unsigned spins = 0; !dead && __any(max(umax4v(umax4v(0u, kv[0]), kv[1]), umax4v(umax4v(0u, kv[2]), kv[3])) == SENT); ++spins) {
            if (spins > SPIN_CAP) { dead = true; break; }
            CLOBBER;
            TRV(51, spins + 1);
#pragma unroll
            for (int i = 0; i < 4; ++i) kv[i] = ld16_sc1(rs, ko, ks + i * (2 * KVP_G));
          }
          TRC(23);
          f32x4 o[4][2];
#pragma unroll
          for (int m = 0; m < 4; ++m)
#pragma unroll
            for (int i = 0; i < 2; ++i) o[m][i] = MFMA(as_frag(kv[2 * i + 1]), as_frag(qn[m]), MFMA(as_frag(kv[2 * i]), as_frag(qp[m]), zero));
#pragma unroll
          for (int m = 0; m < 4; ++m) st16_sc1(rs, l16, otile + m * 2048, nosent(pack(o[m][0], o[m][1])));
        }
      });
#pragma unroll
      for (int i = 0; i < 8; ++i) asm volatile("" : "=v"(acc[i >> 2][i & 3]));
      TRC(25); BAR;
    } else {
      __builtin_amdgcn_s_setprio(3);
      if (l0) {
        WAITV(3);
        WAITL0;
        TRC(1); BAR;
      }
      LOCALS;
      u32x4 xr[2][2];
      const unsigned lin = lane * 16, vlin = lin + hw * 1024;
      const int jr = JROT(j);
      u32x4 sent4 = {SENT, SENT, SENT, SENT};
      OPAQUE_V(sent4);
      const unsigned cb0 = ob_off(g, j, l), cb2 = xb_off(g, l) + j * 16384 - 8 * 4096;
      const unsigned cb1 = WS_KVP + (unsigned)j * KVP_J + (unsigned)g * KVP_G + (unsigned)(l >> 1) * KVP_L - 4 * 4096;
      auto ldres = [&](int slot) {
        const int fr = lane & 15, fq = lane >> 4, ch = (fq & 1) * 2 + (fq >> 1);
#pragma unroll
        for (int i = 0; i < 4; ++i)
          xr[i >> 1][i & 1] = lds_ld<u32x4>(lds0 + L_R2 + slot * XS + (hw * 32 + ((i >> 1) ^ 1) * 16 + fr) * 128 + (((ch + 4 * (i & 1)) ^ ((fr >> 1) & 7)) * 16));
      };
      auto aslot = [&](int t) { return lds0 + L_R2 + (t % 3) * XS + hw * 1024; };
      auto wslot = [&](int t) { return lds0 + L_R1 + (t & 3) * 24576 + hw * 1024; };
      const bool l0f = l == 0;
      const rsrc_t rA = mk_rsrc(pA, zA);
      auto kseq = [&](int t) { return (kb + t - ((t < 4 && t <= dj) ? 1 : 0)) & 15; };
      auto offA = [&](int t) { return bA + (unsigned)kseq(t) * kA; };
      auto offW = [&](int t) { return bW + (unsigned)kseq(t) * kW; };
      auto ldA = [&](int t) { dma4<4096>(rA, vA, offA(t), pcA, aslot(t)); };
      auto ldWh = [&](int t, unsigned o, int hf) { dma3<8192>(mk_rsrc(pW, zW), vW, o + (hf ? hW : 0u), pcW, wslot(t) + hf * 4096); };
      const bool slowj = l0f && SLOWC(0, j);
      auto packedW = [&]() {
        pW = p.ws; zW = WS_SPAN; bW = WS_WP + (unsigned)j * (16 * 24576); kW = 24576; pcW = 8192; hW = 4096; vW = vlin;
      };
      auto ldW = [&](int t) { const unsigned o = offW(t); ldWh(t, o, 0); ldWh(t, o, 1); };
      const unsigned cmask = 0xfu | (isb ? 0u : 0xf0u) | (l == 3 ? 0u : 0xf00u);
      auto clean = [&](int t) {
        if (t < 4 || (t < 12 && ((cmask >> t) & 1))) st16_sc1(rs, vlin, (t < 4 ? cb0 : (t < 8 ? cb1 : cb2)) + (unsigned)t * 4096, sent4);
      };
      unsigned flo;
      unsigned ready = ~0u;
      int tr = 0;
      u32x4 cv[4];
#pragma unroll
      for (int ii = 0; ii < 4; ++ii) asm volatile("" : "=v"(cv[ii]));
      auto chkrd = [&](int t) {
#pragma unroll
        for (int ii = 0; ii < 4; ++ii) cv[ii] = lds_ld<u32x4>(aslot(t) + lin + ii * 4096);
      };
      auto chkbad = [&]() { return __any(max(umax4v(umax4v(0u, cv[0]), cv[1]), umax4v(umax4v(0u, cv[2]), cv[3])) == SENT) && !l0f; };
      auto refetch = [&](int t) __attribute__((always_inline)) {
        for (unsigned spins = 0; !dead; ++spins) {
          if (spins > SPIN_CAP) { dead = true; break; }
          WAITV(0);
          TRV(t == 1 ? 56 : 57, spins + 1);
          ldA(t);
          ldA(t + 1);
          WAITV(0);
          chkrd(t);
          if (!chkbad()) break;
          __builtin_amdgcn_s_sleep(1);
        }
      };
      if (l0f) {
        u32x2 gw2;
        ld8_asm(gw2, rs, WS_EPOCH + 0u * lin);
        TRC(44); BAR;
        ldW(0); ldA(0);
        dma1(mk_rsrc(p.wphi, 32 * 2048), vlin, j * 4096, lds0 + L_WPHI + hw * 1024);
        ldW(1);
        WAITV(17);
        OPAQUE_V(gw2);
        const u64 gword = ((u64)(unsigned)UNI(gw2[1]) << 32) | (unsigned)UNI(gw2[0]);
        u64 z = gword + 0x9e3779b97f4a7c15ull;
        z = (z ^ (z >> 30)) * 0xbf58476d1ce4e5b9ull;
        z = (z ^ (z >> 27)) * 0x94d049bb133111ebull;
        z = z ^ (z >> 31);
        z = (z == gword) ? ~z : z;
        epoch = u32x2{(unsigned)z, (unsigned)(z >> 32)};
        tr = (j & 3) + 8 * (g & 1);
        WAITV(7);
        TRC(1); BAR;
      } else {
        ldW(1);
      }
      if (W2F) ldW(2);
      if (XD) __builtin_amdgcn_s_sleep(XD);
      ldA(1); ldA(2);
      TRC(40);
      if (!W2F) ldW(2);
#pragma unroll
      for (int i = 0; i < 4; ++i) asm volatile("" : "=v"(xr[i >> 1][i & 1]));
      unsigned oA = offA(3), oW = 0;
      OPAQUE_S(oA);
      TRV(56, 0); TRV(57, 0);
      if (LAW == 3) ldW(3);
      if (tr == 0) ldres(0);
      clean(0);
      WAITV(5 + (W2F ? 0 : 6) + (LAW == 3 ? 6 : 0));
      chkrd(1);
      if (__builtin_expect(chkbad(), 0)) refetch(1);
      const int fr = lane & 15, fq = lane >> 4;
      u32x2 fl;
      f32x4 js[4], jc[4];
      float ra[4], rb[4];
      const bool slowo = SLOWC(2 + (l & 1), j);
      const char* pO;
      unsigned zO, bO, pcO, vO, clo;
      u32x4 wo2[2];
      asm volatile("" : "=v"(wo2[0])); asm volatile("" : "=v"(wo2[1]));
      const unsigned fwo = myrep + fl_line(FL_WO + (isb ? 1 : 0), jr + (lane >> 4)) + (lane & 15) * 8;
      static_for<1, 14>([&](auto t_c) __attribute__((always_inline)) {
        constexpr int t = decltype(t_c)::value;
        constexpr auto rest = [](int s) {
          return s == 0 ? (W2F ? 0 : 6) + (LAW == 3 ? 6 : 0) + 1
                        : (s + LAW <= 15 ? 6 : 0) + (s < 4) + (s == NVAL - 2 || s == 5 || s == 11) + ((s == 7 || s == 8) ? 4 : 0);
        };
        constexpr int NW = t == 1 ? 63 : 4 + rest(t - 1) + ((LAW == 3 && (t > 2 || W2F)) ? rest(t - 2) : 0);
        TRC(1 + t);
        if (t >= NVAL && t <= 6 && __builtin_expect((ready & (1u << kseq(t + 2))) == 0, 0)) {
          WAITV(NW); WAITL0; BAR;
          ready = poll_need(rs, flo, epoch, ready, 1u << kseq(t + 2), dead);
          dma4<4096>(rA, vA, oA, pcA, aslot(t + 2));
        } else if (t == 7 && __builtin_expect(ready != ~0u, 0)) {
          WAITV(NW); WAITL0; BAR;
          poll_all(rs, flo, epoch, dead);
          dma4<4096>(rA, vA, oA, pcA, aslot(t + 2));
        } else {
          bar_dma4<4096, NW>(rA, vA, oA, pcA, aslot(t + 2));
        }
        if (t == 1 && lead) set_flags(rs, fl_line(FL_X + l, g), j, l0f ? fl_line(FL_CS, j) : fl_line(FL_X + l, g), l0f ? g : j, epoch, lane);
        if (t == 7 && slowj) packedW();
        if (t + LAW <= 15) oW = offW(t + LAW);
        if (t < 13) oA = offA(t + 3);
        OPAQUE_S(oA); OPAQUE_S(oW);
        clean(t);
        if (t <= NVAL && !l0f) { WAITV(rest(t - 1) + 4 + (t < 4)); chkrd(t + 1); }
        if (t == WT + 3 && l0f && SLOWC(2, jr + hw)) {
          OPAQUE_V(wo2[0]); OPAQUE_V(wo2[1]);
#pragma unroll
          for (int i = 0; i < 2; ++i) st16_sc1(rs, lin, WS_WO + (unsigned)((jr + hw) * 131072 + g * 8192 + ((j & 3) * 2 + i) * 1024), wo2[i]);
        }
        if (t == WT + 6 && l0f && lead) set_flags(rs, fl_line(FL_WO, j), g, fl_line(FL_WO, j), g, epoch, lane);
        if ((t < 4 || (t >= 8 && t < 12)) && tr == t) ldres(t % 3);
        if (t == 7) {
#pragma unroll
          for (int i = 0; i < 4; ++i) ld16_asm(jc[i], rs, fq * 768 + hw * 192 + (fr < 12 ? fr : 0) * 16, WS_CS + (unsigned)(((isb ? 16 : 0) + j) * 16 + i * 4) * 768);
        }
        if (t == 8) {
#pragma unroll
          for (int i = 0; i < 4; ++i) ld16_asm(js[i], rs, fq * 1024 + hw * 256 + fr * 16, st_off(g, l) + i * 4096);
        }
        SCHED;
        if (t + LAW <= 15) ldWh(t + LAW, oW, 0);
        SCHED;
        bool bad = false;
        if (t <= NVAL) bad = chkbad();
        if (t == 1) {
          const unsigned fx = fl_line(FL_X + l, g), fc = fl_line(l0f ? FL_CS : FL_PK, jr) + ((j + fq) & 3) * 128;
          flo = myrep + ((l < 2 && lane >= 16) ? fc : fx) + fr * 8;
          OPAQUE_V(flo);
        }
        if (t == 9) {
          pO = slowo ? p.ws : (const char*)(isb ? p.woB : p.woA);
          zO = slowo ? WS_SPAN : 1024 * 2048; bO = slowo ? WS_WO + (unsigned)((isb ? 16 : 0) + j) * 131072 : j * 128; pcO = slowo ? 4096 : 32 * 2048;
          vO = slowo ? vlin : (hw * 8 + (lane >> 3)) * 2048 + wo_swz(lane);
          clo = myrep + ((lane & 16) ? fl_line(FL_CLC + l, (lane & 32) ? j ^ RX : j) : fl_line(FL_CLR + l, g)) + fr * 8;
          OPAQUE_S(pO); OPAQUE_S(zO); OPAQUE_S(bO); OPAQUE_S(pcO); OPAQUE_V(vO); OPAQUE_V(clo);
        }
        if (t == 10) {
#pragma unroll
          for (int i = 0; i < 4; ++i) OPAQUE_V(jc[i]);
          const f32x4 s = (jc[0] + jc[1]) + (jc[2] + jc[3]);
#pragma unroll
          for (int i = 0; i < 4; ++i) { ra[i] = s[i]; OPAQUE_V(ra[i]); }
        }
        if (t == 11) {
#pragma unroll
          for (int i = 0; i < 4; ++i) OPAQUE_V(js[i]);
          const f32x4 s = (js[0] + js[1]) + (js[2] + js[3]);
#pragma unroll
          for (int i = 0; i < 4; ++i) { rb[i] = s[i]; OPAQUE_V(rb[i]); }
        }
        if (t == 12) {
          red2_add(ra[0], ra[1]); red2_add(ra[2], ra[3]); red2_add(rb[0], rb[1]); red2_add(rb[2], rb[3]);
          if (lane < 12) lds_st<f32x4>(lds0 + L_CS + hw * 192 + lane * 16, f32x4{ra[0], ra[1], ra[2], ra[3]});
#pragma unroll
          for (int i = 0; i < 4; ++i) OPAQUE_V(rb[i]);
        }
        if (t == 13) {
          const float m0 = rb[0] * (1.0f / 1024), m1 = rb[2] * (1.0f / 1024);
          const float r0 = __builtin_amdgcn_rsqf(fmaxf(__builtin_fmaf(rb[1], 1.0f / 1024, -m0 * m0), 0.f) + 1e-5f);
          const float r1 = __builtin_amdgcn_rsqf(fmaxf(__builtin_fmaf(rb[3], 1.0f / 1024, -m1 * m1), 0.f) + 1e-5f);
          if (lane < 16) {
            lds_st<f32x2>(lds0 + L_MEAN + 64 + hw * 128 + fr * 8, f32x2{-m0, -m1});
            lds_st<f32x2>(lds0 + L_RSTD + 64 + hw * 128 + fr * 8, f32x2{r0, r1});
          }
        }
        SCHED;
        if (t + LAW <= 15) ldWh(t + LAW, oW, 1);
        if (t == NVAL - 1 || t == 6) {
          WAITV(10);
          OPAQUE_V(fl);
          if (t == 6) ready = __all(is_set(fl, epoch)) ? ~0u : 0u;
          else {
            ready = l0f ? ~0u : (unsigned)__ballot(is_set(fl, epoch));
            if (slowj && __all(is_set(fl, epoch))) packedW();
          }
        }
        if (t == 12) { WAITV(10); OPAQUE_V(fl); }
        if (t == 13 && slowo && __builtin_expect(!__all(is_set(fl, epoch)), 0)) poll_all(rs, fwo, epoch, dead);
        if (t == NVAL - 2 || t == 5) ld8_asm(fl, rs, flo);
        if (t == 11) ld8_asm(fl, rs, fwo);
        if (t == WT && l0f) {
#pragma unroll
          for (int i = 0; i < 2; ++i) ld16in_asm(wo2[i], mk_rsrc(p.woA, 1024 * 2048), (lane >> 3) * 2048 + wo_swz(lane), (g * 64 + (j & 3) * 16 + i * 8) * 2048 + (jr + hw) * 128);
        }
        if constexpr (t <= NVAL) { if (__builtin_expect(bad, 0)) refetch(t + 1); }
      });
      auto stO = [&](int i) { dma4<4096>(mk_rsrc(pO, zO), vO, bO + wo_src(i, j) * (4 * pcO), pcO, lds0 + wo_slot(i) + hw * 1024); };
      auto clcheck = [&]() {
        OPAQUE_V(fl);
        TRV(58, 0);
        if (__builtin_expect(!__all(is_set(fl, epoch)), 0)) { TRV(58, 1); poll_all(rs, clo, epoch, dead); }
      };
      auto accinit = [&]() {
#pragma unroll
        for (int m = 0; m < 2; ++m)
#pragma unroll
          for (int pp = 0; pp < 2; ++pp) {
            auto r0_ = __builtin_amdgcn_permlane16_swap(xr[m][pp][0], xr[m][pp][2], false, false);
            auto r1_ = __builtin_amdgcn_permlane16_swap(xr[m][pp][1], xr[m][pp][3], false, false);
            acc[m][2 * pp] = f32x4{lo16(r0_[0]), hi16(r0_[0]), lo16(r1_[0]), hi16(r1_[0])};
            acc[m][2 * pp + 1] = f32x4{lo16(r0_[1]), hi16(r0_[1]), lo16(r1_[1]), hi16(r1_[1])};
          }
#pragma unroll
        for (int i = 0; i < 8; ++i) OPAQUE_V(acc[i >> 2][i & 3]);
      };
      WAITV(10);
      WAITL0;
      TRC(15); BAR;
      if (lead) set_flags(rs, fl_line(FL_CLR + l, g), j, fl_line(FL_CLC + l, j), g, epoch, lane);
      if (isb) {
        stO(0); stO(2); stO(3);
        WAITV(12);
        TRC(16); BAR;
        stO(1); stO(4); stO(5);
        TRC(17); BAR;
        stO(7); stO(6);
        __builtin_amdgcn_s_setprio(0);
        accinit();
        ld8_asm(fl, rs, clo);
        TRC(18); BAR;
        WAITV(0);
        clcheck();
      } else {
        u32x4 vb[8];
#pragma unroll
        for (int i = 0; i < 8; ++i) asm volatile("" : "=v"(vb[i]));
        stO(0); stO(2); stO(3);
        WAITV(12);
        TRC(16); BAR;
        stO(1);
        if (l0f) {
          const bool wj = hw != 0;
          const char* bp = (const char*)(wj ? p.wqkvB : p.woB);
          unsigned rs_ = wj ? 6144 : 2048, sz = rs_ * 1024, ss = rs_ * 8, s2 = wj ? 16 * 6144 : 128;
          unsigned sb = g * 64 * rs_ + (wj ? (hw - 1) * 2048 + JOBC(j, hw - 1) * 128 : (j & 3) * (16 * 2048) + jr * 128);
          OPAQUE_S(bp); OPAQUE_S(rs_); OPAQUE_S(sz); OPAQUE_S(sb); OPAQUE_S(ss); OPAQUE_S(s2);
          const rsrc_t rb = mk_rsrc(bp, sz);
#pragma unroll
          for (int i = 0; i < 8; ++i) ld16in_asm(vb[i], rb, (lane >> 3) * rs_ + (lane & 7) * 16, sb + (i & 1) * ss + (i >> 1) * s2);
        }
        __builtin_amdgcn_s_setprio(0);
        KV_ADDR;
        bf16x8 sel = rowsum_sel(lane);
        unsigned h1 = (lane >> 3) * KVP_G + (lane & 7) * 16;
        unsigned pw = lds0 + SC_PHIK + (h * 128 + wm * 64 + 32 + fr) * 160 + fq * 8, kw = lds0 + L_PREV + hw * 2048 + lin;
        const unsigned wa = lds0 + L_WPHI + h * 2048 + (fq * 4 + (fr >> 2)) * 64 + (lane & 3) * 8;
        bf16x8 w0 = tr2(wa, wa + 1024), w1 = tr2(wa + 32, wa + 32 + 1024);
        float cq[4];
#pragma unroll
        for (int m = 0; m < 2; ++m) { cq[m] = lds_ld<float>(lds0 + L_RSTD + (16 + wm * 64 + 32 + m * 16 + fr) * 4) * 1.4426950408889634f; OPAQUE_V(cq[m]); }
        OPAQUE_V(sel); OPAQUE_V(h1); OPAQUE_V(pw); OPAQUE_V(kw); OPAQUE_V(w0); OPAQUE_V(w1);
        TRC(17); BAR;
        TRC(18); BAR;
        ld8_asm(fl, rs, clo);
        {
          const f32x4 zero = {0.f, 0.f, 0.f, 0.f};
          f32x4 r[4][2];
#pragma unroll
          for (int m = 0; m < 2; ++m) {
            const bf16x8 f = lds_ld<bf16x8>(kw + m * 1024);
            r[m][0] = MFMA(w0, f, zero); r[m][1] = MFMA(w1, f, zero);
          }
          phik2(r, cq, pw);
        }
        WAITV(0);
#pragma unroll
        for (int i = 0; i < 8; ++i) OPAQUE_V(vb[i]);
        clcheck();
        WAITL0;
        TRC(19); BAR;
        kvpart(va, pa, kvj + (unsigned)g * KVP_G, lane, wave);
        CLOBBER;
        TRC(20); BAR;
        if (H1D > 0) stO(4);
        if (H1D > 1) stO(5);
        const f32x4 zero = {0.f, 0.f, 0.f, 0.f};
        const unsigned s1 = kvj + (unsigned)((j ^ RX) - j) * KVP_J + g * 512 + hw * 128;
        u32x4 v[2] = {ld16_sc1(rs, h1, s1), ld16_sc1(rs, h1, s1 + 8 * KVP_G)};
        TRC(21); TRV(50, 0);
        for (unsigned spins = 0; !dead && __any(umax4v(umax4v(0u, v[0]), v[1]) == SENT); ++spins) {
          if (spins > SPIN_CAP) { dead = true; break; }
          CLOBBER;
          TRV(50, spins + 1);
          v[0] = ld16_sc1(rs, h1, s1); v[1] = ld16_sc1(rs, h1, s1 + 8 * KVP_G);
        }
        TRC(22);
        const f32x4 c = rowsum_fin(MFMA(sel, as_frag(v[1]), MFMA(sel, as_frag(v[0]), zero)));
        if ((lane & 40) == 0)
          st8_sc1(rs, fr * 16 + fq * 8, kvj + (unsigned)g * KVP_G + KVP_SUM + hw * 128, u32x2{min(pk2(c[0], c[1]), SENT - 1), min(pk2(c[2], c[3]), SENT - 1)});
        CLOBBER;
        TRC(43); BAR;
        accinit();
        if (H2D) __builtin_amdgcn_s_sleep(H2D);
        if (H1D < 1) stO(4);
        if (H1D < 2) stO(5);
        stO(6); stO(7);
        if (l0f) {
          if (hw) {
            job_colsum(rs, vb, 1, g, JOBC(j, hw - 1), hw - 1, lane);
            if (SLOWC(1, JOBC(j, hw - 1))) {
#pragma unroll
              for (int i = 0; i < 8; ++i) job_pack(rs, vb[i], i, 1, g, JOBC(j, hw - 1), hw - 1, lane);
            }
          } else {
#pragma unroll
            for (int i = 0; i < 8; ++i)
              if (SLOWC(3, jr + (i >> 1))) st16_sc1(rs, (lane >> 3) * 128 + wo_swz(lane), WS_WO + (unsigned)((16 + jr + (i >> 1)) * 131072 + g * 8192 + ((j & 3) * 2 + (i & 1)) * 1024), vb[i]);
          }
        }
        WAITV(0);
      }
      TRC(25); BAR;
      if (l0f && lead) set_flags(rs, fl_line(FL_PK, j), g, fl_line(FL_WO + 1, j), g, epoch, lane);
    }
    {
      LOCALS;
      const int fr = lane & 15, fq = lane >> 4;
      const int kh = half;
      const unsigned v0 = lane * 16 + hw * 4096 + kh * 2048, v1 = lane * 16 + hw * 4096 + (kh ^ 1) * 2048;
      const int r0 = (JROT(j) >> 1) + 2;
      const unsigned ob0 = ob_off(g, kh, l);
      const unsigned pko = myrep + fl_line(FL_PK, JOBC(j, fq - 1)) + fr * 8;
      u32x4 R[8][4];
      u32x2 pkf;
      auto issue_ = [&](int i) {
        const unsigned so = ob0 + (unsigned)((r0 + i) & 7) * 131072;
        R[i][0] = ld16_sc1(rs, v0, so); R[i][1] = ld16_sc1(rs, v1, so);
        R[i][2] = ld16_sc1(rs, v0 + 1024, so); R[i][3] = ld16_sc1(rs, v1 + 1024, so);
      };
      auto issue = [&](int i) { issue_(i); SCHED; };
      constexpr int LA = LOOKAHEAD;
      TRV(52, 0); TRV(53, 0); TRV(54, 0);
      TRC(26); TRR(61);
      __builtin_amdgcn_s_setprio(3);
      SCHED;
      issue(0);
      __builtin_amdgcn_s_setprio(2);
      auto vmax = [&](int i) { return umax4v(umax4v(umax4v(umax4v(0u, R[i][0]), R[i][1]), R[i][2]), R[i][3]); };
      auto bad = [&](int i) { return __any(umax4v(umax4v(umax4v(umax4v(0u, R[i][0]), R[i][1]), R[i][2]), R[i][3]) == SENT); };
      auto poll = [&](int i, int nfl) {
        for (unsigned spins = 0; !dead; ++spins) {
          if (spins > SPIN_CAP) { dead = true; break; }
          __builtin_amdgcn_s_sleep(2);
          CLOBBER;
          TRV(52 + (i > 2 ? 2 : i), spins + 1);
          issue(i);
#pragma unroll
          for (int k = i + 1; k < i + nfl && k < 8; ++k)
            if (bad(k)) issue(k);
          WAITV(0);
#pragma unroll
          for (int k = i + 1; k < i + nfl && k < 8; ++k)
#pragma unroll
            for (int f = 0; f < 4; ++f) OPAQUE_V(R[k][f]);
          if (!bad(i)) break;
        }
      };
      unsigned bb[4];
      {
        const int kr = fq * 4 + (fr >> 2);
        const int gs = (kr >> 1) & 3;
#pragma unroll
        for (int c = 0; c < 4; ++c) bb[c] = lds0 + kh * 8192 + kr * 128 + ((c ^ gs) * 32) + (lane & 3) * 8;
      }
      bf16x8 bf[2][4];
      auto rdB = [&](int i, int hh) {
        if (hh == 0) {
#pragma unroll
          for (int c = 0; c < 4; ++c) { bb[c] += wo_slot(i) - (i ? wo_slot(i - 1) : 0u); asm("" : "+v"(bb[c])); }
        }
#pragma unroll
        for (int c = 0; c < 4; ++c) bf[hh][c] = tr2(bb[c] + hh * 4096, bb[c] + hh * 4096 + 2048);
      };
      auto mms = [&](int i, int hh) {
#pragma unroll
        for (int n = 0; n < 4; ++n)
#pragma unroll
          for (int m = 0; m < 2; ++m) acc[m][n] = MFMA(bf[hh][n], as_frag(R[i][hh * 2 + m]), acc[m][n]);
      };
      if (!half) {
#pragma unroll
        for (int i = 0; i < 8; ++i) acc[i >> 2][i & 3] = f32x4{0.f, 0.f, 0.f, 0.f};
      }
      const char* obase = (l == 3) ? (const char*)p.out : (const char*)p.ws;
      unsigned osize = (l == 3) ? 2048 * 2048 : WS_SPAN;
      const bool slown = SLOWC((l & 1) ^ 1, j);
      pW = slown ? p.ws : (const char*)(isb ? p.wqkvA : p.wqkvB);
      bW = slown ? WS_WP + (unsigned)((isb ? 0 : 16) + j) * (16 * 24576) : j * 128;
      kW = slown ? 24576 : 64 * 6144; zW = slown ? WS_SPAN : 1024 * 6144; pcW = slown ? 8192 : 2048; hW = slown ? 4096 : 32 * 6144;
      vA = hw * 1024 + lane * 16;
      vW = slown ? vA : (hw * 8 + (lane >> 3)) * 6144 + (((lane & 7) ^ ((((lane >> 4) & 1) | ((hw & 1) << 1)) << 1)) * 16);
      pA = p.ws; zA = WS_SPAN; bA = xb_off(g, l); kA = 16384; pcA = 4096; kb = JROT(j); dj = j & 3;
      OPAQUE_S(pW); OPAQUE_S(bW); OPAQUE_S(kW); OPAQUE_S(zW); OPAQUE_S(pcW); OPAQUE_S(hW); OPAQUE_V(vA); OPAQUE_V(vW); OPAQUE_S(bA);
      unsigned sq = bW + j * kW + kh * hW, dst = lds0 + L_R1 + kh * 4096 + hw * 1024;
      OPAQUE_S(obase); OPAQUE_S(osize); OPAQUE_S(sq); OPAQUE_S(dst);
      const rsrc_t ro = mk_rsrc(obase, osize);
      unsigned so[2];
      unsigned xw = lds0 + SC_XCH + wave * 4096 + lane * 16;
      unsigned xa = lds0 + SC_XCH + (wave ^ 4) * 4096 + lane * 16;
      unsigned sb0 = (l == 3) ? g * (128 * 2048) + j * 128 : xb_off(g, l) + j * 16384;
      {
        const int row = (hw * 2 + kh) * 16 + fr, ch = (fq & 1) * 2 + (fq >> 1);
        const unsigned sw = (l == 3) ? 0u : (unsigned)((row >> 1) & 7), rb = (unsigned)row << ((l == 3) ? 11 : 7);
#pragma unroll
        for (int pp = 0; pp < 2; ++pp) { so[pp] = rb + (((ch + 4 * pp) ^ sw) * 16); OPAQUE_V(so[pp]); }
        OPAQUE_V(xa); OPAQUE_V(xw); OPAQUE_S(sb0);
      }
      SCHED;
#pragma unroll
      for (int i = 1; i < LA; ++i) issue(i);
      rdB(0, 0);
      unsigned mx = vmax(0);
#pragma unroll
      for (int i = 0; i < 8; ++i) {
        SCHED;
        if (__builtin_expect(__any(mx == SENT), 0)) poll(i, LA);
        if (i == 0) TRC(27); else TRQ(27 + i);
        if (i == 3) { pkf = ld8_sc1(rs, pko, 0); SCHED; }
        if (i == 7) {
          OPAQUE_V(pkf);
          if (l == 0 && lead && slown && !__all(is_set(pkf, epoch))) poll_all(rs, pko, epoch, dead);
          TRC(35); BAR;
        }
        if (i == 7 && l < 3) dma3<8192>(mk_rsrc(pW, zW), vW, sq, pcW, dst);
        if (i == 2) __builtin_amdgcn_s_setprio(1);
        if (i == 5) __builtin_amdgcn_s_setprio(0);
        SCHED;
        rdB(i, 1);
        if (i == 7) break;
        if (i + LA < 8) issue_(i + LA);
        mms(i, 0);
#pragma unroll
        for (int k = 0; k < 4; ++k) { SGB(0x008, 1); SGB(0x100, 1); SGB(0x020, 1); SGB(0x008, 1); SGB(0x100, 1); }
        SCHED;
        rdB(i + 1, 0); mx = vmax(i + 1);
        mms(i, 1);
#pragma unroll
        for (int k = 0; k < 4; ++k) { SGB(0x008, 1); SGB(0x100, 2); }
#pragma unroll
        for (int k = 4; k < 8; ++k) { SGB(0x008, 1); SGB(0x002, 8 / (8 - 4)); }
      }
#pragma unroll
      for (int n = 0; n < 4; ++n) acc[1][n] = MFMA(bf[0][n], as_frag(R[7][1]), acc[1][n]);
#pragma unroll
      for (int n = 0; n < 4; ++n) acc[0][n] = MFMA(bf[0][n], as_frag(R[7][0]), acc[0][n]);
#pragma unroll
      for (int k = 0; k < 8; ++k) { SGB(0x008, 1); SGB(0x100, 1); }
      SCHED;
#pragma unroll
      for (int n = 0; n < 4; ++n) acc[1][n] = MFMA(bf[1][n], as_frag(R[7][3]), acc[1][n]);
#pragma unroll
      for (int n = 0; n < 4; ++n) acc[0][n] = MFMA(bf[1][n], as_frag(R[7][2]), acc[0][n]);
#pragma unroll
      for (int n = 0; n < 4; ++n) lds_st<f32x4>(xw + n * 1024, acc[1][n]);
      SGB(0x008, 4);
#pragma unroll
      for (int k = 0; k < 4; ++k) { SGB(0x008, 1); SGB(0x200, 1); }
      SCHED;
      TRC(36);
      WAITL0;
      TRC(37); BAR;
      SCHED;
      f32x4 pa[4];
#pragma unroll
      for (int n = 0; n < 4; ++n) pa[n] = lds_ld<f32x4>(xa + n * 1024);
      SCHED;
      u32x4 o[2];
#pragma unroll
      for (int pp = 0; pp < 2; ++pp) {
        const f32x4 a = acc[0][2 * pp] + pa[2 * pp], b = acc[0][2 * pp + 1] + pa[2 * pp + 1];
        auto r0_ = __builtin_amdgcn_permlane16_swap(pk2(a[0], a[1]), pk2(b[0], b[1]), false, false);
        auto r1_ = __builtin_amdgcn_permlane16_swap(pk2(a[2], a[3]), pk2(b[2], b[3]), false, false);
        o[pp] = nosent(u32x4{r0_[0], r1_[0], r0_[1], r1_[1]});
        st16_sc1(ro, so[pp], sb0, o[pp]);
      }
      if (l < 3) {
        lds_st<u32x4>(lds0 + L_R2 + so[0], o[0]);
        lds_st<u32x4>(lds0 + L_R2 + so[1], o[1]);
        const f32x4 zero = {0.f, 0.f, 0.f, 0.f};
        const bf16x8 ones = {0x3f80, 0x3f80, 0x3f80, 0x3f80, 0x3f80, 0x3f80, 0x3f80, 0x3f80};
        const f32x4 a = MFMA(ones, as_frag(o[1]), MFMA(ones, as_frag(o[0]), zero));
        const f32x4 b = MFMA(as_frag(o[1]), as_frag(o[1]), MFMA(as_frag(o[0]), as_frag(o[0]), zero));
        const float q = (fr & 2) ? ((fr & 1) ? b[3] : b[2]) : ((fr & 1) ? b[1] : b[0]);
        if (fq == (fr >> 2)) st8_sc1(rs, ((hw * 2 + kh) * 16 + fr) * 8, st_off(g, l + 1) + j * 1024, u32x2{__float_as_uint(a[0]), __float_as_uint(q)});
      } else if (lead && blockIdx.x == 0 && lane == 0) {
        st8_sc1(rs, WS_EPOCH, 0, epoch);
      }
    }
    TRC(38); TRR(63);
  }
  STAMP_DUMP;
  asm volatile(".Lpf_end:");
}

extern "C" void kernel_launch(void* const* d_in, const int* in_sizes, int n_in, void* d_out, int out_size,
                              void* d_ws, size_t ws_size, hipStream_t stream) {
  Params p;
  p.x = (const u16*)d_in[0];
  p.wqkvA = (const u16*)d_in[1];
  p.wphi = (const u16*)d_in[2];
  p.woA = (const u16*)d_in[3];
  p.wqkvB = (const u16*)d_in[5];
  p.woB = (const u16*)d_in[6];
  p.out = (u16*)d_out;
  p.ws = (char*)d_ws;
  (void)hipFuncSetAttribute((const void*)mega_kernel, hipFuncAttributeMaxDynamicSharedMemorySize, L_TOTAL);
  mega_kernel<<<256, 512, L_TOTAL, stream>>>(p);
}
```

```cpp
#include <hip/hip_runtime.h>
#include <stdint.h>
#include <type_traits>
constexpr int LOOKAHEAD = 2;
constexpr int NVAL = 4;
#define XD 0
#define WT 1
#define LAW 2
#define POL0 0
#define PKALL 0u
#define PA1 16
#define PA2 16
#define PA3 16
#define H1S 0
#define H1P 0
#define SPR 2
#define A1E 0
#define KG 32768
#define TS 17664
#define KOD 0
#define STG 1
#define JOBP 0
#define ROT 1
#define BD 14
#define W1S 7
#define W2F 0
#define RX 8
#define H1D 2
#define H2D 5

typedef unsigned short u16;
typedef unsigned long long u64;
typedef __attribute__((ext_vector_type(8))) short bf16x8;
typedef __attribute__((ext_vector_type(4))) short s16x4;
typedef __attribute__((ext_vector_type(4))) float f32x4;
typedef __attribute__((ext_vector_type(2))) float f32x2;
typedef __attribute__((ext_vector_type(4))) unsigned u32x4;
typedef __attribute__((ext_vector_type(2))) unsigned u32x2;
typedef __attribute__((ext_vector_type(2))) __bf16 bf16x2_t;
#define LAS __attribute__((address_space(3)))
typedef LAS s16x4* lds_s16x4_ptr;

constexpr unsigned NREP = 8, REP_STRIDE = 17 * 2048;
enum { FL_X = 0, FL_CS = 4, FL_CLR = 5, FL_CLC = 9, FL_PK = 13, FL_WO = 14 };
__device__ __forceinline__ unsigned fl_line(int set, int a) { return (unsigned)(set * 16 + a) * 128; }
constexpr unsigned WS_EPOCH = 512u << 10;
constexpr unsigned WS_CS = 1u << 20;
constexpr unsigned WS_KVP = 4u << 20;
constexpr unsigned KVP_G = KG, KVP_J = 16 * KVP_G, KVP_L = KG == 32768 ? 16384 : 16 * KVP_J, KVP_Z = 8192, KVP_SUM = 8704, KVP_HALO = 12u << 10;
constexpr unsigned SENT = 0xffffffffu;
constexpr unsigned WS_X = 14u << 20, WS_O = 29u << 20, WS_ST = 2u << 20;
__device__ __forceinline__ unsigned xb_off(int g, int l) { return WS_X + (unsigned)(l * 16 + g) * (16 * TS); }
__device__ __forceinline__ unsigned ob_off(int g, int j, int l) { return WS_O + (unsigned)((l * 16 + g) * 16 + j) * TS; }
__device__ __forceinline__ unsigned st_off(int g, int l) { return WS_ST + (unsigned)(l * 16 + g) * (16u << 10); }
constexpr unsigned WS_WP = 48u << 20;
constexpr unsigned WS_WO = 60u << 20;
constexpr unsigned WS_SPAN = 64u << 20;

constexpr unsigned L_R1 = 0;
constexpr unsigned L_R2 = 98304, XS = 16384;
constexpr unsigned L_MISC = 147456;
constexpr unsigned L_MEAN = L_MISC;
constexpr unsigned L_RSTD = L_MISC + 576;
constexpr unsigned L_CS = L_MISC + 1152;
constexpr unsigned L_WPHI = L_MISC + 2688;
constexpr unsigned L_SINK = L_MISC + 6912;
constexpr unsigned L_ZF = L_MISC + 1920;
constexpr unsigned L_PREV = L_MISC + 8192;
constexpr unsigned L_TOTAL = 163840;

#define JROT(j) ((j) & ~3)
#define JOBC(j, w) (((j) & ~3) + (((j) + (w) + 1) & 3))

#define SLOWC(m, jc) ((slowm >> ((m) * 8 + ((jc) & 7))) & 1)

struct Params {
  const u16* x;
  const u16* wqkvA;
  const u16* wphi;
  const u16* woA;
  const u16* wqkvB;
  const u16* woB;
  u16* out;
  char* ws;
};

#define SCHED __builtin_amdgcn_sched_barrier(0)
#define BAR __builtin_amdgcn_s_barrier()
#define SGB(m, n) __builtin_amdgcn_sched_group_barrier(m, n, 0)
#define WAITV(n) asm volatile("s_waitcnt vmcnt(%0)" ::"n"(n) : "memory")
#define WAITL0 asm volatile("s_waitcnt lgkmcnt(0)" ::: "memory")

#define TRV(slot, val) do {} while (0)
#define TRQ(slot) do {} while (0)
#define TRC(slot) do {} while (0)
#define TRR(slot) do {} while (0)
#define STAMP_INIT do {} while (0)
#define STAMP_DUMP do {} while (0)

__device__ __forceinline__ unsigned pk2(float a, float b) {
  f32x2 v = {a, b};
  return __builtin_bit_cast(unsigned, __builtin_convertvector(v, bf16x2_t));
}
__device__ __forceinline__ float lo16(unsigned u) { return __uint_as_float(u << 16); }
__device__ __forceinline__ float hi16(unsigned u) { return __uint_as_float(u & 0xffff0000u); }

template <int A, int B, typename F> __device__ __forceinline__ void static_for(F f) {
  if constexpr (A < B) { f(std::integral_constant<int, A>{}); static_for<A + 1, B>(f); }
}
typedef __amdgpu_buffer_rsrc_t rsrc_t;
__device__ __forceinline__ rsrc_t mk_rsrc(const void* p, unsigned bytes) { return __builtin_amdgcn_make_buffer_rsrc((void*)p, 0, bytes, 0x00020000); }
#define UNI(x) __builtin_amdgcn_readfirstlane(x)
#define DMA_HEAD(P) "s_mov_b32 m0, %4\n\ts_mov_b32 %0, %3\n\ts_nop 2\n\tbuffer_load_dwordx4 %1, %2, %0 offen " P " lds\n\t"
#define DMA_NEXT(P) "s_add_u32 m0, m0, %6\n\ts_add_u32 %0, %0, %5\n\tbuffer_load_dwordx4 %1, %2, %0 offen " P " lds\n\t"
#define DMA_OPS : "=&s"(t) : "v"(voff), "s"(r), "s"(UNI(soff)), "s"(UNI(lds)), "s"(UNI(sstep)), "i"(LSTEP) : "memory", "scc", "m0"
template <int LSTEP, int POL = 1> __device__ __forceinline__ void dma4(rsrc_t r, unsigned voff, unsigned soff, unsigned sstep, unsigned lds) {
  unsigned t;
  if constexpr (POL == 1) asm volatile(DMA_HEAD("sc1") DMA_NEXT("sc1") DMA_NEXT("sc1") DMA_NEXT("sc1") DMA_OPS);
  else if constexpr (POL == 2) asm volatile(DMA_HEAD("nt") DMA_NEXT("nt") DMA_NEXT("nt") DMA_NEXT("nt") DMA_OPS);
  else asm volatile(DMA_HEAD("") DMA_NEXT("") DMA_NEXT("") DMA_NEXT("") DMA_OPS);
}
template <int LSTEP, int POL = 1> __device__ __forceinline__ void dma3(rsrc_t r, unsigned voff, unsigned soff, unsigned sstep, unsigned lds) {
  unsigned t;
  if constexpr (POL == 1) asm volatile(DMA_HEAD("sc1") DMA_NEXT("sc1") DMA_NEXT("sc1") DMA_OPS);
  else if constexpr (POL == 2) asm volatile(DMA_HEAD("nt") DMA_NEXT("nt") DMA_NEXT("nt") DMA_OPS);
  else asm volatile(DMA_HEAD("") DMA_NEXT("") DMA_NEXT("") DMA_OPS);
}
template <int LSTEP, int N> __device__ __forceinline__ void bar_dma4(rsrc_t r, unsigned voff, unsigned soff, unsigned sstep, unsigned lds) {
  unsigned t;
  asm volatile("s_mov_b32 m0, %4\n\ts_mov_b32 %0, %3\n\ts_nop 0\n\ts_waitcnt vmcnt(%7) lgkmcnt(0)\n\ts_barrier\n\t"
               "buffer_load_dwordx4 %1, %2, %0 offen sc1 lds\n\t" DMA_NEXT("sc1") DMA_NEXT("sc1") DMA_NEXT("sc1")
               : "=&s"(t) : "v"(voff), "s"(r), "s"(UNI(soff)), "s"(UNI(lds)), "s"(UNI(sstep)), "i"(LSTEP), "n"(N) : "memory", "scc", "m0");
}
__device__ __forceinline__ void dma1(rsrc_t r, unsigned voff, unsigned soff, unsigned lds) {
  asm volatile("s_mov_b32 m0, %3\n\ts_nop 3\n\tbuffer_load_dwordx4 %0, %1, %2 offen sc1 lds" ::"v"(voff), "s"(r), "s"(UNI(soff)), "s"(UNI(lds)) : "memory", "m0");
}
__device__ __forceinline__ void touch(rsrc_t r, unsigned voff, unsigned lds) {
  asm volatile("s_mov_b32 m0, %2\n\ts_nop 3\n\tbuffer_load_dword %0, %1, 0 offen lds" ::"v"(voff), "s"(r), "s"(UNI(lds)) : "memory", "m0");
}
__device__ __forceinline__ void ld8_asm(u32x2& d, rsrc_t r, unsigned voff) {
  asm volatile("s_nop 4\n\tbuffer_load_dwordx2 %0, %1, %2, 0 offen sc1" : "=v"(d) : "v"(voff), "s"(r) : "memory");
}

__device__ __forceinline__ void ld16in_asm(u32x4& d, rsrc_t r, unsigned voff, unsigned soff) {
  asm volatile("s_nop 4\n\tbuffer_load_dwordx4 %0, %1, %2, %3 offen" : "=v"(d) : "v"(voff), "s"(r), "s"(UNI(soff)) : "memory");
}
__device__ __forceinline__ void ld16_asm(f32x4& d, rsrc_t r, unsigned voff, unsigned soff) {
  asm volatile("s_nop 4\n\tbuffer_load_dwordx4 %0, %1, %2, %3 offen sc1" : "=v"(d) : "v"(voff), "s"(r), "s"(UNI(soff)) : "memory");
}

template <typename T> __device__ __forceinline__ T lds_ld(unsigned a) { return *(LAS T*)(a); }
template <typename T> __device__ __forceinline__ void lds_st(unsigned a, T v) { *(LAS T*)(a) = v; }
__device__ __forceinline__ bf16x8 tr2(unsigned a0, unsigned a1) {
  s16x4 v0 = __builtin_amdgcn_ds_read_tr16_b64_v4i16((lds_s16x4_ptr)(a0));
  s16x4 v1 = __builtin_amdgcn_ds_read_tr16_b64_v4i16((lds_s16x4_ptr)(a1));
  return __builtin_shufflevector(v0, v1, 0, 1, 2, 3, 4, 5, 6, 7);
}
__device__ __forceinline__ bf16x8 as_frag(u32x4 v) { return __builtin_bit_cast(bf16x8, v); }
#define MFMA(a, b, c) __builtin_amdgcn_mfma_f32_16x16x32_bf16(a, b, c, 0, 0, 0)

__device__ __forceinline__ float vmax2(float a, float b) { float inf = __builtin_inff(); asm("" : "+s"(inf)); return __builtin_amdgcn_fmed3f(a, b, inf); }
#define SWAP16(a, b) __builtin_amdgcn_permlane16_swap(__float_as_uint(a), __float_as_uint(b), false, false)
#define SWAP32(a, b) __builtin_amdgcn_permlane32_swap(__float_as_uint(a), __float_as_uint(b), false, false)
__device__ __forceinline__ void red2_max(float& a, float& b) {
  auto r = SWAP16(a, b);
  float x = vmax2(__uint_as_float(r[0]), __uint_as_float(r[1]));
  r = SWAP32(x, x);
  x = vmax2(__uint_as_float(r[0]), __uint_as_float(r[1]));
  r = SWAP16(x, x);
  a = __uint_as_float(r[0]); b = __uint_as_float(r[1]);
}
__device__ __forceinline__ void red2_add(float& a, float& b) {
  auto r = SWAP16(a, b);
  float x = __uint_as_float(r[0]) + __uint_as_float(r[1]);
  r = SWAP32(x, x);
  x = __uint_as_float(r[0]) + __uint_as_float(r[1]);
  r = SWAP16(x, x);
  a = __uint_as_float(r[0]); b = __uint_as_float(r[1]);
}
template <bool MAX, typename M> __device__ __forceinline__ void red4(float& a, float& b, float& c, float& d, M mid) {
  auto op = [](unsigned x, unsigned y) { return MAX ? vmax2(__uint_as_float(x), __uint_as_float(y)) : __uint_as_float(x) + __uint_as_float(y); };
  auto r = SWAP16(a, b), q = SWAP16(c, d);
  const float x = op(r[0], r[1]), y = op(q[0], q[1]);
  r = SWAP32(x, y);
  const float w = mid(op(r[0], r[1]));
  r = SWAP32(w, w);
  q = __builtin_amdgcn_permlane16_swap(r[1], r[1], false, false);
  r = __builtin_amdgcn_permlane16_swap(r[0], r[0], false, false);
  a = __uint_as_float(r[0]); b = __uint_as_float(r[1]); c = __uint_as_float(q[0]); d = __uint_as_float(q[1]);
}
__device__ __forceinline__ float red_max4(float x) {
  auto r = __builtin_amdgcn_permlane16_swap(__float_as_uint(x), __float_as_uint(x), false, false);
  x = vmax2(__uint_as_float(r[0]), __uint_as_float(r[1]));
  r = __builtin_amdgcn_permlane32_swap(__float_as_uint(x), __float_as_uint(x), false, false);
  return vmax2(__uint_as_float(r[0]), __uint_as_float(r[1]));
}
__device__ __forceinline__ float red_add4(float x) {
  auto r = __builtin_amdgcn_permlane16_swap(__float_as_uint(x), __float_as_uint(x), false, false);
  x = __uint_as_float(r[0]) + __uint_as_float(r[1]);
  r = __builtin_amdgcn_permlane32_swap(__float_as_uint(x), __float_as_uint(x), false, false);
  return __uint_as_float(r[0]) + __uint_as_float(r[1]);
}

__device__ __forceinline__ void st16_sc1(rsrc_t r, unsigned voff, unsigned soff, u32x4 v) {
  __builtin_amdgcn_raw_buffer_store_b128(v, r, voff, soff, 16);
  asm volatile("s_nop 1" ::"v"(v) : "memory");
}
__device__ __forceinline__ void st8_sc1(rsrc_t r, unsigned voff, unsigned soff, u32x2 v) { __builtin_amdgcn_raw_buffer_store_b64(v, r, voff, soff, 16); }
__device__ __forceinline__ u32x4 ld16_sc1(rsrc_t r, unsigned voff, unsigned soff) { return __builtin_amdgcn_raw_buffer_load_b128(r, voff, soff, 16); }
template <int AUX> __device__ __forceinline__ u32x4 ld16p(rsrc_t r, unsigned voff, unsigned soff) { return __builtin_amdgcn_raw_buffer_load_b128(r, voff, soff, AUX); }
__device__ __forceinline__ u32x2 ld8_sc1(rsrc_t r, unsigned voff, unsigned soff) { return __builtin_amdgcn_raw_buffer_load_b64(r, voff, soff, 16); }
__device__ __forceinline__ f32x4 ld4f_sc1(rsrc_t r, unsigned voff, unsigned soff) { return __builtin_bit_cast(f32x4, __builtin_amdgcn_raw_buffer_load_b128(r, voff, soff, 16)); }
#define OPAQUE_V(x) asm volatile("" : "+v"(x))
#define OPAQUE_S(x) asm volatile("" : "+s"(x))

#define CLOBBER asm volatile("" ::: "memory")
constexpr unsigned SPIN_CAP = 1u << 17;
__device__ __forceinline__ bool is_set(u32x2 v, u32x2 epoch) { return v[0] == epoch[0] && v[1] == epoch[1]; }
__device__ __forceinline__ void set_flags(rsrc_t rs, unsigned lineA, int colA, unsigned lineB, int colB, u32x2 epoch, int lane) {
  const unsigned off = (lane & 7) * REP_STRIDE + ((lane & 8) ? lineB + colB * 8 : lineA + colA * 8);
  if (lane < 16) st8_sc1(rs, off, 0, epoch);
}
__device__ __forceinline__ unsigned poll_need(rsrc_t rs, unsigned voff, u32x2 epoch, unsigned ready, unsigned need, bool& dead) {
  for (unsigned spins = 0; !dead; ++spins) {
    CLOBBER;
    ready |= (unsigned)__ballot(is_set(ld8_sc1(rs, voff, 0), epoch));
    if ((ready & need) == need) break;
    if (spins > SPIN_CAP) dead = true;
    __builtin_amdgcn_s_sleep(1);
  }
  CLOBBER;
  return dead ? ~0u : ready;
}

__device__ __forceinline__ void poll_all(rsrc_t rs, unsigned voff, u32x2 epoch, bool& dead) {
  for (unsigned spins = 0; !dead; ++spins) {
    CLOBBER;
    if (__all(is_set(ld8_sc1(rs, voff, 0), epoch))) break;
    if (spins > SPIN_CAP) dead = true;
    __builtin_amdgcn_s_sleep(1);
  }
  CLOBBER;
}

constexpr unsigned SC_VF = L_R1 + 32768;
constexpr unsigned SC_PHIK = L_R1 + 49152;
constexpr unsigned SC_XCH = L_R2 + XS;
constexpr unsigned wo_slot(int s) {
  return s < 2 ? L_R2 + (unsigned)(s + 1) * XS : (s < 6 ? L_R1 + (unsigned)(s - 2) * 16384 : (s == 6 ? L_R1 + 81920 : L_R2));
}
__device__ __forceinline__ unsigned wo_src(int s, int j) { return (unsigned)(((JROT(j) >> 1) + 2 + s) & 7); }
__device__ __forceinline__ unsigned wo_swz(int lane) { return (unsigned)(((lane & 7) ^ ((lane >> 3) & 6)) * 16); }
__device__ __forceinline__ unsigned umax3(unsigned a, unsigned b, unsigned c) { return max(max(a, b), c); }
__device__ __forceinline__ unsigned umax4v(unsigned m, const u32x4& v) { return umax3(umax3(m, v[0], v[1]), v[2], v[3]); }
__device__ __forceinline__ u32x4 nosent(u32x4 v) { return u32x4{min(v[0], SENT - 1), min(v[1], SENT - 1), min(v[2], SENT - 1), min(v[3], SENT - 1)}; }

template <typename P, typename F>
__device__ __forceinline__ void qkv_loop(int isb, int wm, int h, int lane, bool first, int& trv, P pre, F fin) {
  constexpr unsigned lds0 = 0;
  f32x4 acc[4][6];
  const int fr = lane & 15, fq = lane >> 4;
  unsigned ab[2], bb[2];
  {
    const int r = wm * 64 + fr;
#pragma unroll
    for (int ks = 0; ks < 2; ++ks) ab[ks] = lds0 + L_R2 + r * 128 + (((ks * 4 + fq) ^ ((fr >> 1) & 7)) * 16);
    const int kr = fq * 8 + (fr >> 2);
    const int gs = ((kr >> 1) & 1) | (((kr >> 3) & 1) << 1);
#pragma unroll
    for (int c = 0; c < 2; ++c) bb[c] = lds0 + L_R1 + kr * 128 + (((h * 2 + c) ^ gs) * 32) + (lane & 3) * 8;
  }
#pragma unroll
  for (int m = 0; m < 4; ++m)
#pragma unroll
    for (int n = 0; n < 6; ++n) acc[m][n] = f32x4{0.f, 0.f, 0.f, 0.f};
  bf16x8 af[2][4], bf[2][6];
  auto rds = [&](int ks, int p) {
#pragma unroll
    for (int m = 0; m < 4; ++m) af[p][m] = lds_ld<bf16x8>(ab[ks] + m * 2048);
#pragma unroll
    for (int n = 0; n < 6; ++n) {
      const unsigned a = bb[n & 1] + (n >> 1) * 8192 + ks * 4096;
      bf[p][n] = tr2(a, a + 512);
    }
  };
  auto mms = [&](int p) {
#pragma unroll
    for (int n = 0; n < 6; ++n)
#pragma unroll
      for (int mm = 0; mm < 4; ++mm) { const int m = (n & 1) ? 3 - mm : mm; acc[m][n] = n < 4 ? MFMA(bf[p][n], af[p][m], acc[m][n]) : MFMA(af[p][m], bf[p][n], acc[m][n]); }
  };
  auto ilv2 = [&]() {
#pragma unroll
    for (int i = 0; i < 16; ++i) { SGB(0x008, 1); SGB(0x100, 1); }
#pragma unroll
    for (int i = 0; i < 4; ++i) { SGB(0x008, 1); SGB(0x002, 1); }
    SGB(0x008, 4);
  };
  auto ilv3 = [&]() {
#pragma unroll
    for (int i = 0; i < 16; ++i) { SGB(0x008, 1); SGB(0x100, 1); }
#pragma unroll
    for (int i = 0; i < 7; ++i) { SGB(0x008, 1); SGB(0x004, 2); }
    SGB(0x008, 1);
  };
  if (!first) { WAITV(3); WAITL0; }
  TRQ(1); BAR;
  int tslot = 2;
  rds(0, 0);
  int sa = 0, sb = 0;
  int da = XS, db = 24576;
  auto step = [&]() {
    ab[0] += da; ab[1] += da;
    bb[0] += db; bb[1] += db;
  };
  auto ring = [&]() {
    sa = (sa == 2) ? 0 : sa + 1;
    sb = (sb == 3) ? 0 : sb + 1;
    da = (sa == 2) ? -2 * (int)XS : (int)XS;
    db = (sb == 3) ? -3 * 24576 : 24576;
  };
#pragma unroll 1
  for (int t = 14; t > 0; --t) {
    SCHED;
    rds(1, 1); mms(0);
    step();
    ilv2();
    SCHED;
    __builtin_amdgcn_s_waitcnt(0x0070);
    TRQ(tslot); tslot++; BAR;
    SCHED;
    rds(0, 0);
    ring();
    mms(1); ilv3();
  }
  SCHED;
  f32x4 cs[4], nm4[4];
  float nmean[4], csv[2];
  auto fold = [&](int n) {
#pragma unroll
    for (int mm = 0; mm < 4; ++mm) {
      const int m = (n & 1) ? 3 - mm : mm;
#pragma unroll
      for (int e = 0; e < 4; ++e) {
        float t = n < 4 ? __builtin_fmaf(nmean[m], cs[n & 3][e], acc[m][n][e]) : __builtin_fmaf(nm4[m][e], csv[n & 1], acc[m][n][e]);
        asm("" : "+v"(t));
        acc[m][n][e] = t;
      }
    }
  };
  {
    unsigned pm = lds0 + L_MEAN + (16 + wm * 64 + fr) * 4, pc = lds0 + L_CS + (h * 32 + fq * 4) * 4;
    unsigned pm4 = lds0 + L_MEAN + (16 + wm * 64 + fq * 4) * 4, pcv = lds0 + L_CS + (128 + h * 32 + fr) * 4;
    asm("" : "+v"(pm)); asm("" : "+v"(pc)); asm("" : "+v"(pm4)); asm("" : "+v"(pcv));
#pragma unroll
    for (int m = 0; m < 4; ++m) nmean[m] = lds_ld<float>(pm + m * 64);
#pragma unroll
    for (int n = 0; n < 4; ++n) cs[n] = lds_ld<f32x4>(pc + ((n >> 1) * 64 + (n & 1) * 16) * 4);
#pragma unroll
    for (int m = 0; m < 4; ++m) nm4[m] = lds_ld<f32x4>(pm4 + m * 64);
#pragma unroll
    for (int n = 0; n < 2; ++n) csv[n] = lds_ld<float>(pcv + n * 64);
    rds(1, 1); mms(0);
    step();
    fold(0); fold(1);
    SGB(0x002, 8);
#pragma unroll
    for (int i = 0; i < 8; ++i) { SGB(0x008, 1); SGB(0x100, 2); }
#pragma unroll
    for (int i = 0; i < 11; ++i) { SGB(0x008, 1); SGB(0x100, 1); SGB(0x002, 2); }
#pragma unroll
    for (int i = 0; i < 5; ++i) { SGB(0x008, 1); SGB(0x002, 3); }
    SCHED;
    __builtin_amdgcn_s_waitcnt(0xC07F);
    TRQ(16); BAR;
    SCHED;
    rds(0, 0);
    fold(5); mms(1); fold(2);
#pragma unroll
    for (int i = 0; i < 16; ++i) { SGB(0x008, 1); SGB(0x100, 1); SGB(0x002, 1); }
#pragma unroll
    for (int i = 0; i < 8; ++i) { SGB(0x008, 1); SGB(0x002, 2); }
    SCHED;
    pre(0);
    SCHED;
    rds(1, 1);
    fold(4); mms(0); fold(3);
    pre(1);
#pragma unroll
    for (int i = 0; i < 16; ++i) { SGB(0x008, 1); SGB(0x100, 1); SGB(0x002, 1); SGB(0x004, 1); }
#pragma unroll
    for (int i = 0; i < 8; ++i) { SGB(0x008, 1); SGB(0x002, 3); SGB(0x004, 1); }
  }
  SCHED;
  __builtin_amdgcn_s_waitcnt(0xC07F);
  TRQ(17); BAR;
  SCHED;
  auto mm6 = [&](int m, int n0 = 0, int n1 = 6) {
#pragma unroll
    for (int nn = n0; nn < n1; ++nn) { const int n = (nn + 2) % 6; acc[m][n] = n < 4 ? MFMA(bf[1][n], af[1][m], acc[m][n]) : MFMA(af[1][m], bf[1][n], acc[m][n]); }
  };
  if (!isb) {
    fin(std::false_type{}, acc, mm6);
#pragma unroll
    for (int i = 0; i < 24; ++i) asm volatile("" : "=v"(acc[i / 6][i % 6]));
#pragma unroll
    for (int i = 0; i < 6; ++i) asm volatile("" : "=v"(bf[1][i]));
#pragma unroll
    for (int i = 0; i < 4; ++i) asm volatile("" : "=v"(af[1][i]));
  }
  OPAQUE_S(isb);
  if (isb) fin(std::true_type{}, acc, mm6);
}
__device__ __forceinline__ u32x4 pack(const f32x4& a, const f32x4& b) { return u32x4{pk2(a[0], a[1]), pk2(a[2], a[3]), pk2(b[0], b[1]), pk2(b[2], b[3])}; }

template <int N> __device__ __forceinline__ void hedgehog(const f32x4 (&r)[4][2], const float (&c)[4], float (&ep)[4][8], float (&en)[4][8]) {
  float mx[4], nm[4];
#pragma unroll
  for (int m = 0; m < N; ++m) {
    const f32x4 a = r[m][0], b = r[m][1];
    mx[m] = vmax2(fmaxf(fmaxf(a[3], b[0]), b[1]), fmaxf(fmaxf(fmaxf(fmaxf(a[0], a[1]), a[2]), b[2]), b[3]));
    nm[m] = vmax2(fmaxf(fmaxf(-a[3], -b[0]), -b[1]), fmaxf(fmaxf(fmaxf(fmaxf(-a[0], -a[1]), -a[2]), -b[2]), -b[3]));
  }
  SCHED;
  red4<true>(mx[0], mx[1], nm[0], nm[1], [](float x) { return x; });
  if (N == 4) red4<true>(mx[2], mx[3], nm[2], nm[3], [](float x) { return x; });
#pragma unroll
  for (int m = 0; m < N; ++m) { mx[m] *= -c[m]; nm[m] *= -c[m]; }
  SCHED;
#pragma unroll
  for (int m = 0; m < N; ++m) {
#pragma unroll
    for (int i = 0; i < 8; ++i) { ep[m][i] = __builtin_fmaf(r[m][i >> 2][i & 3], c[m], mx[m]); en[m][i] = __builtin_fmaf(r[m][i >> 2][i & 3], -c[m], nm[m]); }
    SCHED;
#pragma unroll
    for (int i = 0; i < 8; ++i) { ep[m][i] = __builtin_amdgcn_exp2f(ep[m][i]); en[m][i] = __builtin_amdgcn_exp2f(en[m][i]); }
    SCHED;
  }
}
__device__ __forceinline__ u32x4 pack8(const float (&e)[8], float f) {
  return u32x4{pk2(e[0] * f, e[1] * f), pk2(e[2] * f, e[3] * f), pk2(e[4] * f, e[5] * f), pk2(e[6] * f, e[7] * f)};
}
__device__ __forceinline__ u32x4 pack8(const float (&e)[8]) { return u32x4{pk2(e[0], e[1]), pk2(e[2], e[3]), pk2(e[4], e[5]), pk2(e[6], e[7])}; }

constexpr u64 window_mask(int t, int e) {
  u64 mk = 0;
  for (int ln = 0; ln < 64; ++ln) {
    const int delta = (ln & 15) + 16 - t * 16 - (ln >> 4) * 4 - e;
    if (delta >= 0 && delta <= 14 && !(delta & 1)) mk |= 1ull << ln;
  }
  return mk;
}
__device__ __forceinline__ void phik2(const f32x4 (&r)[4][2], const float (&c)[4], unsigned pw) {
  const f32x4 zero = {0.f, 0.f, 0.f, 0.f};
  const bf16x8 ones = {0x3f80, 0x3f80, 0x3f80, 0x3f80, 0x3f80, 0x3f80, 0x3f80, 0x3f80};
  float ep[4][8], en[4][8];
  f32x4 sp[2], sn[2];
  hedgehog<2>(r, c, ep, en);
#pragma unroll
  for (int m = 0; m < 2; ++m) { sp[m] = MFMA(ones, as_frag(pack8(ep[m])), zero); sn[m] = MFMA(ones, as_frag(pack8(en[m])), zero); }
#pragma unroll
  for (int m = 0; m < 2; ++m) { asm("" : "+v"(sp[m])); asm("" : "+v"(sn[m])); }
  SCHED;
#pragma unroll
  for (int m = 0; m < 2; ++m) {
    const float fp = __builtin_amdgcn_rcpf(sp[m][0]), fn = __builtin_amdgcn_rcpf(sn[m][0]);
#pragma unroll
    for (int i = 0; i < 8; ++i) { ep[m][i] *= fp; en[m][i] *= fn; }
    SCHED;
    const u32x4 pp = pack8(ep[m]), pn = pack8(en[m]);
    const unsigned d = pw + m * (16 * 160);
    lds_st<u32x2>(d, u32x2{pp[0], pp[1]});
    lds_st<u32x2>(d + 32, u32x2{pp[2], pp[3]});
    lds_st<u32x2>(d + 64, u32x2{pn[0], pn[1]});
    lds_st<u32x2>(d + 96, u32x2{pn[2], pn[3]});
    SCHED;
  }
}
__device__ __forceinline__ bf16x8 rowsum_sel(int lane) {
  const int fr = lane & 15;
  const unsigned one = (fr < 8) ? (0x3f80u << (16 * (fr & 1))) : 0u;
  const int e = (fr >> 1) & 3;
  return as_frag(u32x4{e == 0 ? one : 0u, e == 1 ? one : 0u, e == 2 ? one : 0u, e == 3 ? one : 0u});
}
__device__ __forceinline__ f32x4 rowsum_fin(f32x4 c) {
#pragma unroll
  for (int k = 0; k < 4; ++k) {
    const float a = c[k];
    c[k] = a + __int_as_float(__builtin_amdgcn_update_dpp(0, __float_as_int(a), 0x128  , 0xf, 0xf, false));
  }
  return c;
}
__device__ __forceinline__ void job_colsum(rsrc_t rs, const u32x4 (&w)[8], int mat, int g, int jc, int hw, int lane) {
  const bf16x8 sel = rowsum_sel(lane);
  f32x4 c = {0.f, 0.f, 0.f, 0.f};
#pragma unroll
  for (int i = 0; i < 8; ++i) c = MFMA(sel, as_frag(w[i]), c);
  const u32x4 w4 = __builtin_bit_cast(u32x4, rowsum_fin(c));
  unsigned so = WS_CS + (unsigned)(((mat * 16 + jc) * 16 + g) * 768);
  OPAQUE_S(so);
  if ((lane & 40) == 0) st16_sc1(rs, (unsigned)(hw * 64 + (lane & 7) * 8 + (lane >> 4) * 4) * 4, so, w4);
}
__device__ __forceinline__ unsigned img_off(int i, int lane) { return (lane >> 3) * 128 + (((lane & 7) ^ ((((lane >> 4) & 1) | ((i & 1) << 1)) << 1)) * 16); }
__device__ __forceinline__ void job_pack(rsrc_t rs, u32x4 w, int i, int mat, int g, int jc, int hw, int lane) {
  st16_sc1(rs, img_off(i, lane),
           WS_WP + (unsigned)(((mat * 16 + jc) * 16 + g) * 24576 + hw * 8192 + i * 1024), w);
}

__global__ __launch_bounds__(512, 2) void mega_kernel(Params p) {
  extern __shared__ __attribute__((aligned(16))) char smem[];
  const int tid = threadIdx.x;
  const int wave0 = __builtin_amdgcn_readfirstlane(tid >> 6);
  const int xx = blockIdx.x & 7, rr = blockIdx.x >> 3;
  const int g0 = (rr >> 2) * 2 + (xx & 1), j0 = (xx >> 1) * 4 + (rr & 3);
  constexpr unsigned lds0 = 0, lds00 = 0;
  if ((unsigned)(size_t)(LAS char*)smem != 0u) return;
#define LOCALS0 int g = g0, j = j0, l = l0, wave = wave0;                                              \
  OPAQUE_S(g); OPAQUE_S(j); OPAQUE_S(l); OPAQUE_S(wave);                                               \
  const int hw = wave & 3, wm = wave & 1, h = (wave >> 1) & 1;                                         \
  const unsigned myrep = (unsigned)((j >> 2) * 2 + (g & 1)) * REP_STRIDE;                              \
  (void)hw; (void)wm; (void)h; (void)myrep;                                                            \
  int lane = tid; OPAQUE_V(lane); lane &= 63
#define LOCALS LOCALS0; const bool isb = l & 1, lead = (wave == 4); const int half = wave >> 2; (void)isb; (void)lead; (void)half
  const rsrc_t rs = mk_rsrc(p.ws, WS_SPAN);
  const bool cw = wave0 < 4;
  auto slowbit = [](const void* q) { return 1u << ((3 - ((unsigned)(size_t)q >> 7)) & 7); };
  unsigned slowm = slowbit(p.wqkvA) | (slowbit(p.wqkvB) << 8) | (slowbit(p.woA) << 16) | (slowbit(p.woB) << 24);
  slowm |= PKALL;
  OPAQUE_S(slowm);
  if (wave0 == 2) touch(mk_rsrc(p.ws, NREP * REP_STRIDE), min(blockIdx.x * 9 + ((tid & 63) < 9 ? (tid & 63) : 0), NREP * REP_STRIDE / 128 - 1) * 128, lds00 + L_SINK + 512);
  if (wave0 == 3) {
    const char* pc;
    unsigned len;
    asm volatile("s_getpc_b64 %0\n.Lpf_base:\n\ts_mov_b32 %1, .Lpf_end-.Lpf_base" : "=s"(pc), "=s"(len));
    const unsigned off = ((unsigned)rr * 16 + (tid & 15)) * 128;
    touch(mk_rsrc(pc, len), ((int)len > 0 && off + 4 <= len) ? off : 0u, lds00 + L_SINK);
  }
  int trv = 0, trA = 0, trB = 0, trC = 0;
  u32x2 epoch = {0u, 0u};
  bool dead = false;
  STAMP_INIT;
  const char *pA = (const char*)p.x, *pW = (const char*)p.wqkvA;
  unsigned zA = 2048 * 2048, bA = g0 * (128 * 2048), kA = 128, pcA = 32 * 2048, vA;
  unsigned zW = 1024 * 6144, bW = j0 * 128, kW = 64 * 6144, pcW = 2048, hW = 32 * 6144, vW;
  int kb = JROT(j0) + 8 * (g0 & 1), dj = -1;
  {
    const int lane = tid & 63, hw = wave0 & 3;
    vA = (hw * 8 + (lane >> 3)) * 2048 + (((lane & 7) ^ (((hw & 1) * 4 + (lane >> 4)) & 7)) * 16);
    vW = (hw * 8 + (lane >> 3)) * 6144 + (((lane & 7) ^ ((((lane >> 4) & 1) | ((hw & 1) << 1)) << 1)) * 16);
  }

#pragma unroll 1
  for (int l0 = 0; l0 < 4; ++l0) {
    TRC(0); TRR(62);

    f32x4 acc[2][4];
    auto kvpart = [&](unsigned va, unsigned pa, unsigned kvb, int lane, int wave) {
      const f32x4 zero = {0.f, 0.f, 0.f, 0.f};
      const bf16x8 ones = {0x3f80, 0x3f80, 0x3f80, 0x3f80, 0x3f80, 0x3f80, 0x3f80, 0x3f80};
      f32x4 c0 = zero, c1 = zero, z0 = zero, z1 = zero;
#pragma unroll
      for (int ks = 0; ks < 4; ++ks) {
        const bf16x8 v = lds_ld<bf16x8>(va + ks * 2048);
        const bf16x8 a0 = tr2(pa + ks * 32 * 160, pa + (ks * 32 + 16) * 160);
        const bf16x8 a1 = tr2(pa + 32 + ks * 32 * 160, pa + 32 + (ks * 32 + 16) * 160);
        c0 = MFMA(a0, v, c0);
        c1 = MFMA(a1, v, c1);
        z0 = MFMA(a0, ones, z0);
        z1 = MFMA(a1, ones, z1);
      }
      st16_sc1(rs, lane * 16, kvb + (unsigned)wave * 1024, nosent(pack(c0, c1)));
      if (!(wave & 2) && (lane & 15) == 0) st16_sc1(rs, lane, kvb + KVP_Z + (wave >> 2) * 128 + (wave & 1) * 64, nosent(pack(z0, z1)));
    };
#define KV_ADDR                                                                                                            \
    unsigned va = lds0 + SC_VF + (wave >> 2) * 8192 + ((wave >> 1) & 1) * 1024 + lane * 16;                                \
    unsigned pa = lds0 + SC_PHIK + ((wave >> 2) * 128 + fq * 4 + (fr >> 2)) * 160 + (wave & 1) * 64 + (lane & 3) * 8;      \
    unsigned kvj = WS_KVP + (unsigned)j * KVP_J + (unsigned)(l >> 1) * KVP_L;                                              \
    OPAQUE_V(va); OPAQUE_V(pa); OPAQUE_S(kvj)
    if (cw) {
      if (l0 == 0) {
        LOCALS;
        u32x4 v[2][8];
        if (wave < 3) {
          const int jc = JOBC(j, wave), kt = (JROT(j) + 8 * (g & 1) + (g >> 1)) & 15;
          const rsrc_t ra = mk_rsrc(p.wqkvA, 1024 * 6144);
#pragma unroll
          for (int i = 0; i < 8; ++i)
            v[0][i] = __builtin_amdgcn_raw_buffer_load_b128(ra, (lane >> 3) * 6144 + (lane & 7) * 16, (kt * 64 + i * 8) * 6144 + wave * 2048 + jc * 128, 0);
          SCHED;
          TRC(44); BAR; SCHED;
          job_colsum(rs, v[0], 0, g, jc, wave, lane);
          if (SLOWC(0, jc)) {
#pragma unroll
            for (int i = 0; i < 8; ++i) job_pack(rs, v[0][i], i, 0, kt, jc, wave, lane);
          }
        } else {
          const int fr = lane & 15, fq = lane >> 4;
          const rsrc_t rx = mk_rsrc(p.x, 2048 * 2048);
#pragma unroll
          for (int i = 0; i < 16; ++i)
            v[i & 1][i >> 1] = __builtin_amdgcn_raw_buffer_load_b128(rx, fr * 2048 + fq * 16, (g * 128 + (j & 3) * 32 + ((i >> 1) & 1) * 16) * 2048 + ((JROT(j) + 8 * (g & 1)) & 15) * 128 + (i >> 2) * 128 + (i & 1) * 64, 0);
          SCHED;
          TRC(44); BAR; SCHED;
          const f32x4 zero = {0.f, 0.f, 0.f, 0.f};
          const bf16x8 ones = {0x3f80, 0x3f80, 0x3f80, 0x3f80, 0x3f80, 0x3f80, 0x3f80, 0x3f80};
          float sm[8], q[8];
#pragma unroll
          for (int m = 0; m < 8; ++m) {
            const f32x4 a = MFMA(ones, as_frag(v[1][m]), MFMA(ones, as_frag(v[0][m]), zero));
            const f32x4 b = MFMA(as_frag(v[1][m]), as_frag(v[1][m]), MFMA(as_frag(v[0][m]), as_frag(v[0][m]), zero));
            sm[m] = a[0];
            q[m] = (fr & 2) ? ((fr & 1) ? b[3] : b[2]) : ((fr & 1) ? b[1] : b[0]);
          }
          if (fq == (fr >> 2)) {
#pragma unroll
            for (int m = 0; m < 8; ++m) st8_sc1(rs, ((m & 1) * 16 + fr) * 8 + (m >> 1) * 1024, st_off(g, 0) + JROT(j) * 1024 + (j & 3) * 256, u32x2{__float_as_uint(sm[m]), __float_as_uint(q[m])});
          }
        }
      }
      LOCALS;
      unsigned otile, kvo, l16, pr1, pr4;
      int g2, j2, l2, ln2;
      qkv_loop(l & 1, wm, h, lane, l == 0, trv, [&](int st) {
        if (st == 0) {
          g2 = g0; j2 = j0; l2 = l0; ln2 = tid;
          OPAQUE_S(g2); OPAQUE_S(j2); OPAQUE_S(l2); OPAQUE_V(ln2);
          return;
        }
        otile = ob_off(g2, j2, l2) + (unsigned)(wm * 8 + h) * 1024;
        kvo = WS_KVP + (unsigned)j2 * KVP_J + (unsigned)g2 * KVP_G + (unsigned)(l2 >> 1) * KVP_L;
        l16 = (ln2 & 63) * 16;
        pr1 = lds0 + L_RSTD + (16 + wm * 64 + (ln2 & 15)) * 4;
        pr4 = lds0 + L_RSTD + wm * 256 + (ln2 & 48);
        asm("" : "+s"(otile)); asm("" : "+s"(kvo)); asm("" : "+v"(l16)); asm("" : "+v"(pr1)); asm("" : "+v"(pr4));
      }, [&](auto isb_c, f32x4 (&a)[4][6], auto& mm6) {
        const f32x4 zero = {0.f, 0.f, 0.f, 0.f};
        constexpr float L2E = 1.4426950408889634f;
        float rq[4];
#define ilv6(nv) do { _Pragma("unroll") for (int i_ = 0; i_ < 6; ++i_) { SGB(0x008, 1); SGB(0x100, 2); SGB(0x002, nv); } SCHED; } while (0)
        if constexpr (decltype(isb_c)::value) {
          constexpr float QS = 0.17677669529663687f * L2E, NEG = -3.0e38f;
          const bf16x8 ones = {0x3f80, 0x3f80, 0x3f80, 0x3f80, 0x3f80, 0x3f80, 0x3f80, 0x3f80};
          u32x4 kf[5], vf[5], qf[4], pf[4];
          f32x4 rs4[4], bias[2], s0[4], s1[4], dn[4];
          float x[4][4], mx[4];
          unsigned psel0, psel1;
          auto pkkv = [&](int m) { kf[m + 1] = pack(a[m][2] * rq[m], a[m][3] * rq[m]); vf[m + 1] = pack(a[m][4] * rs4[m], a[m][5] * rs4[m]); };
          auto pkq = [&](int m) { qf[m] = pack(a[m][0], a[m][1]); };
          auto st1 = [&](int m) {
            x[m][0] = vmax2(s0[m][0], s0[m][1]); x[m][1] = vmax2(s0[m][2], s0[m][3]); x[m][2] = vmax2(s1[m][0], s1[m][1]); x[m][3] = vmax2(s1[m][2], s1[m][3]);
            mx[m] = vmax2(vmax2(x[m][0], x[m][1]), vmax2(x[m][2], x[m][3]));
          };
          auto st2 = [&](int m) {
            const float mxl = mx[m] * -rq[m];
#pragma unroll
            for (int e = 0; e < 4; ++e) x[m][e] = __builtin_fmaf(x[m][e], rq[m], mxl);
          };
          auto st3 = [&](int m) {
#pragma unroll
            for (int e = 0; e < 4; ++e) x[m][e] = __builtin_amdgcn_exp2f(x[m][e]);
          };
          auto st5 = [&](int m) {
            const unsigned p0 = pk2(x[m][0], x[m][1]), p1 = pk2(x[m][2], x[m][3]);
            pf[m] = u32x4{__builtin_amdgcn_perm(0u, p0, psel0), __builtin_amdgcn_perm(0u, p0, psel1), __builtin_amdgcn_perm(0u, p1, psel0), __builtin_amdgcn_perm(0u, p1, psel1)};
          };
          auto st6 = [&](int m) {
            dn[m] = MFMA(ones, as_frag(pf[m]), zero);
            s0[m] = MFMA(as_frag(u32x4{vf[m][0], vf[m][1], vf[m + 1][0], vf[m + 1][1]}), as_frag(pf[m]), zero);
            s1[m] = MFMA(as_frag(u32x4{vf[m][2], vf[m][3], vf[m + 1][2], vf[m + 1][3]}), as_frag(pf[m]), zero);
            asm("" : "+v"(dn[m]));
          };
          auto st7 = [&](int m) { const float inv = __builtin_amdgcn_rcpf(dn[m][0]); pf[m] = nosent(pack(s0[m] * inv, s1[m] * inv)); };
          mm6(3);
#pragma unroll
          for (int m = 0; m < 4; ++m) rq[m] = lds_ld<float>(pr1 + m * 64);
#pragma unroll
          for (int m = 3; m >= 0; --m) rs4[m] = lds_ld<f32x4>(pr4 + 64 + m * 64);
          unsigned pv_ = l16 + lds0 + L_PREV + h * 2048;
          asm("" : "+v"(pv_));
          static_for<0, 8>([&](auto i_c) __attribute__((always_inline)) {
            constexpr int i = decltype(i_c)::value;
            constexpr u64 mk = window_mask(i >> 2, i & 3);
            bias[i >> 2][i & 3] = __builtin_amdgcn_inverse_ballot_w64(mk) ? 0.f : NEG;
          });
          asm("" : "+v"(bias[0])); asm("" : "+v"(bias[1]));
          const unsigned hso = kvo + KVP_HALO + h * 2048, hsi = hso - (g > 0 ? KVP_G : 0u);
          ilv6(2);
          mm6(2);
          pkkv(3);
          ilv6(3);
          OPAQUE_V(kf[4]); OPAQUE_V(vf[4]);
          if (wm) {
            st16_sc1(rs, l16, hso, nosent(kf[4]));
            st16_sc1(rs, l16, hso + 1024, nosent(vf[4]));
          } else {
            lds_st<u32x4>(pv_, kf[4]);
            lds_st<u32x4>(pv_ + 1024, vf[4]);
          }
          SCHED;
          mm6(1);
          pkq(3); pkkv(2);
          ilv6(4);
          mm6(0);
          pkq(2); pkkv(1);
          ilv6(4);
          s0[3] = MFMA(as_frag(kf[3]), as_frag(qf[3]), bias[0]); s1[3] = MFMA(as_frag(kf[4]), as_frag(qf[3]), bias[1]);
          s0[2] = MFMA(as_frag(kf[2]), as_frag(qf[2]), bias[0]); s1[2] = MFMA(as_frag(kf[3]), as_frag(qf[2]), bias[1]);
          pkq(1); pkkv(0);
#pragma unroll
          for (int i = 0; i < 4; ++i) { SGB(0x008, 1); SGB(0x002, 5); }
          SCHED;
          const bool halo = wm == 0 && g > 0;
          const u32x4 hk = ld16p<PA1>(rs, l16, hsi), hv = ld16p<PA1>(rs, l16, hsi + 1024);
          SCHED;
          s0[1] = MFMA(as_frag(kf[1]), as_frag(qf[1]), bias[0]); s1[1] = MFMA(as_frag(kf[2]), as_frag(qf[1]), bias[1]);
          st1(3); st1(2);
#pragma unroll
          for (int m = 0; m < 4; ++m) rq[m] *= QS;
          SCHED;
          st1(1);
          pkq(0);
          psel0 = __builtin_amdgcn_inverse_ballot_w64(0xaaaaaaaaaaaaaaaaull) ? 0x01000c0cu : 0x0c0c0100u;
          psel1 = __builtin_amdgcn_inverse_ballot_w64(0xaaaaaaaaaaaaaaaaull) ? 0x03020c0cu : 0x0c0c0302u;
          { float d = mx[1]; red4<true>(mx[3], mx[2], mx[1], d, [](float v) { return v; }); }
          s1[0] = MFMA(as_frag(kf[1]), as_frag(qf[0]), bias[1]);
          SCHED;
          st2(3); st2(2); st2(1);
          SCHED;
          st3(3); st3(2); st3(1);
          SCHED;
          st5(3); st6(3); st5(2); st6(2); st5(1); st6(1);
          SCHED;
          WAITL0;
          TRR(60);
          TRC(18); BAR;
          u32x4 pk = lds_ld<u32x4>(pv_), pv = lds_ld<u32x4>(pv_ + 1024);
          st7(3); st7(2); st7(1);
          SCHED;
#pragma unroll
          for (int m = 3; m > 0; --m) st16_sc1(rs, l16, otile + m * 2048, pf[m]);
          if (!wm && g == 0) bias[0] = f32x4{NEG, NEG, NEG, NEG};
          SCHED;
          TRV(55, 0); TRC(21);
          if (halo) {
            pk = hk; pv = hv;
            for (unsigned spins = 0; !dead && __any(umax4v(umax4v(0u, pk), pv) == SENT); ++spins) {
              if (spins > SPIN_CAP) { dead = true; break; }
              CLOBBER;
              TRV(55, spins + 1);
              pk = ld16p<PA1>(rs, l16, hsi); pv = ld16p<PA1>(rs, l16, hsi + 1024);
              OPAQUE_V(pk); OPAQUE_V(pv);
            }
          }
          vf[0] = pv;
          s0[0] = MFMA(as_frag(pk), as_frag(qf[0]), bias[0]);
          st1(0);
          mx[0] = red_max4(mx[0]);
          st2(0); st3(0); st5(0); st6(0); st7(0);
          st16_sc1(rs, l16, otile, pf[0]);
        } else {
          LOCALS;
          const int fr = lane & 15, fq = lane >> 4;
          f32x4 rs4[4], r[4][2];
          const bf16x8 ones = {0x3f80, 0x3f80, 0x3f80, 0x3f80, 0x3f80, 0x3f80, 0x3f80, 0x3f80};
          auto proj = [&](int m, int n, const bf16x8& w0, const bf16x8& w1) {
            const u32x4 f = pack(a[m][n], a[m][n + 1]);
            r[m][0] = MFMA(w0, as_frag(f), zero); r[m][1] = MFMA(w1, as_frag(f), zero);
          };
          mm6(3, 0, 2); mm6(2, 0, 2); mm6(1, 0, 2); mm6(0, 0, 2);
          unsigned wa = lds0 + L_WPHI + h * 2048 + (fq * 4 + (fr >> 2)) * 64 + (lane & 3) * 8, kw = lds0 + L_PREV + hw * 2048 + l16;
          asm("" : "+v"(wa)); asm("" : "+v"(kw));
#pragma unroll
          for (int m = 0; m < 4; ++m) rq[m] = lds_ld<float>(pr1 + m * 64);
#pragma unroll
          for (int m = 0; m < 4; ++m) rs4[m] = lds_ld<f32x4>(pr4 + 64 + m * 64);
          const bf16x8 w0 = tr2(wa, wa + 1024);
          const bf16x8 w1 = tr2(wa + 32, wa + 32 + 1024);
#pragma unroll
          for (int i = 0; i < 8; ++i) { SGB(0x008, 1); SGB(0x100, 2); SGB(0x002, 2); }
          SCHED;
          mm6(3, 2, 4); mm6(2, 2, 4);
          lds_st<u32x4>(kw + 1024, pack(a[3][2], a[3][3]));
          lds_st<u32x4>(kw, pack(a[2][2], a[2][3]));
#pragma unroll
          for (int i = 0; i < 2; ++i) { SGB(0x008, 1); SGB(0x002, 2); SGB(0x008, 1); SGB(0x002, 2); SGB(0x200, 1); }
          SCHED;
          mm6(1, 2, 4); mm6(0, 2, 4);
          proj(0, 2, w0, w1); proj(1, 2, w0, w1);
#pragma unroll
          for (int i = 0; i < 4; ++i) { SGB(0x008, 1); SGB(0x002, 2); }
          SCHED;
          WAITL0;
          TRC(18); BAR;
          SCHED;
          unsigned pw = lds0 + SC_PHIK + (h * 128 + wm * 64 + fr) * 160 + fq * 8, vw = lds0 + SC_VF + h * 8192 + wm * 4096 + l16;
          asm("" : "+v"(pw)); asm("" : "+v"(vw));
#pragma unroll
          for (int m = 0; m < 4; ++m) mm6(m, 4, 6);
#pragma unroll
          for (int m = 0; m < 4; ++m) rq[m] *= L2E;
#pragma unroll
          for (int i = 0; i < 4; ++i)
            lds_st<u32x4>(vw + i * 1024, pack(a[i & 2][4 + (i & 1)] * rs4[i & 2], a[(i & 2) + 1][4 + (i & 1)] * rs4[(i & 2) + 1]));
#pragma unroll
          for (int i = 0; i < 8; ++i) { SGB(0x008, 1); SGB(0x002, 5); }
          SCHED;
          phik2(r, rq, pw);
          KV_ADDR;
          WAITL0;
          TRR(60);
          TRC(19); BAR;
          kvpart(va, pa, kvo, lane, wave);
          CLOBBER;
          TRC(20); BAR;
          u32x4 qp[4], qn[4];
          float sqp[4], sqn[4];
#pragma unroll
          for (int m = 0; m < 4; ++m) proj(m, 0, w0, w1);
          SCHED;
          float ep[4][8], en[4][8];
          hedgehog<4>(r, rq, ep, en);
          const unsigned zo = (lane >> 3) * KVP_G + (lane & 7) * 16, zs = kvj + KVP_Z + h * 128;
          u32x4 zv[2] = {ld16p<PA1>(rs, zo, zs), ld16p<PA1>(rs, zo, zs + 8 * KVP_G)};
          SCHED;
#pragma unroll
          for (int m = 0; m < 4; ++m) {
            qp[m] = pack8(ep[m]); qn[m] = pack8(en[m]);
            sqp[m] = MFMA(ones, as_frag(qp[m]), zero)[0];
            sqn[m] = MFMA(ones, as_frag(qn[m]), zero)[0];
          }
          SCHED;
#pragma unroll
          for (int m = 0; m < 4; ++m) { sqp[m] = __builtin_amdgcn_rcpf(sqp[m]); sqn[m] = __builtin_amdgcn_rcpf(sqn[m]); }
#pragma unroll
          for (int m = 0; m < 4; ++m) { OPAQUE_V(qp[m]); OPAQUE_V(qn[m]); OPAQUE_V(sqp[m]); OPAQUE_V(sqn[m]); }
          unsigned ko = (lane >> 5) * KVP_G + (lane & 31) * 16, ks = kvj + (unsigned)((j ^ RX) - j) * KVP_J + KVP_SUM + h * (8 * KVP_G);
          bf16x8 sel = rowsum_sel(lane);
          OPAQUE_V(ko); OPAQUE_S(ks); OPAQUE_V(sel);
          TRC(21); TRV(50, 0);
          for (unsigned spins = 0; !dead && __any(umax4v(umax4v(0u, zv[0]), zv[1]) == SENT); ++spins) {
            if (spins > SPIN_CAP) { dead = true; break; }
            CLOBBER;
            TRV(50, spins + 1);
            zv[0] = ld16p<PA1>(rs, zo, zs); zv[1] = ld16p<PA1>(rs, zo, zs + 8 * KVP_G);
          }
          TRC(22);
          {
            const f32x4 z = rowsum_fin(MFMA(sel, as_frag(zv[1]), MFMA(sel, as_frag(zv[0]), zero)));
            if ((lane & 40) == 0) lds_st<u32x2>(lds0 + L_ZF + hw * 128 + fr * 16 + fq * 8, u32x2{pk2(z[0], z[1]), pk2(z[2], z[3])});
            WAITL0;
            const bf16x8 zf0 = lds_ld<bf16x8>(lds0 + L_ZF + hw * 128 + fq * 16), zf1 = lds_ld<bf16x8>(lds0 + L_ZF + hw * 128 + 64 + fq * 16);
            float dp[4], dn[4];
#pragma unroll
            for (int m = 0; m < 4; ++m) { dp[m] = MFMA(zf0, as_frag(qp[m]), zero)[0]; dn[m] = MFMA(zf1, as_frag(qn[m]), zero)[0]; }
            SCHED;
#pragma unroll
            for (int m = 0; m < 4; ++m) {
              const float inv = __builtin_amdgcn_rcpf(__builtin_fmaf(dp[m], sqp[m], __builtin_fmaf(dn[m], sqn[m], 1e-6f)));
              sqp[m] *= inv; sqn[m] *= inv;
              OPAQUE_V(sqp[m]); OPAQUE_V(sqn[m]);
            }
          }
          TRV(51, 0);
          TRC(43); BAR;
          if (H2D) __builtin_amdgcn_s_sleep(H2D);
          u32x4 kv[4];
#pragma unroll
          for (int i = 0; i < 4; ++i) kv[i] = ld16p<PA2>(rs, ko, ks + i * (2 * KVP_G));
          SCHED;
#pragma unroll
          for (int m = 0; m < 4; ++m) { qp[m] = pack8(ep[m], sqp[m]); qn[m] = pack8(en[m], sqn[m]); }
#pragma unroll
          for (int m = 0; m < 4; ++m) { OPAQUE_V(qp[m]); OPAQUE_V(qn[m]); }
          SCHED;
          for (unsigned spins = 0; !dead && __any(max(umax4v(umax4v(0u, kv[0]), kv[1]), umax4v(umax4v(0u, kv[2]), kv[3])) == SENT); ++spins) {
            if (spins > SPIN_CAP) { dead = true; break; }
            CLOBBER;
            TRV(51, spins + 1);
#pragma unroll
            for (int i = 0; i < 4; ++i) kv[i] = ld16p<PA2>(rs, ko, ks + i * (2 * KVP_G));
          }
          TRC(23);
          f32x4 o[4][2];
#pragma unroll
          for (int m = 0; m < 4; ++m)
#pragma unroll
            for (int i = 0; i < 2; ++i) o[m][i] = MFMA(as_frag(kv[2 * i + 1]), as_frag(qn[m]), MFMA(as_frag(kv[2 * i]), as_frag(qp[m]), zero));
#pragma unroll
          for (int m = 0; m < 4; ++m) st16_sc1(rs, l16, otile + m * 2048, nosent(pack(o[m][0], o[m][1])));
        }
      });
#pragma unroll
      for (int i = 0; i < 8; ++i) asm volatile("" : "=v"(acc[i >> 2][i & 3]));
    } else {
      __builtin_amdgcn_s_setprio(3);
      if (l0) {
        WAITV(3);
        WAITL0;
        TRC(1); BAR;
      }
      LOCALS;
      u32x4 xr[2][2];
      const unsigned lin = lane * 16, vlin = lin + hw * 1024;
      const int jr = JROT(j);
      u32x4 sent4 = {SENT, SENT, SENT, SENT};
      OPAQUE_V(sent4);
      const unsigned cb0 = ob_off(g, j, l), cb2 = xb_off(g, l) + j * TS;
      const unsigned cb1 = WS_KVP + (unsigned)j * KVP_J + (unsigned)g * KVP_G + (unsigned)(l >> 1) * KVP_L;
      auto ldres = [&](int slot) {
        const int fr = lane & 15, fq = lane >> 4, ch = (fq & 1) * 2 + (fq >> 1);
#pragma unroll
        for (int i = 0; i < 4; ++i)
          xr[i >> 1][i & 1] = lds_ld<u32x4>(lds0 + L_R2 + slot * XS + (hw * 32 + ((i >> 1) ^ 1) * 16 + fr) * 128 + (((ch + 4 * (i & 1)) ^ ((fr >> 1) & 7)) * 16));
      };
      auto aslot = [&](int t) { return lds0 + L_R2 + (t % 3) * XS + hw * 1024; };
      auto wslot = [&](int t) { return lds0 + L_R1 + (t & 3) * 24576 + hw * 1024; };
      const bool l0f = l == 0;
      const rsrc_t rA = mk_rsrc(pA, zA);
      const int od = (KOD && !l0f) ? 4 * (g & 1) : 0;
      auto kseq = [&](int t) { return (kb + t - ((t < 4 && t <= dj) ? 1 : 0) + (t < 4 ? 0 : (t < 12 ? od : -2 * od))) & 15; };
      auto offA = [&](int t) { return bA + (unsigned)kseq(t) * kA; };
      auto offW = [&](int t) { return bW + (unsigned)kseq(t) * kW; };
      auto ldA = [&](int t) { dma4<4096>(rA, vA, offA(t), pcA, aslot(t)); };
      auto ldWh = [&](int t, unsigned o, int hf) { dma3<8192>(mk_rsrc(pW, zW), vW, o + (hf ? hW : 0u), pcW, wslot(t) + hf * 4096); };
      const bool slowj = l0f && SLOWC(0, j);
      auto packedW = [&]() {
        pW = p.ws; zW = WS_SPAN; bW = WS_WP + (unsigned)j * (16 * 24576); kW = 24576; pcW = 8192; hW = 4096; vW = vlin;
      };
      auto ldW = [&](int t) { const unsigned o = offW(t); ldWh(t, o, 0); ldWh(t, o, 1); };
      const unsigned cmask = 0xfu | (isb ? 0u : 0xf0u) | (l == 3 ? 0u : 0xf00u);
      constexpr auto spread = [](int n) { unsigned m = 0; for (int t = 0; t < 26; ++t) if ((t + 1) * n / 13 > t * n / 13) m |= 1u << t; return m; };
      constexpr unsigned sp4 = spread(4), sp8 = spread(8), sp12 = spread(12);
      const unsigned smask = (isb ? (l == 3 ? sp4 : sp8) : sp12) >> ((((g + j * 5) & 15) * 13) >> 4);
      const unsigned cbm = isb ? cb2 : cb1;
      auto clean = [&](int t) __attribute__((always_inline)) {
        if (STG) {
          if (t < 13 && ((smask >> t) & 1)) {
            const unsigned cc = __builtin_popcount(smask & ((1u << t) - 1));
            const unsigned cb = cb0 + (cc >= 4 ? cbm - cb0 : 0u) + (cc >= 8 ? cb2 - cbm : 0u), pc = (cc + g + j) & 3;
            st16_sc1(rs, (lane >> 4) * 4096 + (lane & 15) * 16, cb + hw * 1024 + pc * 256, sent4);
          }
        } else if (t < 4 || (t < 12 && ((cmask >> t) & 1))) {
          const unsigned cb = t < 4 ? cb0 : (t < 8 ? cb1 : cb2), pc = (unsigned)((t + g + j) & 3);
          st16_sc1(rs, (lane >> 4) * 4096 + (lane & 15) * 16, cb + hw * 1024 + pc * 256, sent4);
        }
      };
      unsigned flo;
      unsigned ready = ~0u;
      int tr = 0;
      u32x4 cv[4];
#pragma unroll
      for (int ii = 0; ii < 4; ++ii) asm volatile("" : "=v"(cv[ii]));
      auto chkrd = [&](int t) {
#pragma unroll
        for (int ii = 0; ii < 4; ++ii) cv[ii] = lds_ld<u32x4>(aslot(t) + lin + ii * 4096);
      };
      auto chkbad = [&]() { return __any(max(umax4v(umax4v(0u, cv[0]), cv[1]), umax4v(umax4v(0u, cv[2]), cv[3])) == SENT) && !l0f; };
      auto refetch = [&](int t) __attribute__((always_inline)) {
        for (unsigned spins = 0; !dead; ++spins) {
          if (spins > SPIN_CAP) { dead = true; break; }
          WAITV(0);
          TRV(t == 1 ? 56 : 57, spins + 1);
          ldA(t);
          ldA(t + 1);
          WAITV(0);
          chkrd(t);
          if (!chkbad()) break;
          __builtin_amdgcn_s_sleep(1);
        }
      };
      if (l0f) {
        u32x2 gw2;
        ld8_asm(gw2, rs, WS_EPOCH + 0u * lin);
        TRC(44); BAR;
        ldW(0); ldA(0);
        dma1(mk_rsrc(p.wphi, 32 * 2048), vlin, j * 4096, lds0 + L_WPHI + hw * 1024);
        ldW(1);
        if (A1E) ldA(1);
        WAITV(A1E ? 21 : 17);
        OPAQUE_V(gw2);
        const u64 gword = ((u64)(unsigned)UNI(gw2[1]) << 32) | (unsigned)UNI(gw2[0]);
        u64 z = gword + 0x9e3779b97f4a7c15ull;
        z = (z ^ (z >> 30)) * 0xbf58476d1ce4e5b9ull;
        z = (z ^ (z >> 27)) * 0x94d049bb133111ebull;
        z = z ^ (z >> 31);
        z = (z == gword) ? ~z : z;
        epoch = u32x2{(unsigned)z, (unsigned)(z >> 32)};
        tr = (j & 3) + 8 * (g & 1);
        WAITV(A1E ? 11 : 7);
        TRC(1); BAR;
        if (!A1E) ldA(1);
      } else {
        ldW(1);
        if (XD) __builtin_amdgcn_s_sleep(XD);
        ldA(1);
      }
      ldA(2);
      TRC(40);
      ldW(2);
#pragma unroll
      for (int i = 0; i < 4; ++i) asm volatile("" : "=v"(xr[i >> 1][i & 1]));
      unsigned oA = offA(3), oW = 0;
      OPAQUE_S(oA);
      TRV(56, 0); TRV(57, 0);
      if (LAW == 3) ldW(3);
      if (tr == 0) ldres(0);
      clean(0);
      WAITV(4 + !STG + (W2F ? 0 : 6) + (LAW == 3 ? 6 : 0));
      chkrd(1);
      if (__builtin_expect(chkbad(), 0)) refetch(1);
      const int fr = lane & 15, fq = lane >> 4;
      u32x2 fl;
      f32x4 js[4], jc[4];
      float ra[4], rb[4];
      const bool slowo = SLOWC(2 + (l & 1), j);
      const char* pO;
      unsigned zO, bO, pcO, vO, clo;
      u32x4 wo2[2];
      asm volatile("" : "=v"(wo2[0])); asm volatile("" : "=v"(wo2[1]));
      const unsigned fwo = myrep + fl_line(FL_WO + (isb ? 1 : 0), jr + (lane >> 4)) + (lane & 15) * 8;
      static_for<1, 14>([&](auto t_c) __attribute__((always_inline)) {
        constexpr int t = decltype(t_c)::value;
        constexpr auto rest = [](int s) {
          return s == 0 ? (W2F ? 0 : 6) + (LAW == 3 ? 6 : 0) + !STG
                        : (s + LAW <= 15 ? 6 : 0) + (s < 4 && !STG) + (s == NVAL - 2 || s == 5 || s == 11) + ((s == 7 || s == 8) ? 4 : 0);
        };
        constexpr int NW = t == 1 ? 63 : 4 + rest(t - 1) + ((LAW == 3 && (t > 2 || W2F)) ? rest(t - 2) : 0);
        TRC(1 + t);
        if (t >= NVAL && t <= 6 && __builtin_expect((ready & (1u << kseq(t + 2))) == 0, 0)) {
          WAITV(NW); WAITL0; BAR;
          ready = poll_need(rs, flo, epoch, ready, 1u << kseq(t + 2), dead);
          dma4<4096>(rA, vA, oA, pcA, aslot(t + 2));
        } else if (t == 7 && __builtin_expect(ready != ~0u, 0)) {
          WAITV(NW); WAITL0; BAR;
          poll_all(rs, flo, epoch, dead);
          dma4<4096>(rA, vA, oA, pcA, aslot(t + 2));
        } else {
          bar_dma4<4096, NW>(rA, vA, oA, pcA, aslot(t + 2));
        }
        if (t == 1 && lead) set_flags(rs, fl_line(FL_X + l, g), j, l0f ? fl_line(FL_CS, j) : fl_line(FL_X + l, g), l0f ? g : j, epoch, lane);
        if (t == 7 && slowj) packedW();
        if (t + LAW <= 15) oW = offW(t + LAW);
        if (t < 13) oA = offA(t + 3);
        OPAQUE_S(oA); OPAQUE_S(oW);
        clean(t);
        if (t <= NVAL && !l0f) { WAITV(rest(t - 1) + 4 + (t < 4 && !STG)); chkrd(t + 1); }
        if (t == WT + 3 && l0f && SLOWC(2, jr + hw)) {
          OPAQUE_V(wo2[0]); OPAQUE_V(wo2[1]);
#pragma unroll
          for (int i = 0; i < 2; ++i) st16_sc1(rs, lin, WS_WO + (unsigned)((jr + hw) * 131072 + g * 8192 + ((j & 3) * 2 + i) * 1024), wo2[i]);
        }
        if (t == WT + 6 && l0f && lead) set_flags(rs, fl_line(FL_WO, j), g, fl_line(FL_WO, j), g, epoch, lane);
        if ((t < 4 || (t >= 8 && t < 12)) && tr == t) ldres(t % 3);
        if (t == 7) {
#pragma unroll
          for (int i = 0; i < 4; ++i) ld16_asm(jc[i], rs, fq * 768 + hw * 192 + (fr < 12 ? fr : 0) * 16, WS_CS + (unsigned)(((isb ? 16 : 0) + j) * 16 + i * 4) * 768);
        }
        if (t == 8) {
#pragma unroll
          for (int i = 0; i < 4; ++i) ld16_asm(js[i], rs, fq * 1024 + hw * 256 + fr * 16, st_off(g, l) + i * 4096);
        }
        SCHED;
        if (t + LAW <= 15) ldWh(t + LAW, oW, 0);
        SCHED;
        bool bad = false;
        if (t <= NVAL) bad = chkbad();
        if (t == 1) {
          const unsigned fx = fl_line(FL_X + l, g), fc = fl_line(l0f ? FL_CS : FL_PK, jr) + ((j + fq) & 3) * 128;
          flo = myrep + ((l < 2 && lane >= 16) ? fc : fx) + fr * 8;
          OPAQUE_V(flo);
        }
        if (t == 9) {
          pO = slowo ? p.ws : (const char*)(isb ? p.woB : p.woA);
          zO = slowo ? WS_SPAN : 1024 * 2048; bO = slowo ? WS_WO + (unsigned)((isb ? 16 : 0) + j) * 131072 : j * 128; pcO = slowo ? 4096 : 32 * 2048;
          vO = slowo ? vlin : (hw * 8 + (lane >> 3)) * 2048 + wo_swz(lane);
          clo = myrep + ((lane & 16) ? fl_line(FL_CLC + l, (lane & 32) ? j ^ RX : j) : fl_line(FL_CLR + l, g)) + fr * 8;
          OPAQUE_S(pO); OPAQUE_S(zO); OPAQUE_S(bO); OPAQUE_S(pcO); OPAQUE_V(vO); OPAQUE_V(clo);
        }
        if (t == 10) {
#pragma unroll
          for (int i = 0; i < 4; ++i) OPAQUE_V(jc[i]);
          const f32x4 s = (jc[0] + jc[1]) + (jc[2] + jc[3]);
#pragma unroll
          for (int i = 0; i < 4; ++i) { ra[i] = s[i]; OPAQUE_V(ra[i]); }
        }
        if (t == 11) {
#pragma unroll
          for (int i = 0; i < 4; ++i) OPAQUE_V(js[i]);
          const f32x4 s = (js[0] + js[1]) + (js[2] + js[3]);
#pragma unroll
          for (int i = 0; i < 4; ++i) { rb[i] = s[i]; OPAQUE_V(rb[i]); }
        }
        if (t == 12) {
          red2_add(ra[0], ra[1]); red2_add(ra[2], ra[3]); red2_add(rb[0], rb[1]); red2_add(rb[2], rb[3]);
          if (lane < 12) lds_st<f32x4>(lds0 + L_CS + hw * 192 + lane * 16, f32x4{ra[0], ra[1], ra[2], ra[3]});
#pragma unroll
          for (int i = 0; i < 4; ++i) OPAQUE_V(rb[i]);
        }
        if (t == 13) {
          const float m0 = rb[0] * (1.0f / 1024), m1 = rb[2] * (1.0f / 1024);
          const float r0 = __builtin_amdgcn_rsqf(fmaxf(__builtin_fmaf(rb[1], 1.0f / 1024, -m0 * m0), 0.f) + 1e-5f);
          const float r1 = __builtin_amdgcn_rsqf(fmaxf(__builtin_fmaf(rb[3], 1.0f / 1024, -m1 * m1), 0.f) + 1e-5f);
          if (lane < 16) {
            lds_st<f32x2>(lds0 + L_MEAN + 64 + hw * 128 + fr * 8, f32x2{-m0, -m1});
            lds_st<f32x2>(lds0 + L_RSTD + 64 + hw * 128 + fr * 8, f32x2{r0, r1});
          }
        }
        SCHED;
        if (t + LAW <= 15) ldWh(t + LAW, oW, 1);
        if (t == NVAL - 1 || t == 6) {
          WAITV(10);
          OPAQUE_V(fl);
          if (t == 6) ready = __all(is_set(fl, epoch)) ? ~0u : 0u;
          else {
            ready = l0f ? ~0u : (unsigned)__ballot(is_set(fl, epoch));
            if (slowj && __all(is_set(fl, epoch))) packedW();
          }
        }
        if (t == 12) { WAITV(10); OPAQUE_V(fl); }
        if (t == 13 && slowo && __builtin_expect(!__all(is_set(fl, epoch)), 0)) poll_all(rs, fwo, epoch, dead);
        if (t == NVAL - 2 || t == 5) ld8_asm(fl, rs, flo);
        if (t == 11) ld8_asm(fl, rs, fwo);
        if (t == WT && l0f) {
#pragma unroll
          for (int i = 0; i < 2; ++i) ld16in_asm(wo2[i], mk_rsrc(p.woA, 1024 * 2048), (lane >> 3) * 2048 + wo_swz(lane), (g * 64 + (j & 3) * 16 + i * 8) * 2048 + (jr + hw) * 128);
        }
        if constexpr (t <= NVAL) { if (__builtin_expect(bad, 0)) refetch(t + 1); }
      });
      auto stO = [&](int i) { dma4<4096>(mk_rsrc(pO, zO), vO, bO + wo_src(i, j) * (4 * pcO), pcO, lds0 + wo_slot(i) + hw * 1024); };
      auto clcheck = [&]() {
        OPAQUE_V(fl);
        TRV(58, 0);
        if (__builtin_expect(!__all(is_set(fl, epoch)), 0)) { TRV(58, 1); poll_all(rs, clo, epoch, dead); }
      };
      auto accinit = [&]() {
#pragma unroll
        for (int m = 0; m < 2; ++m)
#pragma unroll
          for (int pp = 0; pp < 2; ++pp) {
            auto r0_ = __builtin_amdgcn_permlane16_swap(xr[m][pp][0], xr[m][pp][2], false, false);
            auto r1_ = __builtin_amdgcn_permlane16_swap(xr[m][pp][1], xr[m][pp][3], false, false);
            acc[m][2 * pp] = f32x4{lo16(r0_[0]), hi16(r0_[0]), lo16(r1_[0]), hi16(r1_[0])};
            acc[m][2 * pp + 1] = f32x4{lo16(r0_[1]), hi16(r0_[1]), lo16(r1_[1]), hi16(r1_[1])};
          }
#pragma unroll
        for (int i = 0; i < 8; ++i) OPAQUE_V(acc[i >> 2][i & 3]);
      };
      WAITV(10);
      WAITL0;
      TRC(15); BAR;
      if (lead) set_flags(rs, fl_line(FL_CLR + l, g), j, fl_line(FL_CLC + l, j), g, epoch, lane);
      if (isb) {
        stO(0); stO(2); stO(3);
        WAITV(12);
        TRC(16); BAR;
        stO(1); stO(4); stO(5);
        __builtin_amdgcn_s_setprio(0);
        TRC(17); BAR;
        __builtin_amdgcn_s_sleep(BD);
        stO(7); stO(6);
        ld8_asm(fl, rs, clo);
        TRC(18); BAR;
        accinit();
        WAITV(0);
        clcheck();
      } else {
        u32x4 vb[8];
#pragma unroll
        for (int i = 0; i < 8; ++i) asm volatile("" : "=v"(vb[i]));
        stO(0); stO(2); stO(3);
        WAITV(12);
        TRC(16); BAR;
        stO(1);
        if (l0f) {
          const bool wj = hw != 0;
          const char* bp = (const char*)(wj ? p.wqkvB : p.woB);
          unsigned rs_ = wj ? 6144 : 2048, sz = rs_ * 1024, ss = rs_ * 8, s2 = wj ? 16 * 6144 : 128;
          unsigned sb = g * 64 * rs_ + (wj ? (hw - 1) * 2048 + JOBC(j, hw - 1) * 128 : (j & 3) * (16 * 2048) + jr * 128);
          OPAQUE_S(bp); OPAQUE_S(rs_); OPAQUE_S(sz); OPAQUE_S(sb); OPAQUE_S(ss); OPAQUE_S(s2);
          const rsrc_t rb = mk_rsrc(bp, sz);
#pragma unroll
          for (int i = 0; i < 8; ++i) ld16in_asm(vb[i], rb, (lane >> 3) * rs_ + (lane & 7) * 16, sb + (i & 1) * ss + (i >> 1) * s2);
        }
        __builtin_amdgcn_s_setprio(0);
        KV_ADDR;
        bf16x8 sel = rowsum_sel(lane);
        unsigned h1 = (lane >> 3) * KVP_G + (lane & 7) * 16;
        unsigned pw = lds0 + SC_PHIK + (h * 128 + wm * 64 + 32 + fr) * 160 + fq * 8, kw = lds0 + L_PREV + hw * 2048 + lin;
        const unsigned wa = lds0 + L_WPHI + h * 2048 + (fq * 4 + (fr >> 2)) * 64 + (lane & 3) * 8;
        bf16x8 w0 = tr2(wa, wa + 1024), w1 = tr2(wa + 32, wa + 32 + 1024);
        float cq[4];
#pragma unroll
        for (int m = 0; m < 2; ++m) { cq[m] = lds_ld<float>(lds0 + L_RSTD + (16 + wm * 64 + 32 + m * 16 + fr) * 4) * 1.4426950408889634f; OPAQUE_V(cq[m]); }
        OPAQUE_V(sel); OPAQUE_V(h1); OPAQUE_V(pw); OPAQUE_V(kw); OPAQUE_V(w0); OPAQUE_V(w1);
        TRC(17); BAR;
        TRC(18); BAR;
        ld8_asm(fl, rs, clo);
        {
          const f32x4 zero = {0.f, 0.f, 0.f, 0.f};
          f32x4 r[4][2];
#pragma unroll
          for (int m = 0; m < 2; ++m) {
            const bf16x8 f = lds_ld<bf16x8>(kw + m * 1024);
            r[m][0] = MFMA(w0, f, zero); r[m][1] = MFMA(w1, f, zero);
          }
          phik2(r, cq, pw);
        }
        WAITV(0);
#pragma unroll
        for (int i = 0; i < 8; ++i) OPAQUE_V(vb[i]);
        clcheck();
        WAITL0;
        TRC(19); BAR;
        kvpart(va, pa, kvj + (unsigned)g * KVP_G, lane, wave);
        CLOBBER;
        TRC(20); BAR;
        auto job = [&]() __attribute__((always_inline)) {
        if (l0f) {
          if (hw) {
            job_colsum(rs, vb, 1, g, JOBC(j, hw - 1), hw - 1, lane);
            if (SLOWC(1, JOBC(j, hw - 1))) {
#pragma unroll
              for (int i = 0; i < 8; ++i) job_pack(rs, vb[i], i, 1, g, JOBC(j, hw - 1), hw - 1, lane);
            }
          } else {
#pragma unroll
            for (int i = 0; i < 8; ++i)
              if (SLOWC(3, jr + (i >> 1))) st16_sc1(rs, (lane >> 3) * 128 + wo_swz(lane), WS_WO + (unsigned)((16 + jr + (i >> 1)) * 131072 + g * 8192 + ((j & 3) * 2 + (i & 1)) * 1024), vb[i]);
          }
        }
        };
        if (JOBP) job();
        if (H1D > 0) stO(4);
        if (H1D > 1) stO(5);
        const f32x4 zero = {0.f, 0.f, 0.f, 0.f};
        const unsigned s1 = kvj + (unsigned)((j ^ RX) - j) * KVP_J + g * 512 + hw * 128;
        if (H1S) __builtin_amdgcn_s_sleep(H1S);
        u32x4 v[2] = {ld16p<PA1>(rs, h1, s1), ld16p<PA1>(rs, h1, s1 + 8 * KVP_G)};
        TRC(21); TRV(50, 0);
        for (unsigned spins = 0; !dead && __any(umax4v(umax4v(0u, v[0]), v[1]) == SENT); ++spins) {
          if (spins > SPIN_CAP) { dead = true; break; }
          CLOBBER;
          TRV(50, spins + 1);
          if (H1P) __builtin_amdgcn_s_sleep(H1P);
          v[0] = ld16p<PA1>(rs, h1, s1); v[1] = ld16p<PA1>(rs, h1, s1 + 8 * KVP_G);
        }
        TRC(22);
        const f32x4 c = rowsum_fin(MFMA(sel, as_frag(v[1]), MFMA(sel, as_frag(v[0]), zero)));
        if ((lane & 40) == 0)
          st8_sc1(rs, fr * 16 + fq * 8, kvj + (unsigned)g * KVP_G + KVP_SUM + hw * 128, u32x2{min(pk2(c[0], c[1]), SENT - 1), min(pk2(c[2], c[3]), SENT - 1)});
        CLOBBER;
        TRC(43); BAR;
        if (JOBP && l0f && lead) set_flags(rs, fl_line(FL_PK, j), g, fl_line(FL_WO + 1, j), g, epoch, lane);
        accinit();
        if (H2D) __builtin_amdgcn_s_sleep(H2D);
        if (H1D < 1) stO(4);
        if (H1D < 2) stO(5);
        stO(6); stO(7);
        if (!JOBP) job();
        WAITV(0);
      }
    }
    {
      LOCALS;
      const int fr = lane & 15, fq = lane >> 4;
      const int kh = half;
      unsigned v0 = lane * 16 + hw * 4096 + kh * 2048, v1 = lane * 16 + hw * 4096 + (kh ^ 1) * 2048;
      const int r0 = (JROT(j) >> 1) + 2;
      const unsigned ob0 = ob_off(g, kh, l);
      const unsigned pko = myrep + fl_line(FL_PK, JOBC(j, fq - 1)) + fr * 8;
      u32x4 R[8][4];
      u32x2 pkf;
      auto issue_ = [&](int i) {
        const unsigned so = ob0 + (unsigned)((r0 + i) & 7) * (2 * TS);
        R[i][0] = ld16_sc1(rs, v0, so); R[i][1] = ld16_sc1(rs, v1, so);
        R[i][2] = ld16_sc1(rs, v0 + 1024, so); R[i][3] = ld16_sc1(rs, v1 + 1024, so);
      };
      auto issue = [&](int i) { issue_(i); SCHED; };
      constexpr int LA = LOOKAHEAD;
      TRV(52, 0); TRV(53, 0); TRV(54, 0);
      TRR(61);
      __builtin_amdgcn_s_setprio(3);
      unsigned so0 = ob0 + (unsigned)(r0 & 7) * (2 * TS);
      OPAQUE_V(v0); OPAQUE_V(v1); OPAQUE_S(so0);
      TRC(25); BAR;
      SCHED;
      R[0][0] = ld16p<PA3>(rs, v0, so0); R[0][1] = ld16p<PA3>(rs, v1, so0);
      R[0][2] = ld16p<PA3>(rs, v0 + 1024, so0); R[0][3] = ld16p<PA3>(rs, v1 + 1024, so0);
      SCHED;
      __builtin_amdgcn_s_setprio(2);
      if (!JOBP && l == 0 && lead) set_flags(rs, fl_line(FL_PK, j), g, fl_line(FL_WO + 1, j), g, epoch, lane);

      auto vmax = [&](int i) { return umax4v(umax4v(umax4v(umax4v(0u, R[i][0]), R[i][1]), R[i][2]), R[i][3]); };
      auto bad = [&](int i) { return __any(umax4v(umax4v(umax4v(umax4v(0u, R[i][0]), R[i][1]), R[i][2]), R[i][3]) == SENT); };
      auto poll = [&](int i, int nfl) {
        for (unsigned spins = 0; !dead; ++spins) {
          if (spins > SPIN_CAP) { dead = true; break; }
          __builtin_amdgcn_s_sleep(2);
          CLOBBER;
          TRV(52 + (i > 2 ? 2 : i), spins + 1);
          issue(i);
#pragma unroll
          for (int k = i + 1; k < i + nfl && k < 8; ++k)
            if (bad(k)) issue(k);
          WAITV(0);
#pragma unroll
          for (int k = i + 1; k < i + nfl && k < 8; ++k)
#pragma unroll
            for (int f = 0; f < 4; ++f) OPAQUE_V(R[k][f]);
          if (!bad(i)) break;
        }
      };
      unsigned bb[4];
      {
        const int kr = fq * 4 + (fr >> 2);
        const int gs = (kr >> 1) & 3;
#pragma unroll
        for (int c = 0; c < 4; ++c) bb[c] = lds0 + kh * 8192 + kr * 128 + ((c ^ gs) * 32) + (lane & 3) * 8;
      }
      bf16x8 bf[2][4];
      auto rdB = [&](int i, int hh) {
        if (hh == 0) {
#pragma unroll
          for (int c = 0; c < 4; ++c) { bb[c] += wo_slot(i) - (i ? wo_slot(i - 1) : 0u); asm("" : "+v"(bb[c])); }
        }
#pragma unroll
        for (int c = 0; c < 4; ++c) bf[hh][c] = tr2(bb[c] + hh * 4096, bb[c] + hh * 4096 + 2048);
      };
      auto mms = [&](int i, int hh) {
#pragma unroll
        for (int n = 0; n < 4; ++n)
#pragma unroll
          for (int m = 0; m < 2; ++m) acc[m][n] = MFMA(bf[hh][n], as_frag(R[i][hh * 2 + m]), acc[m][n]);
      };
      if (!half) {
#pragma unroll
        for (int i = 0; i < 8; ++i) acc[i >> 2][i & 3] = f32x4{0.f, 0.f, 0.f, 0.f};
      }
      const char* obase = (l == 3) ? (const char*)p.out : (const char*)p.ws;
      unsigned osize = (l == 3) ? 2048 * 2048 : WS_SPAN;
      const bool slown = SLOWC((l & 1) ^ 1, j);
      pW = slown ? p.ws : (const char*)(isb ? p.wqkvA : p.wqkvB);
      bW = slown ? WS_WP + (unsigned)((isb ? 0 : 16) + j) * (16 * 24576) : j * 128;
      kW = slown ? 24576 : 64 * 6144; zW = slown ? WS_SPAN : 1024 * 6144; pcW = slown ? 8192 : 2048; hW = slown ? 4096 : 32 * 6144;
      vA = hw * 1024 + lane * 16;
      vW = slown ? vA : (hw * 8 + (lane >> 3)) * 6144 + (((lane & 7) ^ ((((lane >> 4) & 1) | ((hw & 1) << 1)) << 1)) * 16);
      pA = p.ws; zA = WS_SPAN; bA = xb_off(g, l); kA = TS; pcA = 4096; kb = JROT(j); dj = j & 3;
      OPAQUE_S(pW); OPAQUE_S(bW); OPAQUE_S(kW); OPAQUE_S(zW); OPAQUE_S(pcW); OPAQUE_S(hW); OPAQUE_V(vA); OPAQUE_V(vW); OPAQUE_S(bA);
      unsigned sq = bW + j * kW + kh * hW, dst = lds0 + L_R1 + kh * 4096 + hw * 1024;
      OPAQUE_S(obase); OPAQUE_S(osize); OPAQUE_S(sq); OPAQUE_S(dst);
      const rsrc_t ro = mk_rsrc(obase, osize);
      unsigned so[2];
      unsigned xw = lds0 + SC_XCH + wave * 4096 + lane * 16;
      unsigned xa = lds0 + SC_XCH + (wave ^ 4) * 4096 + lane * 16;
      unsigned sb0 = (l == 3) ? g * (128 * 2048) + j * 128 : xb_off(g, l) + j * TS;
      {
        const int row = (hw * 2 + kh) * 16 + fr, ch = (fq & 1) * 2 + (fq >> 1);
        const unsigned sw = (l == 3) ? 0u : (unsigned)((row >> 1) & 7), rb = (unsigned)row << ((l == 3) ? 11 : 7);
#pragma unroll
        for (int pp = 0; pp < 2; ++pp) { so[pp] = rb + (((ch + 4 * pp) ^ sw) * 16); OPAQUE_V(so[pp]); }
        OPAQUE_V(xa); OPAQUE_V(xw); OPAQUE_S(sb0);
      }
      SCHED;
#pragma unroll
      for (int i = 1; i < LA; ++i) issue(i);
      rdB(0, 0);
      unsigned mx = vmax(0);
#pragma unroll
      for (int i = 0; i < 8; ++i) {
        SCHED;
        if (__builtin_expect(__any(mx == SENT), 0)) poll(i, LA);
        if (i == 0) TRC(27); else TRQ(27 + i);
        if (i == W1S - 2) { pkf = ld8_sc1(rs, pko, 0); SCHED; }
        if (i == W1S) {
          OPAQUE_V(pkf);
          if (l == 0 && lead && slown && !__all(is_set(pkf, epoch))) poll_all(rs, pko, epoch, dead);
          TRC(35); BAR;
        }
        if (i == 7 && l < 3) dma3<8192>(mk_rsrc(pW, zW), vW, sq, pcW, dst);
        if (i == 2) __builtin_amdgcn_s_setprio(1);
        if (i == 5) __builtin_amdgcn_s_setprio(0);
        SCHED;
        rdB(i, 1);
        if (i == 7) break;
        if (i + LA < 8) issue_(i + LA);
        mms(i, 0);
#pragma unroll
        for (int k = 0; k < 4; ++k) { SGB(0x008, 1); SGB(0x100, 1); SGB(0x020, 1); SGB(0x008, 1); SGB(0x100, 1); }
        SCHED;
        rdB(i + 1, 0); mx = vmax(i + 1);
        mms(i, 1);
#pragma unroll
        for (int k = 0; k < 4; ++k) { SGB(0x008, 1); SGB(0x100, 2); }
#pragma unroll
        for (int k = 4; k < 8; ++k) { SGB(0x008, 1); SGB(0x002, 8 / (8 - 4)); }
      }
#pragma unroll
      for (int n = 0; n < 4; ++n) acc[1][n] = MFMA(bf[0][n], as_frag(R[7][1]), acc[1][n]);
#pragma unroll
      for (int n = 0; n < 4; ++n) acc[0][n] = MFMA(bf[0][n], as_frag(R[7][0]), acc[0][n]);
#pragma unroll
      for (int k = 0; k < 8; ++k) { SGB(0x008, 1); SGB(0x100, 1); }
      SCHED;
#pragma unroll
      for (int n = 0; n < 4; ++n) acc[1][n] = MFMA(bf[1][n], as_frag(R[7][3]), acc[1][n]);
#pragma unroll
      for (int n = 0; n < 4; ++n) acc[0][n] = MFMA(bf[1][n], as_frag(R[7][2]), acc[0][n]);
#pragma unroll
      for (int n = 0; n < 4; ++n) lds_st<f32x4>(xw + n * 1024, acc[1][n]);
      SGB(0x008, 4);
#pragma unroll
      for (int k = 0; k < 4; ++k) { SGB(0x008, 1); SGB(0x200, 1); }
      SCHED;
      TRC(36);
      WAITL0;
      TRC(37); BAR;
      SCHED;
      f32x4 pa[4];
#pragma unroll
      for (int n = 0; n < 4; ++n) pa[n] = lds_ld<f32x4>(xa + n * 1024);
      SCHED;
      u32x4 o[2];
#pragma unroll
      for (int pp = 0; pp < 2; ++pp) {
        const f32x4 a = acc[0][2 * pp] + pa[2 * pp], b = acc[0][2 * pp + 1] + pa[2 * pp + 1];
        auto r0_ = __builtin_amdgcn_permlane16_swap(pk2(a[0], a[1]), pk2(b[0], b[1]), false, false);
        auto r1_ = __builtin_amdgcn_permlane16_swap(pk2(a[2], a[3]), pk2(b[2], b[3]), false, false);
        o[pp] = nosent(u32x4{r0_[0], r1_[0], r0_[1], r1_[1]});
        st16_sc1(ro, so[pp], sb0, o[pp]);
      }
      if (l < 3) {
        lds_st<u32x4>(lds0 + L_R2 + so[0], o[0]);
        lds_st<u32x4>(lds0 + L_R2 + so[1], o[1]);
        const f32x4 zero = {0.f, 0.f, 0.f, 0.f};
        const bf16x8 ones = {0x3f80, 0x3f80, 0x3f80, 0x3f80, 0x3f80, 0x3f80, 0x3f80, 0x3f80};
        const f32x4 a = MFMA(ones, as_frag(o[1]), MFMA(ones, as_frag(o[0]), zero));
        const f32x4 b = MFMA(as_frag(o[1]), as_frag(o[1]), MFMA(as_frag(o[0]), as_frag(o[0]), zero));
        const float q = (fr & 2) ? ((fr & 1) ? b[3] : b[2]) : ((fr & 1) ? b[1] : b[0]);
        if (fq == (fr >> 2)) st8_sc1(rs, ((hw * 2 + kh) * 16 + fr) * 8, st_off(g, l + 1) + j * 1024, u32x2{__float_as_uint(a[0]), __float_as_uint(q)});
      } else if (lead && blockIdx.x == 0 && lane == 0) {
        st8_sc1(rs, WS_EPOCH, 0, epoch);
      }
    }
    TRC(38); TRR(63);
  }
  STAMP_DUMP;
  asm volatile(".Lpf_end:");
}

extern "C" void kernel_launch(void* const* d_in, const int* in_sizes, int n_in, void* d_out, int out_size,
                              void* d_ws, size_t ws_size, hipStream_t stream) {
  Params p;
  p.x = (const u16*)d_in[0];
  p.wqkvA = (const u16*)d_in[1];
  p.wphi = (const u16*)d_in[2];
  p.woA = (const u16*)d_in[3];
  p.wqkvB = (const u16*)d_in[5];
  p.woB = (const u16*)d_in[6];
  p.out = (u16*)d_out;
  p.ws = (char*)d_ws;
  (void)hipFuncSetAttribute((const void*)mega_kernel, hipFuncAttributeMaxDynamicSharedMemorySize, L_TOTAL);
  mega_kernel<<<256, 512, L_TOTAL, stream>>>(p);
}
```

```cpp
#include <hip/hip_runtime.h>
#include <stdint.h>
#include <type_traits>
constexpr int LOOKAHEAD = 2;
constexpr int NVAL = 4;
#define XD 0
#define WT 1
#define LAW 2
#define POL0 0
#define PKALL 0u
#define PA1 16
#define PA2 16
#define PA3 16
#define H1S 0
#define H1P 0
#define SPR 2
#define A1E 0
#define KG 32768
#define TS 17664
#define KOD 0
#define STG 1
#define JOBP 0
#define NORS 0
#define DLY 0
#define PRE 0
#define PACE 0
#define NJ 8
#define KX 0
#define ROT 1
#define BD 14
#define W1S 7
#define W2F 0
#define RX 8
#define H1D 2
#define H2D 5

typedef unsigned short u16;
typedef unsigned long long u64;
typedef __attribute__((ext_vector_type(8))) short bf16x8;
typedef __attribute__((ext_vector_type(4))) short s16x4;
typedef __attribute__((ext_vector_type(4))) float f32x4;
typedef __attribute__((ext_vector_type(2))) float f32x2;
typedef __attribute__((ext_vector_type(4))) unsigned u32x4;
typedef __attribute__((ext_vector_type(2))) unsigned u32x2;
typedef __attribute__((ext_vector_type(2))) __bf16 bf16x2_t;
#define LAS __attribute__((address_space(3)))
typedef LAS s16x4* lds_s16x4_ptr;

constexpr unsigned NREP = 8, REP_STRIDE = 17 * 2048;
enum { FL_X = 0, FL_CS = 4, FL_CLR = 5, FL_CLC = 9, FL_PK = 13, FL_WO = 14 };
__device__ __forceinline__ unsigned fl_line(int set, int a) { return (unsigned)(set * 16 + a) * 128; }
constexpr unsigned WS_EPOCH = 512u << 10;
constexpr unsigned WS_CS = 1u << 20;
constexpr unsigned WS_KVP = 4u << 20;
constexpr unsigned KVP_G = KG, KVP_J = 16 * KVP_G, KVP_L = KG == 32768 ? 16384 : 16 * KVP_J, KVP_Z = 8192, KVP_SUM = 8704, KVP_HALO = 12u << 10;
constexpr unsigned SENT = 0xffffffffu;
constexpr unsigned WS_X = 14u << 20, WS_O = 29u << 20, WS_ST = 2u << 20;
__device__ __forceinline__ unsigned xb_off(int g, int l) { return WS_X + (unsigned)(l * 16 + g) * (16 * TS); }
__device__ __forceinline__ unsigned ob_off(int g, int j, int l) { return WS_O + (unsigned)((l * 16 + g) * 16 + j) * TS; }
__device__ __forceinline__ unsigned st_off(int g, int l) { return WS_ST + (unsigned)(l * 16 + g) * (16u << 10); }
constexpr unsigned WS_WP = 48u << 20;
constexpr unsigned WS_WO = 60u << 20;
constexpr unsigned WS_SPAN = 64u << 20;

constexpr unsigned L_R1 = 0;
constexpr unsigned L_R2 = 98304, XS = 16384;
constexpr unsigned L_MISC = 147456;
constexpr unsigned L_MEAN = L_MISC;
constexpr unsigned L_RSTD = L_MISC + 576;
constexpr unsigned L_CS = L_MISC + 1152;
constexpr unsigned L_WPHI = L_MISC + 2688;
constexpr unsigned L_SINK = L_MISC + 6912;
constexpr unsigned L_ZF = L_MISC + 1920;
constexpr unsigned L_PREV = L_MISC + 8192;
constexpr unsigned L_TOTAL = 163840;

#define JROT(j) ((j) & ~(NJ - 1))
constexpr int NX = NJ / 2;
#define KB0(g, j) (JROT(j) + (16 / NX) * ((g) & (NX - 1)))
#define JOBC(j, w) (((j) & ~3) + (((j) + (w) + 1) & 3))

#define SLOWC(m, jc) ((slowm >> ((m) * 8 + ((jc) & 7))) & 1)

struct Params {
  const u16* x;
  const u16* wqkvA;
  const u16* wphi;
  const u16* woA;
  const u16* wqkvB;
  const u16* woB;
  u16* out;
  char* ws;
};

#define SCHED __builtin_amdgcn_sched_barrier(0)
#define BAR __builtin_amdgcn_s_barrier()
#define SGB(m, n) __builtin_amdgcn_sched_group_barrier(m, n, 0)
#define WAITV(n) asm volatile("s_waitcnt vmcnt(%0)" ::"n"(n) : "memory")
#define WAITL0 asm volatile("s_waitcnt lgkmcnt(0)" ::: "memory")

#define TRV(slot, val) do {} while (0)
#define TRQ(slot) do {} while (0)
#define TRC(slot) do {} while (0)
#define TRR(slot) do {} while (0)
#define STAMP_INIT do {} while (0)
#define STAMP_DUMP do {} while (0)

__device__ __forceinline__ unsigned pk2(float a, float b) {
  f32x2 v = {a, b};
  return __builtin_bit_cast(unsigned, __builtin_convertvector(v, bf16x2_t));
}
__device__ __forceinline__ float lo16(unsigned u) { return __uint_as_float(u << 16); }
__device__ __forceinline__ float hi16(unsigned u) { return __uint_as_float(u & 0xffff0000u); }

template <int A, int B, typename F> __device__ __forceinline__ void static_for(F f) {
  if constexpr (A < B) { f(std::integral_constant<int, A>{}); static_for<A + 1, B>(f); }
}
typedef __amdgpu_buffer_rsrc_t rsrc_t;
__device__ __forceinline__ rsrc_t mk_rsrc(const void* p, unsigned bytes) { return __builtin_amdgcn_make_buffer_rsrc((void*)p, 0, bytes, 0x00020000); }
#define UNI(x) __builtin_amdgcn_readfirstlane(x)
#define DMA_HEAD(P) "s_mov_b32 m0, %4\n\ts_mov_b32 %0, %3\n\ts_nop 2\n\tbuffer_load_dwordx4 %1, %2, %0 offen " P " lds\n\t"
#define DMA_NEXT(P) "s_add_u32 m0, m0, %6\n\ts_add_u32 %0, %0, %5\n\tbuffer_load_dwordx4 %1, %2, %0 offen " P " lds\n\t"
#define DMA_OPS : "=&s"(t) : "v"(voff), "s"(r), "s"(UNI(soff)), "s"(UNI(lds)), "s"(UNI(sstep)), "i"(LSTEP) : "memory", "scc", "m0"
template <int LSTEP, int POL = 1> __device__ __forceinline__ void dma4(rsrc_t r, unsigned voff, unsigned soff, unsigned sstep, unsigned lds) {
  unsigned t;
  if constexpr (POL == 1) asm volatile(DMA_HEAD("sc1") DMA_NEXT("sc1") DMA_NEXT("sc1") DMA_NEXT("sc1") DMA_OPS);
  else if constexpr (POL == 2) asm volatile(DMA_HEAD("nt") DMA_NEXT("nt") DMA_NEXT("nt") DMA_NEXT("nt") DMA_OPS);
  else asm volatile(DMA_HEAD("") DMA_NEXT("") DMA_NEXT("") DMA_NEXT("") DMA_OPS);
}
template <int LSTEP, int POL = 1> __device__ __forceinline__ void dma3(rsrc_t r, unsigned voff, unsigned soff, unsigned sstep, unsigned lds) {
  unsigned t;
  if constexpr (POL == 1) asm volatile(DMA_HEAD("sc1") DMA_NEXT("sc1") DMA_NEXT("sc1") DMA_OPS);
  else if constexpr (POL == 2) asm volatile(DMA_HEAD("nt") DMA_NEXT("nt") DMA_NEXT("nt") DMA_OPS);
  else asm volatile(DMA_HEAD("") DMA_NEXT("") DMA_NEXT("") DMA_OPS);
}
template <int LSTEP, int N> __device__ __forceinline__ void bar_dma4(rsrc_t r, unsigned voff, unsigned soff, unsigned sstep, unsigned lds) {
  unsigned t;
  asm volatile("s_mov_b32 m0, %4\n\ts_mov_b32 %0, %3\n\ts_nop 0\n\ts_waitcnt vmcnt(%7) lgkmcnt(0)\n\ts_barrier\n\t"
               "buffer_load_dwordx4 %1, %2, %0 offen sc1 lds\n\t" DMA_NEXT("sc1") DMA_NEXT("sc1") DMA_NEXT("sc1")
               : "=&s"(t) : "v"(voff), "s"(r), "s"(UNI(soff)), "s"(UNI(lds)), "s"(UNI(sstep)), "i"(LSTEP), "n"(N) : "memory", "scc", "m0");
}
__device__ __forceinline__ void dma1(rsrc_t r, unsigned voff, unsigned soff, unsigned lds) {
  asm volatile("s_mov_b32 m0, %3\n\ts_nop 3\n\tbuffer_load_dwordx4 %0, %1, %2 offen sc1 lds" ::"v"(voff), "s"(r), "s"(UNI(soff)), "s"(UNI(lds)) : "memory", "m0");
}
__device__ __forceinline__ void touch(rsrc_t r, unsigned voff, unsigned lds) {
  asm volatile("s_mov_b32 m0, %2\n\ts_nop 3\n\tbuffer_load_dword %0, %1, 0 offen lds" ::"v"(voff), "s"(r), "s"(UNI(lds)) : "memory", "m0");
}
__device__ __forceinline__ void ld8_asm(u32x2& d, rsrc_t r, unsigned voff) {
  asm volatile("s_nop 4\n\tbuffer_load_dwordx2 %0, %1, %2, 0 offen sc1" : "=v"(d) : "v"(voff), "s"(r) : "memory");
}

__device__ __forceinline__ void ld16in_asm(u32x4& d, rsrc_t r, unsigned voff, unsigned soff) {
  asm volatile("s_nop 4\n\tbuffer_load_dwordx4 %0, %1, %2, %3 offen" : "=v"(d) : "v"(voff), "s"(r), "s"(UNI(soff)) : "memory");
}
__device__ __forceinline__ void ld16_asm(f32x4& d, rsrc_t r, unsigned voff, unsigned soff) {
  asm volatile("s_nop 4\n\tbuffer_load_dwordx4 %0, %1, %2, %3 offen sc1" : "=v"(d) : "v"(voff), "s"(r), "s"(UNI(soff)) : "memory");
}

template <typename T> __device__ __forceinline__ T lds_ld(unsigned a) { return *(LAS T*)(a); }
template <typename T> __device__ __forceinline__ void lds_st(unsigned a, T v) { *(LAS T*)(a) = v; }
__device__ __forceinline__ bf16x8 tr2(unsigned a0, unsigned a1) {
  s16x4 v0 = __builtin_amdgcn_ds_read_tr16_b64_v4i16((lds_s16x4_ptr)(a0));
  s16x4 v1 = __builtin_amdgcn_ds_read_tr16_b64_v4i16((lds_s16x4_ptr)(a1));
  return __builtin_shufflevector(v0, v1, 0, 1, 2, 3, 4, 5, 6, 7);
}
__device__ __forceinline__ bf16x8 as_frag(u32x4 v) { return __builtin_bit_cast(bf16x8, v); }
#define MFMA(a, b, c) __builtin_amdgcn_mfma_f32_16x16x32_bf16(a, b, c, 0, 0, 0)

__device__ __forceinline__ float vmax2(float a, float b) { float inf = __builtin_inff(); asm("" : "+s"(inf)); return __builtin_amdgcn_fmed3f(a, b, inf); }
#define SWAP16(a, b) __builtin_amdgcn_permlane16_swap(__float_as_uint(a), __float_as_uint(b), false, false)
#define SWAP32(a, b) __builtin_amdgcn_permlane32_swap(__float_as_uint(a), __float_as_uint(b), false, false)
__device__ __forceinline__ void red2_max(float& a, float& b) {
  auto r = SWAP16(a, b);
  float x = vmax2(__uint_as_float(r[0]), __uint_as_float(r[1]));
  r = SWAP32(x, x);
  x = vmax2(__uint_as_float(r[0]), __uint_as_float(r[1]));
  r = SWAP16(x, x);
  a = __uint_as_float(r[0]); b = __uint_as_float(r[1]);
}
__device__ __forceinline__ void red2_add(float& a, float& b) {
  auto r = SWAP16(a, b);
  float x = __uint_as_float(r[0]) + __uint_as_float(r[1]);
  r = SWAP32(x, x);
  x = __uint_as_float(r[0]) + __uint_as_float(r[1]);
  r = SWAP16(x, x);
  a = __uint_as_float(r[0]); b = __uint_as_float(r[1]);
}
template <bool MAX, typename M> __device__ __forceinline__ void red4(float& a, float& b, float& c, float& d, M mid) {
  auto op = [](unsigned x, unsigned y) { return MAX ? vmax2(__uint_as_float(x), __uint_as_float(y)) : __uint_as_float(x) + __uint_as_float(y); };
  auto r = SWAP16(a, b), q = SWAP16(c, d);
  const float x = op(r[0], r[1]), y = op(q[0], q[1]);
  r = SWAP32(x, y);
  const float w = mid(op(r[0], r[1]));
  r = SWAP32(w, w);
  q = __builtin_amdgcn_permlane16_swap(r[1], r[1], false, false);
  r = __builtin_amdgcn_permlane16_swap(r[0], r[0], false, false);
  a = __uint_as_float(r[0]); b = __uint_as_float(r[1]); c = __uint_as_float(q[0]); d = __uint_as_float(q[1]);
}
__device__ __forceinline__ float red_max4(float x) {
  auto r = __builtin_amdgcn_permlane16_swap(__float_as_uint(x), __float_as_uint(x), false, false);
  x = vmax2(__uint_as_float(r[0]), __uint_as_float(r[1]));
  r = __builtin_amdgcn_permlane32_swap(__float_as_uint(x), __float_as_uint(x), false, false);
  return vmax2(__uint_as_float(r[0]), __uint_as_float(r[1]));
}
__device__ __forceinline__ float red_add4(float x) {
  auto r = __builtin_amdgcn_permlane16_swap(__float_as_uint(x), __float_as_uint(x), false, false);
  x = __uint_as_float(r[0]) + __uint_as_float(r[1]);
  r = __builtin_amdgcn_permlane32_swap(__float_as_uint(x), __float_as_uint(x), false, false);
  return __uint_as_float(r[0]) + __uint_as_float(r[1]);
}

__device__ __forceinline__ void st16_sc1(rsrc_t r, unsigned voff, unsigned soff, u32x4 v) {
  __builtin_amdgcn_raw_buffer_store_b128(v, r, voff, soff, 16);
  asm volatile("s_nop 1" ::"v"(v) : "memory");
}
__device__ __forceinline__ void st8_sc1(rsrc_t r, unsigned voff, unsigned soff, u32x2 v) { __builtin_amdgcn_raw_buffer_store_b64(v, r, voff, soff, 16); }
__device__ __forceinline__ u32x4 ld16_sc1(rsrc_t r, unsigned voff, unsigned soff) { return __builtin_amdgcn_raw_buffer_load_b128(r, voff, soff, 16); }
template <int AUX> __device__ __forceinline__ u32x4 ld16p(rsrc_t r, unsigned voff, unsigned soff) { return __builtin_amdgcn_raw_buffer_load_b128(r, voff, soff, AUX); }
__device__ __forceinline__ u32x2 ld8_sc1(rsrc_t r, unsigned voff, unsigned soff) { return __builtin_amdgcn_raw_buffer_load_b64(r, voff, soff, 16); }
__device__ __forceinline__ f32x4 ld4f_sc1(rsrc_t r, unsigned voff, unsigned soff) { return __builtin_bit_cast(f32x4, __builtin_amdgcn_raw_buffer_load_b128(r, voff, soff, 16)); }
#define OPAQUE_V(x) asm volatile("" : "+v"(x))
#define OPAQUE_S(x) asm volatile("" : "+s"(x))

#define CLOBBER asm volatile("" ::: "memory")
constexpr unsigned SPIN_CAP = 1u << 17;
__device__ __forceinline__ bool is_set(u32x2 v, u32x2 epoch) { return v[0] == epoch[0] && v[1] == epoch[1]; }
__device__ __forceinline__ void set_flags(rsrc_t rs, unsigned lineA, int colA, unsigned lineB, int colB, u32x2 epoch, int lane) {
  const unsigned off = (lane & 7) * REP_STRIDE + ((lane & 8) ? lineB + colB * 8 : lineA + colA * 8);
  if (lane < 16) st8_sc1(rs, off, 0, epoch);
}
__device__ __forceinline__ unsigned poll_need(rsrc_t rs, unsigned voff, u32x2 epoch, unsigned ready, unsigned need, bool& dead) {
  for (unsigned spins = 0; !dead; ++spins) {
    CLOBBER;
    ready |= (unsigned)__ballot(is_set(ld8_sc1(rs, voff, 0), epoch));
    if ((ready & need) == need) break;
    if (spins > SPIN_CAP) dead = true;
    __builtin_amdgcn_s_sleep(1);
  }
  CLOBBER;
  return dead ? ~0u : ready;
}

__device__ __forceinline__ void poll_all(rsrc_t rs, unsigned voff, u32x2 epoch, bool& dead) {
  for (unsigned spins = 0; !dead; ++spins) {
    CLOBBER;
    if (__all(is_set(ld8_sc1(rs, voff, 0), epoch))) break;
    if (spins > SPIN_CAP) dead = true;
    __builtin_amdgcn_s_sleep(1);
  }
  CLOBBER;
}

constexpr unsigned SC_VF = L_R1 + 32768;
constexpr unsigned SC_PHIK = L_R1 + 49152;
constexpr unsigned SC_XCH = L_R2 + XS;
constexpr unsigned wo_slot(int s) {
  return s < 2 ? L_R2 + (unsigned)(s + 1) * XS : (s < 6 ? L_R1 + (unsigned)(s - 2) * 16384 : (s == 6 ? L_R1 + 81920 : L_R2));
}
__device__ __forceinline__ unsigned wo_src(int s, int j) { return (unsigned)(((JROT(j) >> 1) + NJ / 2 + s) & 7); }
__device__ __forceinline__ unsigned wo_swz(int lane) { return (unsigned)(((lane & 7) ^ ((lane >> 3) & 6)) * 16); }
__device__ __forceinline__ unsigned umax3(unsigned a, unsigned b, unsigned c) { return max(max(a, b), c); }
__device__ __forceinline__ unsigned umax4v(unsigned m, const u32x4& v) { return umax3(umax3(m, v[0], v[1]), v[2], v[3]); }
__device__ __forceinline__ u32x4 nosent(u32x4 v) { return u32x4{min(v[0], SENT - 1), min(v[1], SENT - 1), min(v[2], SENT - 1), min(v[3], SENT - 1)}; }

template <typename P, typename F>
__device__ __forceinline__ void qkv_loop(int isb, int wm, int h, int lane, bool first, int& trv, P pre, F fin) {
  constexpr unsigned lds0 = 0;
  f32x4 acc[4][6];
  const int fr = lane & 15, fq = lane >> 4;
  unsigned ab[2], bb[2];
  {
    const int r = wm * 64 + fr;
#pragma unroll
    for (int ks = 0; ks < 2; ++ks) ab[ks] = lds0 + L_R2 + r * 128 + (((ks * 4 + fq) ^ ((fr >> 1) & 7)) * 16);
    const int kr = fq * 8 + (fr >> 2);
    const int gs = ((kr >> 1) & 1) | (((kr >> 3) & 1) << 1);
#pragma unroll
    for (int c = 0; c < 2; ++c) bb[c] = lds0 + L_R1 + kr * 128 + (((h * 2 + c) ^ gs) * 32) + (lane & 3) * 8;
  }
#pragma unroll
  for (int m = 0; m < 4; ++m)
#pragma unroll
    for (int n = 0; n < 6; ++n) acc[m][n] = f32x4{0.f, 0.f, 0.f, 0.f};
  bf16x8 af[2][4], bf[2][6];
  auto rds = [&](int ks, int p) {
#pragma unroll
    for (int m = 0; m < 4; ++m) af[p][m] = lds_ld<bf16x8>(ab[ks] + m * 2048);
#pragma unroll
    for (int n = 0; n < 6; ++n) {
      const unsigned a = bb[n & 1] + (n >> 1) * 8192 + ks * 4096;
      bf[p][n] = tr2(a, a + 512);
    }
  };
  auto mms = [&](int p) {
#pragma unroll
    for (int n = 0; n < 6; ++n)
#pragma unroll
      for (int mm = 0; mm < 4; ++mm) { const int m = (n & 1) ? 3 - mm : mm; acc[m][n] = n < 4 ? MFMA(bf[p][n], af[p][m], acc[m][n]) : MFMA(af[p][m], bf[p][n], acc[m][n]); }
  };
  auto ilv2 = [&]() {
#pragma unroll
    for (int i = 0; i < 16; ++i) { SGB(0x008, 1); SGB(0x100, 1); }
#pragma unroll
    for (int i = 0; i < 4; ++i) { SGB(0x008, 1); SGB(0x002, 1); }
    SGB(0x008, 4);
  };
  auto ilv3 = [&]() {
#pragma unroll
    for (int i = 0; i < 16; ++i) { SGB(0x008, 1); SGB(0x100, 1); }
#pragma unroll
    for (int i = 0; i < 7; ++i) { SGB(0x008, 1); SGB(0x004, 2); }
    SGB(0x008, 1);
  };
  if (!first) { WAITV(3); WAITL0; }
  TRQ(1); BAR;
  int tslot = 2;
  rds(0, 0);
  int sa = 0, sb = 0;
  int da = XS, db = 24576;
  auto step = [&]() {
    ab[0] += da; ab[1] += da;
    bb[0] += db; bb[1] += db;
  };
  auto ring = [&]() {
    sa = (sa == 2) ? 0 : sa + 1;
    sb = (sb == 3) ? 0 : sb + 1;
    da = (sa == 2) ? -2 * (int)XS : (int)XS;
    db = (sb == 3) ? -3 * 24576 : 24576;
  };
#pragma unroll 1
  for (int t = 14; t > 0; --t) {
    SCHED;
    rds(1, 1); mms(0);
    step();
    ilv2();
    SCHED;
    __builtin_amdgcn_s_waitcnt(0x0070);
    TRQ(tslot); tslot++; BAR;
    SCHED;
    rds(0, 0);
    ring();
    mms(1); ilv3();
  }
  SCHED;
  f32x4 cs[4], nm4[4];
  float nmean[4], csv[2];
  auto fold = [&](int n) {
#pragma unroll
    for (int mm = 0; mm < 4; ++mm) {
      const int m = (n & 1) ? 3 - mm : mm;
#pragma unroll
      for (int e = 0; e < 4; ++e) {
        float t = n < 4 ? __builtin_fmaf(nmean[m], cs[n & 3][e], acc[m][n][e]) : __builtin_fmaf(nm4[m][e], csv[n & 1], acc[m][n][e]);
        asm("" : "+v"(t));
        acc[m][n][e] = t;
      }
    }
  };
  {
    unsigned pm = lds0 + L_MEAN + (16 + wm * 64 + fr) * 4, pc = lds0 + L_CS + (h * 32 + fq * 4) * 4;
    unsigned pm4 = lds0 + L_MEAN + (16 + wm * 64 + fq * 4) * 4, pcv = lds0 + L_CS + (128 + h * 32 + fr) * 4;
    asm("" : "+v"(pm)); asm("" : "+v"(pc)); asm("" : "+v"(pm4)); asm("" : "+v"(pcv));
#pragma unroll
    for (int m = 0; m < 4; ++m) nmean[m] = lds_ld<float>(pm + m * 64);
#pragma unroll
    for (int n = 0; n < 4; ++n) cs[n] = lds_ld<f32x4>(pc + ((n >> 1) * 64 + (n & 1) * 16) * 4);
#pragma unroll
    for (int m = 0; m < 4; ++m) nm4[m] = lds_ld<f32x4>(pm4 + m * 64);
#pragma unroll
    for (int n = 0; n < 2; ++n) csv[n] = lds_ld<float>(pcv + n * 64);
    rds(1, 1); mms(0);
    step();
    fold(0); fold(1);
    SGB(0x002, 8);
#pragma unroll
    for (int i = 0; i < 8; ++i) { SGB(0x008, 1); SGB(0x100, 2); }
#pragma unroll
    for (int i = 0; i < 11; ++i) { SGB(0x008, 1); SGB(0x100, 1); SGB(0x002, 2); }
#pragma unroll
    for (int i = 0; i < 5; ++i) { SGB(0x008, 1); SGB(0x002, 3); }
    SCHED;
    __builtin_amdgcn_s_waitcnt(0xC07F);
    TRQ(16); BAR;
    SCHED;
    rds(0, 0);
    fold(5); mms(1); fold(2);
#pragma unroll
    for (int i = 0; i < 16; ++i) { SGB(0x008, 1); SGB(0x100, 1); SGB(0x002, 1); }
#pragma unroll
    for (int i = 0; i < 8; ++i) { SGB(0x008, 1); SGB(0x002, 2); }
    SCHED;
    pre(0);
    SCHED;
    rds(1, 1);
    fold(4); mms(0); fold(3);
    pre(1);
#pragma unroll
    for (int i = 0; i < 16; ++i) { SGB(0x008, 1); SGB(0x100, 1); SGB(0x002, 1); SGB(0x004, 1); }
#pragma unroll
    for (int i = 0; i < 8; ++i) { SGB(0x008, 1); SGB(0x002, 3); SGB(0x004, 1); }
  }
  SCHED;
  __builtin_amdgcn_s_waitcnt(0xC07F);
  TRQ(17); BAR;
  SCHED;
  auto mm6 = [&](int m, int n0 = 0, int n1 = 6) {
#pragma unroll
    for (int nn = n0; nn < n1; ++nn) { const int n = (nn + 2) % 6; acc[m][n] = n < 4 ? MFMA(bf[1][n], af[1][m], acc[m][n]) : MFMA(af[1][m], bf[1][n], acc[m][n]); }
  };
  if (!isb) {
    fin(std::false_type{}, acc, mm6);
#pragma unroll
    for (int i = 0; i < 24; ++i) asm volatile("" : "=v"(acc[i / 6][i % 6]));
#pragma unroll
    for (int i = 0; i < 6; ++i) asm volatile("" : "=v"(bf[1][i]));
#pragma unroll
    for (int i = 0; i < 4; ++i) asm volatile("" : "=v"(af[1][i]));
  }
  OPAQUE_S(isb);
  if (isb) fin(std::true_type{}, acc, mm6);
}
__device__ __forceinline__ u32x4 pack(const f32x4& a, const f32x4& b) { return u32x4{pk2(a[0], a[1]), pk2(a[2], a[3]), pk2(b[0], b[1]), pk2(b[2], b[3])}; }

template <int N> __device__ __forceinline__ void hedgehog(const f32x4 (&r)[4][2], const float (&c)[4], float (&ep)[4][8], float (&en)[4][8]) {
  float mx[4], nm[4];
#pragma unroll
  for (int m = 0; m < N; ++m) {
    const f32x4 a = r[m][0], b = r[m][1];
    mx[m] = vmax2(fmaxf(fmaxf(a[3], b[0]), b[1]), fmaxf(fmaxf(fmaxf(fmaxf(a[0], a[1]), a[2]), b[2]), b[3]));
    nm[m] = vmax2(fmaxf(fmaxf(-a[3], -b[0]), -b[1]), fmaxf(fmaxf(fmaxf(fmaxf(-a[0], -a[1]), -a[2]), -b[2]), -b[3]));
  }
  SCHED;
  red4<true>(mx[0], mx[1], nm[0], nm[1], [](float x) { return x; });
  if (N == 4) red4<true>(mx[2], mx[3], nm[2], nm[3], [](float x) { return x; });
#pragma unroll
  for (int m = 0; m < N; ++m) { mx[m] *= -c[m]; nm[m] *= -c[m]; }
  SCHED;
#pragma unroll
  for (int m = 0; m < N; ++m) {
#pragma unroll
    for (int i = 0; i < 8; ++i) { ep[m][i] = __builtin_fmaf(r[m][i >> 2][i & 3], c[m], mx[m]); en[m][i] = __builtin_fmaf(r[m][i >> 2][i & 3], -c[m], nm[m]); }
    SCHED;
#pragma unroll
    for (int i = 0; i < 8; ++i) { ep[m][i] = __builtin_amdgcn_exp2f(ep[m][i]); en[m][i] = __builtin_amdgcn_exp2f(en[m][i]); }
    SCHED;
  }
}
__device__ __forceinline__ u32x4 pack8(const float (&e)[8], float f) {
  return u32x4{pk2(e[0] * f, e[1] * f), pk2(e[2] * f, e[3] * f), pk2(e[4] * f, e[5] * f), pk2(e[6] * f, e[7] * f)};
}
__device__ __forceinline__ u32x4 pack8(const float (&e)[8]) { return u32x4{pk2(e[0], e[1]), pk2(e[2], e[3]), pk2(e[4], e[5]), pk2(e[6], e[7])}; }

constexpr u64 window_mask(int t, int e) {
  u64 mk = 0;
  for (int ln = 0; ln < 64; ++ln) {
    const int delta = (ln & 15) + 16 - t * 16 - (ln >> 4) * 4 - e;
    if (delta >= 0 && delta <= 14 && !(delta & 1)) mk |= 1ull << ln;
  }
  return mk;
}
__device__ __forceinline__ void phik2(const f32x4 (&r)[4][2], const float (&c)[4], unsigned pw) {
  const f32x4 zero = {0.f, 0.f, 0.f, 0.f};
  const bf16x8 ones = {0x3f80, 0x3f80, 0x3f80, 0x3f80, 0x3f80, 0x3f80, 0x3f80, 0x3f80};
  float ep[4][8], en[4][8];
  f32x4 sp[2], sn[2];
  hedgehog<2>(r, c, ep, en);
#pragma unroll
  for (int m = 0; m < 2; ++m) { sp[m] = MFMA(ones, as_frag(pack8(ep[m])), zero); sn[m] = MFMA(ones, as_frag(pack8(en[m])), zero); }
#pragma unroll
  for (int m = 0; m < 2; ++m) { asm("" : "+v"(sp[m])); asm("" : "+v"(sn[m])); }
  SCHED;
#pragma unroll
  for (int m = 0; m < 2; ++m) {
    const float fp = __builtin_amdgcn_rcpf(sp[m][0]), fn = __builtin_amdgcn_rcpf(sn[m][0]);
#pragma unroll
    for (int i = 0; i < 8; ++i) { ep[m][i] *= fp; en[m][i] *= fn; }
    SCHED;
    const u32x4 pp = pack8(ep[m]), pn = pack8(en[m]);
    const unsigned d = pw + m * (16 * 160);
    lds_st<u32x2>(d, u32x2{pp[0], pp[1]});
    lds_st<u32x2>(d + 32, u32x2{pp[2], pp[3]});
    lds_st<u32x2>(d + 64, u32x2{pn[0], pn[1]});
    lds_st<u32x2>(d + 96, u32x2{pn[2], pn[3]});
    SCHED;
  }
}
__device__ __forceinline__ bf16x8 rowsum_sel(int lane) {
  const int fr = lane & 15;
  const unsigned one = (fr < 8) ? (0x3f80u << (16 * (fr & 1))) : 0u;
  const int e = (fr >> 1) & 3;
  return as_frag(u32x4{e == 0 ? one : 0u, e == 1 ? one : 0u, e == 2 ? one : 0u, e == 3 ? one : 0u});
}
__device__ __forceinline__ f32x4 rowsum_fin(f32x4 c) {
#pragma unroll
  for (int k = 0; k < 4; ++k) {
    const float a = c[k];
    c[k] = a + __int_as_float(__builtin_amdgcn_update_dpp(0, __float_as_int(a), 0x128  , 0xf, 0xf, false));
  }
  return c;
}
__device__ __forceinline__ void job_colsum(rsrc_t rs, const u32x4 (&w)[8], int mat, int g, int jc, int hw, int lane) {
  const bf16x8 sel = rowsum_sel(lane);
  f32x4 c = {0.f, 0.f, 0.f, 0.f};
#pragma unroll
  for (int i = 0; i < 8; ++i) c = MFMA(sel, as_frag(w[i]), c);
  const u32x4 w4 = __builtin_bit_cast(u32x4, rowsum_fin(c));
  unsigned so = WS_CS + (unsigned)(((mat * 16 + jc) * 16 + g) * 768);
  OPAQUE_S(so);
  if ((lane & 40) == 0) st16_sc1(rs, (unsigned)(hw * 64 + (lane & 7) * 8 + (lane >> 4) * 4) * 4, so, w4);
}
__device__ __forceinline__ unsigned img_off(int i, int lane) { return (lane >> 3) * 128 + (((lane & 7) ^ ((((lane >> 4) & 1) | ((i & 1) << 1)) << 1)) * 16); }
__device__ __forceinline__ void job_pack(rsrc_t rs, u32x4 w, int i, int mat, int g, int jc, int hw, int lane) {
  st16_sc1(rs, img_off(i, lane),
           WS_WP + (unsigned)(((mat * 16 + jc) * 16 + g) * 24576 + hw * 8192 + i * 1024), w);
}

__global__ __launch_bounds__(512, 2) void mega_kernel(Params p) {
  extern __shared__ __attribute__((aligned(16))) char smem[];
  const int tid = threadIdx.x;
  const int wave0 = __builtin_amdgcn_readfirstlane(tid >> 6);
  const int xx = blockIdx.x & 7, rr = blockIdx.x >> 3;
  const int g0 = (rr / NJ) * NX + (xx & (NX - 1)), j0 = (xx / NX) * NJ + (rr & (NJ - 1));
  constexpr unsigned lds0 = 0, lds00 = 0;
  if ((unsigned)(size_t)(LAS char*)smem != 0u) return;
#define LOCALS0 int g = g0, j = j0, l = l0, wave = wave0;                                              \
  OPAQUE_S(g); OPAQUE_S(j); OPAQUE_S(l); OPAQUE_S(wave);                                               \
  const int hw = wave & 3, wm = wave & 1, h = (wave >> 1) & 1;                                         \
  const unsigned myrep = (unsigned)(JROT(j) / 2 + (g & (NX - 1))) * REP_STRIDE;                              \
  (void)hw; (void)wm; (void)h; (void)myrep;                                                            \
  int lane = tid; OPAQUE_V(lane); lane &= 63
#define LOCALS LOCALS0; const bool isb = l & 1, lead = (wave == 4); const int half = wave >> 2; (void)isb; (void)lead; (void)half
  const rsrc_t rs = mk_rsrc(p.ws, WS_SPAN);
  const bool cw = wave0 < 4;
  auto slowbit = [](const void* q) { return 1u << ((3 - ((unsigned)(size_t)q >> 7)) & 7); };
  unsigned slowm = slowbit(p.wqkvA) | (slowbit(p.wqkvB) << 8) | (slowbit(p.woA) << 16) | (slowbit(p.woB) << 24);
  slowm |= PKALL;
  OPAQUE_S(slowm);
  if (wave0 == 2) touch(mk_rsrc(p.ws, NREP * REP_STRIDE), min(blockIdx.x * 9 + ((tid & 63) < 9 ? (tid & 63) : 0), NREP * REP_STRIDE / 128 - 1) * 128, lds00 + L_SINK + 512);
  if (wave0 == 3) {
    const char* pc;
    unsigned len;
    asm volatile("s_getpc_b64 %0\n.Lpf_base:\n\ts_mov_b32 %1, .Lpf_end-.Lpf_base" : "=s"(pc), "=s"(len));
    const unsigned off = ((unsigned)rr * 16 + (tid & 15)) * 128;
    touch(mk_rsrc(pc, len), ((int)len > 0 && off + 4 <= len) ? off : 0u, lds00 + L_SINK);
  }
  int trv = 0, trA = 0, trB = 0, trC = 0;
  u32x2 epoch = {0u, 0u};
  bool dead = false;
  STAMP_INIT;
  const char *pA = (const char*)p.x, *pW = (const char*)p.wqkvA;
  unsigned zA = 2048 * 2048, bA = g0 * (128 * 2048), kA = 128, pcA = 32 * 2048, vA;
  unsigned zW = 1024 * 6144, bW = j0 * 128, kW = 64 * 6144, pcW = 2048, hW = 32 * 6144, vW;
  int kb = KB0(g0, j0), dj = -1;
  {
    const int lane = tid & 63, hw = wave0 & 3;
    vA = (hw * 8 + (lane >> 3)) * 2048 + (((lane & 7) ^ (((hw & 1) * 4 + (lane >> 4)) & 7)) * 16);
    vW = (hw * 8 + (lane >> 3)) * 6144 + (((lane & 7) ^ ((((lane >> 4) & 1) | ((hw & 1) << 1)) << 1)) * 16);
  }

#pragma unroll 1
  for (int l0 = 0; l0 < 4; ++l0) {
    TRC(0); TRR(62);

    f32x4 acc[2][4];
    auto kvpart = [&](unsigned va, unsigned pa, unsigned kvb, int lane, int wave) {
      const f32x4 zero = {0.f, 0.f, 0.f, 0.f};
      const bf16x8 ones = {0x3f80, 0x3f80, 0x3f80, 0x3f80, 0x3f80, 0x3f80, 0x3f80, 0x3f80};
      f32x4 c0 = zero, c1 = zero, z0 = zero, z1 = zero;
#pragma unroll
      for (int ks = 0; ks < 4; ++ks) {
        const bf16x8 v = lds_ld<bf16x8>(va + ks * 2048);
        const bf16x8 a0 = tr2(pa + ks * 32 * 160, pa + (ks * 32 + 16) * 160);
        const bf16x8 a1 = tr2(pa + 32 + ks * 32 * 160, pa + 32 + (ks * 32 + 16) * 160);
        c0 = MFMA(a0, v, c0);
        c1 = MFMA(a1, v, c1);
        z0 = MFMA(a0, ones, z0);
        z1 = MFMA(a1, ones, z1);
      }
      st16_sc1(rs, lane * 16, kvb + (unsigned)wave * 1024, nosent(pack(c0, c1)));
      if (!(wave & 2) && (lane & 15) == 0) st16_sc1(rs, lane, kvb + KVP_Z + (wave >> 2) * 128 + (wave & 1) * 64, nosent(pack(z0, z1)));
    };
#define KV_ADDR                                                                                                            \
    unsigned va = lds0 + SC_VF + (wave >> 2) * 8192 + ((wave >> 1) & 1) * 1024 + lane * 16;                                \
    unsigned pa = lds0 + SC_PHIK + ((wave >> 2) * 128 + fq * 4 + (fr >> 2)) * 160 + (wave & 1) * 64 + (lane & 3) * 8;      \
    unsigned kvj = WS_KVP + (unsigned)j * KVP_J + (unsigned)(l >> 1) * KVP_L;                                              \
    OPAQUE_V(va); OPAQUE_V(pa); OPAQUE_S(kvj)
    if (cw) {
      if (l0 == 0) {
        LOCALS;
        u32x4 v[2][8];
        if (wave < 3) {
          const int jc = JOBC(j, wave), kt = (KB0(g, j) + (unsigned)g / NX) & 15;
          const rsrc_t ra = mk_rsrc(p.wqkvA, 1024 * 6144);
#pragma unroll
          for (int i = 0; i < 8; ++i)
            v[0][i] = __builtin_amdgcn_raw_buffer_load_b128(ra, (lane >> 3) * 6144 + (lane & 7) * 16, (kt * 64 + i * 8) * 6144 + wave * 2048 + jc * 128, 0);
          SCHED;
          TRC(44); BAR; SCHED;
          job_colsum(rs, v[0], 0, g, jc, wave, lane);
          if (SLOWC(0, jc)) {
#pragma unroll
            for (int i = 0; i < 8; ++i) job_pack(rs, v[0][i], i, 0, kt, jc, wave, lane);
          }
        } else {
          const int fr = lane & 15, fq = lane >> 4;
          const rsrc_t rx = mk_rsrc(p.x, 2048 * 2048);
#pragma unroll
          for (int i = 0; i < 16; ++i)
            v[i & 1][i >> 1] = __builtin_amdgcn_raw_buffer_load_b128(rx, fr * 2048 + fq * 16, (g * 128 + (j & (NJ - 1)) * (128 / NJ) + ((i >> 1) % (8 / NJ)) * 16) * 2048 + ((KB0(g, j) + (i >> 1) / (8 / NJ)) & 15) * 128 + (i & 1) * 64, 0);
          SCHED;
          TRC(44); BAR; SCHED;
          const f32x4 zero = {0.f, 0.f, 0.f, 0.f};
          const bf16x8 ones = {0x3f80, 0x3f80, 0x3f80, 0x3f80, 0x3f80, 0x3f80, 0x3f80, 0x3f80};
          float sm[8], q[8];
#pragma unroll
          for (int m = 0; m < 8; ++m) {
            const f32x4 a = MFMA(ones, as_frag(v[1][m]), MFMA(ones, as_frag(v[0][m]), zero));
            const f32x4 b = MFMA(as_frag(v[1][m]), as_frag(v[1][m]), MFMA(as_frag(v[0][m]), as_frag(v[0][m]), zero));
            sm[m] = a[0];
            q[m] = (fr & 2) ? ((fr & 1) ? b[3] : b[2]) : ((fr & 1) ? b[1] : b[0]);
          }
          if (fq == (fr >> 2)) {
#pragma unroll
            for (int m = 0; m < 8; ++m) st8_sc1(rs, ((m % (8 / NJ)) * 16 + fr) * 8 + (m / (8 / NJ)) * 1024, st_off(g, 0) + JROT(j) * 1024 + (j & (NJ - 1)) * (1024 / NJ), u32x2{__float_as_uint(sm[m]), __float_as_uint(q[m])});
          }
        }
      }
      LOCALS;
      unsigned otile, kvo, l16, pr1, pr4;
      int g2, j2, l2, ln2;
      qkv_loop(l & 1, wm, h, lane, l == 0, trv, [&](int st) {
        if (st == 0) {
          g2 = g0; j2 = j0; l2 = l0; ln2 = tid;
          OPAQUE_S(g2); OPAQUE_S(j2); OPAQUE_S(l2); OPAQUE_V(ln2);
          return;
        }
        otile = ob_off(g2, j2, l2) + (unsigned)(wm * 8 + h) * 1024;
        kvo = WS_KVP + (unsigned)j2 * KVP_J + (unsigned)g2 * KVP_G + (unsigned)(l2 >> 1) * KVP_L;
        l16 = (ln2 & 63) * 16;
        pr1 = lds0 + L_RSTD + (16 + wm * 64 + (ln2 & 15)) * 4;
        pr4 = lds0 + L_RSTD + wm * 256 + (ln2 & 48);
        asm("" : "+s"(otile)); asm("" : "+s"(kvo)); asm("" : "+v"(l16)); asm("" : "+v"(pr1)); asm("" : "+v"(pr4));
      }, [&](auto isb_c, f32x4 (&a)[4][6], auto& mm6) {
        const f32x4 zero = {0.f, 0.f, 0.f, 0.f};
        constexpr float L2E = 1.4426950408889634f;
        float rq[4];
#define ilv6(nv) do { _Pragma("unroll") for (int i_ = 0; i_ < 6; ++i_) { SGB(0x008, 1); SGB(0x100, 2); SGB(0x002, nv); } SCHED; } while (0)
        if constexpr (decltype(isb_c)::value) {
          constexpr float QS = 0.17677669529663687f * L2E, NEG = -3.0e38f;
          const bf16x8 ones = {0x3f80, 0x3f80, 0x3f80, 0x3f80, 0x3f80, 0x3f80, 0x3f80, 0x3f80};
          u32x4 kf[5], vf[5], qf[4], pf[4];
          f32x4 rs4[4], bias[2], s0[4], s1[4], dn[4];
          float x[4][4], mx[4];
          unsigned psel0, psel1;
          auto pkkv = [&](int m) { kf[m + 1] = pack(a[m][2] * rq[m], a[m][3] * rq[m]); vf[m + 1] = pack(a[m][4] * rs4[m], a[m][5] * rs4[m]); };
          auto pkq = [&](int m) { qf[m] = pack(a[m][0], a[m][1]); };
          auto st1 = [&](int m) {
            x[m][0] = vmax2(s0[m][0], s0[m][1]); x[m][1] = vmax2(s0[m][2], s0[m][3]); x[m][2] = vmax2(s1[m][0], s1[m][1]); x[m][3] = vmax2(s1[m][2], s1[m][3]);
            mx[m] = vmax2(vmax2(x[m][0], x[m][1]), vmax2(x[m][2], x[m][3]));
          };
          auto st2 = [&](int m) {
            const float mxl = mx[m] * -rq[m];
#pragma unroll
            for (int e = 0; e < 4; ++e) x[m][e] = __builtin_fmaf(x[m][e], rq[m], mxl);
          };
          auto st3 = [&](int m) {
#pragma unroll
            for (int e = 0; e < 4; ++e) x[m][e] = __builtin_amdgcn_exp2f(x[m][e]);
          };
          auto st5 = [&](int m) {
            const unsigned p0 = pk2(x[m][0], x[m][1]), p1 = pk2(x[m][2], x[m][3]);
            pf[m] = u32x4{__builtin_amdgcn_perm(0u, p0, psel0), __builtin_amdgcn_perm(0u, p0, psel1), __builtin_amdgcn_perm(0u, p1, psel0), __builtin_amdgcn_perm(0u, p1, psel1)};
          };
          auto st6 = [&](int m) {
            dn[m] = MFMA(ones, as_frag(pf[m]), zero);
            s0[m] = MFMA(as_frag(u32x4{vf[m][0], vf[m][1], vf[m + 1][0], vf[m + 1][1]}), as_frag(pf[m]), zero);
            s1[m] = MFMA(as_frag(u32x4{vf[m][2], vf[m][3], vf[m + 1][2], vf[m + 1][3]}), as_frag(pf[m]), zero);
            asm("" : "+v"(dn[m]));
          };
          auto st7 = [&](int m) { const float inv = __builtin_amdgcn_rcpf(dn[m][0]); pf[m] = nosent(pack(s0[m] * inv, s1[m] * inv)); };
          mm6(3);
#pragma unroll
          for (int m = 0; m < 4; ++m) rq[m] = lds_ld<float>(pr1 + m * 64);
#pragma unroll
          for (int m = 3; m >= 0; --m) rs4[m] = lds_ld<f32x4>(pr4 + 64 + m * 64);
          unsigned pv_ = l16 + lds0 + L_PREV + h * 2048;
          asm("" : "+v"(pv_));
          static_for<0, 8>([&](auto i_c) __attribute__((always_inline)) {
            constexpr int i = decltype(i_c)::value;
            constexpr u64 mk = window_mask(i >> 2, i & 3);
            bias[i >> 2][i & 3] = __builtin_amdgcn_inverse_ballot_w64(mk) ? 0.f : NEG;
          });
          asm("" : "+v"(bias[0])); asm("" : "+v"(bias[1]));
          const unsigned hso = kvo + KVP_HALO + h * 2048, hsi = hso - (g > 0 ? KVP_G : 0u);
          ilv6(2);
          mm6(2);
          pkkv(3);
          ilv6(3);
          OPAQUE_V(kf[4]); OPAQUE_V(vf[4]);
          if (wm) {
            st16_sc1(rs, l16, hso, nosent(kf[4]));
            st16_sc1(rs, l16, hso + 1024, nosent(vf[4]));
          } else {
            lds_st<u32x4>(pv_, kf[4]);
            lds_st<u32x4>(pv_ + 1024, vf[4]);
          }
          SCHED;
          mm6(1);
          pkq(3); pkkv(2);
          ilv6(4);
          mm6(0);
          pkq(2); pkkv(1);
          ilv6(4);
          s0[3] = MFMA(as_frag(kf[3]), as_frag(qf[3]), bias[0]); s1[3] = MFMA(as_frag(kf[4]), as_frag(qf[3]), bias[1]);
          s0[2] = MFMA(as_frag(kf[2]), as_frag(qf[2]), bias[0]); s1[2] = MFMA(as_frag(kf[3]), as_frag(qf[2]), bias[1]);
          pkq(1); pkkv(0);
#pragma unroll
          for (int i = 0; i < 4; ++i) { SGB(0x008, 1); SGB(0x002, 5); }
          SCHED;
          const bool halo = wm == 0 && g > 0;
          const u32x4 hk = ld16p<PA1>(rs, l16, hsi), hv = ld16p<PA1>(rs, l16, hsi + 1024);
          SCHED;
          s0[1] = MFMA(as_frag(kf[1]), as_frag(qf[1]), bias[0]); s1[1] = MFMA(as_frag(kf[2]), as_frag(qf[1]), bias[1]);
          st1(3); st1(2);
#pragma unroll
          for (int m = 0; m < 4; ++m) rq[m] *= QS;
          SCHED;
          st1(1);
          pkq(0);
          psel0 = __builtin_amdgcn_inverse_ballot_w64(0xaaaaaaaaaaaaaaaaull) ? 0x01000c0cu : 0x0c0c0100u;
          psel1 = __builtin_amdgcn_inverse_ballot_w64(0xaaaaaaaaaaaaaaaaull) ? 0x03020c0cu : 0x0c0c0302u;
          { float d = mx[1]; red4<true>(mx[3], mx[2], mx[1], d, [](float v) { return v; }); }
          s1[0] = MFMA(as_frag(kf[1]), as_frag(qf[0]), bias[1]);
          SCHED;
          st2(3); st2(2); st2(1);
          SCHED;
          st3(3); st3(2); st3(1);
          SCHED;
          st5(3); st6(3); st5(2); st6(2); st5(1); st6(1);
          SCHED;
          WAITL0;
          TRR(60);
          TRC(18); BAR;
          u32x4 pk = lds_ld<u32x4>(pv_), pv = lds_ld<u32x4>(pv_ + 1024);
          st7(3); st7(2); st7(1);
          SCHED;
#pragma unroll
          for (int m = 3; m > 0; --m) st16_sc1(rs, l16, otile + m * 2048, pf[m]);
          if (!wm && g == 0) bias[0] = f32x4{NEG, NEG, NEG, NEG};
          SCHED;
          TRV(55, 0); TRC(21);
          if (halo) {
            pk = hk; pv = hv;
            for (unsigned spins = 0; !dead && __any(umax4v(umax4v(0u, pk), pv) == SENT); ++spins) {
              if (spins > SPIN_CAP) { dead = true; break; }
              CLOBBER;
              TRV(55, spins + 1);
              pk = ld16p<PA1>(rs, l16, hsi); pv = ld16p<PA1>(rs, l16, hsi + 1024);
              OPAQUE_V(pk); OPAQUE_V(pv);
            }
          }
          vf[0] = pv;
          s0[0] = MFMA(as_frag(pk), as_frag(qf[0]), bias[0]);
          st1(0);
          mx[0] = red_max4(mx[0]);
          st2(0); st3(0); st5(0); st6(0); st7(0);
          st16_sc1(rs, l16, otile, pf[0]);
        } else {
          LOCALS;
          const int fr = lane & 15, fq = lane >> 4;
          f32x4 rs4[4], r[4][2];
          const bf16x8 ones = {0x3f80, 0x3f80, 0x3f80, 0x3f80, 0x3f80, 0x3f80, 0x3f80, 0x3f80};
          auto proj = [&](int m, int n, const bf16x8& w0, const bf16x8& w1) {
            const u32x4 f = pack(a[m][n], a[m][n + 1]);
            r[m][0] = MFMA(w0, as_frag(f), zero); r[m][1] = MFMA(w1, as_frag(f), zero);
          };
          mm6(3, 0, 2); mm6(2, 0, 2); mm6(1, 0, 2); mm6(0, 0, 2);
          unsigned wa = lds0 + L_WPHI + h * 2048 + (fq * 4 + (fr >> 2)) * 64 + (lane & 3) * 8, kw = lds0 + L_PREV + hw * 2048 + l16;
          asm("" : "+v"(wa)); asm("" : "+v"(kw));
#pragma unroll
          for (int m = 0; m < 4; ++m) rq[m] = lds_ld<float>(pr1 + m * 64);
#pragma unroll
          for (int m = 0; m < 4; ++m) rs4[m] = lds_ld<f32x4>(pr4 + 64 + m * 64);
          const bf16x8 w0 = tr2(wa, wa + 1024);
          const bf16x8 w1 = tr2(wa + 32, wa + 32 + 1024);
#pragma unroll
          for (int i = 0; i < 8; ++i) { SGB(0x008, 1); SGB(0x100, 2); SGB(0x002, 2); }
          SCHED;
          mm6(3, 2, 4); mm6(2, 2, 4);
          lds_st<u32x4>(kw + 1024, pack(a[3][2], a[3][3]));
          lds_st<u32x4>(kw, pack(a[2][2], a[2][3]));
#pragma unroll
          for (int i = 0; i < 2; ++i) { SGB(0x008, 1); SGB(0x002, 2); SGB(0x008, 1); SGB(0x002, 2); SGB(0x200, 1); }
          SCHED;
          mm6(1, 2, 4); mm6(0, 2, 4);
          proj(0, 2, w0, w1); proj(1, 2, w0, w1);
#pragma unroll
          for (int i = 0; i < 4; ++i) { SGB(0x008, 1); SGB(0x002, 2); }
          SCHED;
          WAITL0;
          TRC(18); BAR;
          SCHED;
          unsigned pw = lds0 + SC_PHIK + (h * 128 + wm * 64 + fr) * 160 + fq * 8, vw = lds0 + SC_VF + h * 8192 + wm * 4096 + l16;
          asm("" : "+v"(pw)); asm("" : "+v"(vw));
#pragma unroll
          for (int m = 0; m < 4; ++m) mm6(m, 4, 6);
#pragma unroll
          for (int m = 0; m < 4; ++m) rq[m] *= L2E;
#pragma unroll
          for (int i = 0; i < 4; ++i)
            lds_st<u32x4>(vw + i * 1024, pack(a[i & 2][4 + (i & 1)] * rs4[i & 2], a[(i & 2) + 1][4 + (i & 1)] * rs4[(i & 2) + 1]));
#pragma unroll
          for (int i = 0; i < 8; ++i) { SGB(0x008, 1); SGB(0x002, 5); }
          SCHED;
          phik2(r, rq, pw);
          KV_ADDR;
          WAITL0;
          TRR(60);
          TRC(19); BAR;
          kvpart(va, pa, kvo, lane, wave);
          CLOBBER;
          TRC(20); BAR;
          u32x4 qp[4], qn[4];
          float sqp[4], sqn[4];
#pragma unroll
          for (int m = 0; m < 4; ++m) proj(m, 0, w0, w1);
          SCHED;
          float ep[4][8], en[4][8];
          hedgehog<4>(r, rq, ep, en);
          const unsigned zo = (lane >> 3) * KVP_G + (lane & 7) * 16, zs = kvj + KVP_Z + h * 128;
          u32x4 zv[2] = {ld16p<PA1>(rs, zo, zs), ld16p<PA1>(rs, zo, zs + 8 * KVP_G)};
          SCHED;
#pragma unroll
          for (int m = 0; m < 4; ++m) {
            qp[m] = pack8(ep[m]); qn[m] = pack8(en[m]);
            sqp[m] = MFMA(ones, as_frag(qp[m]), zero)[0];
            sqn[m] = MFMA(ones, as_frag(qn[m]), zero)[0];
          }
          SCHED;
#pragma unroll
          for (int m = 0; m < 4; ++m) { sqp[m] = __builtin_amdgcn_rcpf(sqp[m]); sqn[m] = __builtin_amdgcn_rcpf(sqn[m]); }
#pragma unroll
          for (int m = 0; m < 4; ++m) { OPAQUE_V(qp[m]); OPAQUE_V(qn[m]); OPAQUE_V(sqp[m]); OPAQUE_V(sqn[m]); }
          unsigned ko = (lane >> 5) * KVP_G + (lane & 31) * 16, ks = kvj + (unsigned)((j ^ RX) - j) * KVP_J + KVP_SUM + h * (8 * KVP_G);
          bf16x8 sel = rowsum_sel(lane);
          OPAQUE_V(ko); OPAQUE_S(ks); OPAQUE_V(sel);
          TRC(21); TRV(50, 0);
          for (unsigned spins = 0; !dead && __any(umax4v(umax4v(0u, zv[0]), zv[1]) == SENT); ++spins) {
            if (spins > SPIN_CAP) { dead = true; break; }
            CLOBBER;
            TRV(50, spins + 1);
            zv[0] = ld16p<PA1>(rs, zo, zs); zv[1] = ld16p<PA1>(rs, zo, zs + 8 * KVP_G);
          }
          TRC(22);
          {
            const f32x4 z = rowsum_fin(MFMA(sel, as_frag(zv[1]), MFMA(sel, as_frag(zv[0]), zero)));
            if ((lane & 40) == 0) lds_st<u32x2>(lds0 + L_ZF + hw * 128 + fr * 16 + fq * 8, u32x2{pk2(z[0], z[1]), pk2(z[2], z[3])});
            WAITL0;
            const bf16x8 zf0 = lds_ld<bf16x8>(lds0 + L_ZF + hw * 128 + fq * 16), zf1 = lds_ld<bf16x8>(lds0 + L_ZF + hw * 128 + 64 + fq * 16);
            float dp[4], dn[4];
#pragma unroll
            for (int m = 0; m < 4; ++m) { dp[m] = MFMA(zf0, as_frag(qp[m]), zero)[0]; dn[m] = MFMA(zf1, as_frag(qn[m]), zero)[0]; }
            SCHED;
#pragma unroll
            for (int m = 0; m < 4; ++m) {
              const float inv = __builtin_amdgcn_rcpf(__builtin_fmaf(dp[m], sqp[m], __builtin_fmaf(dn[m], sqn[m], 1e-6f)));
              sqp[m] *= inv; sqn[m] *= inv;
              OPAQUE_V(sqp[m]); OPAQUE_V(sqn[m]);
            }
          }
          TRV(51, 0);
          TRC(43); BAR;
          if (H2D) __builtin_amdgcn_s_sleep(H2D);
          u32x4 kv[4];
#pragma unroll
          for (int i = 0; i < 4; ++i) kv[i] = ld16p<PA2>(rs, ko, ks + i * (2 * KVP_G));
          SCHED;
#pragma unroll
          for (int m = 0; m < 4; ++m) { qp[m] = pack8(ep[m], sqp[m]); qn[m] = pack8(en[m], sqn[m]); }
#pragma unroll
          for (int m = 0; m < 4; ++m) { OPAQUE_V(qp[m]); OPAQUE_V(qn[m]); }
          SCHED;
          for (unsigned spins = 0; !dead && __any(max(umax4v(umax4v(0u, kv[0]), kv[1]), umax4v(umax4v(0u, kv[2]), kv[3])) == SENT); ++spins) {
            if (spins > SPIN_CAP) { dead = true; break; }
            CLOBBER;
            TRV(51, spins + 1);
#pragma unroll
            for (int i = 0; i < 4; ++i) kv[i] = ld16p<PA2>(rs, ko, ks + i * (2 * KVP_G));
          }
          TRC(23);
          f32x4 o[4][2];
#pragma unroll
          for (int m = 0; m < 4; ++m)
#pragma unroll
            for (int i = 0; i < 2; ++i) o[m][i] = MFMA(as_frag(kv[2 * i + 1]), as_frag(qn[m]), MFMA(as_frag(kv[2 * i]), as_frag(qp[m]), zero));
#pragma unroll
          for (int m = 0; m < 4; ++m) st16_sc1(rs, l16, otile + m * 2048, nosent(pack(o[m][0], o[m][1])));
        }
      });
#pragma unroll
      for (int i = 0; i < 8; ++i) asm volatile("" : "=v"(acc[i >> 2][i & 3]));
    } else {
      __builtin_amdgcn_s_setprio(3);
      if (l0) {
        WAITV(3);
        WAITL0;
        TRC(1); BAR;
      }
      LOCALS;
      u32x4 xr[2][2];
      const unsigned lin = lane * 16, vlin = lin + hw * 1024;
      const int jr = j & ~3;
      u32x4 sent4 = {SENT, SENT, SENT, SENT};
      OPAQUE_V(sent4);
      const unsigned cb0 = ob_off(g, j, l), cb2 = xb_off(g, l) + j * TS;
      const unsigned cb1 = WS_KVP + (unsigned)j * KVP_J + (unsigned)g * KVP_G + (unsigned)(l >> 1) * KVP_L;
      auto ldres = [&](int slot) {
        const int fr = lane & 15, fq = lane >> 4, ch = (fq & 1) * 2 + (fq >> 1);
#pragma unroll
        for (int i = 0; i < 4; ++i)
          xr[i >> 1][i & 1] = lds_ld<u32x4>(lds0 + L_R2 + slot * XS + (hw * 32 + ((i >> 1) ^ 1) * 16 + fr) * 128 + (((ch + 4 * (i & 1)) ^ ((fr >> 1) & 7)) * 16));
      };
      auto aslot = [&](int t) { return lds0 + L_R2 + (t % 3) * XS + hw * 1024; };
      auto wslot = [&](int t) { return lds0 + L_R1 + (t & 3) * 24576 + hw * 1024; };
      const bool l0f = l == 0;
      const rsrc_t rA = mk_rsrc(pA, zA);
      auto kseq = [&](int t) { return (kb + t - (((KX || t < NJ) && t <= dj) ? 1 : 0)) & 15; };
      auto offA = [&](int t) { return bA + (unsigned)kseq(t) * kA; };
      auto offW = [&](int t) { return bW + (unsigned)kseq(t) * kW; };
      auto ldA = [&](int t) { dma4<4096>(rA, vA, offA(t), pcA, aslot(t)); };
      auto ldWh = [&](int t, unsigned o, int hf) { dma3<8192>(mk_rsrc(pW, zW), vW, o + (hf ? hW : 0u), pcW, wslot(t) + hf * 4096); };
      const bool slowj = l0f && SLOWC(0, j);
      auto packedW = [&]() {
        pW = p.ws; zW = WS_SPAN; bW = WS_WP + (unsigned)j * (16 * 24576); kW = 24576; pcW = 8192; hW = 4096; vW = vlin;
      };
      auto ldW = [&](int t) { const unsigned o = offW(t); ldWh(t, o, 0); ldWh(t, o, 1); };
      const unsigned cmask = 0xfu | (isb ? 0u : 0xf0u) | (l == 3 ? 0u : 0xf00u);
      constexpr auto spread = [](int n) { unsigned m = 0; for (int t = 0; t < 26; ++t) if ((t + 1) * n / 13 > t * n / 13) m |= 1u << t; return m; };
      constexpr unsigned sp4 = spread(4), sp8 = spread(8), sp12 = spread(12);
      const unsigned smask = (isb ? (l == 3 ? sp4 : sp8) : sp12) >> ((((g + j * 5) & 15) * 13) >> 4);
      const unsigned cbm = isb ? cb2 : cb1;
      auto clean = [&](int t) __attribute__((always_inline)) {
        if (STG) {
          if (t < 13 && ((smask >> t) & 1) && (!NORS || l0f)) {
            const unsigned cc = __builtin_popcount(smask & ((1u << t) - 1));
            const unsigned cb = cb0 + (cc >= 4 ? cbm - cb0 : 0u) + (cc >= 8 ? cb2 - cbm : 0u), pc = (cc + g + j) & 3;
            st16_sc1(rs, (lane >> 4) * 4096 + (lane & 15) * 16, cb + hw * 1024 + pc * 256, sent4);
          }
        } else if (t < 4 || (t < 12 && ((cmask >> t) & 1))) {
          const unsigned cb = t < 4 ? cb0 : (t < 8 ? cb1 : cb2), pc = (unsigned)((t + g + j) & 3);
          st16_sc1(rs, (lane >> 4) * 4096 + (lane & 15) * 16, cb + hw * 1024 + pc * 256, sent4);
        }
      };
      unsigned flo;
      unsigned ready = ~0u;
      u32x4 cv[4];
#pragma unroll
      for (int ii = 0; ii < 4; ++ii) asm volatile("" : "=v"(cv[ii]));
      auto chkrd = [&](int t) {
#pragma unroll
        for (int ii = 0; ii < 4; ++ii) cv[ii] = lds_ld<u32x4>(aslot(t) + lin + ii * 4096);
      };
      auto chkbad = [&]() { return __any(max(umax4v(umax4v(0u, cv[0]), cv[1]), umax4v(umax4v(0u, cv[2]), cv[3])) == SENT) && !l0f; };
      auto refetch = [&](int t) __attribute__((always_inline)) {
        for (unsigned spins = 0; !dead; ++spins) {
          if (spins > SPIN_CAP) { dead = true; break; }
          WAITV(0);
          TRV(t == 1 ? 56 : 57, spins + 1);
          ldA(t);
          ldA(t + 1);
          WAITV(0);
          chkrd(t);
          if (!chkbad()) break;
          __builtin_amdgcn_s_sleep(1);
        }
      };
      if (l0f) {
        u32x2 gw2;
        ld8_asm(gw2, rs, WS_EPOCH + 0u * lin);
        TRC(44); BAR;
        ldW(0); ldA(0);
        dma1(mk_rsrc(p.wphi, 32 * 2048), vlin, j * 4096, lds0 + L_WPHI + hw * 1024);
        ldW(1);
        if (A1E) ldA(1);
        WAITV(A1E ? 21 : 17);
        OPAQUE_V(gw2);
        const u64 gword = ((u64)(unsigned)UNI(gw2[1]) << 32) | (unsigned)UNI(gw2[0]);
        u64 z = gword + 0x9e3779b97f4a7c15ull;
        z = (z ^ (z >> 30)) * 0xbf58476d1ce4e5b9ull;
        z = (z ^ (z >> 27)) * 0x94d049bb133111ebull;
        z = z ^ (z >> 31);
        z = (z == gword) ? ~z : z;
        epoch = u32x2{(unsigned)z, (unsigned)(z >> 32)};
        WAITV(A1E ? 11 : 7);
        TRC(1); BAR;
        if (!A1E) ldA(1);
      } else {
        ldW(1);
        if (XD) __builtin_amdgcn_s_sleep(XD);
        ldA(1);
      }
      ldA(2);
      TRC(40);
      ldW(2);
#pragma unroll
      for (int i = 0; i < 4; ++i) asm volatile("" : "=v"(xr[i >> 1][i & 1]));
      unsigned oA = offA(3), oW = 0;
      OPAQUE_S(oA);
      TRV(56, 0); TRV(57, 0);
      if (LAW == 3) ldW(3);
      if (!l0f) ldres(0);
      clean(0);
      WAITV(4 + !STG + (W2F ? 0 : 6) + (LAW == 3 ? 6 : 0));
      chkrd(1);
      if (__builtin_expect(chkbad(), 0)) refetch(1);
      const int fr = lane & 15, fq = lane >> 4;
      u32x2 fl;
      f32x4 js[4], jc[4];
      float ra[4], rb[4];
      const bool slowo = SLOWC(2 + (l & 1), j);
      const char* pO;
      unsigned zO, bO, pcO, vO, clo;
      u32x4 wo2[2];
      asm volatile("" : "=v"(wo2[0])); asm volatile("" : "=v"(wo2[1]));
      const unsigned fwo = myrep + fl_line(FL_WO + (isb ? 1 : 0), jr + (lane >> 4)) + (lane & 15) * 8;
      static_for<1, 14>([&](auto t_c) __attribute__((always_inline)) {
        constexpr int t = decltype(t_c)::value;
        constexpr auto rest = [](int s) {
          return s == 0 ? (W2F ? 0 : 6) + (LAW == 3 ? 6 : 0) + !STG
                        : (s + LAW <= 15 ? 6 : 0) + (s < 4 && !STG) + (s == NVAL - 2 || s == 5 || s == 11) + ((s == 7 || s == 8) ? 4 : 0);
        };
        constexpr int NW = t == 1 ? 63 : 4 + rest(t - 1) + ((LAW == 3 && (t > 2 || W2F)) ? rest(t - 2) : 0);
        TRC(1 + t);
        if (t >= NVAL && t <= 6 && __builtin_expect((ready & (1u << kseq(t + 2))) == 0, 0)) {
          WAITV(NW); WAITL0; BAR;
          ready = poll_need(rs, flo, epoch, ready, 1u << kseq(t + 2), dead);
          dma4<4096>(rA, vA, oA, pcA, aslot(t + 2));
        } else if (t == 7 && __builtin_expect(ready != ~0u, 0)) {
          WAITV(NW); WAITL0; BAR;
          poll_all(rs, flo, epoch, dead);
          dma4<4096>(rA, vA, oA, pcA, aslot(t + 2));
        } else {
          bar_dma4<4096, NW>(rA, vA, oA, pcA, aslot(t + 2));
        }
        if (t == 1 && lead) set_flags(rs, fl_line(FL_X + l, g), j, l0f ? fl_line(FL_CS, j) : fl_line(FL_X + l, g), l0f ? g : j, epoch, lane);
        if (t == 7 && slowj) packedW();
        if (t + LAW <= 15) oW = offW(t + LAW);
        if (t < 13) oA = offA(t + 3);
        OPAQUE_S(oA); OPAQUE_S(oW);
        clean(t);
        if (t <= NVAL && !l0f) { WAITV(rest(t - 1) + 4 + (t < 4 && !STG)); chkrd(t + 1); }
        if (t == WT + 3 && l0f && SLOWC(2, jr + hw)) {
          OPAQUE_V(wo2[0]); OPAQUE_V(wo2[1]);
#pragma unroll
          for (int i = 0; i < 2; ++i) st16_sc1(rs, lin, WS_WO + (unsigned)((jr + hw) * 131072 + g * 8192 + ((j & 3) * 2 + i) * 1024), wo2[i]);
        }
        if (t == WT + 6 && l0f && lead) set_flags(rs, fl_line(FL_WO, j), g, fl_line(FL_WO, j), g, epoch, lane);
        if (t == 9 && l0f) {
          const int ch = (fq & 1) * 2 + (fq >> 1);
#pragma unroll
          for (int i = 0; i < 4; ++i) ld16in_asm(xr[i >> 1][i & 1], rA, fr * 2048 + ch * 16, (g * 128 + hw * 32 + ((i >> 1) ^ 1) * 16) * 2048 + j * 128 + (i & 1) * 64);
        }
        if (t == 7) {
#pragma unroll
          for (int i = 0; i < 4; ++i) ld16_asm(jc[i], rs, fq * 768 + hw * 192 + (fr < 12 ? fr : 0) * 16, WS_CS + (unsigned)(((isb ? 16 : 0) + j) * 16 + i * 4) * 768);
        }
        if (t == 8) {
#pragma unroll
          for (int i = 0; i < 4; ++i) ld16_asm(js[i], rs, fq * 1024 + hw * 256 + fr * 16, st_off(g, l) + i * 4096);
        }
        SCHED;
        if (t + LAW <= 15) ldWh(t + LAW, oW, 0);
        SCHED;
        bool bad = false;
        if (t <= NVAL) bad = chkbad();
        if (t == 1) {
          const unsigned fx = fl_line(FL_X + l, g), fc = fl_line(l0f ? FL_CS : FL_PK, jr) + ((j + fq) & 3) * 128;
          flo = myrep + ((l < 2 && lane >= 16) ? fc : fx) + fr * 8;
          OPAQUE_V(flo);
        }
        if (t == 9) {
          pO = slowo ? p.ws : (const char*)(isb ? p.woB : p.woA);
          zO = slowo ? WS_SPAN : 1024 * 2048; bO = slowo ? WS_WO + (unsigned)((isb ? 16 : 0) + j) * 131072 : j * 128; pcO = slowo ? 4096 : 32 * 2048;
          vO = slowo ? vlin : (hw * 8 + (lane >> 3)) * 2048 + wo_swz(lane);
          clo = myrep + ((lane & 16) ? fl_line(FL_CLC + l, (lane & 32) ? j ^ RX : j) : fl_line(FL_CLR + l, g)) + fr * 8;
          OPAQUE_S(pO); OPAQUE_S(zO); OPAQUE_S(bO); OPAQUE_S(pcO); OPAQUE_V(vO); OPAQUE_V(clo);
        }
        if (t == 10) {
#pragma unroll
          for (int i = 0; i < 4; ++i) OPAQUE_V(jc[i]);
          const f32x4 s = (jc[0] + jc[1]) + (jc[2] + jc[3]);
#pragma unroll
          for (int i = 0; i < 4; ++i) { ra[i] = s[i]; OPAQUE_V(ra[i]); }
        }
        if (t == 11) {
#pragma unroll
          for (int i = 0; i < 4; ++i) OPAQUE_V(js[i]);
          const f32x4 s = (js[0] + js[1]) + (js[2] + js[3]);
#pragma unroll
          for (int i = 0; i < 4; ++i) { rb[i] = s[i]; OPAQUE_V(rb[i]); }
        }
        if (t == 12) {
          red2_add(ra[0], ra[1]); red2_add(ra[2], ra[3]); red2_add(rb[0], rb[1]); red2_add(rb[2], rb[3]);
          if (lane < 12) lds_st<f32x4>(lds0 + L_CS + hw * 192 + lane * 16, f32x4{ra[0], ra[1], ra[2], ra[3]});
#pragma unroll
          for (int i = 0; i < 4; ++i) OPAQUE_V(rb[i]);
        }
        if (t == 13) {
          const float m0 = rb[0] * (1.0f / 1024), m1 = rb[2] * (1.0f / 1024);
          const float r0 = __builtin_amdgcn_rsqf(fmaxf(__builtin_fmaf(rb[1], 1.0f / 1024, -m0 * m0), 0.f) + 1e-5f);
          const float r1 = __builtin_amdgcn_rsqf(fmaxf(__builtin_fmaf(rb[3], 1.0f / 1024, -m1 * m1), 0.f) + 1e-5f);
          if (lane < 16) {
            lds_st<f32x2>(lds0 + L_MEAN + 64 + hw * 128 + fr * 8, f32x2{-m0, -m1});
            lds_st<f32x2>(lds0 + L_RSTD + 64 + hw * 128 + fr * 8, f32x2{r0, r1});
          }
        }
        SCHED;
        if (t + LAW <= 15) ldWh(t + LAW, oW, 1);
        if (t == NVAL - 1 || t == 6) {
          WAITV(10);
          OPAQUE_V(fl);
          if (t == 6) ready = __all(is_set(fl, epoch)) ? ~0u : 0u;
          else {
            ready = l0f ? ~0u : (unsigned)__ballot(is_set(fl, epoch));
            if (slowj && __all(is_set(fl, epoch))) packedW();
          }
        }
        if (t == 12) { WAITV(10); OPAQUE_V(fl); }
        if (t == 13 && slowo && __builtin_expect(!__all(is_set(fl, epoch)), 0)) poll_all(rs, fwo, epoch, dead);
        if (t == NVAL - 2 || t == 5) ld8_asm(fl, rs, flo);
        if (t == 11) ld8_asm(fl, rs, fwo);
        if (t == WT && l0f) {
#pragma unroll
          for (int i = 0; i < 2; ++i) ld16in_asm(wo2[i], mk_rsrc(p.woA, 1024 * 2048), (lane >> 3) * 2048 + wo_swz(lane), (g * 64 + (j & 3) * 16 + i * 8) * 2048 + (jr + hw) * 128);
        }
        if constexpr (t <= NVAL) { if (__builtin_expect(bad, 0)) refetch(t + 1); }
      });
      auto stO = [&](int i) { dma4<4096>(mk_rsrc(pO, zO), vO, bO + wo_src(i, j) * (4 * pcO), pcO, lds0 + wo_slot(i) + hw * 1024); };
      auto clcheck = [&]() {
        OPAQUE_V(fl);
        TRV(58, 0);
        if (__builtin_expect(!__all(is_set(fl, epoch)), 0)) { TRV(58, 1); poll_all(rs, clo, epoch, dead); }
      };
      auto accinit = [&]() {
#pragma unroll
        for (int m = 0; m < 2; ++m)
#pragma unroll
          for (int pp = 0; pp < 2; ++pp) {
            auto r0_ = __builtin_amdgcn_permlane16_swap(xr[m][pp][0], xr[m][pp][2], false, false);
            auto r1_ = __builtin_amdgcn_permlane16_swap(xr[m][pp][1], xr[m][pp][3], false, false);
            acc[m][2 * pp] = f32x4{lo16(r0_[0]), hi16(r0_[0]), lo16(r1_[0]), hi16(r1_[0])};
            acc[m][2 * pp + 1] = f32x4{lo16(r0_[1]), hi16(r0_[1]), lo16(r1_[1]), hi16(r1_[1])};
          }
#pragma unroll
        for (int i = 0; i < 8; ++i) OPAQUE_V(acc[i >> 2][i & 3]);
      };
      WAITV(10);
      WAITL0;
      TRC(15); BAR;
      if (lead) set_flags(rs, fl_line(FL_CLR + l, g), j, fl_line(FL_CLC + l, j), g, epoch, lane);
      if (isb) {
        stO(0); stO(2); stO(3);
        WAITV(12);
        TRC(16); BAR;
        stO(1); stO(4); stO(5);
        __builtin_amdgcn_s_setprio(0);
        TRC(17); BAR;
        __builtin_amdgcn_s_sleep(BD);
        stO(7); stO(6);
        ld8_asm(fl, rs, clo);
        TRC(18); BAR;
        accinit();
        WAITV(0);
        clcheck();
      } else {
        u32x4 vb[8];
#pragma unroll
        for (int i = 0; i < 8; ++i) asm volatile("" : "=v"(vb[i]));
        stO(0); stO(2); stO(3);
        WAITV(12);
        TRC(16); BAR;
        stO(1);
        if (l0f) {
          const bool wj = hw != 0;
          const char* bp = (const char*)(wj ? p.wqkvB : p.woB);
          unsigned rs_ = wj ? 6144 : 2048, sz = rs_ * 1024, ss = rs_ * 8, s2 = wj ? 16 * 6144 : 128;
          unsigned sb = g * 64 * rs_ + (wj ? (hw - 1) * 2048 + JOBC(j, hw - 1) * 128 : (j & 3) * (16 * 2048) + jr * 128);
          OPAQUE_S(bp); OPAQUE_S(rs_); OPAQUE_S(sz); OPAQUE_S(sb); OPAQUE_S(ss); OPAQUE_S(s2);
          const rsrc_t rb = mk_rsrc(bp, sz);
#pragma unroll
          for (int i = 0; i < 8; ++i) ld16in_asm(vb[i], rb, (lane >> 3) * rs_ + (lane & 7) * 16, sb + (i & 1) * ss + (i >> 1) * s2);
        }
        __builtin_amdgcn_s_setprio(0);
        KV_ADDR;
        bf16x8 sel = rowsum_sel(lane);
        unsigned h1 = (lane >> 3) * KVP_G + (lane & 7) * 16;
        unsigned pw = lds0 + SC_PHIK + (h * 128 + wm * 64 + 32 + fr) * 160 + fq * 8, kw = lds0 + L_PREV + hw * 2048 + lin;
        const unsigned wa = lds0 + L_WPHI + h * 2048 + (fq * 4 + (fr >> 2)) * 64 + (lane & 3) * 8;
        bf16x8 w0 = tr2(wa, wa + 1024), w1 = tr2(wa + 32, wa + 32 + 1024);
        float cq[4];
#pragma unroll
        for (int m = 0; m < 2; ++m) { cq[m] = lds_ld<float>(lds0 + L_RSTD + (16 + wm * 64 + 32 + m * 16 + fr) * 4) * 1.4426950408889634f; OPAQUE_V(cq[m]); }
        OPAQUE_V(sel); OPAQUE_V(h1); OPAQUE_V(pw); OPAQUE_V(kw); OPAQUE_V(w0); OPAQUE_V(w1);
        TRC(17); BAR;
        TRC(18); BAR;
        ld8_asm(fl, rs, clo);
        {
          const f32x4 zero = {0.f, 0.f, 0.f, 0.f};
          f32x4 r[4][2];
#pragma unroll
          for (int m = 0; m < 2; ++m) {
            const bf16x8 f = lds_ld<bf16x8>(kw + m * 1024);
            r[m][0] = MFMA(w0, f, zero); r[m][1] = MFMA(w1, f, zero);
          }
          phik2(r, cq, pw);
        }
        WAITV(0);
#pragma unroll
        for (int i = 0; i < 8; ++i) OPAQUE_V(vb[i]);
        clcheck();
        WAITL0;
        TRC(19); BAR;
        kvpart(va, pa, kvj + (unsigned)g * KVP_G, lane, wave);
        CLOBBER;
        TRC(20); BAR;
        auto job = [&]() __attribute__((always_inline)) {
        if (l0f) {
          if (hw) {
            job_colsum(rs, vb, 1, g, JOBC(j, hw - 1), hw - 1, lane);
            if (SLOWC(1, JOBC(j, hw - 1))) {
#pragma unroll
              for (int i = 0; i < 8; ++i) job_pack(rs, vb[i], i, 1, g, JOBC(j, hw - 1), hw - 1, lane);
            }
          } else {
#pragma unroll
            for (int i = 0; i < 8; ++i)
              if (SLOWC(3, jr + (i >> 1))) st16_sc1(rs, (lane >> 3) * 128 + wo_swz(lane), WS_WO + (unsigned)((16 + jr + (i >> 1)) * 131072 + g * 8192 + ((j & 3) * 2 + (i & 1)) * 1024), vb[i]);
          }
        }
        };
        if (JOBP) job();
        if (H1D > 0) stO(4);
        if (H1D > 1) stO(5);
        const f32x4 zero = {0.f, 0.f, 0.f, 0.f};
        const unsigned s1 = kvj + (unsigned)((j ^ RX) - j) * KVP_J + g * 512 + hw * 128;
        if (H1S) __builtin_amdgcn_s_sleep(H1S);
        u32x4 v[2] = {ld16p<PA1>(rs, h1, s1), ld16p<PA1>(rs, h1, s1 + 8 * KVP_G)};
        TRC(21); TRV(50, 0);
        for (unsigned spins = 0; !dead && __any(umax4v(umax4v(0u, v[0]), v[1]) == SENT); ++spins) {
          if (spins > SPIN_CAP) { dead = true; break; }
          CLOBBER;
          TRV(50, spins + 1);
          if (H1P) __builtin_amdgcn_s_sleep(H1P);
          v[0] = ld16p<PA1>(rs, h1, s1); v[1] = ld16p<PA1>(rs, h1, s1 + 8 * KVP_G);
        }
        TRC(22);
        const f32x4 c = rowsum_fin(MFMA(sel, as_frag(v[1]), MFMA(sel, as_frag(v[0]), zero)));
        if ((lane & 40) == 0)
          st8_sc1(rs, fr * 16 + fq * 8, kvj + (unsigned)g * KVP_G + KVP_SUM + hw * 128, u32x2{min(pk2(c[0], c[1]), SENT - 1), min(pk2(c[2], c[3]), SENT - 1)});
        CLOBBER;
        TRC(43); BAR;
        if (JOBP && l0f && lead) set_flags(rs, fl_line(FL_PK, j), g, fl_line(FL_WO + 1, j), g, epoch, lane);
        accinit();
        if (H2D) __builtin_amdgcn_s_sleep(H2D);
        if (H1D < 1) stO(4);
        if (H1D < 2) stO(5);
        stO(6); stO(7);
        if (!JOBP) job();
        WAITV(0);
      }
    }
    {
      LOCALS;
      const int fr = lane & 15, fq = lane >> 4;
      const int kh = half;
      unsigned v0 = lane * 16 + hw * 4096 + kh * 2048, v1 = lane * 16 + hw * 4096 + (kh ^ 1) * 2048;
      const int r0 = (JROT(j) >> 1) + NJ / 2;
      const unsigned ob0 = ob_off(g, kh, l);
      const unsigned pko = myrep + fl_line(FL_PK, JOBC(j, fq - 1)) + fr * 8;
      u32x4 R[8][4];
      u32x2 pkf;
      auto issue_ = [&](int i) {
        const unsigned so = ob0 + (unsigned)((r0 + i) & 7) * (2 * TS);
        R[i][0] = ld16_sc1(rs, v0, so); R[i][1] = ld16_sc1(rs, v1, so);
        R[i][2] = ld16_sc1(rs, v0 + 1024, so); R[i][3] = ld16_sc1(rs, v1 + 1024, so);
      };
      auto issue = [&](int i) { issue_(i); SCHED; };
      constexpr int LA = LOOKAHEAD;
      TRV(52, 0); TRV(53, 0); TRV(54, 0);
      TRR(61);
      __builtin_amdgcn_s_setprio(3);
      unsigned so0 = ob0 + (unsigned)(r0 & 7) * (2 * TS);
      OPAQUE_V(v0); OPAQUE_V(v1); OPAQUE_S(so0);
      TRC(25); BAR;
      if (DLY && !isb) { for (int d = (g >> 1) * DLY; d > 0; --d) __builtin_amdgcn_s_sleep(1); }
      SCHED;
      R[0][0] = ld16p<PA3>(rs, v0, so0); R[0][1] = ld16p<PA3>(rs, v1, so0);
      R[0][2] = ld16p<PA3>(rs, v0 + 1024, so0); R[0][3] = ld16p<PA3>(rs, v1 + 1024, so0);
      SCHED;
      __builtin_amdgcn_s_setprio(2);
      if (!JOBP && l == 0 && lead) set_flags(rs, fl_line(FL_PK, j), g, fl_line(FL_WO + 1, j), g, epoch, lane);

      auto vmax = [&](int i) { return umax4v(umax4v(umax4v(umax4v(0u, R[i][0]), R[i][1]), R[i][2]), R[i][3]); };
      auto bad = [&](int i) { return __any(umax4v(umax4v(umax4v(umax4v(0u, R[i][0]), R[i][1]), R[i][2]), R[i][3]) == SENT); };
      auto poll = [&](int i, int nfl) {
        for (unsigned spins = 0; !dead; ++spins) {
          if (spins > SPIN_CAP) { dead = true; break; }
          __builtin_amdgcn_s_sleep(2);
          CLOBBER;
          TRV(52 + (i > 2 ? 2 : i), spins + 1);
          issue(i);
#pragma unroll
          for (int k = i + 1; k < i + nfl && k < 8; ++k)
            if (bad(k)) issue(k);
          WAITV(0);
#pragma unroll
          for (int k = i + 1; k < i + nfl && k < 8; ++k)
#pragma unroll
            for (int f = 0; f < 4; ++f) OPAQUE_V(R[k][f]);
          if (!bad(i)) break;
        }
      };
      unsigned bb[4];
      {
        const int kr = fq * 4 + (fr >> 2);
        const int gs = (kr >> 1) & 3;
#pragma unroll
        for (int c = 0; c < 4; ++c) bb[c] = lds0 + kh * 8192 + kr * 128 + ((c ^ gs) * 32) + (lane & 3) * 8;
      }
      bf16x8 bf[2][4];
      auto rdB = [&](int i, int hh) {
        if (hh == 0) {
#pragma unroll
          for (int c = 0; c < 4; ++c) { bb[c] += wo_slot(i) - (i ? wo_slot(i - 1) : 0u); asm("" : "+v"(bb[c])); }
        }
#pragma unroll
        for (int c = 0; c < 4; ++c) bf[hh][c] = tr2(bb[c] + hh * 4096, bb[c] + hh * 4096 + 2048);
      };
      auto mms = [&](int i, int hh) {
#pragma unroll
        for (int n = 0; n < 4; ++n)
#pragma unroll
          for (int m = 0; m < 2; ++m) acc[m][n] = MFMA(bf[hh][n], as_frag(R[i][hh * 2 + m]), acc[m][n]);
      };
      if (!half) {
#pragma unroll
        for (int i = 0; i < 8; ++i) acc[i >> 2][i & 3] = f32x4{0.f, 0.f, 0.f, 0.f};
      }
      const char* obase = (l == 3) ? (const char*)p.out : (const char*)p.ws;
      unsigned osize = (l == 3) ? 2048 * 2048 : WS_SPAN;
      const bool slown = SLOWC((l & 1) ^ 1, j);
      pW = slown ? p.ws : (const char*)(isb ? p.wqkvA : p.wqkvB);
      bW = slown ? WS_WP + (unsigned)((isb ? 0 : 16) + j) * (16 * 24576) : j * 128;
      kW = slown ? 24576 : 64 * 6144; zW = slown ? WS_SPAN : 1024 * 6144; pcW = slown ? 8192 : 2048; hW = slown ? 4096 : 32 * 6144;
      vA = hw * 1024 + lane * 16;
      vW = slown ? vA : (hw * 8 + (lane >> 3)) * 6144 + (((lane & 7) ^ ((((lane >> 4) & 1) | ((hw & 1) << 1)) << 1)) * 16);
      pA = p.ws; zA = WS_SPAN; bA = xb_off(g, l); kA = TS; pcA = 4096; kb = JROT(j) + KX; dj = (j - kb) & 15;
      OPAQUE_S(pW); OPAQUE_S(bW); OPAQUE_S(kW); OPAQUE_S(zW); OPAQUE_S(pcW); OPAQUE_S(hW); OPAQUE_V(vA); OPAQUE_V(vW); OPAQUE_S(bA);
      unsigned sq = bW + j * kW + kh * hW, dst = lds0 + L_R1 + kh * 4096 + hw * 1024;
      OPAQUE_S(obase); OPAQUE_S(osize); OPAQUE_S(sq); OPAQUE_S(dst);
      const rsrc_t ro = mk_rsrc(obase, osize);
      unsigned so[2];
      unsigned xw = lds0 + SC_XCH + wave * 4096 + lane * 16;
      unsigned xa = lds0 + SC_XCH + (wave ^ 4) * 4096 + lane * 16;
      unsigned sb0 = (l == 3) ? g * (128 * 2048) + j * 128 : xb_off(g, l) + j * TS;
      {
        const int row = (hw * 2 + kh) * 16 + fr, ch = (fq & 1) * 2 + (fq >> 1);
        const unsigned sw = (l == 3) ? 0u : (unsigned)((row >> 1) & 7), rb = (unsigned)row << ((l == 3) ? 11 : 7);
#pragma unroll
        for (int pp = 0; pp < 2; ++pp) { so[pp] = rb + (((ch + 4 * pp) ^ sw) * 16); OPAQUE_V(so[pp]); }
        OPAQUE_V(xa); OPAQUE_V(xw); OPAQUE_S(sb0);
      }
      SCHED;
#pragma unroll
      for (int i = 1; i < LA; ++i) issue(i);
      rdB(0, 0);
      if (PRE) {
        f32x4 dm = {0.f, 0.f, 0.f, 0.f};
#pragma unroll 1
        for (int d = 0; d < PRE; ++d) {
#pragma unroll
          for (int c = 0; c < 4; ++c) dm = MFMA(bf[0][c], bf[0][(c + 1) & 3], dm);
        }
        asm volatile("" ::"v"(dm));
        SCHED;
      }
      unsigned mx = vmax(0);
#pragma unroll
      for (int i = 0; i < 8; ++i) {
        SCHED;
        if (__builtin_expect(__any(mx == SENT), 0)) poll(i, LA);
        if (i == 0) TRC(27); else TRQ(27 + i);
        if (i == W1S - 2) { pkf = ld8_sc1(rs, pko, 0); SCHED; }
        if (i == W1S) {
          OPAQUE_V(pkf);
          if (l == 0 && lead && slown && !__all(is_set(pkf, epoch))) poll_all(rs, pko, epoch, dead);
          TRC(35); BAR;
        }
        if (i == 7 && l < 3) dma3<8192>(mk_rsrc(pW, zW), vW, sq, pcW, dst);
        if (PACE && !isb) __builtin_amdgcn_s_sleep(PACE);
        if (i == 2) __builtin_amdgcn_s_setprio(1);
        if (i == 5) __builtin_amdgcn_s_setprio(0);
        SCHED;
        rdB(i, 1);
        if (i == 7) break;
        if (i + LA < 8) issue_(i + LA);
        mms(i, 0);
#pragma unroll
        for (int k = 0; k < 4; ++k) { SGB(0x008, 1); SGB(0x100, 1); SGB(0x020, 1); SGB(0x008, 1); SGB(0x100, 1); }
        SCHED;
        rdB(i + 1, 0); mx = vmax(i + 1);
        mms(i, 1);
#pragma unroll
        for (int k = 0; k < 4; ++k) { SGB(0x008, 1); SGB(0x100, 2); }
#pragma unroll
        for (int k = 4; k < 8; ++k) { SGB(0x008, 1); SGB(0x002, 8 / (8 - 4)); }
      }
#pragma unroll
      for (int n = 0; n < 4; ++n) acc[1][n] = MFMA(bf[0][n], as_frag(R[7][1]), acc[1][n]);
#pragma unroll
      for (int n = 0; n < 4; ++n) acc[0][n] = MFMA(bf[0][n], as_frag(R[7][0]), acc[0][n]);
#pragma unroll
      for (int k = 0; k < 8; ++k) { SGB(0x008, 1); SGB(0x100, 1); }
      SCHED;
#pragma unroll
      for (int n = 0; n < 4; ++n) acc[1][n] = MFMA(bf[1][n], as_frag(R[7][3]), acc[1][n]);
#pragma unroll
      for (int n = 0; n < 4; ++n) acc[0][n] = MFMA(bf[1][n], as_frag(R[7][2]), acc[0][n]);
#pragma unroll
      for (int n = 0; n < 4; ++n) lds_st<f32x4>(xw + n * 1024, acc[1][n]);
      SGB(0x008, 4);
#pragma unroll
      for (int k = 0; k < 4; ++k) { SGB(0x008, 1); SGB(0x200, 1); }
      SCHED;
      TRC(36);
      WAITL0;
      TRC(37); BAR;
      SCHED;
      f32x4 pa[4];
#pragma unroll
      for (int n = 0; n < 4; ++n) pa[n] = lds_ld<f32x4>(xa + n * 1024);
      SCHED;
      u32x4 o[2];
#pragma unroll
      for (int pp = 0; pp < 2; ++pp) {
        const f32x4 a = acc[0][2 * pp] + pa[2 * pp], b = acc[0][2 * pp + 1] + pa[2 * pp + 1];
        auto r0_ = __builtin_amdgcn_permlane16_swap(pk2(a[0], a[1]), pk2(b[0], b[1]), false, false);
        auto r1_ = __builtin_amdgcn_permlane16_swap(pk2(a[2], a[3]), pk2(b[2], b[3]), false, false);
        o[pp] = nosent(u32x4{r0_[0], r1_[0], r0_[1], r1_[1]});
        st16_sc1(ro, so[pp], sb0, o[pp]);
      }
      if (l < 3) {
        lds_st<u32x4>(lds0 + L_R2 + so[0], o[0]);
        lds_st<u32x4>(lds0 + L_R2 + so[1], o[1]);
        const f32x4 zero = {0.f, 0.f, 0.f, 0.f};
        const bf16x8 ones = {0x3f80, 0x3f80, 0x3f80, 0x3f80, 0x3f80, 0x3f80, 0x3f80, 0x3f80};
        const f32x4 a = MFMA(ones, as_frag(o[1]), MFMA(ones, as_frag(o[0]), zero));
        const f32x4 b = MFMA(as_frag(o[1]), as_frag(o[1]), MFMA(as_frag(o[0]), as_frag(o[0]), zero));
        const float q = (fr & 2) ? ((fr & 1) ? b[3] : b[2]) : ((fr & 1) ? b[1] : b[0]);
        if (fq == (fr >> 2)) st8_sc1(rs, ((hw * 2 + kh) * 16 + fr) * 8, st_off(g, l + 1) + j * 1024, u32x2{__float_as_uint(a[0]), __float_as_uint(q)});
      } else if (lead && blockIdx.x == 0 && lane == 0) {
        st8_sc1(rs, WS_EPOCH, 0, epoch);
      }
    }
    TRC(38); TRR(63);
  }
  STAMP_DUMP;
  asm volatile(".Lpf_end:");
}

extern "C" void kernel_launch(void* const* d_in, const int* in_sizes, int n_in, void* d_out, int out_size,
                              void* d_ws, size_t ws_size, hipStream_t stream) {
  Params p;
  p.x = (const u16*)d_in[0];
  p.wqkvA = (const u16*)d_in[1];
  p.wphi = (const u16*)d_in[2];
  p.woA = (const u16*)d_in[3];
  p.wqkvB = (const u16*)d_in[5];
  p.woB = (const u16*)d_in[6];
  p.out = (u16*)d_out;
  p.ws = (char*)d_ws;
  (void)hipFuncSetAttribute((const void*)mega_kernel, hipFuncAttributeMaxDynamicSharedMemorySize, L_TOTAL);
  mega_kernel<<<256, 512, L_TOTAL, stream>>>(p);
}
```

```cpp
#include <hip/hip_runtime.h>
#include <stdint.h>
#include <type_traits>
constexpr int LOOKAHEAD = 2;
constexpr int NVAL = 4;
#define XD 0
#define WT 1
#define LAW 2
#define POL0 0
#define PKALL 0u
#define PA1 16
#define PA2 16
#define PA3 16
#define H1S 0
#define H1P 0
#define SPR 2
#define A1E 0
#define KG 32768
#define TS 17664
#define KOD 0
#define STG 1
#define JOBP 0
#define NORS 0
#define DLY 0
#define PRE 0
#define PACE 0
#define NJ 8
#define KX 0
#define ROT 1
#define BD 14
#define W1S 7
#define W2F 0
#define RX 8
#define H1D 2
#define H2D 5

typedef unsigned short u16;
typedef unsigned long long u64;
typedef __attribute__((ext_vector_type(8))) short bf16x8;
typedef __attribute__((ext_vector_type(4))) short s16x4;
typedef __attribute__((ext_vector_type(4))) float f32x4;
typedef __attribute__((ext_vector_type(2))) float f32x2;
typedef __attribute__((ext_vector_type(4))) unsigned u32x4;
typedef __attribute__((ext_vector_type(2))) unsigned u32x2;
typedef __attribute__((ext_vector_type(2))) __bf16 bf16x2_t;
#define LAS __attribute__((address_space(3)))
typedef LAS s16x4* lds_s16x4_ptr;

constexpr unsigned NREP = 8, REP_STRIDE = 17 * 2048;
enum { FL_X = 0, FL_CS = 4, FL_CLR = 5, FL_CLC = 9, FL_PK = 13, FL_WO = 14 };
__device__ __forceinline__ unsigned fl_line(int set, int a) { return (unsigned)(set * 16 + a) * 128; }
constexpr unsigned WS_EPOCH = 512u << 10;
constexpr unsigned WS_CS = 1u << 20;
constexpr unsigned WS_T = 4u << 20, TD = 256 * TS;
constexpr unsigned WS_KVP = WS_T + 2 * TD, KVP_G = 16 * TS, KVP_J = TS, KVP_L = 5 * TD, KVP_Z = 8192, KVP_SUM = 8704, KVP_HALO = 12u << 10;
constexpr unsigned SENT = 0xffffffffu;
constexpr unsigned WS_ST = 2u << 20;
__device__ __forceinline__ unsigned ob_off(int g, int j, int l) { return WS_T + (unsigned)((l >> 1) * 5 + (l & 1)) * TD + (unsigned)(g * 16 + j) * TS; }
__device__ __forceinline__ unsigned xb_off(int g, int l) { return ob_off(g, 0, l) + 3 * TD; }
__device__ __forceinline__ unsigned st_off(int g, int l) { return WS_ST + (unsigned)(l * 16 + g) * (16u << 10); }
constexpr unsigned WS_WP = 48u << 20;
constexpr unsigned WS_WO = 60u << 20;
constexpr unsigned WS_SPAN = 64u << 20;

constexpr unsigned L_R1 = 0;
constexpr unsigned L_R2 = 98304, XS = 16384;
constexpr unsigned L_MISC = 147456;
constexpr unsigned L_MEAN = L_MISC;
constexpr unsigned L_RSTD = L_MISC + 576;
constexpr unsigned L_CS = L_MISC + 1152;
constexpr unsigned L_WPHI = L_MISC + 2688;
constexpr unsigned L_SINK = L_MISC + 6912;
constexpr unsigned L_ZF = L_MISC + 1920;
constexpr unsigned L_PREV = L_MISC + 8192;
constexpr unsigned L_TOTAL = 163840;

#define JROT(j) ((j) & ~(NJ - 1))
constexpr int NX = NJ / 2;
#define KB0(g, j) (JROT(j) + (16 / NX) * ((g) & (NX - 1)))
#define JOBC(j, w) (((j) & ~3) + (((j) + (w) + 1) & 3))

#define SLOWC(m, jc) ((slowm >> ((m) * 8 + ((jc) & 7))) & 1)

struct Params {
  const u16* x;
  const u16* wqkvA;
  const u16* wphi;
  const u16* woA;
  const u16* wqkvB;
  const u16* woB;
  u16* out;
  char* ws;
};

#define SCHED __builtin_amdgcn_sched_barrier(0)
#define BAR __builtin_amdgcn_s_barrier()
#define SGB(m, n) __builtin_amdgcn_sched_group_barrier(m, n, 0)
#define WAITV(n) asm volatile("s_waitcnt vmcnt(%0)" ::"n"(n) : "memory")
#define WAITL0 asm volatile("s_waitcnt lgkmcnt(0)" ::: "memory")

#define TRV(slot, val) do {} while (0)
#define TRQ(slot) do {} while (0)
#define TRC(slot) do {} while (0)
#define TRR(slot) do {} while (0)
#define STAMP_INIT do {} while (0)
#define STAMP_DUMP do {} while (0)

__device__ __forceinline__ unsigned pk2(float a, float b) {
  f32x2 v = {a, b};
  return __builtin_bit_cast(unsigned, __builtin_convertvector(v, bf16x2_t));
}
__device__ __forceinline__ float lo16(unsigned u) { return __uint_as_float(u << 16); }
__device__ __forceinline__ float hi16(unsigned u) { return __uint_as_float(u & 0xffff0000u); }

template <int A, int B, typename F> __device__ __forceinline__ void static_for(F f) {
  if constexpr (A < B) { f(std::integral_constant<int, A>{}); static_for<A + 1, B>(f); }
}
typedef __amdgpu_buffer_rsrc_t rsrc_t;
__device__ __forceinline__ rsrc_t mk_rsrc(const void* p, unsigned bytes) { return __builtin_amdgcn_make_buffer_rsrc((void*)p, 0, bytes, 0x00020000); }
#define UNI(x) __builtin_amdgcn_readfirstlane(x)
#define DMA_HEAD(P) "s_mov_b32 m0, %4\n\ts_mov_b32 %0, %3\n\ts_nop 2\n\tbuffer_load_dwordx4 %1, %2, %0 offen " P " lds\n\t"
#define DMA_NEXT(P) "s_add_u32 m0, m0, %6\n\ts_add_u32 %0, %0, %5\n\tbuffer_load_dwordx4 %1, %2, %0 offen " P " lds\n\t"
#define DMA_OPS : "=&s"(t) : "v"(voff), "s"(r), "s"(UNI(soff)), "s"(UNI(lds)), "s"(UNI(sstep)), "i"(LSTEP) : "memory", "scc", "m0"
template <int LSTEP, int POL = 1> __device__ __forceinline__ void dma4(rsrc_t r, unsigned voff, unsigned soff, unsigned sstep, unsigned lds) {
  unsigned t;
  if constexpr (POL == 1) asm volatile(DMA_HEAD("sc1") DMA_NEXT("sc1") DMA_NEXT("sc1") DMA_NEXT("sc1") DMA_OPS);
  else if constexpr (POL == 2) asm volatile(DMA_HEAD("nt") DMA_NEXT("nt") DMA_NEXT("nt") DMA_NEXT("nt") DMA_OPS);
  else asm volatile(DMA_HEAD("") DMA_NEXT("") DMA_NEXT("") DMA_NEXT("") DMA_OPS);
}
template <int LSTEP, int POL = 1> __device__ __forceinline__ void dma3(rsrc_t r, unsigned voff, unsigned soff, unsigned sstep, unsigned lds) {
  unsigned t;
  if constexpr (POL == 1) asm volatile(DMA_HEAD("sc1") DMA_NEXT("sc1") DMA_NEXT("sc1") DMA_OPS);
  else if constexpr (POL == 2) asm volatile(DMA_HEAD("nt") DMA_NEXT("nt") DMA_NEXT("nt") DMA_OPS);
  else asm volatile(DMA_HEAD("") DMA_NEXT("") DMA_NEXT("") DMA_OPS);
}
template <int LSTEP, int N> __device__ __forceinline__ void bar_dma4(rsrc_t r, unsigned voff, unsigned soff, unsigned sstep, unsigned lds) {
  unsigned t;
  asm volatile("s_mov_b32 m0, %4\n\ts_mov_b32 %0, %3\n\ts_nop 0\n\ts_waitcnt vmcnt(%7) lgkmcnt(0)\n\ts_barrier\n\t"
               "buffer_load_dwordx4 %1, %2, %0 offen sc1 lds\n\t" DMA_NEXT("sc1") DMA_NEXT("sc1") DMA_NEXT("sc1")
               : "=&s"(t) : "v"(voff), "s"(r), "s"(UNI(soff)), "s"(UNI(lds)), "s"(UNI(sstep)), "i"(LSTEP), "n"(N) : "memory", "scc", "m0");
}
__device__ __forceinline__ void dma1(rsrc_t r, unsigned voff, unsigned soff, unsigned lds) {
  asm volatile("s_mov_b32 m0, %3\n\ts_nop 3\n\tbuffer_load_dwordx4 %0, %1, %2 offen sc1 lds" ::"v"(voff), "s"(r), "s"(UNI(soff)), "s"(UNI(lds)) : "memory", "m0");
}
__device__ __forceinline__ void touch(rsrc_t r, unsigned voff, unsigned lds) {
  asm volatile("s_mov_b32 m0, %2\n\ts_nop 3\n\tbuffer_load_dword %0, %1, 0 offen lds" ::"v"(voff), "s"(r), "s"(UNI(lds)) : "memory", "m0");
}
__device__ __forceinline__ void ld8_asm(u32x2& d, rsrc_t r, unsigned voff) {
  asm volatile("s_nop 4\n\tbuffer_load_dwordx2 %0, %1, %2, 0 offen sc1" : "=v"(d) : "v"(voff), "s"(r) : "memory");
}

__device__ __forceinline__ void ld16in_asm(u32x4& d, rsrc_t r, unsigned voff, unsigned soff) {
  asm volatile("s_nop 4\n\tbuffer_load_dwordx4 %0, %1, %2, %3 offen" : "=v"(d) : "v"(voff), "s"(r), "s"(UNI(soff)) : "memory");
}
__device__ __forceinline__ void ld16_asm(f32x4& d, rsrc_t r, unsigned voff, unsigned soff) {
  asm volatile("s_nop 4\n\tbuffer_load_dwordx4 %0, %1, %2, %3 offen sc1" : "=v"(d) : "v"(voff), "s"(r), "s"(UNI(soff)) : "memory");
}

template <typename T> __device__ __forceinline__ T lds_ld(unsigned a) { return *(LAS T*)(a); }
template <typename T> __device__ __forceinline__ void lds_st(unsigned a, T v) { *(LAS T*)(a) = v; }
__device__ __forceinline__ bf16x8 tr2(unsigned a0, unsigned a1) {
  s16x4 v0 = __builtin_amdgcn_ds_read_tr16_b64_v4i16((lds_s16x4_ptr)(a0));
  s16x4 v1 = __builtin_amdgcn_ds_read_tr16_b64_v4i16((lds_s16x4_ptr)(a1));
  return __builtin_shufflevector(v0, v1, 0, 1, 2, 3, 4, 5, 6, 7);
}
__device__ __forceinline__ bf16x8 as_frag(u32x4 v) { return __builtin_bit_cast(bf16x8, v); }
#define MFMA(a, b, c) __builtin_amdgcn_mfma_f32_16x16x32_bf16(a, b, c, 0, 0, 0)

__device__ __forceinline__ float vmax2(float a, float b) { float inf = __builtin_inff(); asm("" : "+s"(inf)); return __builtin_amdgcn_fmed3f(a, b, inf); }
#define SWAP16(a, b) __builtin_amdgcn_permlane16_swap(__float_as_uint(a), __float_as_uint(b), false, false)
#define SWAP32(a, b) __builtin_amdgcn_permlane32_swap(__float_as_uint(a), __float_as_uint(b), false, false)
__device__ __forceinline__ void red2_max(float& a, float& b) {
  auto r = SWAP16(a, b);
  float x = vmax2(__uint_as_float(r[0]), __uint_as_float(r[1]));
  r = SWAP32(x, x);
  x = vmax2(__uint_as_float(r[0]), __uint_as_float(r[1]));
  r = SWAP16(x, x);
  a = __uint_as_float(r[0]); b = __uint_as_float(r[1]);
}
__device__ __forceinline__ void red2_add(float& a, float& b) {
  auto r = SWAP16(a, b);
  float x = __uint_as_float(r[0]) + __uint_as_float(r[1]);
  r = SWAP32(x, x);
  x = __uint_as_float(r[0]) + __uint_as_float(r[1]);
  r = SWAP16(x, x);
  a = __uint_as_float(r[0]); b = __uint_as_float(r[1]);
}
template <bool MAX, typename M> __device__ __forceinline__ void red4(float& a, float& b, float& c, float& d, M mid) {
  auto op = [](unsigned x, unsigned y) { return MAX ? vmax2(__uint_as_float(x), __uint_as_float(y)) : __uint_as_float(x) + __uint_as_float(y); };
  auto r = SWAP16(a, b), q = SWAP16(c, d);
  const float x = op(r[0], r[1]), y = op(q[0], q[1]);
  r = SWAP32(x, y);
  const float w = mid(op(r[0], r[1]));
  r = SWAP32(w, w);
  q = __builtin_amdgcn_permlane16_swap(r[1], r[1], false, false);
  r = __builtin_amdgcn_permlane16_swap(r[0], r[0], false, false);
  a = __uint_as_float(r[0]); b = __uint_as_float(r[1]); c = __uint_as_float(q[0]); d = __uint_as_float(q[1]);
}
__device__ __forceinline__ float red_max4(float x) {
  auto r = __builtin_amdgcn_permlane16_swap(__float_as_uint(x), __float_as_uint(x), false, false);
  x = vmax2(__uint_as_float(r[0]), __uint_as_float(r[1]));
  r = __builtin_amdgcn_permlane32_swap(__float_as_uint(x), __float_as_uint(x), false, false);
  return vmax2(__uint_as_float(r[0]), __uint_as_float(r[1]));
}
__device__ __forceinline__ float red_add4(float x) {
  auto r = __builtin_amdgcn_permlane16_swap(__float_as_uint(x), __float_as_uint(x), false, false);
  x = __uint_as_float(r[0]) + __uint_as_float(r[1]);
  r = __builtin_amdgcn_permlane32_swap(__float_as_uint(x), __float_as_uint(x), false, false);
  return __uint_as_float(r[0]) + __uint_as_float(r[1]);
}

__device__ __forceinline__ void st16_sc1(rsrc_t r, unsigned voff, unsigned soff, u32x4 v) {
  __builtin_amdgcn_raw_buffer_store_b128(v, r, voff, soff, 16);
  asm volatile("s_nop 1" ::"v"(v) : "memory");
}
__device__ __forceinline__ void st8_sc1(rsrc_t r, unsigned voff, unsigned soff, u32x2 v) { __builtin_amdgcn_raw_buffer_store_b64(v, r, voff, soff, 16); }
__device__ __forceinline__ u32x4 ld16_sc1(rsrc_t r, unsigned voff, unsigned soff) { return __builtin_amdgcn_raw_buffer_load_b128(r, voff, soff, 16); }
template <int AUX> __device__ __forceinline__ u32x4 ld16p(rsrc_t r, unsigned voff, unsigned soff) { return __builtin_amdgcn_raw_buffer_load_b128(r, voff, soff, AUX); }
__device__ __forceinline__ u32x2 ld8_sc1(rsrc_t r, unsigned voff, unsigned soff) { return __builtin_amdgcn_raw_buffer_load_b64(r, voff, soff, 16); }
__device__ __forceinline__ f32x4 ld4f_sc1(rsrc_t r, unsigned voff, unsigned soff) { return __builtin_bit_cast(f32x4, __builtin_amdgcn_raw_buffer_load_b128(r, voff, soff, 16)); }
#define OPAQUE_V(x) asm volatile("" : "+v"(x))
#define OPAQUE_S(x) asm volatile("" : "+s"(x))

#define CLOBBER asm volatile("" ::: "memory")
constexpr unsigned SPIN_CAP = 1u << 17;
__device__ __forceinline__ bool is_set(u32x2 v, u32x2 epoch) { return v[0] == epoch[0] && v[1] == epoch[1]; }
__device__ __forceinline__ void set_flags(rsrc_t rs, unsigned lineA, int colA, unsigned lineB, int colB, u32x2 epoch, int lane) {
  const unsigned off = (lane & 7) * REP_STRIDE + ((lane & 8) ? lineB + colB * 8 : lineA + colA * 8);
  if (lane < 16) st8_sc1(rs, off, 0, epoch);
}
__device__ __forceinline__ unsigned poll_need(rsrc_t rs, unsigned voff, u32x2 epoch, unsigned ready, unsigned need, bool& dead) {
  for (unsigned spins = 0; !dead; ++spins) {
    CLOBBER;
    ready |= (unsigned)__ballot(is_set(ld8_sc1(rs, voff, 0), epoch));
    if ((ready & need) == need) break;
    if (spins > SPIN_CAP) dead = true;
    __builtin_amdgcn_s_sleep(1);
  }
  CLOBBER;
  return dead ? ~0u : ready;
}

__device__ __forceinline__ void poll_all(rsrc_t rs, unsigned voff, u32x2 epoch, bool& dead) {
  for (unsigned spins = 0; !dead; ++spins) {
    CLOBBER;
    if (__all(is_set(ld8_sc1(rs, voff, 0), epoch))) break;
    if (spins > SPIN_CAP) dead = true;
    __builtin_amdgcn_s_sleep(1);
  }
  CLOBBER;
}

constexpr unsigned SC_VF = L_R1 + 32768;
constexpr unsigned SC_PHIK = L_R1 + 49152;
constexpr unsigned SC_XCH = L_R2 + XS;
constexpr unsigned wo_slot(int s) {
  return s < 2 ? L_R2 + (unsigned)(s + 1) * XS : (s < 6 ? L_R1 + (unsigned)(s - 2) * 16384 : (s == 6 ? L_R1 + 81920 : L_R2));
}
__device__ __forceinline__ unsigned wo_src(int s, int j) { return (unsigned)(((JROT(j) >> 1) + NJ / 2 + s) & 7); }
__device__ __forceinline__ unsigned wo_swz(int lane) { return (unsigned)(((lane & 7) ^ ((lane >> 3) & 6)) * 16); }
__device__ __forceinline__ unsigned umax3(unsigned a, unsigned b, unsigned c) { return max(max(a, b), c); }
__device__ __forceinline__ unsigned umax4v(unsigned m, const u32x4& v) { return umax3(umax3(m, v[0], v[1]), v[2], v[3]); }
__device__ __forceinline__ u32x4 nosent(u32x4 v) { return u32x4{min(v[0], SENT - 1), min(v[1], SENT - 1), min(v[2], SENT - 1), min(v[3], SENT - 1)}; }

template <typename P, typename F>
__device__ __forceinline__ void qkv_loop(int isb, int wm, int h, int lane, bool first, int& trv, P pre, F fin) {
  constexpr unsigned lds0 = 0;
  f32x4 acc[4][6];
  const int fr = lane & 15, fq = lane >> 4;
  unsigned ab[2], bb[2];
  {
    const int r = wm * 64 + fr;
#pragma unroll
    for (int ks = 0; ks < 2; ++ks) ab[ks] = lds0 + L_R2 + r * 128 + (((ks * 4 + fq) ^ ((fr >> 1) & 7)) * 16);
    const int kr = fq * 8 + (fr >> 2);
    const int gs = ((kr >> 1) & 1) | (((kr >> 3) & 1) << 1);
#pragma unroll
    for (int c = 0; c < 2; ++c) bb[c] = lds0 + L_R1 + kr * 128 + (((h * 2 + c) ^ gs) * 32) + (lane & 3) * 8;
  }
#pragma unroll
  for (int m = 0; m < 4; ++m)
#pragma unroll
    for (int n = 0; n < 6; ++n) acc[m][n] = f32x4{0.f, 0.f, 0.f, 0.f};
  bf16x8 af[2][4], bf[2][6];
  auto rds = [&](int ks, int p) {
#pragma unroll
    for (int m = 0; m < 4; ++m) af[p][m] = lds_ld<bf16x8>(ab[ks] + m * 2048);
#pragma unroll
    for (int n = 0; n < 6; ++n) {
      const unsigned a = bb[n & 1] + (n >> 1) * 8192 + ks * 4096;
      bf[p][n] = tr2(a, a + 512);
    }
  };
  auto mms = [&](int p) {
#pragma unroll
    for (int n = 0; n < 6; ++n)
#pragma unroll
      for (int mm = 0; mm < 4; ++mm) { const int m = (n & 1) ? 3 - mm : mm; acc[m][n] = n < 4 ? MFMA(bf[p][n], af[p][m], acc[m][n]) : MFMA(af[p][m], bf[p][n], acc[m][n]); }
  };
  auto ilv2 = [&]() {
#pragma unroll
    for (int i = 0; i < 16; ++i) { SGB(0x008, 1); SGB(0x100, 1); }
#pragma unroll
    for (int i = 0; i < 4; ++i) { SGB(0x008, 1); SGB(0x002, 1); }
    SGB(0x008, 4);
  };
  auto ilv3 = [&]() {
#pragma unroll
    for (int i = 0; i < 16; ++i) { SGB(0x008, 1); SGB(0x100, 1); }
#pragma unroll
    for (int i = 0; i < 7; ++i) { SGB(0x008, 1); SGB(0x004, 2); }
    SGB(0x008, 1);
  };
  if (!first) { WAITV(3); WAITL0; }
  TRQ(1); BAR;
  int tslot = 2;
  rds(0, 0);
  int sa = 0, sb = 0;
  int da = XS, db = 24576;
  auto step = [&]() {
    ab[0] += da; ab[1] += da;
    bb[0] += db; bb[1] += db;
  };
  auto ring = [&]() {
    sa = (sa == 2) ? 0 : sa + 1;
    sb = (sb == 3) ? 0 : sb + 1;
    da = (sa == 2) ? -2 * (int)XS : (int)XS;
    db = (sb == 3) ? -3 * 24576 : 24576;
  };
#pragma unroll 1
  for (int t = 14; t > 0; --t) {
    SCHED;
    rds(1, 1); mms(0);
    step();
    ilv2();
    SCHED;
    __builtin_amdgcn_s_waitcnt(0x0070);
    TRQ(tslot); tslot++; BAR;
    SCHED;
    rds(0, 0);
    ring();
    mms(1); ilv3();
  }
  SCHED;
  f32x4 cs[4], nm4[4];
  float nmean[4], csv[2];
  auto fold = [&](int n) {
#pragma unroll
    for (int mm = 0; mm < 4; ++mm) {
      const int m = (n & 1) ? 3 - mm : mm;
#pragma unroll
      for (int e = 0; e < 4; ++e) {
        float t = n < 4 ? __builtin_fmaf(nmean[m], cs[n & 3][e], acc[m][n][e]) : __builtin_fmaf(nm4[m][e], csv[n & 1], acc[m][n][e]);
        asm("" : "+v"(t));
        acc[m][n][e] = t;
      }
    }
  };
  {
    unsigned pm = lds0 + L_MEAN + (16 + wm * 64 + fr) * 4, pc = lds0 + L_CS + (h * 32 + fq * 4) * 4;
    unsigned pm4 = lds0 + L_MEAN + (16 + wm * 64 + fq * 4) * 4, pcv = lds0 + L_CS + (128 + h * 32 + fr) * 4;
    asm("" : "+v"(pm)); asm("" : "+v"(pc)); asm("" : "+v"(pm4)); asm("" : "+v"(pcv));
#pragma unroll
    for (int m = 0; m < 4; ++m) nmean[m] = lds_ld<float>(pm + m * 64);
#pragma unroll
    for (int n = 0; n < 4; ++n) cs[n] = lds_ld<f32x4>(pc + ((n >> 1) * 64 + (n & 1) * 16) * 4);
#pragma unroll
    for (int m = 0; m < 4; ++m) nm4[m] = lds_ld<f32x4>(pm4 + m * 64);
#pragma unroll
    for (int n = 0; n < 2; ++n) csv[n] = lds_ld<float>(pcv + n * 64);
    rds(1, 1); mms(0);
    step();
    fold(0); fold(1);
    SGB(0x002, 8);
#pragma unroll
    for (int i = 0; i < 8; ++i) { SGB(0x008, 1); SGB(0x100, 2); }
#pragma unroll
    for (int i = 0; i < 11; ++i) { SGB(0x008, 1); SGB(0x100, 1); SGB(0x002, 2); }
#pragma unroll
    for (int i = 0; i < 5; ++i) { SGB(0x008, 1); SGB(0x002, 3); }
    SCHED;
    __builtin_amdgcn_s_waitcnt(0xC07F);
    TRQ(16); BAR;
    SCHED;
    rds(0, 0);
    fold(5); mms(1); fold(2);
#pragma unroll
    for (int i = 0; i < 16; ++i) { SGB(0x008, 1); SGB(0x100, 1); SGB(0x002, 1); }
#pragma unroll
    for (int i = 0; i < 8; ++i) { SGB(0x008, 1); SGB(0x002, 2); }
    SCHED;
    pre(0);
    SCHED;
    rds(1, 1);
    fold(4); mms(0); fold(3);
    pre(1);
#pragma unroll
    for (int i = 0; i < 16; ++i) { SGB(0x008, 1); SGB(0x100, 1); SGB(0x002, 1); SGB(0x004, 1); }
#pragma unroll
    for (int i = 0; i < 8; ++i) { SGB(0x008, 1); SGB(0x002, 3); SGB(0x004, 1); }
  }
  SCHED;
  __builtin_amdgcn_s_waitcnt(0xC07F);
  TRQ(17); BAR;
  SCHED;
  auto mm6 = [&](int m, int n0 = 0, int n1 = 6) {
#pragma unroll
    for (int nn = n0; nn < n1; ++nn) { const int n = (nn + 2) % 6; acc[m][n] = n < 4 ? MFMA(bf[1][n], af[1][m], acc[m][n]) : MFMA(af[1][m], bf[1][n], acc[m][n]); }
  };
  if (!isb) {
    fin(std::false_type{}, acc, mm6);
#pragma unroll
    for (int i = 0; i < 24; ++i) asm volatile("" : "=v"(acc[i / 6][i % 6]));
#pragma unroll
    for (int i = 0; i < 6; ++i) asm volatile("" : "=v"(bf[1][i]));
#pragma unroll
    for (int i = 0; i < 4; ++i) asm volatile("" : "=v"(af[1][i]));
  }
  OPAQUE_S(isb);
  if (isb) fin(std::true_type{}, acc, mm6);
}
__device__ __forceinline__ u32x4 pack(const f32x4& a, const f32x4& b) { return u32x4{pk2(a[0], a[1]), pk2(a[2], a[3]), pk2(b[0], b[1]), pk2(b[2], b[3])}; }

template <int N> __device__ __forceinline__ void hedgehog(const f32x4 (&r)[4][2], const float (&c)[4], float (&ep)[4][8], float (&en)[4][8]) {
  float mx[4], nm[4];
#pragma unroll
  for (int m = 0; m < N; ++m) {
    const f32x4 a = r[m][0], b = r[m][1];
    mx[m] = vmax2(fmaxf(fmaxf(a[3], b[0]), b[1]), fmaxf(fmaxf(fmaxf(fmaxf(a[0], a[1]), a[2]), b[2]), b[3]));
    nm[m] = vmax2(fmaxf(fmaxf(-a[3], -b[0]), -b[1]), fmaxf(fmaxf(fmaxf(fmaxf(-a[0], -a[1]), -a[2]), -b[2]), -b[3]));
  }
  SCHED;
  red4<true>(mx[0], mx[1], nm[0], nm[1], [](float x) { return x; });
  if (N == 4) red4<true>(mx[2], mx[3], nm[2], nm[3], [](float x) { return x; });
#pragma unroll
  for (int m = 0; m < N; ++m) { mx[m] *= -c[m]; nm[m] *= -c[m]; }
  SCHED;
#pragma unroll
  for (int m = 0; m < N; ++m) {
#pragma unroll
    for (int i = 0; i < 8; ++i) { ep[m][i] = __builtin_fmaf(r[m][i >> 2][i & 3], c[m], mx[m]); en[m][i] = __builtin_fmaf(r[m][i >> 2][i & 3], -c[m], nm[m]); }
    SCHED;
#pragma unroll
    for (int i = 0; i < 8; ++i) { ep[m][i] = __builtin_amdgcn_exp2f(ep[m][i]); en[m][i] = __builtin_amdgcn_exp2f(en[m][i]); }
    SCHED;
  }
}
__device__ __forceinline__ u32x4 pack8(const float (&e)[8], float f) {
  return u32x4{pk2(e[0] * f, e[1] * f), pk2(e[2] * f, e[3] * f), pk2(e[4] * f, e[5] * f), pk2(e[6] * f, e[7] * f)};
}
__device__ __forceinline__ u32x4 pack8(const float (&e)[8]) { return u32x4{pk2(e[0], e[1]), pk2(e[2], e[3]), pk2(e[4], e[5]), pk2(e[6], e[7])}; }

constexpr u64 window_mask(int t, int e) {
  u64 mk = 0;
  for (int ln = 0; ln < 64; ++ln) {
    const int delta = (ln & 15) + 16 - t * 16 - (ln >> 4) * 4 - e;
    if (delta >= 0 && delta <= 14 && !(delta & 1)) mk |= 1ull << ln;
  }
  return mk;
}
__device__ __forceinline__ void phik2(const f32x4 (&r)[4][2], const float (&c)[4], unsigned pw) {
  const f32x4 zero = {0.f, 0.f, 0.f, 0.f};
  const bf16x8 ones = {0x3f80, 0x3f80, 0x3f80, 0x3f80, 0x3f80, 0x3f80, 0x3f80, 0x3f80};
  float ep[4][8], en[4][8];
  f32x4 sp[2], sn[2];
  hedgehog<2>(r, c, ep, en);
#pragma unroll
  for (int m = 0; m < 2; ++m) { sp[m] = MFMA(ones, as_frag(pack8(ep[m])), zero); sn[m] = MFMA(ones, as_frag(pack8(en[m])), zero); }
#pragma unroll
  for (int m = 0; m < 2; ++m) { asm("" : "+v"(sp[m])); asm("" : "+v"(sn[m])); }
  SCHED;
#pragma unroll
  for (int m = 0; m < 2; ++m) {
    const float fp = __builtin_amdgcn_rcpf(sp[m][0]), fn = __builtin_amdgcn_rcpf(sn[m][0]);
#pragma unroll
    for (int i = 0; i < 8; ++i) { ep[m][i] *= fp; en[m][i] *= fn; }
    SCHED;
    const u32x4 pp = pack8(ep[m]), pn = pack8(en[m]);
    const unsigned d = pw + m * (16 * 160);
    lds_st<u32x2>(d, u32x2{pp[0], pp[1]});
    lds_st<u32x2>(d + 32, u32x2{pp[2], pp[3]});
    lds_st<u32x2>(d + 64, u32x2{pn[0], pn[1]});
    lds_st<u32x2>(d + 96, u32x2{pn[2], pn[3]});
    SCHED;
  }
}
__device__ __forceinline__ bf16x8 rowsum_sel(int lane) {
  const int fr = lane & 15;
  const unsigned one = (fr < 8) ? (0x3f80u << (16 * (fr & 1))) : 0u;
  const int e = (fr >> 1) & 3;
  return as_frag(u32x4{e == 0 ? one : 0u, e == 1 ? one : 0u, e == 2 ? one : 0u, e == 3 ? one : 0u});
}
__device__ __forceinline__ f32x4 rowsum_fin(f32x4 c) {
#pragma unroll
  for (int k = 0; k < 4; ++k) {
    const float a = c[k];
    c[k] = a + __int_as_float(__builtin_amdgcn_update_dpp(0, __float_as_int(a), 0x128  , 0xf, 0xf, false));
  }
  return c;
}
__device__ __forceinline__ void job_colsum(rsrc_t rs, const u32x4 (&w)[8], int mat, int g, int jc, int hw, int lane) {
  const bf16x8 sel = rowsum_sel(lane);
  f32x4 c = {0.f, 0.f, 0.f, 0.f};
#pragma unroll
  for (int i = 0; i < 8; ++i) c = MFMA(sel, as_frag(w[i]), c);
  const u32x4 w4 = __builtin_bit_cast(u32x4, rowsum_fin(c));
  unsigned so = WS_CS + (unsigned)(((mat * 16 + jc) * 16 + g) * 768);
  OPAQUE_S(so);
  if ((lane & 40) == 0) st16_sc1(rs, (unsigned)(hw * 64 + (lane & 7) * 8 + (lane >> 4) * 4) * 4, so, w4);
}
__device__ __forceinline__ unsigned img_off(int i, int lane) { return (lane >> 3) * 128 + (((lane & 7) ^ ((((lane >> 4) & 1) | ((i & 1) << 1)) << 1)) * 16); }
__device__ __forceinline__ void job_pack(rsrc_t rs, u32x4 w, int i, int mat, int g, int jc, int hw, int lane) {
  st16_sc1(rs, img_off(i, lane),
           WS_WP + (unsigned)(((mat * 16 + jc) * 16 + g) * 24576 + hw * 8192 + i * 1024), w);
}

__global__ __launch_bounds__(512, 2) void mega_kernel(Params p) {
  extern __shared__ __attribute__((aligned(16))) char smem[];
  const int tid = threadIdx.x;
  const int wave0 = __builtin_amdgcn_readfirstlane(tid >> 6);
  const int xx = blockIdx.x & 7, rr = blockIdx.x >> 3;
  const int g0 = (rr / NJ) * NX + (xx & (NX - 1)), j0 = (xx / NX) * NJ + (rr & (NJ - 1));
  constexpr unsigned lds0 = 0, lds00 = 0;
  if ((unsigned)(size_t)(LAS char*)smem != 0u) return;
#define LOCALS0 int g = g0, j = j0, l = l0, wave = wave0;                                              \
  OPAQUE_S(g); OPAQUE_S(j); OPAQUE_S(l); OPAQUE_S(wave);                                               \
  const int hw = wave & 3, wm = wave & 1, h = (wave >> 1) & 1;                                         \
  const unsigned myrep = (unsigned)(JROT(j) / 2 + (g & (NX - 1))) * REP_STRIDE;                              \
  (void)hw; (void)wm; (void)h; (void)myrep;                                                            \
  int lane = tid; OPAQUE_V(lane); lane &= 63
#define LOCALS LOCALS0; const bool isb = l & 1, lead = (wave == 4); const int half = wave >> 2; (void)isb; (void)lead; (void)half
  const rsrc_t rs = mk_rsrc(p.ws, WS_SPAN);
  const bool cw = wave0 < 4;
  auto slowbit = [](const void* q) { return 1u << ((3 - ((unsigned)(size_t)q >> 7)) & 7); };
  unsigned slowm = slowbit(p.wqkvA) | (slowbit(p.wqkvB) << 8) | (slowbit(p.woA) << 16) | (slowbit(p.woB) << 24);
  slowm |= PKALL;
  OPAQUE_S(slowm);
  if (wave0 == 2) touch(mk_rsrc(p.ws, NREP * REP_STRIDE), min(blockIdx.x * 9 + ((tid & 63) < 9 ? (tid & 63) : 0), NREP * REP_STRIDE / 128 - 1) * 128, lds00 + L_SINK + 512);
  if (wave0 == 3) {
    const char* pc;
    unsigned len;
    asm volatile("s_getpc_b64 %0\n.Lpf_base:\n\ts_mov_b32 %1, .Lpf_end-.Lpf_base" : "=s"(pc), "=s"(len));
    const unsigned off = ((unsigned)rr * 16 + (tid & 15)) * 128;
    touch(mk_rsrc(pc, len), ((int)len > 0 && off + 4 <= len) ? off : 0u, lds00 + L_SINK);
  }
  int trv = 0, trA = 0, trB = 0, trC = 0;
  u32x2 epoch = {0u, 0u};
  bool dead = false;
  STAMP_INIT;
  const char *pA = (const char*)p.x, *pW = (const char*)p.wqkvA;
  unsigned zA = 2048 * 2048, bA = g0 * (128 * 2048), kA = 128, pcA = 32 * 2048, vA;
  unsigned zW = 1024 * 6144, bW = j0 * 128, kW = 64 * 6144, pcW = 2048, hW = 32 * 6144, vW;
  int kb = KB0(g0, j0), dj = -1;
  {
    const int lane = tid & 63, hw = wave0 & 3;
    vA = (hw * 8 + (lane >> 3)) * 2048 + (((lane & 7) ^ (((hw & 1) * 4 + (lane >> 4)) & 7)) * 16);
    vW = (hw * 8 + (lane >> 3)) * 6144 + (((lane & 7) ^ ((((lane >> 4) & 1) | ((hw & 1) << 1)) << 1)) * 16);
  }

#pragma unroll 1
  for (int l0 = 0; l0 < 4; ++l0) {
    TRC(0); TRR(62);

    f32x4 acc[2][4];
    auto kvpart = [&](unsigned va, unsigned pa, unsigned kvb, int lane, int wave) {
      const f32x4 zero = {0.f, 0.f, 0.f, 0.f};
      const bf16x8 ones = {0x3f80, 0x3f80, 0x3f80, 0x3f80, 0x3f80, 0x3f80, 0x3f80, 0x3f80};
      f32x4 c0 = zero, c1 = zero, z0 = zero, z1 = zero;
#pragma unroll
      for (int ks = 0; ks < 4; ++ks) {
        const bf16x8 v = lds_ld<bf16x8>(va + ks * 2048);
        const bf16x8 a0 = tr2(pa + ks * 32 * 160, pa + (ks * 32 + 16) * 160);
        const bf16x8 a1 = tr2(pa + 32 + ks * 32 * 160, pa + 32 + (ks * 32 + 16) * 160);
        c0 = MFMA(a0, v, c0);
        c1 = MFMA(a1, v, c1);
        z0 = MFMA(a0, ones, z0);
        z1 = MFMA(a1, ones, z1);
      }
      st16_sc1(rs, lane * 16, kvb + (unsigned)wave * 1024, nosent(pack(c0, c1)));
      if (!(wave & 2) && (lane & 15) == 0) st16_sc1(rs, lane, kvb + KVP_Z + (wave >> 2) * 128 + (wave & 1) * 64, nosent(pack(z0, z1)));
    };
#define KV_ADDR                                                                                                            \
    unsigned va = lds0 + SC_VF + (wave >> 2) * 8192 + ((wave >> 1) & 1) * 1024 + lane * 16;                                \
    unsigned pa = lds0 + SC_PHIK + ((wave >> 2) * 128 + fq * 4 + (fr >> 2)) * 160 + (wave & 1) * 64 + (lane & 3) * 8;      \
    unsigned kvj = WS_KVP + (unsigned)j * KVP_J + (unsigned)(l >> 1) * KVP_L;                                              \
    OPAQUE_V(va); OPAQUE_V(pa); OPAQUE_S(kvj)
    if (cw) {
      if (l0 == 0) {
        LOCALS;
        u32x4 v[2][8];
        if (wave < 3) {
          const int jc = JOBC(j, wave), kt = (KB0(g, j) + (unsigned)g / NX) & 15;
          const rsrc_t ra = mk_rsrc(p.wqkvA, 1024 * 6144);
#pragma unroll
          for (int i = 0; i < 8; ++i)
            v[0][i] = __builtin_amdgcn_raw_buffer_load_b128(ra, (lane >> 3) * 6144 + (lane & 7) * 16, (kt * 64 + i * 8) * 6144 + wave * 2048 + jc * 128, 0);
          SCHED;
          TRC(44); BAR; SCHED;
          job_colsum(rs, v[0], 0, g, jc, wave, lane);
          if (SLOWC(0, jc)) {
#pragma unroll
            for (int i = 0; i < 8; ++i) job_pack(rs, v[0][i], i, 0, kt, jc, wave, lane);
          }
        } else {
          const int fr = lane & 15, fq = lane >> 4;
          const rsrc_t rx = mk_rsrc(p.x, 2048 * 2048);
#pragma unroll
          for (int i = 0; i < 16; ++i)
            v[i & 1][i >> 1] = __builtin_amdgcn_raw_buffer_load_b128(rx, fr * 2048 + fq * 16, (g * 128 + (j & (NJ - 1)) * (128 / NJ) + ((i >> 1) % (8 / NJ)) * 16) * 2048 + ((KB0(g, j) + (i >> 1) / (8 / NJ)) & 15) * 128 + (i & 1) * 64, 0);
          SCHED;
          TRC(44); BAR; SCHED;
          const f32x4 zero = {0.f, 0.f, 0.f, 0.f};
          const bf16x8 ones = {0x3f80, 0x3f80, 0x3f80, 0x3f80, 0x3f80, 0x3f80, 0x3f80, 0x3f80};
          float sm[8], q[8];
#pragma unroll
          for (int m = 0; m < 8; ++m) {
            const f32x4 a = MFMA(ones, as_frag(v[1][m]), MFMA(ones, as_frag(v[0][m]), zero));
            const f32x4 b = MFMA(as_frag(v[1][m]), as_frag(v[1][m]), MFMA(as_frag(v[0][m]), as_frag(v[0][m]), zero));
            sm[m] = a[0];
            q[m] = (fr & 2) ? ((fr & 1) ? b[3] : b[2]) : ((fr & 1) ? b[1] : b[0]);
          }
          if (fq == (fr >> 2)) {
#pragma unroll
            for (int m = 0; m < 8; ++m) st8_sc1(rs, ((m % (8 / NJ)) * 16 + fr) * 8 + (m / (8 / NJ)) * 1024, st_off(g, 0) + JROT(j) * 1024 + (j & (NJ - 1)) * (1024 / NJ), u32x2{__float_as_uint(sm[m]), __float_as_uint(q[m])});
          }
        }
      }
      LOCALS;
      unsigned otile, kvo, l16, pr1, pr4;
      int g2, j2, l2, ln2;
      qkv_loop(l & 1, wm, h, lane, l == 0, trv, [&](int st) {
        if (st == 0) {
          g2 = g0; j2 = j0; l2 = l0; ln2 = tid;
          OPAQUE_S(g2); OPAQUE_S(j2); OPAQUE_S(l2); OPAQUE_V(ln2);
          return;
        }
        otile = ob_off(g2, j2, l2) + (unsigned)(wm * 8 + h) * 1024;
        kvo = WS_KVP + (unsigned)j2 * KVP_J + (unsigned)g2 * KVP_G + (unsigned)(l2 >> 1) * KVP_L;
        l16 = (ln2 & 63) * 16;
        pr1 = lds0 + L_RSTD + (16 + wm * 64 + (ln2 & 15)) * 4;
        pr4 = lds0 + L_RSTD + wm * 256 + (ln2 & 48);
        asm("" : "+s"(otile)); asm("" : "+s"(kvo)); asm("" : "+v"(l16)); asm("" : "+v"(pr1)); asm("" : "+v"(pr4));
      }, [&](auto isb_c, f32x4 (&a)[4][6], auto& mm6) {
        const f32x4 zero = {0.f, 0.f, 0.f, 0.f};
        constexpr float L2E = 1.4426950408889634f;
        float rq[4];
#define ilv6(nv) do { _Pragma("unroll") for (int i_ = 0; i_ < 6; ++i_) { SGB(0x008, 1); SGB(0x100, 2); SGB(0x002, nv); } SCHED; } while (0)
        if constexpr (decltype(isb_c)::value) {
          constexpr float QS = 0.17677669529663687f * L2E, NEG = -3.0e38f;
          const bf16x8 ones = {0x3f80, 0x3f80, 0x3f80, 0x3f80, 0x3f80, 0x3f80, 0x3f80, 0x3f80};
          u32x4 kf[5], vf[5], qf[4], pf[4];
          f32x4 rs4[4], bias[2], s0[4], s1[4], dn[4];
          float x[4][4], mx[4];
          unsigned psel0, psel1;
          auto pkkv = [&](int m) { kf[m + 1] = pack(a[m][2] * rq[m], a[m][3] * rq[m]); vf[m + 1] = pack(a[m][4] * rs4[m], a[m][5] * rs4[m]); };
          auto pkq = [&](int m) { qf[m] = pack(a[m][0], a[m][1]); };
          auto st1 = [&](int m) {
            x[m][0] = vmax2(s0[m][0], s0[m][1]); x[m][1] = vmax2(s0[m][2], s0[m][3]); x[m][2] = vmax2(s1[m][0], s1[m][1]); x[m][3] = vmax2(s1[m][2], s1[m][3]);
            mx[m] = vmax2(vmax2(x[m][0], x[m][1]), vmax2(x[m][2], x[m][3]));
          };
          auto st2 = [&](int m) {
            const float mxl = mx[m] * -rq[m];
#pragma unroll
            for (int e = 0; e < 4; ++e) x[m][e] = __builtin_fmaf(x[m][e], rq[m], mxl);
          };
          auto st3 = [&](int m) {
#pragma unroll
            for (int e = 0; e < 4; ++e) x[m][e] = __builtin_amdgcn_exp2f(x[m][e]);
          };
          auto st5 = [&](int m) {
            const unsigned p0 = pk2(x[m][0], x[m][1]), p1 = pk2(x[m][2], x[m][3]);
            pf[m] = u32x4{__builtin_amdgcn_perm(0u, p0, psel0), __builtin_amdgcn_perm(0u, p0, psel1), __builtin_amdgcn_perm(0u, p1, psel0), __builtin_amdgcn_perm(0u, p1, psel1)};
          };
          auto st6 = [&](int m) {
            dn[m] = MFMA(ones, as_frag(pf[m]), zero);
            s0[m] = MFMA(as_frag(u32x4{vf[m][0], vf[m][1], vf[m + 1][0], vf[m + 1][1]}), as_frag(pf[m]), zero);
            s1[m] = MFMA(as_frag(u32x4{vf[m][2], vf[m][3], vf[m + 1][2], vf[m + 1][3]}), as_frag(pf[m]), zero);
            asm("" : "+v"(dn[m]));
          };
          auto st7 = [&](int m) { const float inv = __builtin_amdgcn_rcpf(dn[m][0]); pf[m] = nosent(pack(s0[m] * inv, s1[m] * inv)); };
          mm6(3);
#pragma unroll
          for (int m = 0; m < 4; ++m) rq[m] = lds_ld<float>(pr1 + m * 64);
#pragma unroll
          for (int m = 3; m >= 0; --m) rs4[m] = lds_ld<f32x4>(pr4 + 64 + m * 64);
          unsigned pv_ = l16 + lds0 + L_PREV + h * 2048;
          asm("" : "+v"(pv_));
          static_for<0, 8>([&](auto i_c) __attribute__((always_inline)) {
            constexpr int i = decltype(i_c)::value;
            constexpr u64 mk = window_mask(i >> 2, i & 3);
            bias[i >> 2][i & 3] = __builtin_amdgcn_inverse_ballot_w64(mk) ? 0.f : NEG;
          });
          asm("" : "+v"(bias[0])); asm("" : "+v"(bias[1]));
          const unsigned hso = kvo + KVP_HALO + h * 2048, hsi = hso - (g > 0 ? KVP_G : 0u);
          ilv6(2);
          mm6(2);
          pkkv(3);
          ilv6(3);
          OPAQUE_V(kf[4]); OPAQUE_V(vf[4]);
          if (wm) {
            st16_sc1(rs, l16, hso, nosent(kf[4]));
            st16_sc1(rs, l16, hso + 1024, nosent(vf[4]));
          } else {
            lds_st<u32x4>(pv_, kf[4]);
            lds_st<u32x4>(pv_ + 1024, vf[4]);
          }
          SCHED;
          mm6(1);
          pkq(3); pkkv(2);
          ilv6(4);
          mm6(0);
          pkq(2); pkkv(1);
          ilv6(4);
          s0[3] = MFMA(as_frag(kf[3]), as_frag(qf[3]), bias[0]); s1[3] = MFMA(as_frag(kf[4]), as_frag(qf[3]), bias[1]);
          s0[2] = MFMA(as_frag(kf[2]), as_frag(qf[2]), bias[0]); s1[2] = MFMA(as_frag(kf[3]), as_frag(qf[2]), bias[1]);
          pkq(1); pkkv(0);
#pragma unroll
          for (int i = 0; i < 4; ++i) { SGB(0x008, 1); SGB(0x002, 5); }
          SCHED;
          const bool halo = wm == 0 && g > 0;
          const u32x4 hk = ld16p<PA1>(rs, l16, hsi), hv = ld16p<PA1>(rs, l16, hsi + 1024);
          SCHED;
          s0[1] = MFMA(as_frag(kf[1]), as_frag(qf[1]), bias[0]); s1[1] = MFMA(as_frag(kf[2]), as_frag(qf[1]), bias[1]);
          st1(3); st1(2);
#pragma unroll
          for (int m = 0; m < 4; ++m) rq[m] *= QS;
          SCHED;
          st1(1);
          pkq(0);
          psel0 = __builtin_amdgcn_inverse_ballot_w64(0xaaaaaaaaaaaaaaaaull) ? 0x01000c0cu : 0x0c0c0100u;
          psel1 = __builtin_amdgcn_inverse_ballot_w64(0xaaaaaaaaaaaaaaaaull) ? 0x03020c0cu : 0x0c0c0302u;
          { float d = mx[1]; red4<true>(mx[3], mx[2], mx[1], d, [](float v) { return v; }); }
          s1[0] = MFMA(as_frag(kf[1]), as_frag(qf[0]), bias[1]);
          SCHED;
          st2(3); st2(2); st2(1);
          SCHED;
          st3(3); st3(2); st3(1);
          SCHED;
          st5(3); st6(3); st5(2); st6(2); st5(1); st6(1);
          SCHED;
          WAITL0;
          TRR(60);
          TRC(18); BAR;
          u32x4 pk = lds_ld<u32x4>(pv_), pv = lds_ld<u32x4>(pv_ + 1024);
          st7(3); st7(2); st7(1);
          SCHED;
#pragma unroll
          for (int m = 3; m > 0; --m) st16_sc1(rs, l16, otile + m * 2048, pf[m]);
          if (!wm && g == 0) bias[0] = f32x4{NEG, NEG, NEG, NEG};
          SCHED;
          TRV(55, 0); TRC(21);
          if (halo) {
            pk = hk; pv = hv;
            for (unsigned spins = 0; !dead && __any(umax4v(umax4v(0u, pk), pv) == SENT); ++spins) {
              if (spins > SPIN_CAP) { dead = true; break; }
              CLOBBER;
              TRV(55, spins + 1);
              pk = ld16p<PA1>(rs, l16, hsi); pv = ld16p<PA1>(rs, l16, hsi + 1024);
              OPAQUE_V(pk); OPAQUE_V(pv);
            }
          }
          vf[0] = pv;
          s0[0] = MFMA(as_frag(pk), as_frag(qf[0]), bias[0]);
          st1(0);
          mx[0] = red_max4(mx[0]);
          st2(0); st3(0); st5(0); st6(0); st7(0);
          st16_sc1(rs, l16, otile, pf[0]);
        } else {
          LOCALS;
          const int fr = lane & 15, fq = lane >> 4;
          f32x4 rs4[4], r[4][2];
          const bf16x8 ones = {0x3f80, 0x3f80, 0x3f80, 0x3f80, 0x3f80, 0x3f80, 0x3f80, 0x3f80};
          auto proj = [&](int m, int n, const bf16x8& w0, const bf16x8& w1) {
            const u32x4 f = pack(a[m][n], a[m][n + 1]);
            r[m][0] = MFMA(w0, as_frag(f), zero); r[m][1] = MFMA(w1, as_frag(f), zero);
          };
          mm6(3, 0, 2); mm6(2, 0, 2); mm6(1, 0, 2); mm6(0, 0, 2);
          unsigned wa = lds0 + L_WPHI + h * 2048 + (fq * 4 + (fr >> 2)) * 64 + (lane & 3) * 8, kw = lds0 + L_PREV + hw * 2048 + l16;
          asm("" : "+v"(wa)); asm("" : "+v"(kw));
#pragma unroll
          for (int m = 0; m < 4; ++m) rq[m] = lds_ld<float>(pr1 + m * 64);
#pragma unroll
          for (int m = 0; m < 4; ++m) rs4[m] = lds_ld<f32x4>(pr4 + 64 + m * 64);
          const bf16x8 w0 = tr2(wa, wa + 1024);
          const bf16x8 w1 = tr2(wa + 32, wa + 32 + 1024);
#pragma unroll
          for (int i = 0; i < 8; ++i) { SGB(0x008, 1); SGB(0x100, 2); SGB(0x002, 2); }
          SCHED;
          mm6(3, 2, 4); mm6(2, 2, 4);
          lds_st<u32x4>(kw + 1024, pack(a[3][2], a[3][3]));
          lds_st<u32x4>(kw, pack(a[2][2], a[2][3]));
#pragma unroll
          for (int i = 0; i < 2; ++i) { SGB(0x008, 1); SGB(0x002, 2); SGB(0x008, 1); SGB(0x002, 2); SGB(0x200, 1); }
          SCHED;
          mm6(1, 2, 4); mm6(0, 2, 4);
          proj(0, 2, w0, w1); proj(1, 2, w0, w1);
#pragma unroll
          for (int i = 0; i < 4; ++i) { SGB(0x008, 1); SGB(0x002, 2); }
          SCHED;
          WAITL0;
          TRC(18); BAR;
          SCHED;
          unsigned pw = lds0 + SC_PHIK + (h * 128 + wm * 64 + fr) * 160 + fq * 8, vw = lds0 + SC_VF + h * 8192 + wm * 4096 + l16;
          asm("" : "+v"(pw)); asm("" : "+v"(vw));
#pragma unroll
          for (int m = 0; m < 4; ++m) mm6(m, 4, 6);
#pragma unroll
          for (int m = 0; m < 4; ++m) rq[m] *= L2E;
#pragma unroll
          for (int i = 0; i < 4; ++i)
            lds_st<u32x4>(vw + i * 1024, pack(a[i & 2][4 + (i & 1)] * rs4[i & 2], a[(i & 2) + 1][4 + (i & 1)] * rs4[(i & 2) + 1]));
#pragma unroll
          for (int i = 0; i < 8; ++i) { SGB(0x008, 1); SGB(0x002, 5); }
          SCHED;
          phik2(r, rq, pw);
          KV_ADDR;
          WAITL0;
          TRR(60);
          TRC(19); BAR;
          kvpart(va, pa, kvo, lane, wave);
          CLOBBER;
          TRC(20); BAR;
          u32x4 qp[4], qn[4];
          float sqp[4], sqn[4];
#pragma unroll
          for (int m = 0; m < 4; ++m) proj(m, 0, w0, w1);
          SCHED;
          float ep[4][8], en[4][8];
          hedgehog<4>(r, rq, ep, en);
          const unsigned zo = (lane >> 3) * KVP_G + (lane & 7) * 16, zs = kvj + KVP_Z + h * 128;
          u32x4 zv[2] = {ld16p<PA1>(rs, zo, zs), ld16p<PA1>(rs, zo, zs + 8 * KVP_G)};
          SCHED;
#pragma unroll
          for (int m = 0; m < 4; ++m) {
            qp[m] = pack8(ep[m]); qn[m] = pack8(en[m]);
            sqp[m] = MFMA(ones, as_frag(qp[m]), zero)[0];
            sqn[m] = MFMA(ones, as_frag(qn[m]), zero)[0];
          }
          SCHED;
#pragma unroll
          for (int m = 0; m < 4; ++m) { sqp[m] = __builtin_amdgcn_rcpf(sqp[m]); sqn[m] = __builtin_amdgcn_rcpf(sqn[m]); }
#pragma unroll
          for (int m = 0; m < 4; ++m) { OPAQUE_V(qp[m]); OPAQUE_V(qn[m]); OPAQUE_V(sqp[m]); OPAQUE_V(sqn[m]); }
          unsigned ko = (lane >> 5) * KVP_G + (lane & 31) * 16, ks = kvj + (unsigned)((j ^ RX) - j) * KVP_J + KVP_SUM + h * (8 * KVP_G);
          bf16x8 sel = rowsum_sel(lane);
          OPAQUE_V(ko); OPAQUE_S(ks); OPAQUE_V(sel);
          TRC(21); TRV(50, 0);
          for (unsigned spins = 0; !dead && __any(umax4v(umax4v(0u, zv[0]), zv[1]) == SENT); ++spins) {
            if (spins > SPIN_CAP) { dead = true; break; }
            CLOBBER;
            TRV(50, spins + 1);
            zv[0] = ld16p<PA1>(rs, zo, zs); zv[1] = ld16p<PA1>(rs, zo, zs + 8 * KVP_G);
          }
          TRC(22);
          {
            const f32x4 z = rowsum_fin(MFMA(sel, as_frag(zv[1]), MFMA(sel, as_frag(zv[0]), zero)));
            if ((lane & 40) == 0) lds_st<u32x2>(lds0 + L_ZF + hw * 128 + fr * 16 + fq * 8, u32x2{pk2(z[0], z[1]), pk2(z[2], z[3])});
            WAITL0;
            const bf16x8 zf0 = lds_ld<bf16x8>(lds0 + L_ZF + hw * 128 + fq * 16), zf1 = lds_ld<bf16x8>(lds0 + L_ZF + hw * 128 + 64 + fq * 16);
            float dp[4], dn[4];
#pragma unroll
            for (int m = 0; m < 4; ++m) { dp[m] = MFMA(zf0, as_frag(qp[m]), zero)[0]; dn[m] = MFMA(zf1, as_frag(qn[m]), zero)[0]; }
            SCHED;
#pragma unroll
            for (int m = 0; m < 4; ++m) {
              const float inv = __builtin_amdgcn_rcpf(__builtin_fmaf(dp[m], sqp[m], __builtin_fmaf(dn[m], sqn[m], 1e-6f)));
              sqp[m] *= inv; sqn[m] *= inv;
              OPAQUE_V(sqp[m]); OPAQUE_V(sqn[m]);
            }
          }
          TRV(51, 0);
          TRC(43); BAR;
          if (H2D) __builtin_amdgcn_s_sleep(H2D);
          u32x4 kv[4];
#pragma unroll
          for (int i = 0; i < 4; ++i) kv[i] = ld16p<PA2>(rs, ko, ks + i * (2 * KVP_G));
          SCHED;
#pragma unroll
          for (int m = 0; m < 4; ++m) { qp[m] = pack8(ep[m], sqp[m]); qn[m] = pack8(en[m], sqn[m]); }
#pragma unroll
          for (int m = 0; m < 4; ++m) { OPAQUE_V(qp[m]); OPAQUE_V(qn[m]); }
          SCHED;
          for (unsigned spins = 0; !dead && __any(max(umax4v(umax4v(0u, kv[0]), kv[1]), umax4v(umax4v(0u, kv[2]), kv[3])) == SENT); ++spins) {
            if (spins > SPIN_CAP) { dead = true; break; }
            CLOBBER;
            TRV(51, spins + 1);
#pragma unroll
            for (int i = 0; i < 4; ++i) kv[i] = ld16p<PA2>(rs, ko, ks + i * (2 * KVP_G));
          }
          TRC(23);
          f32x4 o[4][2];
#pragma unroll
          for (int m = 0; m < 4; ++m)
#pragma unroll
            for (int i = 0; i < 2; ++i) o[m][i] = MFMA(as_frag(kv[2 * i + 1]), as_frag(qn[m]), MFMA(as_frag(kv[2 * i]), as_frag(qp[m]), zero));
#pragma unroll
          for (int m = 0; m < 4; ++m) st16_sc1(rs, l16, otile + m * 2048, nosent(pack(o[m][0], o[m][1])));
        }
      });
#pragma unroll
      for (int i = 0; i < 8; ++i) asm volatile("" : "=v"(acc[i >> 2][i & 3]));
    } else {
      __builtin_amdgcn_s_setprio(3);
      if (l0) {
        WAITV(3);
        WAITL0;
        TRC(1); BAR;
      }
      LOCALS;
      u32x4 xr[2][2];
      const unsigned lin = lane * 16, vlin = lin + hw * 1024;
      const int jr = j & ~3;
      u32x4 sent4 = {SENT, SENT, SENT, SENT};
      OPAQUE_V(sent4);
      auto ldres = [&](int slot) {
        const int fr = lane & 15, fq = lane >> 4, ch = (fq & 1) * 2 + (fq >> 1);
#pragma unroll
        for (int i = 0; i < 4; ++i)
          xr[i >> 1][i & 1] = lds_ld<u32x4>(lds0 + L_R2 + slot * XS + (hw * 32 + ((i >> 1) ^ 1) * 16 + fr) * 128 + (((ch + 4 * (i & 1)) ^ ((fr >> 1) & 7)) * 16));
      };
      auto aslot = [&](int t) { return lds0 + L_R2 + (t % 3) * XS + hw * 1024; };
      auto wslot = [&](int t) { return lds0 + L_R1 + (t & 3) * 24576 + hw * 1024; };
      const bool l0f = l == 0;
      const rsrc_t rA = mk_rsrc(pA, zA);
      auto kseq = [&](int t) { return (kb + t - (((KX || t < NJ) && t <= dj) ? 1 : 0)) & 15; };
      auto offA = [&](int t) { return bA + (unsigned)kseq(t) * kA; };
      auto offW = [&](int t) { return bW + (unsigned)kseq(t) * kW; };
      auto ldA = [&](int t) { dma4<4096>(rA, vA, offA(t), pcA, aslot(t)); };
      auto ldWh = [&](int t, unsigned o, int hf) { dma3<8192>(mk_rsrc(pW, zW), vW, o + (hf ? hW : 0u), pcW, wslot(t) + hf * 4096); };
      const bool slowj = l0f && SLOWC(0, j);
      auto packedW = [&]() {
        pW = p.ws; zW = WS_SPAN; bW = WS_WP + (unsigned)j * (16 * 24576); kW = 24576; pcW = 8192; hW = 4096; vW = vlin;
      };
      auto ldW = [&](int t) { const unsigned o = offW(t); ldWh(t, o, 0); ldWh(t, o, 1); };
      constexpr auto spread = [](int n) { unsigned m = 0; for (int t = 0; t < 26; ++t) if ((t + 1) * n / 13 > t * n / 13) m |= 1u << t; return m; };
      constexpr unsigned sp7 = spread(7), sp3 = spread(3);
      const unsigned smask = (l0f ? sp7 : sp3) >> ((((g + j * 5) & 15) * 13) >> 4), cb0 = ob_off(g, j, l) + hw * 1024;
      auto clean = [&](int t, int second) __attribute__((always_inline)) {
        if (t < 13 && !isb && (!second || ((smask >> t) & 1))) {
          const unsigned cc = t + second + __builtin_popcount(smask & ((1u << t) - 1));
          st16_sc1(rs, (lane >> 4) * 4096 + (lane & 15) * 16, cb0 + (cc >> 2) * TD + ((cc + g + j) & 3) * 256, sent4);
        }
      };
      unsigned flo;
      unsigned ready = ~0u;
      u32x4 cv[4];
#pragma unroll
      for (int ii = 0; ii < 4; ++ii) asm volatile("" : "=v"(cv[ii]));
      auto chkrd = [&](int t) {
#pragma unroll
        for (int ii = 0; ii < 4; ++ii) cv[ii] = lds_ld<u32x4>(aslot(t) + lin + ii * 4096);
      };
      auto chkbad = [&]() { return __any(max(umax4v(umax4v(0u, cv[0]), cv[1]), umax4v(umax4v(0u, cv[2]), cv[3])) == SENT) && !l0f; };
      auto refetch = [&](int t) __attribute__((always_inline)) {
        for (unsigned spins = 0; !dead; ++spins) {
          if (spins > SPIN_CAP) { dead = true; break; }
          WAITV(0);
          TRV(t == 1 ? 56 : 57, spins + 1);
          ldA(t);
          ldA(t + 1);
          WAITV(0);
          chkrd(t);
          if (!chkbad()) break;
          __builtin_amdgcn_s_sleep(1);
        }
      };
      if (l0f) {
        u32x2 gw2;
        ld8_asm(gw2, rs, WS_EPOCH + 0u * lin);
        TRC(44); BAR;
        ldW(0); ldA(0);
        dma1(mk_rsrc(p.wphi, 32 * 2048), vlin, j * 4096, lds0 + L_WPHI + hw * 1024);
        ldW(1);
        if (A1E) ldA(1);
        WAITV(A1E ? 21 : 17);
        OPAQUE_V(gw2);
        const u64 gword = ((u64)(unsigned)UNI(gw2[1]) << 32) | (unsigned)UNI(gw2[0]);
        u64 z = gword + 0x9e3779b97f4a7c15ull;
        z = (z ^ (z >> 30)) * 0xbf58476d1ce4e5b9ull;
        z = (z ^ (z >> 27)) * 0x94d049bb133111ebull;
        z = z ^ (z >> 31);
        z = (z == gword) ? ~z : z;
        epoch = u32x2{(unsigned)z, (unsigned)(z >> 32)};
        WAITV(A1E ? 11 : 7);
        TRC(1); BAR;
        if (!A1E) ldA(1);
      } else {
        ldW(1);
        if (XD) __builtin_amdgcn_s_sleep(XD);
        ldA(1);
      }
      ldA(2);
      TRC(40);
      ldW(2);
#pragma unroll
      for (int i = 0; i < 4; ++i) asm volatile("" : "=v"(xr[i >> 1][i & 1]));
      unsigned oA = offA(3), oW = 0;
      OPAQUE_S(oA);
      TRV(56, 0); TRV(57, 0);
      if (LAW == 3) ldW(3);
      if (!l0f) ldres(0);
      clean(0, 0); clean(0, 1);
      WAITV(4 + (W2F ? 0 : 6) + (LAW == 3 ? 6 : 0));
      chkrd(1);
      if (__builtin_expect(chkbad(), 0)) refetch(1);
      const int fr = lane & 15, fq = lane >> 4;
      u32x2 fl;
      f32x4 js[4], jc[4];
      float ra[4], rb[4];
      const bool slowo = SLOWC(2 + (l & 1), j);
      const char* pO;
      unsigned zO, bO, pcO, vO, clo;
      u32x4 wo2[2];
      asm volatile("" : "=v"(wo2[0])); asm volatile("" : "=v"(wo2[1]));
      const unsigned fwo = myrep + fl_line(FL_WO + (isb ? 1 : 0), jr + (lane >> 4)) + (lane & 15) * 8;
      static_for<1, 14>([&](auto t_c) __attribute__((always_inline)) {
        constexpr int t = decltype(t_c)::value;
        constexpr auto rest = [](int s) {
          return s == 0 ? (W2F ? 0 : 6) + (LAW == 3 ? 6 : 0)
                        : (s + LAW <= 15 ? 6 : 0) + (s == NVAL - 2 || s == 5 || s == 11) + ((s == 7 || s == 8) ? 4 : 0);
        };
        constexpr int NW = t == 1 ? 63 : 4 + rest(t - 1) + ((LAW == 3 && (t > 2 || W2F)) ? rest(t - 2) : 0);
        TRC(1 + t);
        if (t >= NVAL && t <= 6 && __builtin_expect((ready & (1u << kseq(t + 2))) == 0, 0)) {
          WAITV(NW); WAITL0; BAR;
          ready = poll_need(rs, flo, epoch, ready, 1u << kseq(t + 2), dead);
          dma4<4096>(rA, vA, oA, pcA, aslot(t + 2));
        } else if (t == 7 && __builtin_expect(ready != ~0u, 0)) {
          WAITV(NW); WAITL0; BAR;
          poll_all(rs, flo, epoch, dead);
          dma4<4096>(rA, vA, oA, pcA, aslot(t + 2));
        } else {
          bar_dma4<4096, NW>(rA, vA, oA, pcA, aslot(t + 2));
        }
        if (t == 1 && lead) set_flags(rs, fl_line(FL_X + l, g), j, l0f ? fl_line(FL_CS, j) : fl_line(FL_X + l, g), l0f ? g : j, epoch, lane);
        if (t == 7 && slowj) packedW();
        if (t + LAW <= 15) oW = offW(t + LAW);
        if (t < 13) oA = offA(t + 3);
        OPAQUE_S(oA); OPAQUE_S(oW);
        clean(t, 0);
        if (t <= NVAL && !l0f) { WAITV(rest(t - 1) + 4); chkrd(t + 1); }
        if (t == WT + 3 && l0f && SLOWC(2, jr + hw)) {
          OPAQUE_V(wo2[0]); OPAQUE_V(wo2[1]);
#pragma unroll
          for (int i = 0; i < 2; ++i) st16_sc1(rs, lin, WS_WO + (unsigned)((jr + hw) * 131072 + g * 8192 + ((j & 3) * 2 + i) * 1024), wo2[i]);
        }
        if (t == WT + 6 && l0f && lead) set_flags(rs, fl_line(FL_WO, j), g, fl_line(FL_WO, j), g, epoch, lane);
        if (t == 9 && l0f) {
          const int ch = (fq & 1) * 2 + (fq >> 1);
#pragma unroll
          for (int i = 0; i < 4; ++i) ld16in_asm(xr[i >> 1][i & 1], rA, fr * 2048 + ch * 16, (g * 128 + hw * 32 + ((i >> 1) ^ 1) * 16) * 2048 + j * 128 + (i & 1) * 64);
        }
        if (t == 7) {
#pragma unroll
          for (int i = 0; i < 4; ++i) ld16_asm(jc[i], rs, fq * 768 + hw * 192 + (fr < 12 ? fr : 0) * 16, WS_CS + (unsigned)(((isb ? 16 : 0) + j) * 16 + i * 4) * 768);
        }
        if (t == 8) {
#pragma unroll
          for (int i = 0; i < 4; ++i) ld16_asm(js[i], rs, fq * 1024 + hw * 256 + fr * 16, st_off(g, l) + i * 4096);
        }
        SCHED;
        if (t + LAW <= 15) ldWh(t + LAW, oW, 0);
        clean(t, 1);
        SCHED;
        bool bad = false;
        if (t <= NVAL) bad = chkbad();
        if (t == 1) {
          const unsigned fx = fl_line(FL_X + l, g), fc = fl_line(l0f ? FL_CS : FL_PK, jr) + ((j + fq) & 3) * 128;
          flo = myrep + ((l < 2 && lane >= 16) ? fc : fx) + fr * 8;
          OPAQUE_V(flo);
        }
        if (t == 9) {
          pO = slowo ? p.ws : (const char*)(isb ? p.woB : p.woA);
          zO = slowo ? WS_SPAN : 1024 * 2048; bO = slowo ? WS_WO + (unsigned)((isb ? 16 : 0) + j) * 131072 : j * 128; pcO = slowo ? 4096 : 32 * 2048;
          vO = slowo ? vlin : (hw * 8 + (lane >> 3)) * 2048 + wo_swz(lane);
          clo = myrep + ((lane & 16) ? fl_line(FL_CLC + l, (lane & 32) ? j ^ RX : j) : fl_line(FL_CLR + l, g)) + fr * 8;
          OPAQUE_S(pO); OPAQUE_S(zO); OPAQUE_S(bO); OPAQUE_S(pcO); OPAQUE_V(vO); OPAQUE_V(clo);
        }
        if (t == 10) {
#pragma unroll
          for (int i = 0; i < 4; ++i) OPAQUE_V(jc[i]);
          const f32x4 s = (jc[0] + jc[1]) + (jc[2] + jc[3]);
#pragma unroll
          for (int i = 0; i < 4; ++i) { ra[i] = s[i]; OPAQUE_V(ra[i]); }
        }
        if (t == 11) {
#pragma unroll
          for (int i = 0; i < 4; ++i) OPAQUE_V(js[i]);
          const f32x4 s = (js[0] + js[1]) + (js[2] + js[3]);
#pragma unroll
          for (int i = 0; i < 4; ++i) { rb[i] = s[i]; OPAQUE_V(rb[i]); }
        }
        if (t == 12) {
          red2_add(ra[0], ra[1]); red2_add(ra[2], ra[3]); red2_add(rb[0], rb[1]); red2_add(rb[2], rb[3]);
          if (lane < 12) lds_st<f32x4>(lds0 + L_CS + hw * 192 + lane * 16, f32x4{ra[0], ra[1], ra[2], ra[3]});
#pragma unroll
          for (int i = 0; i < 4; ++i) OPAQUE_V(rb[i]);
        }
        if (t == 13) {
          const float m0 = rb[0] * (1.0f / 1024), m1 = rb[2] * (1.0f / 1024);
          const float r0 = __builtin_amdgcn_rsqf(fmaxf(__builtin_fmaf(rb[1], 1.0f / 1024, -m0 * m0), 0.f) + 1e-5f);
          const float r1 = __builtin_amdgcn_rsqf(fmaxf(__builtin_fmaf(rb[3], 1.0f / 1024, -m1 * m1), 0.f) + 1e-5f);
          if (lane < 16) {
            lds_st<f32x2>(lds0 + L_MEAN + 64 + hw * 128 + fr * 8, f32x2{-m0, -m1});
            lds_st<f32x2>(lds0 + L_RSTD + 64 + hw * 128 + fr * 8, f32x2{r0, r1});
          }
        }
        SCHED;
        if (t + LAW <= 15) ldWh(t + LAW, oW, 1);
        if (t == NVAL - 1 || t == 6) {
          WAITV(10);
          OPAQUE_V(fl);
          if (t == 6) ready = __all(is_set(fl, epoch)) ? ~0u : 0u;
          else {
            ready = l0f ? ~0u : (unsigned)__ballot(is_set(fl, epoch));
            if (slowj && __all(is_set(fl, epoch))) packedW();
          }
        }
        if (t == 12) { WAITV(10); OPAQUE_V(fl); }
        if (t == 13 && slowo && __builtin_expect(!__all(is_set(fl, epoch)), 0)) poll_all(rs, fwo, epoch, dead);
        if (t == NVAL - 2 || t == 5) ld8_asm(fl, rs, flo);
        if (t == 11) ld8_asm(fl, rs, fwo);
        if (t == WT && l0f) {
#pragma unroll
          for (int i = 0; i < 2; ++i) ld16in_asm(wo2[i], mk_rsrc(p.woA, 1024 * 2048), (lane >> 3) * 2048 + wo_swz(lane), (g * 64 + (j & 3) * 16 + i * 8) * 2048 + (jr + hw) * 128);
        }
        if constexpr (t <= NVAL) { if (__builtin_expect(bad, 0)) refetch(t + 1); }
      });
      auto stO = [&](int i) { dma4<4096>(mk_rsrc(pO, zO), vO, bO + wo_src(i, j) * (4 * pcO), pcO, lds0 + wo_slot(i) + hw * 1024); };
      auto clcheck = [&]() {
        OPAQUE_V(fl);
        TRV(58, 0);
        if (__builtin_expect(!__all(is_set(fl, epoch)), 0)) { TRV(58, 1); poll_all(rs, clo, epoch, dead); }
      };
      auto accinit = [&]() {
#pragma unroll
        for (int m = 0; m < 2; ++m)
#pragma unroll
          for (int pp = 0; pp < 2; ++pp) {
            auto r0_ = __builtin_amdgcn_permlane16_swap(xr[m][pp][0], xr[m][pp][2], false, false);
            auto r1_ = __builtin_amdgcn_permlane16_swap(xr[m][pp][1], xr[m][pp][3], false, false);
            acc[m][2 * pp] = f32x4{lo16(r0_[0]), hi16(r0_[0]), lo16(r1_[0]), hi16(r1_[0])};
            acc[m][2 * pp + 1] = f32x4{lo16(r0_[1]), hi16(r0_[1]), lo16(r1_[1]), hi16(r1_[1])};
          }
#pragma unroll
        for (int i = 0; i < 8; ++i) OPAQUE_V(acc[i >> 2][i & 3]);
      };
      WAITV(10);
      WAITL0;
      TRC(15); BAR;
      if (isb) {
        stO(0); stO(2); stO(3);
        WAITV(12);
        TRC(16); BAR;
        stO(1); stO(4); stO(5);
        __builtin_amdgcn_s_setprio(0);
        TRC(17); BAR;
        __builtin_amdgcn_s_sleep(BD);
        stO(7); stO(6);
        TRC(18); BAR;
        accinit();
        WAITV(0);
      } else {
        if (lead) set_flags(rs, fl_line(FL_CLR + l, g), j, fl_line(FL_CLC + l, j), g, epoch, lane);
        u32x4 vb[8];
#pragma unroll
        for (int i = 0; i < 8; ++i) asm volatile("" : "=v"(vb[i]));
        stO(0); stO(2); stO(3);
        WAITV(12);
        TRC(16); BAR;
        stO(1);
        if (l0f) {
          const bool wj = hw != 0;
          const char* bp = (const char*)(wj ? p.wqkvB : p.woB);
          unsigned rs_ = wj ? 6144 : 2048, sz = rs_ * 1024, ss = rs_ * 8, s2 = wj ? 16 * 6144 : 128;
          unsigned sb = g * 64 * rs_ + (wj ? (hw - 1) * 2048 + JOBC(j, hw - 1) * 128 : (j & 3) * (16 * 2048) + jr * 128);
          OPAQUE_S(bp); OPAQUE_S(rs_); OPAQUE_S(sz); OPAQUE_S(sb); OPAQUE_S(ss); OPAQUE_S(s2);
          const rsrc_t rb = mk_rsrc(bp, sz);
#pragma unroll
          for (int i = 0; i < 8; ++i) ld16in_asm(vb[i], rb, (lane >> 3) * rs_ + (lane & 7) * 16, sb + (i & 1) * ss + (i >> 1) * s2);
        }
        __builtin_amdgcn_s_setprio(0);
        KV_ADDR;
        bf16x8 sel = rowsum_sel(lane);
        unsigned h1 = (lane >> 3) * KVP_G + (lane & 7) * 16;
        unsigned pw = lds0 + SC_PHIK + (h * 128 + wm * 64 + 32 + fr) * 160 + fq * 8, kw = lds0 + L_PREV + hw * 2048 + lin;
        const unsigned wa = lds0 + L_WPHI + h * 2048 + (fq * 4 + (fr >> 2)) * 64 + (lane & 3) * 8;
        bf16x8 w0 = tr2(wa, wa + 1024), w1 = tr2(wa + 32, wa + 32 + 1024);
        float cq[4];
#pragma unroll
        for (int m = 0; m < 2; ++m) { cq[m] = lds_ld<float>(lds0 + L_RSTD + (16 + wm * 64 + 32 + m * 16 + fr) * 4) * 1.4426950408889634f; OPAQUE_V(cq[m]); }
        OPAQUE_V(sel); OPAQUE_V(h1); OPAQUE_V(pw); OPAQUE_V(kw); OPAQUE_V(w0); OPAQUE_V(w1);
        TRC(17); BAR;
        TRC(18); BAR;
        ld8_asm(fl, rs, clo);
        {
          const f32x4 zero = {0.f, 0.f, 0.f, 0.f};
          f32x4 r[4][2];
#pragma unroll
          for (int m = 0; m < 2; ++m) {
            const bf16x8 f = lds_ld<bf16x8>(kw + m * 1024);
            r[m][0] = MFMA(w0, f, zero); r[m][1] = MFMA(w1, f, zero);
          }
          phik2(r, cq, pw);
        }
        WAITV(0);
#pragma unroll
        for (int i = 0; i < 8; ++i) OPAQUE_V(vb[i]);
        clcheck();
        WAITL0;
        TRC(19); BAR;
        kvpart(va, pa, kvj + (unsigned)g * KVP_G, lane, wave);
        CLOBBER;
        TRC(20); BAR;
        auto job = [&]() __attribute__((always_inline)) {
        if (l0f) {
          if (hw) {
            job_colsum(rs, vb, 1, g, JOBC(j, hw - 1), hw - 1, lane);
            if (SLOWC(1, JOBC(j, hw - 1))) {
#pragma unroll
              for (int i = 0; i < 8; ++i) job_pack(rs, vb[i], i, 1, g, JOBC(j, hw - 1), hw - 1, lane);
            }
          } else {
#pragma unroll
            for (int i = 0; i < 8; ++i)
              if (SLOWC(3, jr + (i >> 1))) st16_sc1(rs, (lane >> 3) * 128 + wo_swz(lane), WS_WO + (unsigned)((16 + jr + (i >> 1)) * 131072 + g * 8192 + ((j & 3) * 2 + (i & 1)) * 1024), vb[i]);
          }
        }
        };
        if (JOBP) job();
        if (H1D > 0) stO(4);
        if (H1D > 1) stO(5);
        const f32x4 zero = {0.f, 0.f, 0.f, 0.f};
        const unsigned s1 = kvj + (unsigned)((j ^ RX) - j) * KVP_J + g * 512 + hw * 128;
        if (H1S) __builtin_amdgcn_s_sleep(H1S);
        u32x4 v[2] = {ld16p<PA1>(rs, h1, s1), ld16p<PA1>(rs, h1, s1 + 8 * KVP_G)};
        TRC(21); TRV(50, 0);
        for (unsigned spins = 0; !dead && __any(umax4v(umax4v(0u, v[0]), v[1]) == SENT); ++spins) {
          if (spins > SPIN_CAP) { dead = true; break; }
          CLOBBER;
          TRV(50, spins + 1);
          if (H1P) __builtin_amdgcn_s_sleep(H1P);
          v[0] = ld16p<PA1>(rs, h1, s1); v[1] = ld16p<PA1>(rs, h1, s1 + 8 * KVP_G);
        }
        TRC(22);
        const f32x4 c = rowsum_fin(MFMA(sel, as_frag(v[1]), MFMA(sel, as_frag(v[0]), zero)));
        if ((lane & 40) == 0)
          st8_sc1(rs, fr * 16 + fq * 8, kvj + (unsigned)g * KVP_G + KVP_SUM + hw * 128, u32x2{min(pk2(c[0], c[1]), SENT - 1), min(pk2(c[2], c[3]), SENT - 1)});
        CLOBBER;
        TRC(43); BAR;
        if (JOBP && l0f && lead) set_flags(rs, fl_line(FL_PK, j), g, fl_line(FL_WO + 1, j), g, epoch, lane);
        accinit();
        if (H2D) __builtin_amdgcn_s_sleep(H2D);
        if (H1D < 1) stO(4);
        if (H1D < 2) stO(5);
        stO(6); stO(7);
        if (!JOBP) job();
        WAITV(0);
      }
    }
    {
      LOCALS;
      const int fr = lane & 15, fq = lane >> 4;
      const int kh = half;
      unsigned v0 = lane * 16 + hw * 4096 + kh * 2048, v1 = lane * 16 + hw * 4096 + (kh ^ 1) * 2048;
      const int r0 = (JROT(j) >> 1) + NJ / 2;
      const unsigned ob0 = ob_off(g, kh, l);
      const unsigned pko = myrep + fl_line(FL_PK, JOBC(j, fq - 1)) + fr * 8;
      u32x4 R[8][4];
      u32x2 pkf;
      auto issue_ = [&](int i) {
        const unsigned so = ob0 + (unsigned)((r0 + i) & 7) * (2 * TS);
        R[i][0] = ld16_sc1(rs, v0, so); R[i][1] = ld16_sc1(rs, v1, so);
        R[i][2] = ld16_sc1(rs, v0 + 1024, so); R[i][3] = ld16_sc1(rs, v1 + 1024, so);
      };
      auto issue = [&](int i) { issue_(i); SCHED; };
      constexpr int LA = LOOKAHEAD;
      TRV(52, 0); TRV(53, 0); TRV(54, 0);
      TRR(61);
      __builtin_amdgcn_s_setprio(3);
      unsigned so0 = ob0 + (unsigned)(r0 & 7) * (2 * TS);
      OPAQUE_V(v0); OPAQUE_V(v1); OPAQUE_S(so0);
      TRC(25); BAR;
      if (DLY && !isb) { for (int d = (g >> 1) * DLY; d > 0; --d) __builtin_amdgcn_s_sleep(1); }
      SCHED;
      R[0][0] = ld16p<PA3>(rs, v0, so0); R[0][1] = ld16p<PA3>(rs, v1, so0);
      R[0][2] = ld16p<PA3>(rs, v0 + 1024, so0); R[0][3] = ld16p<PA3>(rs, v1 + 1024, so0);
      SCHED;
      __builtin_amdgcn_s_setprio(2);
      if (!JOBP && l == 0 && lead) set_flags(rs, fl_line(FL_PK, j), g, fl_line(FL_WO + 1, j), g, epoch, lane);

      auto vmax = [&](int i) { return umax4v(umax4v(umax4v(umax4v(0u, R[i][0]), R[i][1]), R[i][2]), R[i][3]); };
      auto bad = [&](int i) { return __any(umax4v(umax4v(umax4v(umax4v(0u, R[i][0]), R[i][1]), R[i][2]), R[i][3]) == SENT); };
      auto poll = [&](int i, int nfl) {
        for (unsigned spins = 0; !dead; ++spins) {
          if (spins > SPIN_CAP) { dead = true; break; }
          __builtin_amdgcn_s_sleep(2);
          CLOBBER;
          TRV(52 + (i > 2 ? 2 : i), spins + 1);
          issue(i);
#pragma unroll
          for (int k = i + 1; k < i + nfl && k < 8; ++k)
            if (bad(k)) issue(k);
          WAITV(0);
#pragma unroll
          for (int k = i + 1; k < i + nfl && k < 8; ++k)
#pragma unroll
            for (int f = 0; f < 4; ++f) OPAQUE_V(R[k][f]);
          if (!bad(i)) break;
        }
      };
      unsigned bb[4];
      {
        const int kr = fq * 4 + (fr >> 2);
        const int gs = (kr >> 1) & 3;
#pragma unroll
        for (int c = 0; c < 4; ++c) bb[c] = lds0 + kh * 8192 + kr * 128 + ((c ^ gs) * 32) + (lane & 3) * 8;
      }
      bf16x8 bf[2][4];
      auto rdB = [&](int i, int hh) {
        if (hh == 0) {
#pragma unroll
          for (int c = 0; c < 4; ++c) { bb[c] += wo_slot(i) - (i ? wo_slot(i - 1) : 0u); asm("" : "+v"(bb[c])); }
        }
#pragma unroll
        for (int c = 0; c < 4; ++c) bf[hh][c] = tr2(bb[c] + hh * 4096, bb[c] + hh * 4096 + 2048);
      };
      auto mms = [&](int i, int hh) {
#pragma unroll
        for (int n = 0; n < 4; ++n)
#pragma unroll
          for (int m = 0; m < 2; ++m) acc[m][n] = MFMA(bf[hh][n], as_frag(R[i][hh * 2 + m]), acc[m][n]);
      };
      if (!half) {
#pragma unroll
        for (int i = 0; i < 8; ++i) acc[i >> 2][i & 3] = f32x4{0.f, 0.f, 0.f, 0.f};
      }
      const char* obase = (l == 3) ? (const char*)p.out : (const char*)p.ws;
      unsigned osize = (l == 3) ? 2048 * 2048 : WS_SPAN;
      const bool slown = SLOWC((l & 1) ^ 1, j);
      pW = slown ? p.ws : (const char*)(isb ? p.wqkvA : p.wqkvB);
      bW = slown ? WS_WP + (unsigned)((isb ? 0 : 16) + j) * (16 * 24576) : j * 128;
      kW = slown ? 24576 : 64 * 6144; zW = slown ? WS_SPAN : 1024 * 6144; pcW = slown ? 8192 : 2048; hW = slown ? 4096 : 32 * 6144;
      vA = hw * 1024 + lane * 16;
      vW = slown ? vA : (hw * 8 + (lane >> 3)) * 6144 + (((lane & 7) ^ ((((lane >> 4) & 1) | ((hw & 1) << 1)) << 1)) * 16);
      pA = p.ws; zA = WS_SPAN; bA = xb_off(g, l); kA = TS; pcA = 4096; kb = JROT(j) + KX; dj = (j - kb) & 15;
      OPAQUE_S(pW); OPAQUE_S(bW); OPAQUE_S(kW); OPAQUE_S(zW); OPAQUE_S(pcW); OPAQUE_S(hW); OPAQUE_V(vA); OPAQUE_V(vW); OPAQUE_S(bA);
      unsigned sq = bW + j * kW + kh * hW, dst = lds0 + L_R1 + kh * 4096 + hw * 1024;
      OPAQUE_S(obase); OPAQUE_S(osize); OPAQUE_S(sq); OPAQUE_S(dst);
      const rsrc_t ro = mk_rsrc(obase, osize);
      unsigned so[2];
      unsigned xw = lds0 + SC_XCH + wave * 4096 + lane * 16;
      unsigned xa = lds0 + SC_XCH + (wave ^ 4) * 4096 + lane * 16;
      unsigned sb0 = (l == 3) ? g * (128 * 2048) + j * 128 : xb_off(g, l) + j * TS;
      {
        const int row = (hw * 2 + kh) * 16 + fr, ch = (fq & 1) * 2 + (fq >> 1);
        const unsigned sw = (l == 3) ? 0u : (unsigned)((row >> 1) & 7), rb = (unsigned)row << ((l == 3) ? 11 : 7);
#pragma unroll
        for (int pp = 0; pp < 2; ++pp) { so[pp] = rb + (((ch + 4 * pp) ^ sw) * 16); OPAQUE_V(so[pp]); }
        OPAQUE_V(xa); OPAQUE_V(xw); OPAQUE_S(sb0);
      }
      SCHED;
#pragma unroll
      for (int i = 1; i < LA; ++i) issue(i);
      rdB(0, 0);
      if (PRE) {
        f32x4 dm = {0.f, 0.f, 0.f, 0.f};
#pragma unroll 1
        for (int d = 0; d < PRE; ++d) {
#pragma unroll
          for (int c = 0; c < 4; ++c) dm = MFMA(bf[0][c], bf[0][(c + 1) & 3], dm);
        }
        asm volatile("" ::"v"(dm));
        SCHED;
      }
      unsigned mx = vmax(0);
#pragma unroll
      for (int i = 0; i < 8; ++i) {
        SCHED;
        if (__builtin_expect(__any(mx == SENT), 0)) poll(i, LA);
        if (i == 0) TRC(27); else TRQ(27 + i);
        if (i == W1S - 2) { pkf = ld8_sc1(rs, pko, 0); SCHED; }
        if (i == W1S) {
          OPAQUE_V(pkf);
          if (l == 0 && lead && slown && !__all(is_set(pkf, epoch))) poll_all(rs, pko, epoch, dead);
          TRC(35); BAR;
        }
        if (i == 7 && l < 3) dma3<8192>(mk_rsrc(pW, zW), vW, sq, pcW, dst);
        if (PACE && !isb) __builtin_amdgcn_s_sleep(PACE);
        if (i == 2) __builtin_amdgcn_s_setprio(1);
        if (i == 5) __builtin_amdgcn_s_setprio(0);
        SCHED;
        rdB(i, 1);
        if (i == 7) break;
        if (i + LA < 8) issue_(i + LA);
        mms(i, 0);
#pragma unroll
        for (int k = 0; k < 4; ++k) { SGB(0x008, 1); SGB(0x100, 1); SGB(0x020, 1); SGB(0x008, 1); SGB(0x100, 1); }
        SCHED;
        rdB(i + 1, 0); mx = vmax(i + 1);
        mms(i, 1);
#pragma unroll
        for (int k = 0; k < 4; ++k) { SGB(0x008, 1); SGB(0x100, 2); }
#pragma unroll
        for (int k = 4; k < 8; ++k) { SGB(0x008, 1); SGB(0x002, 8 / (8 - 4)); }
      }
#pragma unroll
      for (int n = 0; n < 4; ++n) acc[1][n] = MFMA(bf[0][n], as_frag(R[7][1]), acc[1][n]);
#pragma unroll
      for (int n = 0; n < 4; ++n) acc[0][n] = MFMA(bf[0][n], as_frag(R[7][0]), acc[0][n]);
#pragma unroll
      for (int k = 0; k < 8; ++k) { SGB(0x008, 1); SGB(0x100, 1); }
      SCHED;
#pragma unroll
      for (int n = 0; n < 4; ++n) acc[1][n] = MFMA(bf[1][n], as_frag(R[7][3]), acc[1][n]);
#pragma unroll
      for (int n = 0; n < 4; ++n) acc[0][n] = MFMA(bf[1][n], as_frag(R[7][2]), acc[0][n]);
#pragma unroll
      for (int n = 0; n < 4; ++n) lds_st<f32x4>(xw + n * 1024, acc[1][n]);
      SGB(0x008, 4);
#pragma unroll
      for (int k = 0; k < 4; ++k) { SGB(0x008, 1); SGB(0x200, 1); }
      SCHED;
      TRC(36);
      WAITL0;
      TRC(37); BAR;
      SCHED;
      f32x4 pa[4];
#pragma unroll
      for (int n = 0; n < 4; ++n) pa[n] = lds_ld<f32x4>(xa + n * 1024);
      SCHED;
      u32x4 o[2];
#pragma unroll
      for (int pp = 0; pp < 2; ++pp) {
        const f32x4 a = acc[0][2 * pp] + pa[2 * pp], b = acc[0][2 * pp + 1] + pa[2 * pp + 1];
        auto r0_ = __builtin_amdgcn_permlane16_swap(pk2(a[0], a[1]), pk2(b[0], b[1]), false, false);
        auto r1_ = __builtin_amdgcn_permlane16_swap(pk2(a[2], a[3]), pk2(b[2], b[3]), false, false);
        o[pp] = nosent(u32x4{r0_[0], r1_[0], r0_[1], r1_[1]});
        st16_sc1(ro, so[pp], sb0, o[pp]);
      }
      if (l < 3) {
        lds_st<u32x4>(lds0 + L_R2 + so[0], o[0]);
        lds_st<u32x4>(lds0 + L_R2 + so[1], o[1]);
        const f32x4 zero = {0.f, 0.f, 0.f, 0.f};
        const bf16x8 ones = {0x3f80, 0x3f80, 0x3f80, 0x3f80, 0x3f80, 0x3f80, 0x3f80, 0x3f80};
        const f32x4 a = MFMA(ones, as_frag(o[1]), MFMA(ones, as_frag(o[0]), zero));
        const f32x4 b = MFMA(as_frag(o[1]), as_frag(o[1]), MFMA(as_frag(o[0]), as_frag(o[0]), zero));
        const float q = (fr & 2) ? ((fr & 1) ? b[3] : b[2]) : ((fr & 1) ? b[1] : b[0]);
        if (fq == (fr >> 2)) st8_sc1(rs, ((hw * 2 + kh) * 16 + fr) * 8, st_off(g, l + 1) + j * 1024, u32x2{__float_as_uint(a[0]), __float_as_uint(q)});
      } else if (lead && blockIdx.x == 0 && lane == 0) {
        st8_sc1(rs, WS_EPOCH, 0, epoch);
      }
    }
    TRC(38); TRR(63);
  }
  STAMP_DUMP;
  asm volatile(".Lpf_end:");
}

extern "C" void kernel_launch(void* const* d_in, const int* in_sizes, int n_in, void* d_out, int out_size,
                              void* d_ws, size_t ws_size, hipStream_t stream) {
  Params p;
  p.x = (const u16*)d_in[0];
  p.wqkvA = (const u16*)d_in[1];
  p.wphi = (const u16*)d_in[2];
  p.woA = (const u16*)d_in[3];
  p.wqkvB = (const u16*)d_in[5];
  p.woB = (const u16*)d_in[6];
  p.out = (u16*)d_out;
  p.ws = (char*)d_ws;
  (void)hipFuncSetAttribute((const void*)mega_kernel, hipFuncAttributeMaxDynamicSharedMemorySize, L_TOTAL);
  mega_kernel<<<256, 512, L_TOTAL, stream>>>(p);
}
```

```cpp
#include <hip/hip_runtime.h>
#include <stdint.h>
#include <type_traits>
constexpr int LOOKAHEAD = 2;
constexpr int NVAL = 4;
#define XD 0
#define WT 1
#define LAW 2
#define POL0 0
#define PKALL 0u
#define PA1 16
#define PA2 16
#define PA3 16
#define H1S 0
#define H1P 0
#define SPR 2
#define A1E 0
#define KG 32768
#define TS 17664
#define KOD 0
#define STG 1
#define JOBP 0
#define NORS 0
#define DLY 0
#define PRE 0
#define PACE 4
#define NJ 8
#define KX 0
#define PM 2
#define ROT 1
#define BD 8
#define W1S 7
#define W2F 0
#define RX 8
#define H1D 2
#define H2D 5

typedef unsigned short u16;
typedef unsigned long long u64;
typedef __attribute__((ext_vector_type(8))) short bf16x8;
typedef __attribute__((ext_vector_type(4))) short s16x4;
typedef __attribute__((ext_vector_type(4))) float f32x4;
typedef __attribute__((ext_vector_type(2))) float f32x2;
typedef __attribute__((ext_vector_type(4))) unsigned u32x4;
typedef __attribute__((ext_vector_type(2))) unsigned u32x2;
typedef __attribute__((ext_vector_type(2))) __bf16 bf16x2_t;
#define LAS __attribute__((address_space(3)))
typedef LAS s16x4* lds_s16x4_ptr;

constexpr unsigned NREP = 8, REP_STRIDE = 17 * 2048;
enum { FL_X = 0, FL_CS = 4, FL_CLR = 5, FL_CLC = 9, FL_PK = 13, FL_WO = 14 };
__device__ __forceinline__ unsigned fl_line(int set, int a) { return (unsigned)(set * 16 + a) * 128; }
constexpr unsigned WS_EPOCH = 512u << 10;
constexpr unsigned WS_CS = 1u << 20;
constexpr unsigned WS_KVP = 4u << 20;
constexpr unsigned KVP_G = KG, KVP_J = 16 * KVP_G, KVP_L = KG == 32768 ? 16384 : 16 * KVP_J, KVP_Z = 8192, KVP_SUM = 8704, KVP_HALO = 12u << 10;
constexpr unsigned SENT = 0xffffffffu;
constexpr unsigned WS_X = 14u << 20, WS_O = 29u << 20, WS_ST = 2u << 20;
__device__ __forceinline__ unsigned xb_off(int g, int l) { return WS_X + (unsigned)(l * 16 + g) * (16 * TS); }
__device__ __forceinline__ unsigned ob_off(int g, int j, int l) { return WS_O + (unsigned)((l * 16 + g) * 16 + j) * TS; }
__device__ __forceinline__ unsigned st_off(int g, int l) { return WS_ST + (unsigned)(l * 16 + g) * (16u << 10); }
constexpr unsigned WS_WP = 48u << 20;
constexpr unsigned WS_WO = 60u << 20;
constexpr unsigned WS_SPAN = 64u << 20;

constexpr unsigned L_R1 = 0;
constexpr unsigned L_R2 = 98304, XS = 16384;
constexpr unsigned L_MISC = 147456;
constexpr unsigned L_MEAN = L_MISC;
constexpr unsigned L_RSTD = L_MISC + 576;
constexpr unsigned L_CS = L_MISC + 1152;
constexpr unsigned L_WPHI = L_MISC + 2688;
constexpr unsigned L_SINK = L_MISC + 6912;
constexpr unsigned L_ZF = L_MISC + 1920;
constexpr unsigned L_PREV = L_MISC + 8192;
constexpr unsigned L_TOTAL = 163840;

#define JROT(j) ((j) & ~(NJ - 1))
constexpr int NX = NJ / 2;
#define KB0(g, j) (JROT(j) + (16 / NX) * ((g) & (NX - 1)))
#define JOBC(j, w) (((j) & ~3) + (((j) + (w) + 1) & 3))

#define SLOWC(m, jc) ((slowm >> ((m) * 8 + ((jc) & 7))) & 1)

struct Params {
  const u16* x;
  const u16* wqkvA;
  const u16* wphi;
  const u16* woA;
  const u16* wqkvB;
  const u16* woB;
  u16* out;
  char* ws;
};

#define SCHED __builtin_amdgcn_sched_barrier(0)
#define BAR __builtin_amdgcn_s_barrier()
#define SGB(m, n) __builtin_amdgcn_sched_group_barrier(m, n, 0)
#define WAITV(n) asm volatile("s_waitcnt vmcnt(%0)" ::"n"(n) : "memory")
#define WAITL0 asm volatile("s_waitcnt lgkmcnt(0)" ::: "memory")

#define TRV(slot, val) do {} while (0)
#define TRQ(slot) do {} while (0)
#define TRC(slot) do {} while (0)
#define TRR(slot) do {} while (0)
#define STAMP_INIT do {} while (0)
#define STAMP_DUMP do {} while (0)

__device__ __forceinline__ unsigned pk2(float a, float b) {
  f32x2 v = {a, b};
  return __builtin_bit_cast(unsigned, __builtin_convertvector(v, bf16x2_t));
}
__device__ __forceinline__ float lo16(unsigned u) { return __uint_as_float(u << 16); }
__device__ __forceinline__ float hi16(unsigned u) { return __uint_as_float(u & 0xffff0000u); }

template <int A, int B, typename F> __device__ __forceinline__ void static_for(F f) {
  if constexpr (A < B) { f(std::integral_constant<int, A>{}); static_for<A + 1, B>(f); }
}
typedef __amdgpu_buffer_rsrc_t rsrc_t;
__device__ __forceinline__ rsrc_t mk_rsrc(const void* p, unsigned bytes) { return __builtin_amdgcn_make_buffer_rsrc((void*)p, 0, bytes, 0x00020000); }
#define UNI(x) __builtin_amdgcn_readfirstlane(x)
#define DMA_HEAD(P) "s_mov_b32 m0, %4\n\ts_mov_b32 %0, %3\n\ts_nop 2\n\tbuffer_load_dwordx4 %1, %2, %0 offen " P " lds\n\t"
#define DMA_NEXT(P) "s_add_u32 m0, m0, %6\n\ts_add_u32 %0, %0, %5\n\tbuffer_load_dwordx4 %1, %2, %0 offen " P " lds\n\t"
#define DMA_OPS : "=&s"(t) : "v"(voff), "s"(r), "s"(UNI(soff)), "s"(UNI(lds)), "s"(UNI(sstep)), "i"(LSTEP) : "memory", "scc", "m0"
template <int LSTEP, int POL = 1> __device__ __forceinline__ void dma4(rsrc_t r, unsigned voff, unsigned soff, unsigned sstep, unsigned lds) {
  unsigned t;
  if constexpr (POL == 1) asm volatile(DMA_HEAD("sc1") DMA_NEXT("sc1") DMA_NEXT("sc1") DMA_NEXT("sc1") DMA_OPS);
  else if constexpr (POL == 2) asm volatile(DMA_HEAD("nt") DMA_NEXT("nt") DMA_NEXT("nt") DMA_NEXT("nt") DMA_OPS);
  else asm volatile(DMA_HEAD("") DMA_NEXT("") DMA_NEXT("") DMA_NEXT("") DMA_OPS);
}
template <int LSTEP, int POL = 1> __device__ __forceinline__ void dma3(rsrc_t r, unsigned voff, unsigned soff, unsigned sstep, unsigned lds) {
  unsigned t;
  if constexpr (POL == 1) asm volatile(DMA_HEAD("sc1") DMA_NEXT("sc1") DMA_NEXT("sc1") DMA_OPS);
  else if constexpr (POL == 2) asm volatile(DMA_HEAD("nt") DMA_NEXT("nt") DMA_NEXT("nt") DMA_OPS);
  else asm volatile(DMA_HEAD("") DMA_NEXT("") DMA_NEXT("") DMA_OPS);
}
template <int LSTEP, int N> __device__ __forceinline__ void bar_dma4(rsrc_t r, unsigned voff, unsigned soff, unsigned sstep, unsigned lds) {
  unsigned t;
  asm volatile("s_mov_b32 m0, %4\n\ts_mov_b32 %0, %3\n\ts_nop 0\n\ts_waitcnt vmcnt(%7) lgkmcnt(0)\n\ts_barrier\n\t"
               "buffer_load_dwordx4 %1, %2, %0 offen sc1 lds\n\t" DMA_NEXT("sc1") DMA_NEXT("sc1") DMA_NEXT("sc1")
               : "=&s"(t) : "v"(voff), "s"(r), "s"(UNI(soff)), "s"(UNI(lds)), "s"(UNI(sstep)), "i"(LSTEP), "n"(N) : "memory", "scc", "m0");
}
__device__ __forceinline__ void dma1(rsrc_t r, unsigned voff, unsigned soff, unsigned lds) {
  asm volatile("s_mov_b32 m0, %3\n\ts_nop 3\n\tbuffer_load_dwordx4 %0, %1, %2 offen sc1 lds" ::"v"(voff), "s"(r), "s"(UNI(soff)), "s"(UNI(lds)) : "memory", "m0");
}
__device__ __forceinline__ void touch(rsrc_t r, unsigned voff, unsigned lds) {
  asm volatile("s_mov_b32 m0, %2\n\ts_nop 3\n\tbuffer_load_dword %0, %1, 0 offen lds" ::"v"(voff), "s"(r), "s"(UNI(lds)) : "memory", "m0");
}
__device__ __forceinline__ void ld8_asm(u32x2& d, rsrc_t r, unsigned voff) {
  asm volatile("s_nop 4\n\tbuffer_load_dwordx2 %0, %1, %2, 0 offen sc1" : "=v"(d) : "v"(voff), "s"(r) : "memory");
}

__device__ __forceinline__ void ld16in_asm(u32x4& d, rsrc_t r, unsigned voff, unsigned soff) {
  asm volatile("s_nop 4\n\tbuffer_load_dwordx4 %0, %1, %2, %3 offen" : "=v"(d) : "v"(voff), "s"(r), "s"(UNI(soff)) : "memory");
}
__device__ __forceinline__ void ld16_asm(f32x4& d, rsrc_t r, unsigned voff, unsigned soff) {
  asm volatile("s_nop 4\n\tbuffer_load_dwordx4 %0, %1, %2, %3 offen sc1" : "=v"(d) : "v"(voff), "s"(r), "s"(UNI(soff)) : "memory");
}

template <typename T> __device__ __forceinline__ T lds_ld(unsigned a) { return *(LAS T*)(a); }
template <typename T> __device__ __forceinline__ void lds_st(unsigned a, T v) { *(LAS T*)(a) = v; }
__device__ __forceinline__ bf16x8 tr2(unsigned a0, unsigned a1) {
  s16x4 v0 = __builtin_amdgcn_ds_read_tr16_b64_v4i16((lds_s16x4_ptr)(a0));
  s16x4 v1 = __builtin_amdgcn_ds_read_tr16_b64_v4i16((lds_s16x4_ptr)(a1));
  return __builtin_shufflevector(v0, v1, 0, 1, 2, 3, 4, 5, 6, 7);
}
__device__ __forceinline__ bf16x8 as_frag(u32x4 v) { return __builtin_bit_cast(bf16x8, v); }
#define MFMA(a, b, c) __builtin_amdgcn_mfma_f32_16x16x32_bf16(a, b, c, 0, 0, 0)

__device__ __forceinline__ float vmax2(float a, float b) { float inf = __builtin_inff(); asm("" : "+s"(inf)); return __builtin_amdgcn_fmed3f(a, b, inf); }
#define SWAP16(a, b) __builtin_amdgcn_permlane16_swap(__float_as_uint(a), __float_as_uint(b), false, false)
#define SWAP32(a, b) __builtin_amdgcn_permlane32_swap(__float_as_uint(a), __float_as_uint(b), false, false)
__device__ __forceinline__ void red2_max(float& a, float& b) {
  auto r = SWAP16(a, b);
  float x = vmax2(__uint_as_float(r[0]), __uint_as_float(r[1]));
  r = SWAP32(x, x);
  x = vmax2(__uint_as_float(r[0]), __uint_as_float(r[1]));
  r = SWAP16(x, x);
  a = __uint_as_float(r[0]); b = __uint_as_float(r[1]);
}
__device__ __forceinline__ void red2_add(float& a, float& b) {
  auto r = SWAP16(a, b);
  float x = __uint_as_float(r[0]) + __uint_as_float(r[1]);
  r = SWAP32(x, x);
  x = __uint_as_float(r[0]) + __uint_as_float(r[1]);
  r = SWAP16(x, x);
  a = __uint_as_float(r[0]); b = __uint_as_float(r[1]);
}
template <bool MAX, typename M> __device__ __forceinline__ void red4(float& a, float& b, float& c, float& d, M mid) {
  auto op = [](unsigned x, unsigned y) { return MAX ? vmax2(__uint_as_float(x), __uint_as_float(y)) : __uint_as_float(x) + __uint_as_float(y); };
  auto r = SWAP16(a, b), q = SWAP16(c, d);
  const float x = op(r[0], r[1]), y = op(q[0], q[1]);
  r = SWAP32(x, y);
  const float w = mid(op(r[0], r[1]));
  r = SWAP32(w, w);
  q = __builtin_amdgcn_permlane16_swap(r[1], r[1], false, false);
  r = __builtin_amdgcn_permlane16_swap(r[0], r[0], false, false);
  a = __uint_as_float(r[0]); b = __uint_as_float(r[1]); c = __uint_as_float(q[0]); d = __uint_as_float(q[1]);
}
__device__ __forceinline__ float red_max4(float x) {
  auto r = __builtin_amdgcn_permlane16_swap(__float_as_uint(x), __float_as_uint(x), false, false);
  x = vmax2(__uint_as_float(r[0]), __uint_as_float(r[1]));
  r = __builtin_amdgcn_permlane32_swap(__float_as_uint(x), __float_as_uint(x), false, false);
  return vmax2(__uint_as_float(r[0]), __uint_as_float(r[1]));
}
__device__ __forceinline__ float red_add4(float x) {
  auto r = __builtin_amdgcn_permlane16_swap(__float_as_uint(x), __float_as_uint(x), false, false);
  x = __uint_as_float(r[0]) + __uint_as_float(r[1]);
  r = __builtin_amdgcn_permlane32_swap(__float_as_uint(x), __float_as_uint(x), false, false);
  return __uint_as_float(r[0]) + __uint_as_float(r[1]);
}

__device__ __forceinline__ void st16_sc1(rsrc_t r, unsigned voff, unsigned soff, u32x4 v) {
  __builtin_amdgcn_raw_buffer_store_b128(v, r, voff, soff, 16);
  asm volatile("s_nop 1" ::"v"(v) : "memory");
}
__device__ __forceinline__ void st8_sc1(rsrc_t r, unsigned voff, unsigned soff, u32x2 v) { __builtin_amdgcn_raw_buffer_store_b64(v, r, voff, soff, 16); }
__device__ __forceinline__ u32x4 ld16_sc1(rsrc_t r, unsigned voff, unsigned soff) { return __builtin_amdgcn_raw_buffer_load_b128(r, voff, soff, 16); }
template <int AUX> __device__ __forceinline__ u32x4 ld16p(rsrc_t r, unsigned voff, unsigned soff) { return __builtin_amdgcn_raw_buffer_load_b128(r, voff, soff, AUX); }
__device__ __forceinline__ u32x2 ld8_sc1(rsrc_t r, unsigned voff, unsigned soff) { return __builtin_amdgcn_raw_buffer_load_b64(r, voff, soff, 16); }
__device__ __forceinline__ f32x4 ld4f_sc1(rsrc_t r, unsigned voff, unsigned soff) { return __builtin_bit_cast(f32x4, __builtin_amdgcn_raw_buffer_load_b128(r, voff, soff, 16)); }
#define OPAQUE_V(x) asm volatile("" : "+v"(x))
#define OPAQUE_S(x) asm volatile("" : "+s"(x))

#define CLOBBER asm volatile("" ::: "memory")
constexpr unsigned SPIN_CAP = 1u << 17;
__device__ __forceinline__ bool is_set(u32x2 v, u32x2 epoch) { return v[0] == epoch[0] && v[1] == epoch[1]; }
__device__ __forceinline__ void set_flags(rsrc_t rs, unsigned lineA, int colA, unsigned lineB, int colB, u32x2 epoch, int lane) {
  const unsigned off = (lane & 7) * REP_STRIDE + ((lane & 8) ? lineB + colB * 8 : lineA + colA * 8);
  if (lane < 16) st8_sc1(rs, off, 0, epoch);
}
__device__ __forceinline__ unsigned poll_need(rsrc_t rs, unsigned voff, u32x2 epoch, unsigned ready, unsigned need, bool& dead) {
  for (unsigned spins = 0; !dead; ++spins) {
    CLOBBER;
    ready |= (unsigned)__ballot(is_set(ld8_sc1(rs, voff, 0), epoch));
    if ((ready & need) == need) break;
    if (spins > SPIN_CAP) dead = true;
    __builtin_amdgcn_s_sleep(1);
  }
  CLOBBER;
  return dead ? ~0u : ready;
}

__device__ __forceinline__ void poll_all(rsrc_t rs, unsigned voff, u32x2 epoch, bool& dead) {
  for (unsigned spins = 0; !dead; ++spins) {
    CLOBBER;
    if (__all(is_set(ld8_sc1(rs, voff, 0), epoch))) break;
    if (spins > SPIN_CAP) dead = true;
    __builtin_amdgcn_s_sleep(1);
  }
  CLOBBER;
}

constexpr unsigned SC_VF = L_R1 + 32768;
constexpr unsigned SC_PHIK = L_R1 + 49152;
constexpr unsigned SC_XCH = L_R2 + XS;
constexpr unsigned wo_slot(int s) {
  return s < 2 ? L_R2 + (unsigned)(s + 1) * XS : (s < 6 ? L_R1 + (unsigned)(s - 2) * 16384 : (s == 6 ? L_R1 + 81920 : L_R2));
}
__device__ __forceinline__ unsigned wo_src(int s, int j) { return (unsigned)(((JROT(j) >> 1) + NJ / 2 + s) & 7); }
__device__ __forceinline__ unsigned wo_swz(int lane) { return (unsigned)(((lane & 7) ^ ((lane >> 3) & 6)) * 16); }
__device__ __forceinline__ unsigned umax3(unsigned a, unsigned b, unsigned c) { return max(max(a, b), c); }
__device__ __forceinline__ unsigned umax4v(unsigned m, const u32x4& v) { return umax3(umax3(m, v[0], v[1]), v[2], v[3]); }
__device__ __forceinline__ u32x4 nosent(u32x4 v) { return u32x4{min(v[0], SENT - 1), min(v[1], SENT - 1), min(v[2], SENT - 1), min(v[3], SENT - 1)}; }

template <typename P, typename F>
__device__ __forceinline__ void qkv_loop(int isb, int wm, int h, int lane, bool first, int& trv, P pre, F fin) {
  constexpr unsigned lds0 = 0;
  f32x4 acc[4][6];
  const int fr = lane & 15, fq = lane >> 4;
  unsigned ab[2], bb[2];
  {
    const int r = wm * 64 + fr;
#pragma unroll
    for (int ks = 0; ks < 2; ++ks) ab[ks] = lds0 + L_R2 + r * 128 + (((ks * 4 + fq) ^ ((fr >> 1) & 7)) * 16);
    const int kr = fq * 8 + (fr >> 2);
    const int gs = ((kr >> 1) & 1) | (((kr >> 3) & 1) << 1);
#pragma unroll
    for (int c = 0; c < 2; ++c) bb[c] = lds0 + L_R1 + kr * 128 + (((h * 2 + c) ^ gs) * 32) + (lane & 3) * 8;
  }
#pragma unroll
  for (int m = 0; m < 4; ++m)
#pragma unroll
    for (int n = 0; n < 6; ++n) acc[m][n] = f32x4{0.f, 0.f, 0.f, 0.f};
  bf16x8 af[2][4], bf[2][6];
  auto rds = [&](int ks, int p, int rot = 0) {
#pragma unroll
    for (int m = 0; m < 4; ++m) af[p][m] = lds_ld<bf16x8>(ab[ks] + m * 2048);
#pragma unroll
    for (int nn = 0; nn < 6; ++nn) {
      const int n = (nn + rot) % 6;
      const unsigned a = bb[n & 1] + (n >> 1) * 8192 + ks * 4096;
      bf[p][n] = tr2(a, a + 512);
    }
  };
  auto mms = [&](int p, int rot = 0, int cnt = 6) {
#pragma unroll
    for (int nn = 0; nn < cnt; ++nn)
#pragma unroll
      for (int mm = 0; mm < 4; ++mm) { const int n = (nn + rot) % 6, m = (n & 1) ? 3 - mm : mm; acc[m][n] = n < 4 ? MFMA(bf[p][n], af[p][m], acc[m][n]) : MFMA(af[p][m], bf[p][n], acc[m][n]); }
  };
  auto ilv2 = [&]() {
#pragma unroll
    for (int i = 0; i < 16; ++i) { SGB(0x008, 1); SGB(0x100, 1); }
#pragma unroll
    for (int i = 0; i < 4; ++i) { SGB(0x008, 1); SGB(0x002, 1); }
    SGB(0x008, 4);
  };
  auto ilv3 = [&]() {
#pragma unroll
    for (int i = 0; i < 16; ++i) { SGB(0x008, 1); SGB(0x100, 1); }
#pragma unroll
    for (int i = 0; i < 7; ++i) { SGB(0x008, 1); SGB(0x004, 2); }
    SGB(0x008, 1);
  };
  if (!first) { WAITV(3); WAITL0; }
  TRQ(1); BAR;
  int tslot = 2;
  rds(0, 0);
  int sa = 0, sb = 0;
  int da = XS, db = 24576;
  auto step = [&]() {
    ab[0] += da; ab[1] += da;
    bb[0] += db; bb[1] += db;
  };
  auto ring = [&]() {
    sa = (sa == 2) ? 0 : sa + 1;
    sb = (sb == 3) ? 0 : sb + 1;
    da = (sa == 2) ? -2 * (int)XS : (int)XS;
    db = (sb == 3) ? -3 * 24576 : 24576;
  };
#pragma unroll 1
  for (int t = 14; t > 0; --t) {
    SCHED;
    rds(1, 1); mms(0);
    step();
    ilv2();
    SCHED;
    __builtin_amdgcn_s_waitcnt(0x0070);
    TRQ(tslot); tslot++; BAR;
    SCHED;
    rds(0, 0);
    ring();
    mms(1); ilv3();
  }
  SCHED;
  f32x4 cs[4], nm4[4];
  float nmean[4], csv[2];
  auto fold = [&](int n) {
#pragma unroll
    for (int mm = 0; mm < 4; ++mm) {
      const int m = (n & 1) ? 3 - mm : mm;
#pragma unroll
      for (int e = 0; e < 4; ++e) {
        float t = n < 4 ? __builtin_fmaf(nmean[m], cs[n & 3][e], acc[m][n][e]) : __builtin_fmaf(nm4[m][e], csv[n & 1], acc[m][n][e]);
        asm("" : "+v"(t));
        acc[m][n][e] = t;
      }
    }
  };
  {
    unsigned pm = lds0 + L_MEAN + (16 + wm * 64 + fr) * 4, pc = lds0 + L_CS + (h * 32 + fq * 4) * 4;
    unsigned pm4 = lds0 + L_MEAN + (16 + wm * 64 + fq * 4) * 4, pcv = lds0 + L_CS + (128 + h * 32 + fr) * 4;
    asm("" : "+v"(pm)); asm("" : "+v"(pc)); asm("" : "+v"(pm4)); asm("" : "+v"(pcv));
#pragma unroll
    for (int m = 0; m < 4; ++m) nmean[m] = lds_ld<float>(pm + m * 64);
#pragma unroll
    for (int n = 0; n < 4; ++n) cs[n ^ 2] = lds_ld<f32x4>(pc + (((n ^ 2) >> 1) * 64 + (n & 1) * 16) * 4);
#pragma unroll
    for (int m = 0; m < 4; ++m) nm4[m] = lds_ld<f32x4>(pm4 + m * 64);
#pragma unroll
    for (int n = 0; n < 2; ++n) csv[n] = lds_ld<float>(pcv + n * 64);
    rds(1, 1, 4); mms(0, 2);
    step();
    fold(2); fold(3);
    SGB(0x002, 8);
#pragma unroll
    for (int i = 0; i < 8; ++i) { SGB(0x008, 1); SGB(0x100, 2); }
#pragma unroll
    for (int i = 0; i < 11; ++i) { SGB(0x008, 1); SGB(0x100, 1); SGB(0x002, 2); }
#pragma unroll
    for (int i = 0; i < 5; ++i) { SGB(0x008, 1); SGB(0x002, 3); }
    SCHED;
    __builtin_amdgcn_s_waitcnt(0xC07F);
    TRQ(16); BAR;
    SCHED;
    rds(0, 0, 2);
    mms(1, 4); fold(4); fold(5);
#pragma unroll
    for (int i = 0; i < 8; ++i) { SGB(0x008, 1); SGB(0x100, 1); SGB(0x002, 1); }
#pragma unroll
    for (int i = 0; i < 8; ++i) { SGB(0x008, 1); SGB(0x100, 1); SGB(0x002, 2); }
#pragma unroll
    for (int i = 0; i < 8; ++i) { SGB(0x008, 1); SGB(0x002, 2); }
    SCHED;
    pre(0);
    SCHED;
    rds(1, 1, 2);
    mms(0, 2, 4); fold(0); fold(1);
    pre(1);
#pragma unroll
    for (int i = 0; i < 16; ++i) { SGB(0x008, 1); SGB(0x100, 1); SGB(0x002, 2); SGB(0x004, 1); }
  }
  SCHED;
  auto mm6 = [&](int m, int n0 = 0, int n1 = 8) {
#pragma unroll
    for (int nn = n0; nn < n1; ++nn) {
      const int n = nn < 4 ? nn + 2 : nn & 1, p = (nn >> 1) != 2;
      acc[m][n] = n < 4 ? MFMA(bf[p][n], af[p][m], acc[m][n]) : MFMA(af[p][m], bf[p][n], acc[m][n]);
    }
  };
  if (!isb) {
    fin(std::false_type{}, acc, mm6);
#pragma unroll
    for (int i = 0; i < 24; ++i) asm volatile("" : "=v"(acc[i / 6][i % 6]));
#pragma unroll
    for (int i = 0; i < 6; ++i) asm volatile("" : "=v"(bf[1][i]));
#pragma unroll
    for (int i = 0; i < 8; ++i) asm volatile("" : "=v"(af[i >> 2][i & 3]));
    asm volatile("" : "=v"(bf[0][0])); asm volatile("" : "=v"(bf[0][1]));
  }
  OPAQUE_S(isb);
  if (isb) fin(std::true_type{}, acc, mm6);
}
__device__ __forceinline__ u32x4 pack(const f32x4& a, const f32x4& b) { return u32x4{pk2(a[0], a[1]), pk2(a[2], a[3]), pk2(b[0], b[1]), pk2(b[2], b[3])}; }

template <int N> __device__ __forceinline__ void hedgehog(const f32x4 (&r)[4][2], const float (&c)[4], float (&ep)[4][8], float (&en)[4][8]) {
  float mx[4], nm[4];
#pragma unroll
  for (int m = 0; m < N; ++m) {
    const f32x4 a = r[m][0], b = r[m][1];
    mx[m] = vmax2(fmaxf(fmaxf(a[3], b[0]), b[1]), fmaxf(fmaxf(fmaxf(fmaxf(a[0], a[1]), a[2]), b[2]), b[3]));
    nm[m] = vmax2(fmaxf(fmaxf(-a[3], -b[0]), -b[1]), fmaxf(fmaxf(fmaxf(fmaxf(-a[0], -a[1]), -a[2]), -b[2]), -b[3]));
  }
  SCHED;
  red4<true>(mx[0], mx[1], nm[0], nm[1], [](float x) { return x; });
  if (N == 4) red4<true>(mx[2], mx[3], nm[2], nm[3], [](float x) { return x; });
#pragma unroll
  for (int m = 0; m < N; ++m) { mx[m] *= -c[m]; nm[m] *= -c[m]; }
  SCHED;
#pragma unroll
  for (int m = 0; m < N; ++m) {
#pragma unroll
    for (int i = 0; i < 8; ++i) { ep[m][i] = __builtin_fmaf(r[m][i >> 2][i & 3], c[m], mx[m]); en[m][i] = __builtin_fmaf(r[m][i >> 2][i & 3], -c[m], nm[m]); }
    SCHED;
#pragma unroll
    for (int i = 0; i < 8; ++i) { ep[m][i] = __builtin_amdgcn_exp2f(ep[m][i]); en[m][i] = __builtin_amdgcn_exp2f(en[m][i]); }
    SCHED;
  }
}
__device__ __forceinline__ u32x4 pack8(const float (&e)[8], float f) {
  return u32x4{pk2(e[0] * f, e[1] * f), pk2(e[2] * f, e[3] * f), pk2(e[4] * f, e[5] * f), pk2(e[6] * f, e[7] * f)};
}
__device__ __forceinline__ u32x4 pack8(const float (&e)[8]) { return u32x4{pk2(e[0], e[1]), pk2(e[2], e[3]), pk2(e[4], e[5]), pk2(e[6], e[7])}; }

constexpr u64 window_mask(int t, int e) {
  u64 mk = 0;
  for (int ln = 0; ln < 64; ++ln) {
    const int delta = (ln & 15) + 16 - t * 16 - (ln >> 4) * 4 - e;
    if (delta >= 0 && delta <= 14 && !(delta & 1)) mk |= 1ull << ln;
  }
  return mk;
}
__device__ __forceinline__ void phik2(const f32x4 (&r)[4][2], const float (&c)[4], unsigned pw) {
  const f32x4 zero = {0.f, 0.f, 0.f, 0.f};
  const bf16x8 ones = {0x3f80, 0x3f80, 0x3f80, 0x3f80, 0x3f80, 0x3f80, 0x3f80, 0x3f80};
  float ep[4][8], en[4][8];
  f32x4 sp[2], sn[2];
  hedgehog<2>(r, c, ep, en);
#pragma unroll
  for (int m = 0; m < 2; ++m) { sp[m] = MFMA(ones, as_frag(pack8(ep[m])), zero); sn[m] = MFMA(ones, as_frag(pack8(en[m])), zero); }
#pragma unroll
  for (int m = 0; m < 2; ++m) { asm("" : "+v"(sp[m])); asm("" : "+v"(sn[m])); }
  SCHED;
#pragma unroll
  for (int m = 0; m < 2; ++m) {
    const float fp = __builtin_amdgcn_rcpf(sp[m][0]), fn = __builtin_amdgcn_rcpf(sn[m][0]);
#pragma unroll
    for (int i = 0; i < 8; ++i) { ep[m][i] *= fp; en[m][i] *= fn; }
    SCHED;
    const u32x4 pp = pack8(ep[m]), pn = pack8(en[m]);
    const unsigned d = pw + m * (16 * 160);
    lds_st<u32x2>(d, u32x2{pp[0], pp[1]});
    lds_st<u32x2>(d + 32, u32x2{pp[2], pp[3]});
    lds_st<u32x2>(d + 64, u32x2{pn[0], pn[1]});
    lds_st<u32x2>(d + 96, u32x2{pn[2], pn[3]});
    SCHED;
  }
}
__device__ __forceinline__ bf16x8 rowsum_sel(int lane) {
  const int fr = lane & 15;
  const unsigned one = (fr < 8) ? (0x3f80u << (16 * (fr & 1))) : 0u;
  const int e = (fr >> 1) & 3;
  return as_frag(u32x4{e == 0 ? one : 0u, e == 1 ? one : 0u, e == 2 ? one : 0u, e == 3 ? one : 0u});
}
__device__ __forceinline__ f32x4 rowsum_fin(f32x4 c) {
#pragma unroll
  for (int k = 0; k < 4; ++k) {
    const float a = c[k];
    c[k] = a + __int_as_float(__builtin_amdgcn_update_dpp(0, __float_as_int(a), 0x128  , 0xf, 0xf, false));
  }
  return c;
}
__device__ __forceinline__ void job_colsum(rsrc_t rs, const u32x4 (&w)[8], int mat, int g, int jc, int hw, int lane) {
  const bf16x8 sel = rowsum_sel(lane);
  f32x4 c = {0.f, 0.f, 0.f, 0.f};
#pragma unroll
  for (int i = 0; i < 8; ++i) c = MFMA(sel, as_frag(w[i]), c);
  const u32x4 w4 = __builtin_bit_cast(u32x4, rowsum_fin(c));
  unsigned so = WS_CS + (unsigned)(((mat * 16 + jc) * 16 + g) * 768);
  OPAQUE_S(so);
  if ((lane & 40) == 0) st16_sc1(rs, (unsigned)(hw * 64 + (lane & 7) * 8 + (lane >> 4) * 4) * 4, so, w4);
}
__device__ __forceinline__ unsigned img_off(int i, int lane) { return (lane >> 3) * 128 + (((lane & 7) ^ ((((lane >> 4) & 1) | ((i & 1) << 1)) << 1)) * 16); }
__device__ __forceinline__ void job_pack(rsrc_t rs, u32x4 w, int i, int mat, int g, int jc, int hw, int lane) {
  st16_sc1(rs, img_off(i, lane),
           WS_WP + (unsigned)(((mat * 16 + jc) * 16 + g) * 24576 + hw * 8192 + i * 1024), w);
}

__global__ __launch_bounds__(512, 2) void mega_kernel(Params p) {
  extern __shared__ __attribute__((aligned(16))) char smem[];
  const int tid = threadIdx.x;
  const int wave0 = __builtin_amdgcn_readfirstlane(tid >> 6);
  const int xx = blockIdx.x & 7, rr = blockIdx.x >> 3;
  const int g0 = (rr / NJ) * NX + (xx & (NX - 1)), j0 = (xx / NX) * NJ + (rr & (NJ - 1));
  constexpr unsigned lds0 = 0, lds00 = 0;
  if ((unsigned)(size_t)(LAS char*)smem != 0u) return;
#define LOCALS0 int g = g0, j = j0, l = l0, wave = wave0;                                              \
  OPAQUE_S(g); OPAQUE_S(j); OPAQUE_S(l); OPAQUE_S(wave);                                               \
  const int hw = wave & 3, wm = wave & 1, h = (wave >> 1) & 1;                                         \
  const unsigned myrep = (unsigned)(JROT(j) / 2 + (g & (NX - 1))) * REP_STRIDE;                              \
  (void)hw; (void)wm; (void)h; (void)myrep;                                                            \
  int lane = tid; OPAQUE_V(lane); lane &= 63
#define LOCALS LOCALS0; const bool isb = l & 1, lead = (wave == 4); const int half = wave >> 2; (void)isb; (void)lead; (void)half
  const rsrc_t rs = mk_rsrc(p.ws, WS_SPAN);
  const bool cw = wave0 < 4;
  int trv = 0, trA = 0, trB = 0, trC = 0;
  STAMP_INIT;
  TRC(0); TRR(62);
  if (wave0 == 3) {
    const char* pc;
    unsigned len;
    asm volatile("s_getpc_b64 %0\n.Lpf_base:\n\ts_mov_b32 %1, .Lpf_end-.Lpf_base" : "=s"(pc), "=s"(len));
    const unsigned off = ((unsigned)rr * 16 + (tid & 15)) * 128;
    touch(mk_rsrc(pc, len), ((int)len > 0 && off + 4 <= len) ? off : 0u, lds00 + L_SINK);
  }
  if (wave0 == 2) touch(mk_rsrc(p.ws, NREP * REP_STRIDE), min(blockIdx.x * 9 + ((tid & 63) < 9 ? (tid & 63) : 0), NREP * REP_STRIDE / 128 - 1) * 128, lds00 + L_SINK + 512);
  auto slowbit = [](const void* q) { return 1u << ((3 - ((unsigned)(size_t)q >> 7)) & 7); };
  unsigned slowm = slowbit(p.wqkvA) | (slowbit(p.wqkvB) << 8) | (slowbit(p.woA) << 16) | (slowbit(p.woB) << 24);
  slowm |= PKALL;
  OPAQUE_S(slowm);
  u32x2 epoch = {0u, 0u};
  bool dead = false;
      if (cw) {
        const int l0 = 0;
        LOCALS;
        u32x4 v[2][8];
        if (wave < 3) {
          const int jc = JOBC(j, wave), kt = (KB0(g, j) + (unsigned)g / NX) & 15;
          const rsrc_t ra = mk_rsrc(p.wqkvA, 1024 * 6144);
#pragma unroll
          for (int i = 0; i < 8; ++i)
            v[0][i] = __builtin_amdgcn_raw_buffer_load_b128(ra, (lane >> 3) * 6144 + (lane & 7) * 16, (kt * 64 + i * 8) * 6144 + wave * 2048 + jc * 128, 0);
          SCHED;
          TRC(44); BAR; SCHED;
          job_colsum(rs, v[0], 0, g, jc, wave, lane);
          if (SLOWC(0, jc)) {
#pragma unroll
            for (int i = 0; i < 8; ++i) job_pack(rs, v[0][i], i, 0, kt, jc, wave, lane);
          }
        } else {
          const int fr = lane & 15, fq = lane >> 4;
          const rsrc_t rx = mk_rsrc(p.x, 2048 * 2048);
#pragma unroll
          for (int i = 0; i < 16; ++i)
            v[i & 1][i >> 1] = __builtin_amdgcn_raw_buffer_load_b128(rx, fr * 2048 + fq * 16, (g * 128 + (j & (NJ - 1)) * (128 / NJ) + ((i >> 1) % (8 / NJ)) * 16) * 2048 + ((KB0(g, j) + (i >> 1) / (8 / NJ)) & 15) * 128 + (i & 1) * 64, 0);
          SCHED;
          TRC(44); BAR; SCHED;
          const f32x4 zero = {0.f, 0.f, 0.f, 0.f};
          const bf16x8 ones = {0x3f80, 0x3f80, 0x3f80, 0x3f80, 0x3f80, 0x3f80, 0x3f80, 0x3f80};
          float sm[8], q[8];
#pragma unroll
          for (int m = 0; m < 8; ++m) {
            const f32x4 a = MFMA(ones, as_frag(v[1][m]), MFMA(ones, as_frag(v[0][m]), zero));
            const f32x4 b = MFMA(as_frag(v[1][m]), as_frag(v[1][m]), MFMA(as_frag(v[0][m]), as_frag(v[0][m]), zero));
            sm[m] = a[0];
            q[m] = (fr & 2) ? ((fr & 1) ? b[3] : b[2]) : ((fr & 1) ? b[1] : b[0]);
          }
          if (fq == (fr >> 2)) {
#pragma unroll
            for (int m = 0; m < 8; ++m) st8_sc1(rs, ((m % (8 / NJ)) * 16 + fr) * 8 + (m / (8 / NJ)) * 1024, st_off(g, 0) + JROT(j) * 1024 + (j & (NJ - 1)) * (1024 / NJ), u32x2{__float_as_uint(sm[m]), __float_as_uint(q[m])});
          }
        }
      }
  const char *pA = (const char*)p.x, *pW = (const char*)p.wqkvA;
  unsigned zA = 2048 * 2048, bA = g0 * (128 * 2048), kA = 128, pcA = 32 * 2048, vA;
  unsigned zW = 1024 * 6144, bW = j0 * 128, kW = 64 * 6144, pcW = 2048, hW = 32 * 6144, vW;
  int kb = KB0(g0, j0), dj = -1;
  {
    const int lane = tid & 63, hw = wave0 & 3;
    vA = (hw * 8 + (lane >> 3)) * 2048 + (((lane & 7) ^ (((hw & 1) * 4 + (lane >> 4)) & 7)) * 16);
    vW = (hw * 8 + (lane >> 3)) * 6144 + (((lane & 7) ^ ((((lane >> 4) & 1) | ((hw & 1) << 1)) << 1)) * 16);
  }

#pragma unroll 1
  for (int l0 = 0; l0 < 4; ++l0) {
    if (l0) { TRC(0); TRR(62); }

    f32x4 acc[2][4];
    auto kvpart = [&](unsigned va, unsigned pa, unsigned kvb, int lane, int wave) {
      const f32x4 zero = {0.f, 0.f, 0.f, 0.f};
      const bf16x8 ones = {0x3f80, 0x3f80, 0x3f80, 0x3f80, 0x3f80, 0x3f80, 0x3f80, 0x3f80};
      f32x4 c0 = zero, c1 = zero, z0 = zero, z1 = zero;
#pragma unroll
      for (int ks = 0; ks < 4; ++ks) {
        const bf16x8 v = lds_ld<bf16x8>(va + ks * 2048);
        const bf16x8 a0 = tr2(pa + ks * 32 * 160, pa + (ks * 32 + 16) * 160);
        const bf16x8 a1 = tr2(pa + 32 + ks * 32 * 160, pa + 32 + (ks * 32 + 16) * 160);
        c0 = MFMA(a0, v, c0);
        c1 = MFMA(a1, v, c1);
        z0 = MFMA(a0, ones, z0);
        z1 = MFMA(a1, ones, z1);
      }
      st16_sc1(rs, lane * 16, kvb + (unsigned)wave * 1024, nosent(pack(c0, c1)));
      if (!(wave & 2) && (lane & 15) == 0) st16_sc1(rs, lane, kvb + KVP_Z + (wave >> 2) * 128 + (wave & 1) * 64, nosent(pack(z0, z1)));
    };
#define KV_ADDR                                                                                                            \
    unsigned va = lds0 + SC_VF + (wave >> 2) * 8192 + ((wave >> 1) & 1) * 1024 + lane * 16;                                \
    unsigned pa = lds0 + SC_PHIK + ((wave >> 2) * 128 + fq * 4 + (fr >> 2)) * 160 + (wave & 1) * 64 + (lane & 3) * 8;      \
    unsigned kvj = WS_KVP + (unsigned)j * KVP_J + (unsigned)(l >> 1) * KVP_L;                                              \
    OPAQUE_V(va); OPAQUE_V(pa); OPAQUE_S(kvj)
    if (cw) {
      LOCALS;
      unsigned otile, kvo, l16, pr1, pr4;
      int g2, j2, l2, ln2;
      qkv_loop(l & 1, wm, h, lane, l == 0, trv, [&](int st) {
        if (st == 0) {
          g2 = g0; j2 = j0; l2 = l0; ln2 = tid;
          OPAQUE_S(g2); OPAQUE_S(j2); OPAQUE_S(l2); OPAQUE_V(ln2);
          return;
        }
        otile = ob_off(g2, j2, l2) + (unsigned)(wm * 8 + h) * 1024;
        kvo = WS_KVP + (unsigned)j2 * KVP_J + (unsigned)g2 * KVP_G + (unsigned)(l2 >> 1) * KVP_L;
        l16 = (ln2 & 63) * 16;
        pr1 = lds0 + L_RSTD + (16 + wm * 64 + (ln2 & 15)) * 4;
        pr4 = lds0 + L_RSTD + wm * 256 + (ln2 & 48);
        asm("" : "+s"(otile)); asm("" : "+s"(kvo)); asm("" : "+v"(l16)); asm("" : "+v"(pr1)); asm("" : "+v"(pr4));
      }, [&](auto isb_c, f32x4 (&a)[4][6], auto& mm6) {
        const f32x4 zero = {0.f, 0.f, 0.f, 0.f};
        constexpr float L2E = 1.4426950408889634f;
        float rq[4];
#define ilv8(nv) do { _Pragma("unroll") for (int i_ = 0; i_ < 8; ++i_) { SGB(0x008, 1); SGB(0x100, 2); SGB(0x002, nv); } SCHED; } while (0)
        if constexpr (decltype(isb_c)::value) {
          constexpr float QS = 0.17677669529663687f * L2E, NEG = -3.0e38f;
          const bf16x8 ones = {0x3f80, 0x3f80, 0x3f80, 0x3f80, 0x3f80, 0x3f80, 0x3f80, 0x3f80};
          u32x4 kf[5], vf[5], qf[4], pf[4];
          f32x4 rs4[4], bias[2], s0[4], s1[4], dn[4];
          float x[4][4], mx[4];
          unsigned psel0, psel1;
          auto pkkv = [&](int m) { kf[m + 1] = pack(a[m][2] * rq[m], a[m][3] * rq[m]); vf[m + 1] = pack(a[m][4] * rs4[m], a[m][5] * rs4[m]); };
          auto pkq = [&](int m) { qf[m] = pack(a[m][0], a[m][1]); };
          auto st1 = [&](int m) {
            x[m][0] = vmax2(s0[m][0], s0[m][1]); x[m][1] = vmax2(s0[m][2], s0[m][3]); x[m][2] = vmax2(s1[m][0], s1[m][1]); x[m][3] = vmax2(s1[m][2], s1[m][3]);
            mx[m] = vmax2(vmax2(x[m][0], x[m][1]), vmax2(x[m][2], x[m][3]));
          };
          auto st2 = [&](int m) {
            const float mxl = mx[m] * -rq[m];
#pragma unroll
            for (int e = 0; e < 4; ++e) x[m][e] = __builtin_fmaf(x[m][e], rq[m], mxl);
          };
          auto st3 = [&](int m) {
#pragma unroll
            for (int e = 0; e < 4; ++e) x[m][e] = __builtin_amdgcn_exp2f(x[m][e]);
          };
          auto st5 = [&](int m) {
            const unsigned p0 = pk2(x[m][0], x[m][1]), p1 = pk2(x[m][2], x[m][3]);
            pf[m] = u32x4{__builtin_amdgcn_perm(0u, p0, psel0), __builtin_amdgcn_perm(0u, p0, psel1), __builtin_amdgcn_perm(0u, p1, psel0), __builtin_amdgcn_perm(0u, p1, psel1)};
          };
          auto st6 = [&](int m) {
            dn[m] = MFMA(ones, as_frag(pf[m]), zero);
            s0[m] = MFMA(as_frag(u32x4{vf[m][0], vf[m][1], vf[m + 1][0], vf[m + 1][1]}), as_frag(pf[m]), zero);
            s1[m] = MFMA(as_frag(u32x4{vf[m][2], vf[m][3], vf[m + 1][2], vf[m + 1][3]}), as_frag(pf[m]), zero);
            asm("" : "+v"(dn[m]));
          };
          auto st7 = [&](int m) { const float inv = __builtin_amdgcn_rcpf(dn[m][0]); pf[m] = nosent(pack(s0[m] * inv, s1[m] * inv)); };
          mm6(3);
#pragma unroll
          for (int m = 0; m < 4; ++m) rq[m] = lds_ld<float>(pr1 + m * 64);
#pragma unroll
          for (int m = 3; m >= 0; --m) rs4[m] = lds_ld<f32x4>(pr4 + 64 + m * 64);
          unsigned pv_ = l16 + lds0 + L_PREV + h * 2048;
          asm("" : "+v"(pv_));
          static_for<0, 8>([&](auto i_c) __attribute__((always_inline)) {
            constexpr int i = decltype(i_c)::value;
            constexpr u64 mk = window_mask(i >> 2, i & 3);
            bias[i >> 2][i & 3] = __builtin_amdgcn_inverse_ballot_w64(mk) ? 0.f : NEG;
          });
          asm("" : "+v"(bias[0])); asm("" : "+v"(bias[1]));
          const unsigned hso = kvo + KVP_HALO + h * 2048, hsi = hso - (g > 0 ? KVP_G : 0u);
          ilv8(2);
          mm6(2);
          pkkv(3);
          ilv8(2);
          OPAQUE_V(kf[4]); OPAQUE_V(vf[4]);
          if (wm) {
            st16_sc1(rs, l16, hso, nosent(kf[4]));
            st16_sc1(rs, l16, hso + 1024, nosent(vf[4]));
          } else {
            lds_st<u32x4>(pv_, kf[4]);
            lds_st<u32x4>(pv_ + 1024, vf[4]);
          }
          SCHED;
          mm6(1);
          pkq(3); pkkv(2);
          ilv8(3);
          WAITL0;
          TRQ(17); BAR;
          SCHED;
          mm6(0);
          pkq(2); pkkv(1);
          ilv8(3);
          s0[3] = MFMA(as_frag(kf[3]), as_frag(qf[3]), bias[0]); s1[3] = MFMA(as_frag(kf[4]), as_frag(qf[3]), bias[1]);
          s0[2] = MFMA(as_frag(kf[2]), as_frag(qf[2]), bias[0]); s1[2] = MFMA(as_frag(kf[3]), as_frag(qf[2]), bias[1]);
          pkq(1); pkkv(0);
#pragma unroll
          for (int i = 0; i < 4; ++i) { SGB(0x008, 1); SGB(0x002, 5); }
          SCHED;
          const bool halo = wm == 0 && g > 0;
          const u32x4 hk = ld16p<PA1>(rs, l16, hsi), hv = ld16p<PA1>(rs, l16, hsi + 1024);
          SCHED;
          s0[1] = MFMA(as_frag(kf[1]), as_frag(qf[1]), bias[0]); s1[1] = MFMA(as_frag(kf[2]), as_frag(qf[1]), bias[1]);
          st1(3); st1(2);
#pragma unroll
          for (int m = 0; m < 4; ++m) rq[m] *= QS;
          SCHED;
          st1(1);
          pkq(0);
          psel0 = __builtin_amdgcn_inverse_ballot_w64(0xaaaaaaaaaaaaaaaaull) ? 0x01000c0cu : 0x0c0c0100u;
          psel1 = __builtin_amdgcn_inverse_ballot_w64(0xaaaaaaaaaaaaaaaaull) ? 0x03020c0cu : 0x0c0c0302u;
          { float d = mx[1]; red4<true>(mx[3], mx[2], mx[1], d, [](float v) { return v; }); }
          s1[0] = MFMA(as_frag(kf[1]), as_frag(qf[0]), bias[1]);
          SCHED;
          st2(3); st2(2); st2(1);
          SCHED;
          st3(3); st3(2); st3(1);
          SCHED;
          st5(3); st6(3); st5(2); st6(2); st5(1); st6(1);
          SCHED;
          TRR(60);
          TRC(18);
          u32x4 pk = lds_ld<u32x4>(pv_), pv = lds_ld<u32x4>(pv_ + 1024);
          st7(3); st7(2); st7(1);
          SCHED;
#pragma unroll
          for (int m = 3; m > 0; --m) st16_sc1(rs, l16, otile + m * 2048, pf[m]);
          if (!wm && g == 0) bias[0] = f32x4{NEG, NEG, NEG, NEG};
          SCHED;
          TRV(55, 0); TRC(21);
          if (halo) {
            pk = hk; pv = hv;
            for (unsigned spins = 0; !dead && __any(umax4v(umax4v(0u, pk), pv) == SENT); ++spins) {
              if (spins > SPIN_CAP) { dead = true; break; }
              CLOBBER;
              TRV(55, spins + 1);
              pk = ld16p<PA1>(rs, l16, hsi); pv = ld16p<PA1>(rs, l16, hsi + 1024);
              OPAQUE_V(pk); OPAQUE_V(pv);
            }
          }
          vf[0] = pv;
          s0[0] = MFMA(as_frag(pk), as_frag(qf[0]), bias[0]);
          st1(0);
          mx[0] = red_max4(mx[0]);
          st2(0); st3(0); st5(0); st6(0); st7(0);
          st16_sc1(rs, l16, otile, pf[0]);
        } else {
          LOCALS;
          const int fr = lane & 15, fq = lane >> 4;
          f32x4 rs4[4], r[4][2];
          const bf16x8 ones = {0x3f80, 0x3f80, 0x3f80, 0x3f80, 0x3f80, 0x3f80, 0x3f80, 0x3f80};
          auto proj = [&](int m, int n, const bf16x8& w0, const bf16x8& w1) {
            const u32x4 f = pack(a[m][n], a[m][n + 1]);
            r[m][0] = MFMA(w0, as_frag(f), zero); r[m][1] = MFMA(w1, as_frag(f), zero);
          };
          mm6(3, 0, 2); mm6(2, 0, 2); mm6(1, 0, 2); mm6(0, 0, 2);
          unsigned wa = lds0 + L_WPHI + h * 2048 + (fq * 4 + (fr >> 2)) * 64 + (lane & 3) * 8, kw = lds0 + L_PREV + hw * 2048 + l16;
          asm("" : "+v"(wa)); asm("" : "+v"(kw));
#pragma unroll
          for (int m = 0; m < 4; ++m) rq[m] = lds_ld<float>(pr1 + m * 64);
#pragma unroll
          for (int m = 0; m < 4; ++m) rs4[m] = lds_ld<f32x4>(pr4 + 64 + m * 64);
          const bf16x8 w0 = tr2(wa, wa + 1024);
          const bf16x8 w1 = tr2(wa + 32, wa + 32 + 1024);
#pragma unroll
          for (int i = 0; i < 8; ++i) { SGB(0x008, 1); SGB(0x100, 2); SGB(0x002, 2); }
          SCHED;
          mm6(3, 2, 4); mm6(2, 2, 4);
          lds_st<u32x4>(kw + 1024, pack(a[3][2], a[3][3]));
          lds_st<u32x4>(kw, pack(a[2][2], a[2][3]));
#pragma unroll
          for (int i = 0; i < 2; ++i) { SGB(0x008, 1); SGB(0x002, 2); SGB(0x008, 1); SGB(0x002, 2); SGB(0x200, 1); }
          SCHED;
          mm6(1, 2, 4); mm6(0, 2, 4);
          proj(0, 2, w0, w1); proj(1, 2, w0, w1);
#pragma unroll
          for (int i = 0; i < 4; ++i) { SGB(0x008, 1); SGB(0x002, 2); }
          SCHED;
          WAITL0;
          TRQ(17); BAR;
          SCHED;
          unsigned pw = lds0 + SC_PHIK + (h * 128 + wm * 64 + fr) * 160 + fq * 8, vw = lds0 + SC_VF + h * 8192 + wm * 4096 + l16;
          asm("" : "+v"(pw)); asm("" : "+v"(vw));
#pragma unroll
          for (int m = 0; m < 4; ++m) mm6(m, 4, 8);
#pragma unroll
          for (int m = 0; m < 4; ++m) rq[m] *= L2E;
#pragma unroll
          for (int i = 0; i < 4; ++i)
            lds_st<u32x4>(vw + i * 1024, pack(a[i & 2][4 + (i & 1)] * rs4[i & 2], a[(i & 2) + 1][4 + (i & 1)] * rs4[(i & 2) + 1]));
#pragma unroll
          for (int i = 0; i < 16; ++i) { SGB(0x008, 1); SGB(0x002, 3); }
          SCHED;
          phik2(r, rq, pw);
          KV_ADDR;
          WAITL0;
          TRR(60);
          TRC(19); BAR;
          kvpart(va, pa, kvo, lane, wave);
          CLOBBER;
          TRC(20); BAR;
          u32x4 qp[4], qn[4];
          float sqp[4], sqn[4];
#pragma unroll
          for (int m = 0; m < 4; ++m) proj(m, 0, w0, w1);
          SCHED;
          float ep[4][8], en[4][8];
          hedgehog<4>(r, rq, ep, en);
          const unsigned zo = (lane >> 3) * KVP_G + (lane & 7) * 16, zs = kvj + KVP_Z + h * 128;
          u32x4 zv[2] = {ld16p<PA1>(rs, zo, zs), ld16p<PA1>(rs, zo, zs + 8 * KVP_G)};
          SCHED;
#pragma unroll
          for (int m = 0; m < 4; ++m) {
            qp[m] = pack8(ep[m]); qn[m] = pack8(en[m]);
            sqp[m] = MFMA(ones, as_frag(qp[m]), zero)[0];
            sqn[m] = MFMA(ones, as_frag(qn[m]), zero)[0];
          }
          SCHED;
#pragma unroll
          for (int m = 0; m < 4; ++m) { sqp[m] = __builtin_amdgcn_rcpf(sqp[m]); sqn[m] = __builtin_amdgcn_rcpf(sqn[m]); }
#pragma unroll
          for (int m = 0; m < 4; ++m) { OPAQUE_V(qp[m]); OPAQUE_V(qn[m]); OPAQUE_V(sqp[m]); OPAQUE_V(sqn[m]); }
          unsigned ko = (lane >> 5) * KVP_G + (lane & 31) * 16, ks = kvj + (unsigned)((j ^ RX) - j) * KVP_J + KVP_SUM + h * (8 * KVP_G);
          bf16x8 sel = rowsum_sel(lane);
          OPAQUE_V(ko); OPAQUE_S(ks); OPAQUE_V(sel);
          TRC(21); TRV(50, 0);
          for (unsigned spins = 0; !dead && __any(umax4v(umax4v(0u, zv[0]), zv[1]) == SENT); ++spins) {
            if (spins > SPIN_CAP) { dead = true; break; }
            CLOBBER;
            TRV(50, spins + 1);
            zv[0] = ld16p<PA1>(rs, zo, zs); zv[1] = ld16p<PA1>(rs, zo, zs + 8 * KVP_G);
          }
          TRC(22);
          {
            const f32x4 z = rowsum_fin(MFMA(sel, as_frag(zv[1]), MFMA(sel, as_frag(zv[0]), zero)));
            if ((lane & 40) == 0) lds_st<u32x2>(lds0 + L_ZF + hw * 128 + fr * 16 + fq * 8, u32x2{pk2(z[0], z[1]), pk2(z[2], z[3])});
            WAITL0;
            const bf16x8 zf0 = lds_ld<bf16x8>(lds0 + L_ZF + hw * 128 + fq * 16), zf1 = lds_ld<bf16x8>(lds0 + L_ZF + hw * 128 + 64 + fq * 16);
            float dp[4], dn[4];
#pragma unroll
            for (int m = 0; m < 4; ++m) { dp[m] = MFMA(zf0, as_frag(qp[m]), zero)[0]; dn[m] = MFMA(zf1, as_frag(qn[m]), zero)[0]; }
            SCHED;
#pragma unroll
            for (int m = 0; m < 4; ++m) {
              const float inv = __builtin_amdgcn_rcpf(__builtin_fmaf(dp[m], sqp[m], __builtin_fmaf(dn[m], sqn[m], 1e-6f)));
              sqp[m] *= inv; sqn[m] *= inv;
              OPAQUE_V(sqp[m]); OPAQUE_V(sqn[m]);
            }
          }
          TRV(51, 0);
          TRC(43); BAR;
          if (H2D) __builtin_amdgcn_s_sleep(H2D);
          u32x4 kv[4];
#pragma unroll
          for (int i = 0; i < 4; ++i) kv[i] = ld16p<PA2>(rs, ko, ks + i * (2 * KVP_G));
          SCHED;
#pragma unroll
          for (int m = 0; m < 4; ++m) { qp[m] = pack8(ep[m], sqp[m]); qn[m] = pack8(en[m], sqn[m]); }
#pragma unroll
          for (int m = 0; m < 4; ++m) { OPAQUE_V(qp[m]); OPAQUE_V(qn[m]); }
          SCHED;
          for (unsigned spins = 0; !dead && __any(max(umax4v(umax4v(0u, kv[0]), kv[1]), umax4v(umax4v(0u, kv[2]), kv[3])) == SENT); ++spins) {
            if (spins > SPIN_CAP) { dead = true; break; }
            CLOBBER;
            TRV(51, spins + 1);
#pragma unroll
            for (int i = 0; i < 4; ++i) kv[i] = ld16p<PA2>(rs, ko, ks + i * (2 * KVP_G));
          }
          TRC(23);
          f32x4 o[4][2];
#pragma unroll
          for (int m = 0; m < 4; ++m)
#pragma unroll
            for (int i = 0; i < 2; ++i) o[m][i] = MFMA(as_frag(kv[2 * i + 1]), as_frag(qn[m]), MFMA(as_frag(kv[2 * i]), as_frag(qp[m]), zero));
#pragma unroll
          for (int m = 0; m < 4; ++m) st16_sc1(rs, l16, otile + m * 2048, nosent(pack(o[m][0], o[m][1])));
        }
      });
#pragma unroll
      for (int i = 0; i < 8; ++i) asm volatile("" : "=v"(acc[i >> 2][i & 3]));
    } else {
      __builtin_amdgcn_s_setprio(3);
      if (l0) {
        WAITV(3);
        WAITL0;
        TRC(1); BAR;
      }
      LOCALS;
      u32x4 xr[2][2];
      const unsigned lin = lane * 16, vlin = lin + hw * 1024;
      const int jr = j & ~3;
      u32x4 sent4 = {SENT, SENT, SENT, SENT};
      OPAQUE_V(sent4);
      const unsigned cb0 = ob_off(g, j, l), cb2 = xb_off(g, l) + j * TS;
      const unsigned cb1 = WS_KVP + (unsigned)j * KVP_J + (unsigned)g * KVP_G + (unsigned)(l >> 1) * KVP_L;
      unsigned ra0;
      int tr = 0;
      auto ldres = [&](int t) {
        if (tr == t) {
#pragma unroll
          for (int i = 0; i < 4; ++i) xr[i >> 1][i & 1] = lds_ld<u32x4>((ra0 ^ ((i & 1) * 64)) + (t % 3) * XS + ((i >> 1) ^ 1) * 2048);
        }
      };
      auto aslot = [&](int t) { return lds0 + L_R2 + (t % 3) * XS + hw * 1024; };
      auto wslot = [&](int t) { return lds0 + L_R1 + (t & 3) * 24576 + hw * 1024; };
      const bool l0f = l == 0;
      const rsrc_t rA = mk_rsrc(pA, zA);
      auto kseq = [&](int t) { return (kb + t - (((KX || t < NJ) && t <= dj) ? 1 : 0)) & 15; };
      auto offA = [&](int t) { return bA + (unsigned)kseq(t) * kA; };
      auto offW = [&](int t) { return bW + (unsigned)kseq(t) * kW; };
      auto ldA = [&](int t) { dma4<4096>(rA, vA, offA(t), pcA, aslot(t)); };
      auto ldWh = [&](int t, unsigned o, int hf) { dma3<8192>(mk_rsrc(pW, zW), vW, o + (hf ? hW : 0u), pcW, wslot(t) + hf * 4096); };
      const bool slowj = l0f && SLOWC(0, j);
      auto packedW = [&]() {
        pW = p.ws; zW = WS_SPAN; bW = WS_WP + (unsigned)j * (16 * 24576); kW = 24576; pcW = 8192; hW = 4096; vW = vlin;
      };
      auto ldW = [&](int t) { const unsigned o = offW(t); ldWh(t, o, 0); ldWh(t, o, 1); };
      const unsigned cmask = 0xfu | (isb ? 0u : 0xf0u) | (l == 3 ? 0u : 0xf00u);
      constexpr auto spread = [](int n) { unsigned m = 0; for (int t = 0; t < 26; ++t) if ((t + 1) * n / 13 > t * n / 13) m |= 1u << t; return m; };
      constexpr unsigned sp4 = spread(4), sp8 = spread(8), sp12 = spread(12);
      const unsigned smask = (isb ? (l == 3 ? sp4 : sp8) : sp12) >> ((((g + j * 5) & 15) * 13) >> 4);
      const unsigned cbm = isb ? cb2 : cb1;
      auto clean = [&](int t) __attribute__((always_inline)) {
        if (STG) {
          if (t < 13 && ((smask >> t) & 1) && (!NORS || l0f)) {
            const unsigned cc = __builtin_popcount(smask & ((1u << t) - 1));
            const unsigned cb = cb0 + (cc >= 4 ? cbm - cb0 : 0u) + (cc >= 8 ? cb2 - cbm : 0u), pc = (cc + g + j) & 3;
            st16_sc1(rs, (lane >> 4) * 4096 + (lane & 15) * 16, cb + hw * 1024 + pc * 256, sent4);
          }
        } else if (t < 4 || (t < 12 && ((cmask >> t) & 1))) {
          const unsigned cb = t < 4 ? cb0 : (t < 8 ? cb1 : cb2), pc = (unsigned)((t + g + j) & 3);
          st16_sc1(rs, (lane >> 4) * 4096 + (lane & 15) * 16, cb + hw * 1024 + pc * 256, sent4);
        }
      };
      unsigned flo;
      unsigned ready = ~0u;
      u32x4 cv[4];
#pragma unroll
      for (int ii = 0; ii < 4; ++ii) asm volatile("" : "=v"(cv[ii]));
      auto chkrd = [&](int t) {
#pragma unroll
        for (int ii = 0; ii < 4; ++ii) cv[ii] = lds_ld<u32x4>(aslot(t) + lin + ii * 4096);
      };
      auto chkbad = [&]() { return __any(max(umax4v(umax4v(0u, cv[0]), cv[1]), umax4v(umax4v(0u, cv[2]), cv[3])) == SENT) && !l0f; };
      auto refetch = [&](int t) __attribute__((always_inline)) {
        for (unsigned spins = 0; !dead; ++spins) {
          if (spins > SPIN_CAP) { dead = true; break; }
          WAITV(0);
          TRV(t == 1 ? 56 : 57, spins + 1);
          ldA(t);
          ldA(t + 1);
          WAITV(0);
          chkrd(t);
          if (!chkbad()) break;
          __builtin_amdgcn_s_sleep(1);
        }
      };
      if (l0f) {
        u32x2 gw2;
        ld8_asm(gw2, rs, WS_EPOCH + 0u * lin);
        TRC(44); BAR;
        ldW(0); ldA(0);
        dma1(mk_rsrc(p.wphi, 32 * 2048), vlin, j * 4096, lds0 + L_WPHI + hw * 1024);
        ldW(1);
        if (A1E) ldA(1);
        WAITV(A1E ? 21 : 17);
        OPAQUE_V(gw2);
        const u64 gword = ((u64)(unsigned)UNI(gw2[1]) << 32) | (unsigned)UNI(gw2[0]);
        u64 z = gword + 0x9e3779b97f4a7c15ull;
        z = (z ^ (z >> 30)) * 0xbf58476d1ce4e5b9ull;
        z = (z ^ (z >> 27)) * 0x94d049bb133111ebull;
        z = z ^ (z >> 31);
        z = (z == gword) ? ~z : z;
        epoch = u32x2{(unsigned)z, (unsigned)(z >> 32)};
        tr = (j - kb) & 15;
        WAITV(A1E ? 11 : 7);
        TRC(1); BAR;
        if (!A1E) ldA(1);
      } else {
        ldW(1);
        if (XD) __builtin_amdgcn_s_sleep(XD);
        ldA(1);
      }
      ldA(2);
      TRC(40);
      ldW(2);
#pragma unroll
      for (int i = 0; i < 4; ++i) asm volatile("" : "=v"(xr[i >> 1][i & 1]));
      unsigned oA = offA(3), oW = 0;
      OPAQUE_S(oA);
      TRV(56, 0); TRV(57, 0);
      if (LAW == 3) ldW(3);
      ra0 = lds0 + L_R2 + (hw * 32 + (lane & 15)) * 128 + (((((lane >> 4) & 1) * 2 + (lane >> 5)) ^ ((lane >> 1) & 7)) * 16);
      OPAQUE_V(ra0);
      ldres(0);
      clean(0);
      WAITV(4 + !STG + (W2F ? 0 : 6) + (LAW == 3 ? 6 : 0));
      chkrd(1);
      if (__builtin_expect(chkbad(), 0)) refetch(1);
      const int fr = lane & 15, fq = lane >> 4;
      u32x2 fl;
      f32x4 js[4], jc[4];
      float ra[4], rb[4];
      const bool slowo = SLOWC(2 + (l & 1), j);
      const char* pO;
      unsigned zO, bO, pcO, vO, clo;
      u32x4 wo2[2];
      asm volatile("" : "=v"(wo2[0])); asm volatile("" : "=v"(wo2[1]));
      const unsigned fwo = myrep + fl_line(FL_WO + (isb ? 1 : 0), jr + (lane >> 4)) + (lane & 15) * 8;
      static_for<1, 14>([&](auto t_c) __attribute__((always_inline)) {
        constexpr int t = decltype(t_c)::value;
        constexpr auto rest = [](int s) {
          return s == 0 ? (W2F ? 0 : 6) + (LAW == 3 ? 6 : 0) + !STG
                        : (s + LAW <= 15 ? 6 : 0) + (s < 4 && !STG) + (s == NVAL - 2 || s == 5 || s == 11) + ((s == 7 || s == 8) ? 4 : 0);
        };
        constexpr int NW = t == 1 ? 63 : 4 + rest(t - 1) + ((LAW == 3 && (t > 2 || W2F)) ? rest(t - 2) : 0);
        TRC(1 + t);
        if (t >= NVAL && t <= 6 && __builtin_expect((ready & (1u << kseq(t + 2))) == 0, 0)) {
          WAITV(NW); WAITL0; BAR;
          ready = poll_need(rs, flo, epoch, ready, 1u << kseq(t + 2), dead);
          dma4<4096>(rA, vA, oA, pcA, aslot(t + 2));
        } else if (t == 7 && __builtin_expect(ready != ~0u, 0)) {
          WAITV(NW); WAITL0; BAR;
          poll_all(rs, flo, epoch, dead);
          dma4<4096>(rA, vA, oA, pcA, aslot(t + 2));
        } else {
          bar_dma4<4096, NW>(rA, vA, oA, pcA, aslot(t + 2));
        }
        if (t == 1 && lead) set_flags(rs, fl_line(FL_X + l, g), j, l0f ? fl_line(FL_CS, j) : fl_line(FL_X + l, g), l0f ? g : j, epoch, lane);
        if (t == 7 && slowj) packedW();
        if (t + LAW <= 15) oW = offW(t + LAW);
        if (t < 13) oA = offA(t + 3);
        OPAQUE_S(oA); OPAQUE_S(oW);
        clean(t);
        if (t <= NVAL && !l0f) { WAITV(rest(t - 1) + 4 + (t < 4 && !STG)); chkrd(t + 1); }
        if (t == WT + 3 && l0f && SLOWC(2, jr + hw)) {
          OPAQUE_V(wo2[0]); OPAQUE_V(wo2[1]);
#pragma unroll
          for (int i = 0; i < 2; ++i) st16_sc1(rs, lin, WS_WO + (unsigned)((jr + hw) * 131072 + g * 8192 + ((j & 3) * 2 + i) * 1024), wo2[i]);
        }
        if (t == WT + 6 && l0f && lead) set_flags(rs, fl_line(FL_WO, j), g, fl_line(FL_WO, j), g, epoch, lane);
        ldres(t);
        if (t == 7) {
#pragma unroll
          for (int i = 0; i < 4; ++i) ld16_asm(jc[i], rs, fq * 768 + hw * 192 + (fr < 12 ? fr : 0) * 16, WS_CS + (unsigned)(((isb ? 16 : 0) + j) * 16 + i * 4) * 768);
        }
        if (t == 8) {
#pragma unroll
          for (int i = 0; i < 4; ++i) ld16_asm(js[i], rs, fq * 1024 + hw * 256 + fr * 16, st_off(g, l) + i * 4096);
        }
        SCHED;
        if (t + LAW <= 15) ldWh(t + LAW, oW, 0);
        SCHED;
        bool bad = false;
        if (t <= NVAL) bad = chkbad();
        if (t == 1) {
          const unsigned fx = fl_line(FL_X + l, g), fc = fl_line(l0f ? FL_CS : FL_PK, jr) + ((j + fq) & 3) * 128;
          flo = myrep + ((l < 2 && lane >= 16) ? fc : fx) + fr * 8;
          OPAQUE_V(flo);
        }
        if (t == 9) {
          pO = slowo ? p.ws : (const char*)(isb ? p.woB : p.woA);
          zO = slowo ? WS_SPAN : 1024 * 2048; bO = slowo ? WS_WO + (unsigned)((isb ? 16 : 0) + j) * 131072 : j * 128; pcO = slowo ? 4096 : 32 * 2048;
          vO = slowo ? vlin : (hw * 8 + (lane >> 3)) * 2048 + wo_swz(lane);
          clo = myrep + ((lane & 16) ? fl_line(FL_CLC + l, (lane & 32) ? j ^ RX : j) : fl_line(FL_CLR + l, g)) + fr * 8;
          OPAQUE_S(pO); OPAQUE_S(zO); OPAQUE_S(bO); OPAQUE_S(pcO); OPAQUE_V(vO); OPAQUE_V(clo);
        }
        if (t == 10) {
#pragma unroll
          for (int i = 0; i < 4; ++i) OPAQUE_V(jc[i]);
          const f32x4 s = (jc[0] + jc[1]) + (jc[2] + jc[3]);
#pragma unroll
          for (int i = 0; i < 4; ++i) { ra[i] = s[i]; OPAQUE_V(ra[i]); }
        }
        if (t == 11) {
#pragma unroll
          for (int i = 0; i < 4; ++i) OPAQUE_V(js[i]);
          const f32x4 s = (js[0] + js[1]) + (js[2] + js[3]);
#pragma unroll
          for (int i = 0; i < 4; ++i) { rb[i] = s[i]; OPAQUE_V(rb[i]); }
        }
        if (t == 12) {
          red2_add(ra[0], ra[1]); red2_add(ra[2], ra[3]); red2_add(rb[0], rb[1]); red2_add(rb[2], rb[3]);
          if (lane < 12) lds_st<f32x4>(lds0 + L_CS + hw * 192 + lane * 16, f32x4{ra[0], ra[1], ra[2], ra[3]});
#pragma unroll
          for (int i = 0; i < 4; ++i) OPAQUE_V(rb[i]);
        }
        if (t == 13) {
          const float m0 = rb[0] * (1.0f / 1024), m1 = rb[2] * (1.0f / 1024);
          const float r0 = __builtin_amdgcn_rsqf(fmaxf(__builtin_fmaf(rb[1], 1.0f / 1024, -m0 * m0), 0.f) + 1e-5f);
          const float r1 = __builtin_amdgcn_rsqf(fmaxf(__builtin_fmaf(rb[3], 1.0f / 1024, -m1 * m1), 0.f) + 1e-5f);
          if (lane < 16) {
            lds_st<f32x2>(lds0 + L_MEAN + 64 + hw * 128 + fr * 8, f32x2{-m0, -m1});
            lds_st<f32x2>(lds0 + L_RSTD + 64 + hw * 128 + fr * 8, f32x2{r0, r1});
          }
        }
        SCHED;
        if (t + LAW <= 15) ldWh(t + LAW, oW, 1);
        if (t == NVAL - 1 || t == 6) {
          WAITV(10);
          OPAQUE_V(fl);
          if (t == 6) ready = __all(is_set(fl, epoch)) ? ~0u : 0u;
          else {
            ready = l0f ? ~0u : (unsigned)__ballot(is_set(fl, epoch));
            if (slowj && __all(is_set(fl, epoch))) packedW();
          }
        }
        if (t == 12) { WAITV(10); OPAQUE_V(fl); }
        if (t == 13 && slowo && __builtin_expect(!__all(is_set(fl, epoch)), 0)) poll_all(rs, fwo, epoch, dead);
        if (t == NVAL - 2 || t == 5) ld8_asm(fl, rs, flo);
        if (t == 11) ld8_asm(fl, rs, fwo);
        if (t == WT && l0f) {
#pragma unroll
          for (int i = 0; i < 2; ++i) ld16in_asm(wo2[i], mk_rsrc(p.woA, 1024 * 2048), (lane >> 3) * 2048 + wo_swz(lane), (g * 64 + (j & 3) * 16 + i * 8) * 2048 + (jr + hw) * 128);
        }
        if constexpr (t <= NVAL) { if (__builtin_expect(bad, 0)) refetch(t + 1); }
      });
      auto stO = [&](int i) { dma4<4096>(mk_rsrc(pO, zO), vO, bO + wo_src(i, j) * (4 * pcO), pcO, lds0 + wo_slot(i) + hw * 1024); };
      auto clcheck = [&]() {
        OPAQUE_V(fl);
        TRV(58, 0);
        if (__builtin_expect(!__all(is_set(fl, epoch)), 0)) { TRV(58, 1); poll_all(rs, clo, epoch, dead); }
      };
      auto accinit = [&]() {
#pragma unroll
        for (int m = 0; m < 2; ++m)
#pragma unroll
          for (int pp = 0; pp < 2; ++pp) {
            auto r0_ = __builtin_amdgcn_permlane16_swap(xr[m][pp][0], xr[m][pp][2], false, false);
            auto r1_ = __builtin_amdgcn_permlane16_swap(xr[m][pp][1], xr[m][pp][3], false, false);
            acc[m][2 * pp] = f32x4{lo16(r0_[0]), hi16(r0_[0]), lo16(r1_[0]), hi16(r1_[0])};
            acc[m][2 * pp + 1] = f32x4{lo16(r0_[1]), hi16(r0_[1]), lo16(r1_[1]), hi16(r1_[1])};
          }
#pragma unroll
        for (int i = 0; i < 8; ++i) OPAQUE_V(acc[i >> 2][i & 3]);
      };
      WAITV(10);
      WAITL0;
      TRC(15); BAR;
      if (lead) set_flags(rs, fl_line(FL_CLR + l, g), j, fl_line(FL_CLC + l, j), g, epoch, lane);
      if (isb) {
        stO(0); stO(2); stO(3);
        WAITV(12);
        TRC(16); BAR;
        stO(1); stO(4); stO(5);
        __builtin_amdgcn_s_setprio(0);
        TRC(17); BAR;
        __builtin_amdgcn_s_sleep(BD);
        stO(7); stO(6);
        ld8_asm(fl, rs, clo);
        accinit();
        WAITV(0);
        clcheck();
      } else {
        u32x4 vb[8];
#pragma unroll
        for (int i = 0; i < 8; ++i) asm volatile("" : "=v"(vb[i]));
        stO(0); stO(2); stO(3);
        ldres(14);
        WAITV(12);
        WAITL0;
        TRC(16); BAR;
        stO(1);
        ldres(15);
        if (l0f) {
          const bool wj = hw != 0;
          const char* bp = (const char*)(wj ? p.wqkvB : p.woB);
          unsigned rs_ = wj ? 6144 : 2048, sz = rs_ * 1024, ss = rs_ * 8, s2 = wj ? 16 * 6144 : 128;
          unsigned sb = g * 64 * rs_ + (wj ? (hw - 1) * 2048 + JOBC(j, hw - 1) * 128 : (j & 3) * (16 * 2048) + jr * 128);
          OPAQUE_S(bp); OPAQUE_S(rs_); OPAQUE_S(sz); OPAQUE_S(sb); OPAQUE_S(ss); OPAQUE_S(s2);
          const rsrc_t rb = mk_rsrc(bp, sz);
#pragma unroll
          for (int i = 0; i < 8; ++i) ld16in_asm(vb[i], rb, (lane >> 3) * rs_ + (lane & 7) * 16, sb + (i & 1) * ss + (i >> 1) * s2);
        }
        __builtin_amdgcn_s_setprio(0);
        KV_ADDR;
        bf16x8 sel = rowsum_sel(lane);
        unsigned h1 = (lane >> 3) * KVP_G + (lane & 7) * 16;
        unsigned pw = lds0 + SC_PHIK + (h * 128 + wm * 64 + 32 + fr) * 160 + fq * 8, kw = lds0 + L_PREV + hw * 2048 + lin;
        const unsigned wa = lds0 + L_WPHI + h * 2048 + (fq * 4 + (fr >> 2)) * 64 + (lane & 3) * 8;
        bf16x8 w0 = tr2(wa, wa + 1024), w1 = tr2(wa + 32, wa + 32 + 1024);
        float cq[4];
#pragma unroll
        for (int m = 0; m < 2; ++m) { cq[m] = lds_ld<float>(lds0 + L_RSTD + (16 + wm * 64 + 32 + m * 16 + fr) * 4) * 1.4426950408889634f; OPAQUE_V(cq[m]); }
        OPAQUE_V(sel); OPAQUE_V(h1); OPAQUE_V(pw); OPAQUE_V(kw); OPAQUE_V(w0); OPAQUE_V(w1);
        TRC(17); BAR;
        ld8_asm(fl, rs, clo);
        {
          const f32x4 zero = {0.f, 0.f, 0.f, 0.f};
          f32x4 r[4][2];
#pragma unroll
          for (int m = 0; m < 2; ++m) {
            const bf16x8 f = lds_ld<bf16x8>(kw + m * 1024);
            r[m][0] = MFMA(w0, f, zero); r[m][1] = MFMA(w1, f, zero);
          }
          phik2(r, cq, pw);
        }
        WAITV(0);
#pragma unroll
        for (int i = 0; i < 8; ++i) OPAQUE_V(vb[i]);
        clcheck();
        WAITL0;
        TRC(19); BAR;
        kvpart(va, pa, kvj + (unsigned)g * KVP_G, lane, wave);
        CLOBBER;
        TRC(20); BAR;
        auto job = [&]() __attribute__((always_inline)) {
        if (l0f) {
          if (hw) {
            job_colsum(rs, vb, 1, g, JOBC(j, hw - 1), hw - 1, lane);
            if (SLOWC(1, JOBC(j, hw - 1))) {
#pragma unroll
              for (int i = 0; i < 8; ++i) job_pack(rs, vb[i], i, 1, g, JOBC(j, hw - 1), hw - 1, lane);
            }
          } else {
#pragma unroll
            for (int i = 0; i < 8; ++i)
              if (SLOWC(3, jr + (i >> 1))) st16_sc1(rs, (lane >> 3) * 128 + wo_swz(lane), WS_WO + (unsigned)((16 + jr + (i >> 1)) * 131072 + g * 8192 + ((j & 3) * 2 + (i & 1)) * 1024), vb[i]);
          }
        }
        };
        if (JOBP) job();
        if (H1D > 0) stO(4);
        if (H1D > 1) stO(5);
        const f32x4 zero = {0.f, 0.f, 0.f, 0.f};
        const unsigned s1 = kvj + (unsigned)((j ^ RX) - j) * KVP_J + g * 512 + hw * 128;
        if (H1S) __builtin_amdgcn_s_sleep(H1S);
        u32x4 v[2] = {ld16p<PA1>(rs, h1, s1), ld16p<PA1>(rs, h1, s1 + 8 * KVP_G)};
        TRC(21); TRV(50, 0);
        for (unsigned spins = 0; !dead && __any(umax4v(umax4v(0u, v[0]), v[1]) == SENT); ++spins) {
          if (spins > SPIN_CAP) { dead = true; break; }
          CLOBBER;
          TRV(50, spins + 1);
          if (H1P) __builtin_amdgcn_s_sleep(H1P);
          v[0] = ld16p<PA1>(rs, h1, s1); v[1] = ld16p<PA1>(rs, h1, s1 + 8 * KVP_G);
        }
        TRC(22);
        const f32x4 c = rowsum_fin(MFMA(sel, as_frag(v[1]), MFMA(sel, as_frag(v[0]), zero)));
        if ((lane & 40) == 0)
          st8_sc1(rs, fr * 16 + fq * 8, kvj + (unsigned)g * KVP_G + KVP_SUM + hw * 128, u32x2{min(pk2(c[0], c[1]), SENT - 1), min(pk2(c[2], c[3]), SENT - 1)});
        CLOBBER;
        TRC(43); BAR;
        if (JOBP && l0f && lead) set_flags(rs, fl_line(FL_PK, j), g, fl_line(FL_WO + 1, j), g, epoch, lane);
        accinit();
        if (H2D) __builtin_amdgcn_s_sleep(H2D);
        if (H1D < 1) stO(4);
        if (H1D < 2) stO(5);
        stO(6); stO(7);
        if (!JOBP) job();
        WAITV(0);
      }
    }
    {
      LOCALS;
      const int fr = lane & 15, fq = lane >> 4;
      const int kh = half;
      unsigned v0 = lane * 16 + hw * 4096 + kh * 2048, v1 = lane * 16 + hw * 4096 + (kh ^ 1) * 2048;
      const int r0 = (JROT(j) >> 1) + NJ / 2;
      const unsigned ob0 = ob_off(g, kh, l);
      const unsigned pko = myrep + fl_line(FL_PK, JOBC(j, fq - 1)) + fr * 8;
      u32x4 R[8][4];
      u32x2 pkf;
      auto issue_ = [&](int i) {
        const unsigned so = ob0 + (unsigned)((r0 + i) & 7) * (2 * TS);
        R[i][0] = ld16_sc1(rs, v0, so); R[i][1] = ld16_sc1(rs, v1, so);
        R[i][2] = ld16_sc1(rs, v0 + 1024, so); R[i][3] = ld16_sc1(rs, v1 + 1024, so);
      };
      auto issue = [&](int i) { issue_(i); SCHED; };
      constexpr int LA = LOOKAHEAD;
      TRV(52, 0); TRV(53, 0); TRV(54, 0);
      TRR(61);
      __builtin_amdgcn_s_setprio(3);
      unsigned so0 = ob0 + (unsigned)(r0 & 7) * (2 * TS);
      OPAQUE_V(v0); OPAQUE_V(v1); OPAQUE_S(so0);
      TRC(25); BAR;
      if (DLY && !isb) { for (int d = (g >> 1) * DLY; d > 0; --d) __builtin_amdgcn_s_sleep(1); }
      SCHED;
      R[0][0] = ld16p<PA3>(rs, v0, so0); R[0][1] = ld16p<PA3>(rs, v1, so0);
      R[0][2] = ld16p<PA3>(rs, v0 + 1024, so0); R[0][3] = ld16p<PA3>(rs, v1 + 1024, so0);
      SCHED;
      __builtin_amdgcn_s_setprio(2);
      if (!JOBP && l == 0 && lead) set_flags(rs, fl_line(FL_PK, j), g, fl_line(FL_WO + 1, j), g, epoch, lane);

      auto vmax = [&](int i) { return umax4v(umax4v(umax4v(umax4v(0u, R[i][0]), R[i][1]), R[i][2]), R[i][3]); };
      auto bad = [&](int i) { return __any(umax4v(umax4v(umax4v(umax4v(0u, R[i][0]), R[i][1]), R[i][2]), R[i][3]) == SENT); };
      auto poll = [&](int i, int nfl) {
        for (unsigned spins = 0; !dead; ++spins) {
          if (spins > SPIN_CAP) { dead = true; break; }
          __builtin_amdgcn_s_sleep(2);
          CLOBBER;
          TRV(52 + (i > 2 ? 2 : i), spins + 1);
          issue(i);
#pragma unroll
          for (int k = i + 1; k < i + nfl && k < 8; ++k)
            if (bad(k)) issue(k);
          WAITV(0);
#pragma unroll
          for (int k = i + 1; k < i + nfl && k < 8; ++k)
#pragma unroll
            for (int f = 0; f < 4; ++f) OPAQUE_V(R[k][f]);
          if (!bad(i)) break;
        }
      };
      unsigned bb[4];
      {
        const int kr = fq * 4 + (fr >> 2);
        const int gs = (kr >> 1) & 3;
#pragma unroll
        for (int c = 0; c < 4; ++c) bb[c] = lds0 + kh * 8192 + kr * 128 + ((c ^ gs) * 32) + (lane & 3) * 8;
      }
      bf16x8 bf[2][4];
      auto rdB = [&](int i, int hh) {
        if (hh == 0) {
#pragma unroll
          for (int c = 0; c < 4; ++c) { bb[c] += wo_slot(i) - (i ? wo_slot(i - 1) : 0u); asm("" : "+v"(bb[c])); }
        }
#pragma unroll
        for (int c = 0; c < 4; ++c) bf[hh][c] = tr2(bb[c] + hh * 4096, bb[c] + hh * 4096 + 2048);
      };
      auto mms = [&](int i, int hh) {
#pragma unroll
        for (int n = 0; n < 4; ++n)
#pragma unroll
          for (int m = 0; m < 2; ++m) acc[m][n] = MFMA(bf[hh][n], as_frag(R[i][hh * 2 + m]), acc[m][n]);
      };
      if (!half) {
#pragma unroll
        for (int i = 0; i < 8; ++i) acc[i >> 2][i & 3] = f32x4{0.f, 0.f, 0.f, 0.f};
      }
      const char* obase = (l == 3) ? (const char*)p.out : (const char*)p.ws;
      unsigned osize = (l == 3) ? 2048 * 2048 : WS_SPAN;
      const bool slown = SLOWC((l & 1) ^ 1, j);
      pW = slown ? p.ws : (const char*)(isb ? p.wqkvA : p.wqkvB);
      bW = slown ? WS_WP + (unsigned)((isb ? 0 : 16) + j) * (16 * 24576) : j * 128;
      kW = slown ? 24576 : 64 * 6144; zW = slown ? WS_SPAN : 1024 * 6144; pcW = slown ? 8192 : 2048; hW = slown ? 4096 : 32 * 6144;
      vA = hw * 1024 + lane * 16;
      vW = slown ? vA : (hw * 8 + (lane >> 3)) * 6144 + (((lane & 7) ^ ((((lane >> 4) & 1) | ((hw & 1) << 1)) << 1)) * 16);
      pA = p.ws; zA = WS_SPAN; bA = xb_off(g, l); kA = TS; pcA = 4096; kb = JROT(j) + KX; dj = (j - kb) & 15;
      OPAQUE_S(pW); OPAQUE_S(bW); OPAQUE_S(kW); OPAQUE_S(zW); OPAQUE_S(pcW); OPAQUE_S(hW); OPAQUE_V(vA); OPAQUE_V(vW); OPAQUE_S(bA);
      unsigned sq = bW + j * kW + kh * hW, dst = lds0 + L_R1 + kh * 4096 + hw * 1024;
      OPAQUE_S(obase); OPAQUE_S(osize); OPAQUE_S(sq); OPAQUE_S(dst);
      const rsrc_t ro = mk_rsrc(obase, osize);
      unsigned so[2];
      unsigned xw = lds0 + SC_XCH + wave * 4096 + lane * 16;
      unsigned xa = lds0 + SC_XCH + (wave ^ 4) * 4096 + lane * 16;
      unsigned sb0 = (l == 3) ? g * (128 * 2048) + j * 128 : xb_off(g, l) + j * TS;
      {
        const int row = (hw * 2 + kh) * 16 + fr, ch = (fq & 1) * 2 + (fq >> 1);
        const unsigned sw = (l == 3) ? 0u : (unsigned)((row >> 1) & 7), rb = (unsigned)row << ((l == 3) ? 11 : 7);
#pragma unroll
        for (int pp = 0; pp < 2; ++pp) { so[pp] = rb + (((ch + 4 * pp) ^ sw) * 16); OPAQUE_V(so[pp]); }
        OPAQUE_V(xa); OPAQUE_V(xw); OPAQUE_S(sb0);
      }
      SCHED;
#pragma unroll
      for (int i = 1; i < LA; ++i) issue(i);
      rdB(0, 0);
      if (PRE) {
        f32x4 dm = {0.f, 0.f, 0.f, 0.f};
#pragma unroll 1
        for (int d = 0; d < PRE; ++d) {
#pragma unroll
          for (int c = 0; c < 4; ++c) dm = MFMA(bf[0][c], bf[0][(c + 1) & 3], dm);
        }
        asm volatile("" ::"v"(dm));
        SCHED;
      }
      unsigned mx = vmax(0);
#pragma unroll
      for (int i = 0; i < 8; ++i) {
        SCHED;
        if (__builtin_expect(__any(mx == SENT), 0)) poll(i, LA);
        if (i == 0) TRC(27); else TRQ(27 + i);
        if (i == W1S - 2) { pkf = ld8_sc1(rs, pko, 0); SCHED; }
        if (i == W1S) {
          OPAQUE_V(pkf);
          if (l == 0 && lead && slown && !__all(is_set(pkf, epoch))) poll_all(rs, pko, epoch, dead);
          TRC(35); BAR;
        }
        if (i == 7 && l < 3) dma3<8192>(mk_rsrc(pW, zW), vW, sq, pcW, dst);
        if (i < 4 && (i < PM ? !isb : l == 0)) __builtin_amdgcn_s_sleep(PACE);
        if (i == 2) __builtin_amdgcn_s_setprio(1);
        if (i == 5) __builtin_amdgcn_s_setprio(0);
        SCHED;
        rdB(i, 1);
        if (i == 7) break;
        if (i + LA < 8) issue_(i + LA);
        mms(i, 0);
#pragma unroll
        for (int k = 0; k < 4; ++k) { SGB(0x008, 1); SGB(0x100, 1); SGB(0x020, 1); SGB(0x008, 1); SGB(0x100, 1); }
        SCHED;
        rdB(i + 1, 0); mx = vmax(i + 1);
        mms(i, 1);
#pragma unroll
        for (int k = 0; k < 4; ++k) { SGB(0x008, 1); SGB(0x100, 2); }
#pragma unroll
        for (int k = 4; k < 8; ++k) { SGB(0x008, 1); SGB(0x002, 8 / (8 - 4)); }
      }
#pragma unroll
      for (int n = 0; n < 4; ++n) acc[1][n] = MFMA(bf[0][n], as_frag(R[7][1]), acc[1][n]);
#pragma unroll
      for (int n = 0; n < 4; ++n) acc[0][n] = MFMA(bf[0][n], as_frag(R[7][0]), acc[0][n]);
#pragma unroll
      for (int k = 0; k < 8; ++k) { SGB(0x008, 1); SGB(0x100, 1); }
      SCHED;
#pragma unroll
      for (int n = 0; n < 4; ++n) acc[1][n] = MFMA(bf[1][n], as_frag(R[7][3]), acc[1][n]);
#pragma unroll
      for (int n = 0; n < 4; ++n) acc[0][n] = MFMA(bf[1][n], as_frag(R[7][2]), acc[0][n]);
#pragma unroll
      for (int n = 0; n < 4; ++n) lds_st<f32x4>(xw + n * 1024, acc[1][n]);
      SGB(0x008, 4);
#pragma unroll
      for (int k = 0; k < 4; ++k) { SGB(0x008, 1); SGB(0x200, 1); }
      SCHED;
      TRC(36);
      WAITL0;
      TRC(37); BAR;
      SCHED;
      f32x4 pa[4];
#pragma unroll
      for (int n = 0; n < 4; ++n) pa[n] = lds_ld<f32x4>(xa + n * 1024);
      SCHED;
      u32x4 o[2];
#pragma unroll
      for (int pp = 0; pp < 2; ++pp) {
        const f32x4 a = acc[0][2 * pp] + pa[2 * pp], b = acc[0][2 * pp + 1] + pa[2 * pp + 1];
        auto r0_ = __builtin_amdgcn_permlane16_swap(pk2(a[0], a[1]), pk2(b[0], b[1]), false, false);
        auto r1_ = __builtin_amdgcn_permlane16_swap(pk2(a[2], a[3]), pk2(b[2], b[3]), false, false);
        o[pp] = nosent(u32x4{r0_[0], r1_[0], r0_[1], r1_[1]});
        st16_sc1(ro, so[pp], sb0, o[pp]);
      }
      if (l < 3) {
        lds_st<u32x4>(lds0 + L_R2 + so[0], o[0]);
        lds_st<u32x4>(lds0 + L_R2 + so[1], o[1]);
        const f32x4 zero = {0.f, 0.f, 0.f, 0.f};
        const bf16x8 ones = {0x3f80, 0x3f80, 0x3f80, 0x3f80, 0x3f80, 0x3f80, 0x3f80, 0x3f80};
        const f32x4 a = MFMA(ones, as_frag(o[1]), MFMA(ones, as_frag(o[0]), zero));
        const f32x4 b = MFMA(as_frag(o[1]), as_frag(o[1]), MFMA(as_frag(o[0]), as_frag(o[0]), zero));
        const float q = (fr & 2) ? ((fr & 1) ? b[3] : b[2]) : ((fr & 1) ? b[1] : b[0]);
        if (fq == (fr >> 2)) st8_sc1(rs, ((hw * 2 + kh) * 16 + fr) * 8, st_off(g, l + 1) + j * 1024, u32x2{__float_as_uint(a[0]), __float_as_uint(q)});
      } else if (lead && blockIdx.x == 0 && lane == 0) {
        st8_sc1(rs, WS_EPOCH, 0, epoch);
      }
    }
    TRC(38); TRR(63);
  }
  STAMP_DUMP;
  asm volatile(".Lpf_end:");
}

extern "C" void kernel_launch(void* const* d_in, const int* in_sizes, int n_in, void* d_out, int out_size,
                              void* d_ws, size_t ws_size, hipStream_t stream) {
  Params p;
  p.x = (const u16*)d_in[0];
  p.wqkvA = (const u16*)d_in[1];
  p.wphi = (const u16*)d_in[2];
  p.woA = (const u16*)d_in[3];
  p.wqkvB = (const u16*)d_in[5];
  p.woB = (const u16*)d_in[6];
  p.out = (u16*)d_out;
  p.ws = (char*)d_ws;
  (void)hipFuncSetAttribute((const void*)mega_kernel, hipFuncAttributeMaxDynamicSharedMemorySize, L_TOTAL);
  mega_kernel<<<256, 512, L_TOTAL, stream>>>(p);
}
```

```cpp
#include <hip/hip_runtime.h>
#include <stdint.h>
#include <type_traits>
#define LA_ 2
#ifndef NV_
#define NV_ 4
#endif
constexpr int LOOKAHEAD = LA_;
constexpr int NVAL = NV_;
#define XD 3
#define WT 1
#define LAW 2
#define POL0 0
#define PKALL 0u
#define PA1 16
#define PA2 16
#define PA3 16
#define H1S 6
#define H1P 0
#define SPR 2
#define A1E 0
#define KG 32768
#define TS 17664
#define KOD 0
#define STG 1
#define JOBP 0
#define NORS 0
#define DLY 0
#define PRE 0
#define PACE 4
#define NJ 8
#define KX 8
#define PM 2
#define AF 0
#define OD 0
#define PS 2
#define ROT 1
#define BD 8
#define W1S 6
#define W2F 0
#define RX 8
#define H1D 2
#define H2D 5

typedef unsigned short u16;
typedef unsigned long long u64;
typedef __attribute__((ext_vector_type(8))) short bf16x8;
typedef __attribute__((ext_vector_type(4))) short s16x4;
typedef __attribute__((ext_vector_type(4))) float f32x4;
typedef __attribute__((ext_vector_type(2))) float f32x2;
typedef __attribute__((ext_vector_type(4))) unsigned u32x4;
typedef __attribute__((ext_vector_type(2))) unsigned u32x2;
typedef __attribute__((ext_vector_type(2))) __bf16 bf16x2_t;
#define LAS __attribute__((address_space(3)))
typedef LAS s16x4* lds_s16x4_ptr;

constexpr unsigned NREP = 8, REP_STRIDE = 17 * 2048;
enum { FL_X = 0, FL_CS = 4, FL_CLR = 5, FL_CLC = 9, FL_PK = 13, FL_WO = 14 };
__device__ __forceinline__ unsigned fl_line(int set, int a) { return (unsigned)(set * 16 + a) * 128; }
constexpr unsigned WS_EPOCH = 512u << 10;
constexpr unsigned WS_CS = 1u << 20;
constexpr unsigned WS_KVP = 4u << 20;
constexpr unsigned KVP_G = KG, KVP_J = 16 * KVP_G, KVP_L = KG == 32768 ? 16384 : 16 * KVP_J, KVP_Z = 8192, KVP_SUM = 8704, KVP_HALO = 12u << 10;
constexpr unsigned SENT = 0xffffffffu;
constexpr unsigned WS_X = 14u << 20, WS_O = 29u << 20, WS_ST = 2u << 20;
__device__ __forceinline__ unsigned xb_off(int g, int l) { return WS_X + (unsigned)(l * 16 + g) * (16 * TS); }
__device__ __forceinline__ unsigned ob_off(int g, int j, int l) { return WS_O + (unsigned)((l * 16 + g) * 16 + j) * TS; }
__device__ __forceinline__ unsigned st_off(int g, int l) { return WS_ST + (unsigned)(l * 16 + g) * (16u << 10); }
constexpr unsigned WS_WP = 48u << 20;
constexpr unsigned WS_WO = 60u << 20;
constexpr unsigned WS_SPAN = 64u << 20;

constexpr unsigned L_R1 = 0;
constexpr unsigned L_R2 = 98304, XS = 16384;
constexpr unsigned L_MISC = 147456;
constexpr unsigned L_MEAN = L_MISC;
constexpr unsigned L_RSTD = L_MISC + 576;
constexpr unsigned L_CS = L_MISC + 1152;
constexpr unsigned L_WPHI = L_MISC + 2688;
constexpr unsigned L_SINK = L_MISC + 6912;
constexpr unsigned L_ZF = L_MISC + 1920;
constexpr unsigned L_PREV = L_MISC + 8192;
constexpr unsigned L_TOTAL = 163840;

#define JROT(j) ((j) & ~(NJ - 1))
constexpr int NX = NJ / 2;
#define KB0(g, j) (JROT(j) + (16 / NX) * ((g) & (NX - 1)))
#define JOBC(j, w) (((j) & ~3) + (((j) + (w) + 1) & 3))

#define SLOWC(m, jc) ((slowm >> ((m) * 8 + ((jc) & 7))) & 1)

struct Params {
  const u16* x;
  const u16* wqkvA;
  const u16* wphi;
  const u16* woA;
  const u16* wqkvB;
  const u16* woB;
  u16* out;
  char* ws;
};

#define SCHED __builtin_amdgcn_sched_barrier(0)
#define BAR __builtin_amdgcn_s_barrier()
#define SGB(m, n) __builtin_amdgcn_sched_group_barrier(m, n, 0)
#define WAITV(n) asm volatile("s_waitcnt vmcnt(%0)" ::"n"(n) : "memory")
#define WAITL0 asm volatile("s_waitcnt lgkmcnt(0)" ::: "memory")

#define TRV(slot, val) do {} while (0)
#define TRQ(slot) do {} while (0)
#define TRC(slot) do {} while (0)
#define TRR(slot) do {} while (0)
#define STAMP_INIT do {} while (0)
#define STAMP_DUMP do {} while (0)

__device__ __forceinline__ unsigned pk2(float a, float b) {
  f32x2 v = {a, b};
  return __builtin_bit_cast(unsigned, __builtin_convertvector(v, bf16x2_t));
}
__device__ __forceinline__ float lo16(unsigned u) { return __uint_as_float(u << 16); }
__device__ __forceinline__ float hi16(unsigned u) { return __uint_as_float(u & 0xffff0000u); }

template <int A, int B, typename F> __device__ __forceinline__ void static_for(F f) {
  if constexpr (A < B) { f(std::integral_constant<int, A>{}); static_for<A + 1, B>(f); }
}
typedef __amdgpu_buffer_rsrc_t rsrc_t;
__device__ __forceinline__ rsrc_t mk_rsrc(const void* p, unsigned bytes) { return __builtin_amdgcn_make_buffer_rsrc((void*)p, 0, bytes, 0x00020000); }
#define UNI(x) __builtin_amdgcn_readfirstlane(x)
#define DMA_HEAD(P) "s_mov_b32 m0, %4\n\ts_mov_b32 %0, %3\n\ts_nop 2\n\tbuffer_load_dwordx4 %1, %2, %0 offen " P " lds\n\t"
#define DMA_NEXT(P) "s_add_u32 m0, m0, %6\n\ts_add_u32 %0, %0, %5\n\tbuffer_load_dwordx4 %1, %2, %0 offen " P " lds\n\t"
#define DMA_OPS : "=&s"(t) : "v"(voff), "s"(r), "s"(UNI(soff)), "s"(UNI(lds)), "s"(UNI(sstep)), "i"(LSTEP) : "memory", "scc", "m0"
template <int LSTEP, int POL = 1> __device__ __forceinline__ void dma4(rsrc_t r, unsigned voff, unsigned soff, unsigned sstep, unsigned lds) {
  unsigned t;
  if constexpr (POL == 1) asm volatile(DMA_HEAD("sc1") DMA_NEXT("sc1") DMA_NEXT("sc1") DMA_NEXT("sc1") DMA_OPS);
  else if constexpr (POL == 2) asm volatile(DMA_HEAD("nt") DMA_NEXT("nt") DMA_NEXT("nt") DMA_NEXT("nt") DMA_OPS);
  else asm volatile(DMA_HEAD("") DMA_NEXT("") DMA_NEXT("") DMA_NEXT("") DMA_OPS);
}
template <int LSTEP, int POL = 1> __device__ __forceinline__ void dma3(rsrc_t r, unsigned voff, unsigned soff, unsigned sstep, unsigned lds) {
  unsigned t;
  if constexpr (POL == 1) asm volatile(DMA_HEAD("sc1") DMA_NEXT("sc1") DMA_NEXT("sc1") DMA_OPS);
  else if constexpr (POL == 2) asm volatile(DMA_HEAD("nt") DMA_NEXT("nt") DMA_NEXT("nt") DMA_OPS);
  else asm volatile(DMA_HEAD("") DMA_NEXT("") DMA_NEXT("") DMA_OPS);
}
template <int LSTEP, int N> __device__ __forceinline__ void bar_dma4(rsrc_t r, unsigned voff, unsigned soff, unsigned sstep, unsigned lds) {
  unsigned t;
  asm volatile("s_mov_b32 m0, %4\n\ts_mov_b32 %0, %3\n\ts_nop 0\n\ts_waitcnt vmcnt(%7) lgkmcnt(0)\n\ts_barrier\n\t"
               "buffer_load_dwordx4 %1, %2, %0 offen sc1 lds\n\t" DMA_NEXT("sc1") DMA_NEXT("sc1") DMA_NEXT("sc1")
               : "=&s"(t) : "v"(voff), "s"(r), "s"(UNI(soff)), "s"(UNI(lds)), "s"(UNI(sstep)), "i"(LSTEP), "n"(N) : "memory", "scc", "m0");
}
__device__ __forceinline__ void dma1(rsrc_t r, unsigned voff, unsigned soff, unsigned lds) {
  asm volatile("s_mov_b32 m0, %3\n\ts_nop 3\n\tbuffer_load_dwordx4 %0, %1, %2 offen sc1 lds" ::"v"(voff), "s"(r), "s"(UNI(soff)), "s"(UNI(lds)) : "memory", "m0");
}
__device__ __forceinline__ void touch(rsrc_t r, unsigned voff, unsigned lds) {
  asm volatile("s_mov_b32 m0, %2\n\ts_nop 3\n\tbuffer_load_dword %0, %1, 0 offen lds" ::"v"(voff), "s"(r), "s"(UNI(lds)) : "memory", "m0");
}
__device__ __forceinline__ void ld8_asm(u32x2& d, rsrc_t r, unsigned voff) {
  asm volatile("s_nop 4\n\tbuffer_load_dwordx2 %0, %1, %2, 0 offen sc1" : "=v"(d) : "v"(voff), "s"(r) : "memory");
}

__device__ __forceinline__ void ld16in_asm(u32x4& d, rsrc_t r, unsigned voff, unsigned soff) {
  asm volatile("s_nop 4\n\tbuffer_load_dwordx4 %0, %1, %2, %3 offen" : "=v"(d) : "v"(voff), "s"(r), "s"(UNI(soff)) : "memory");
}
__device__ __forceinline__ void ld16_asm(f32x4& d, rsrc_t r, unsigned voff, unsigned soff) {
  asm volatile("s_nop 4\n\tbuffer_load_dwordx4 %0, %1, %2, %3 offen sc1" : "=v"(d) : "v"(voff), "s"(r), "s"(UNI(soff)) : "memory");
}

template <typename T> __device__ __forceinline__ T lds_ld(unsigned a) { return *(LAS T*)(a); }
template <typename T> __device__ __forceinline__ void lds_st(unsigned a, T v) { *(LAS T*)(a) = v; }
__device__ __forceinline__ bf16x8 tr2(unsigned a0, unsigned a1) {
  s16x4 v0 = __builtin_amdgcn_ds_read_tr16_b64_v4i16((lds_s16x4_ptr)(a0));
  s16x4 v1 = __builtin_amdgcn_ds_read_tr16_b64_v4i16((lds_s16x4_ptr)(a1));
  return __builtin_shufflevector(v0, v1, 0, 1, 2, 3, 4, 5, 6, 7);
}
__device__ __forceinline__ bf16x8 as_frag(u32x4 v) { return __builtin_bit_cast(bf16x8, v); }
#define MFMA(a, b, c) __builtin_amdgcn_mfma_f32_16x16x32_bf16(a, b, c, 0, 0, 0)

__device__ __forceinline__ float vmax2(float a, float b) { float inf = __builtin_inff(); asm("" : "+s"(inf)); return __builtin_amdgcn_fmed3f(a, b, inf); }
#define SWAP16(a, b) __builtin_amdgcn_permlane16_swap(__float_as_uint(a), __float_as_uint(b), false, false)
#define SWAP32(a, b) __builtin_amdgcn_permlane32_swap(__float_as_uint(a), __float_as_uint(b), false, false)
__device__ __forceinline__ void red2_max(float& a, float& b) {
  auto r = SWAP16(a, b);
  float x = vmax2(__uint_as_float(r[0]), __uint_as_float(r[1]));
  r = SWAP32(x, x);
  x = vmax2(__uint_as_float(r[0]), __uint_as_float(r[1]));
  r = SWAP16(x, x);
  a = __uint_as_float(r[0]); b = __uint_as_float(r[1]);
}
__device__ __forceinline__ void red2_add(float& a, float& b) {
  auto r = SWAP16(a, b);
  float x = __uint_as_float(r[0]) + __uint_as_float(r[1]);
  r = SWAP32(x, x);
  x = __uint_as_float(r[0]) + __uint_as_float(r[1]);
  r = SWAP16(x, x);
  a = __uint_as_float(r[0]); b = __uint_as_float(r[1]);
}
template <bool MAX, typename M> __device__ __forceinline__ void red4(float& a, float& b, float& c, float& d, M mid) {
  auto op = [](unsigned x, unsigned y) { return MAX ? vmax2(__uint_as_float(x), __uint_as_float(y)) : __uint_as_float(x) + __uint_as_float(y); };
  auto r = SWAP16(a, b), q = SWAP16(c, d);
  const float x = op(r[0], r[1]), y = op(q[0], q[1]);
  r = SWAP32(x, y);
  const float w = mid(op(r[0], r[1]));
  r = SWAP32(w, w);
  q = __builtin_amdgcn_permlane16_swap(r[1], r[1], false, false);
  r = __builtin_amdgcn_permlane16_swap(r[0], r[0], false, false);
  a = __uint_as_float(r[0]); b = __uint_as_float(r[1]); c = __uint_as_float(q[0]); d = __uint_as_float(q[1]);
}
__device__ __forceinline__ float red_max4(float x) {
  auto r = __builtin_amdgcn_permlane16_swap(__float_as_uint(x), __float_as_uint(x), false, false);
  x = vmax2(__uint_as_float(r[0]), __uint_as_float(r[1]));
  r = __builtin_amdgcn_permlane32_swap(__float_as_uint(x), __float_as_uint(x), false, false);
  return vmax2(__uint_as_float(r[0]), __uint_as_float(r[1]));
}
__device__ __forceinline__ float red_add4(float x) {
  auto r = __builtin_amdgcn_permlane16_swap(__float_as_uint(x), __float_as_uint(x), false, false);
  x = __uint_as_float(r[0]) + __uint_as_float(r[1]);
  r = __builtin_amdgcn_permlane32_swap(__float_as_uint(x), __float_as_uint(x), false, false);
  return __uint_as_float(r[0]) + __uint_as_float(r[1]);
}

__device__ __forceinline__ void st16_sc1(rsrc_t r, unsigned voff, unsigned soff, u32x4 v) {
  __builtin_amdgcn_raw_buffer_store_b128(v, r, voff, soff, 16);
  asm volatile("s_nop 1" ::"v"(v) : "memory");
}
__device__ __forceinline__ void st8_sc1(rsrc_t r, unsigned voff, unsigned soff, u32x2 v) { __builtin_amdgcn_raw_buffer_store_b64(v, r, voff, soff, 16); }
__device__ __forceinline__ u32x4 ld16_sc1(rsrc_t r, unsigned voff, unsigned soff) { return __builtin_amdgcn_raw_buffer_load_b128(r, voff, soff, 16); }
template <int AUX> __device__ __forceinline__ u32x4 ld16p(rsrc_t r, unsigned voff, unsigned soff) { return __builtin_amdgcn_raw_buffer_load_b128(r, voff, soff, AUX); }
__device__ __forceinline__ u32x2 ld8_sc1(rsrc_t r, unsigned voff, unsigned soff) { return __builtin_amdgcn_raw_buffer_load_b64(r, voff, soff, 16); }
__device__ __forceinline__ f32x4 ld4f_sc1(rsrc_t r, unsigned voff, unsigned soff) { return __builtin_bit_cast(f32x4, __builtin_amdgcn_raw_buffer_load_b128(r, voff, soff, 16)); }
#define OPAQUE_V(x) asm volatile("" : "+v"(x))
#define OPAQUE_S(x) asm volatile("" : "+s"(x))

#define CLOBBER asm volatile("" ::: "memory")
constexpr unsigned SPIN_CAP = 1u << 17;
__device__ __forceinline__ bool is_set(u32x2 v, u32x2 epoch) { return v[0] == epoch[0] && v[1] == epoch[1]; }
__device__ __forceinline__ void set_flags(rsrc_t rs, unsigned lineA, int colA, unsigned lineB, int colB, u32x2 epoch, int lane) {
  const unsigned off = (lane & 7) * REP_STRIDE + ((lane & 8) ? lineB + colB * 8 : lineA + colA * 8);
  if (lane < 16) st8_sc1(rs, off, 0, epoch);
}
__device__ __forceinline__ unsigned poll_need(rsrc_t rs, unsigned voff, u32x2 epoch, unsigned ready, unsigned need, bool& dead) {
  for (unsigned spins = 0; !dead; ++spins) {
    CLOBBER;
    ready |= (unsigned)__ballot(is_set(ld8_sc1(rs, voff, 0), epoch));
    if ((ready & need) == need) break;
    if (spins > SPIN_CAP) dead = true;
    __builtin_amdgcn_s_sleep(1);
  }
  CLOBBER;
  return dead ? ~0u : ready;
}

__device__ __forceinline__ void poll_all(rsrc_t rs, unsigned voff, u32x2 epoch, bool& dead) {
  for (unsigned spins = 0; !dead; ++spins) {
    CLOBBER;
    if (__all(is_set(ld8_sc1(rs, voff, 0), epoch))) break;
    if (spins > SPIN_CAP) dead = true;
    __builtin_amdgcn_s_sleep(1);
  }
  CLOBBER;
}

constexpr unsigned SC_VF = L_R1 + 32768;
constexpr unsigned SC_PHIK = L_R1 + 49152;
constexpr unsigned SC_XCH = L_R2 + XS;
constexpr unsigned wo_slot(int s) {
  return s < 2 ? L_R2 + (unsigned)(s + 1) * XS : (s < 6 ? L_R1 + (unsigned)(s - 2) * 16384 : (s == 6 ? L_R1 + 81920 : L_R2));
}
__device__ __forceinline__ unsigned wo_src(int s, int j) { return (unsigned)(((JROT(j) >> 1) + NJ / 2 + s) & 7); }
__device__ __forceinline__ unsigned wo_swz(int lane) { return (unsigned)(((lane & 7) ^ ((lane >> 3) & 6)) * 16); }
__device__ __forceinline__ unsigned umax3(unsigned a, unsigned b, unsigned c) { return max(max(a, b), c); }
__device__ __forceinline__ unsigned umax4v(unsigned m, const u32x4& v) { return umax3(umax3(m, v[0], v[1]), v[2], v[3]); }
__device__ __forceinline__ u32x4 nosent(u32x4 v) { return u32x4{min(v[0], SENT - 1), min(v[1], SENT - 1), min(v[2], SENT - 1), min(v[3], SENT - 1)}; }

template <typename P, typename F>
__device__ __forceinline__ void qkv_loop(int isb, int wm, int h, int lane, bool first, int& trv, P pre, F fin) {
  constexpr unsigned lds0 = 0;
  f32x4 acc[4][6];
  const int fr = lane & 15, fq = lane >> 4;
  unsigned ab[2], bb[2];
  {
    const int r = wm * 64 + fr;
#pragma unroll
    for (int ks = 0; ks < 2; ++ks) ab[ks] = lds0 + L_R2 + r * 128 + (((ks * 4 + fq) ^ ((fr >> 1) & 7)) * 16);
    const int kr = fq * 8 + (fr >> 2);
    const int gs = ((kr >> 1) & 1) | (((kr >> 3) & 1) << 1);
#pragma unroll
    for (int c = 0; c < 2; ++c) bb[c] = lds0 + L_R1 + kr * 128 + (((h * 2 + c) ^ gs) * 32) + (lane & 3) * 8;
  }
#pragma unroll
  for (int m = 0; m < 4; ++m)
#pragma unroll
    for (int n = 0; n < 6; ++n) acc[m][n] = f32x4{0.f, 0.f, 0.f, 0.f};
  bf16x8 af[2][4], bf[2][6];
  auto rds = [&](int ks, int p, int rot = 0) {
#pragma unroll
    for (int m = 0; m < 4; ++m) af[p][m] = lds_ld<bf16x8>(ab[ks] + m * 2048);
#pragma unroll
    for (int nn = 0; nn < 6; ++nn) {
      const int n = (nn + rot) % 6;
      const unsigned a = bb[n & 1] + (n >> 1) * 8192 + ks * 4096;
      bf[p][n] = tr2(a, a + 512);
    }
  };
  auto mms = [&](int p, int rot = 0, int cnt = 6) {
#pragma unroll
    for (int nn = 0; nn < cnt; ++nn)
#pragma unroll
      for (int mm = 0; mm < 4; ++mm) { const int n = (nn + rot) % 6, m = (n & 1) ? 3 - mm : mm; acc[m][n] = n < 4 ? MFMA(bf[p][n], af[p][m], acc[m][n]) : MFMA(af[p][m], bf[p][n], acc[m][n]); }
  };
  auto ilv2 = [&]() {
#pragma unroll
    for (int i = 0; i < 16; ++i) { SGB(0x008, 1); SGB(0x100, 1); }
#pragma unroll
    for (int i = 0; i < 4; ++i) { SGB(0x008, 1); SGB(0x002, 1); }
    SGB(0x008, 4);
  };
  auto ilv3 = [&]() {
#pragma unroll
    for (int i = 0; i < 16; ++i) { SGB(0x008, 1); SGB(0x100, 1); }
#pragma unroll
    for (int i = 0; i < 7; ++i) { SGB(0x008, 1); SGB(0x004, 2); }
    SGB(0x008, 1);
  };
#pragma unroll
  for (int i = 0; i < 24; ++i) OPAQUE_V(acc[i / 6][i % 6]);
  OPAQUE_V(ab[0]); OPAQUE_V(ab[1]); OPAQUE_V(bb[0]); OPAQUE_V(bb[1]);
  if (!first) { WAITV(3); WAITL0; }
  TRQ(1); BAR;
  int tslot = 2;
  rds(0, 0);
  int sa = 0, sb = 0;
  int da = XS, db = 24576;
  auto step = [&]() {
    ab[0] += da; ab[1] += da;
    bb[0] += db; bb[1] += db;
  };
  auto ring = [&]() {
    sa = (sa == 2) ? 0 : sa + 1;
    sb = (sb == 3) ? 0 : sb + 1;
    da = (sa == 2) ? -2 * (int)XS : (int)XS;
    db = (sb == 3) ? -3 * 24576 : 24576;
  };
#pragma unroll 1
  for (int t = 14; t > 0; --t) {
    SCHED;
    rds(1, 1); mms(0);
    step();
    ilv2();
    SCHED;
    __builtin_amdgcn_s_waitcnt(0x0070);
    TRQ(tslot); tslot++; BAR;
    SCHED;
    rds(0, 0);
    ring();
    mms(1); ilv3();
  }
  SCHED;
  f32x4 cs[4], nm4[4];
  float nmean[4], csv[2];
  auto fold = [&](int n) {
#pragma unroll
    for (int mm = 0; mm < 4; ++mm) {
      const int m = (n & 1) ? 3 - mm : mm;
#pragma unroll
      for (int e = 0; e < 4; ++e) {
        float t = n < 4 ? __builtin_fmaf(nmean[m], cs[n & 3][e], acc[m][n][e]) : __builtin_fmaf(nm4[m][e], csv[n & 1], acc[m][n][e]);
        asm("" : "+v"(t));
        acc[m][n][e] = t;
      }
    }
  };
  {
    unsigned pm = lds0 + L_MEAN + (16 + wm * 64 + fr) * 4, pc = lds0 + L_CS + (h * 32 + fq * 4) * 4;
    unsigned pm4 = lds0 + L_MEAN + (16 + wm * 64 + fq * 4) * 4, pcv = lds0 + L_CS + (128 + h * 32 + fr) * 4;
    asm("" : "+v"(pm)); asm("" : "+v"(pc)); asm("" : "+v"(pm4)); asm("" : "+v"(pcv));
#pragma unroll
    for (int m = 0; m < 4; ++m) nmean[m] = lds_ld<float>(pm + m * 64);
#pragma unroll
    for (int n = 0; n < 4; ++n) cs[n ^ 2] = lds_ld<f32x4>(pc + (((n ^ 2) >> 1) * 64 + (n & 1) * 16) * 4);
#pragma unroll
    for (int m = 0; m < 4; ++m) nm4[m] = lds_ld<f32x4>(pm4 + m * 64);
#pragma unroll
    for (int n = 0; n < 2; ++n) csv[n] = lds_ld<float>(pcv + n * 64);
    rds(1, 1, 4); mms(0, 2);
    step();
    fold(2); fold(3);
    SGB(0x002, 8);
#pragma unroll
    for (int i = 0; i < 8; ++i) { SGB(0x008, 1); SGB(0x100, 2); }
#pragma unroll
    for (int i = 0; i < 11; ++i) { SGB(0x008, 1); SGB(0x100, 1); SGB(0x002, 2); }
#pragma unroll
    for (int i = 0; i < 5; ++i) { SGB(0x008, 1); SGB(0x002, 3); }
    SCHED;
    __builtin_amdgcn_s_waitcnt(0xC07F);
    TRQ(16); BAR;
    SCHED;
    rds(0, 0, 2);
    mms(1, 4); fold(4); fold(5);
#pragma unroll
    for (int i = 0; i < 8; ++i) { SGB(0x008, 1); SGB(0x100, 1); SGB(0x002, 1); }
#pragma unroll
    for (int i = 0; i < 8; ++i) { SGB(0x008, 1); SGB(0x100, 1); SGB(0x002, 2); }
#pragma unroll
    for (int i = 0; i < 8; ++i) { SGB(0x008, 1); SGB(0x002, 2); }
    SCHED;
    pre(0);
    SCHED;
    rds(1, 1, 2);
    mms(0, 2, 4); fold(0); fold(1);
    pre(1);
#pragma unroll
    for (int i = 0; i < 16; ++i) { SGB(0x008, 1); SGB(0x100, 1); SGB(0x002, 2); SGB(0x004, 1); }
  }
  SCHED;
  auto mm6 = [&](int m, int n0 = 0, int n1 = 8) {
#pragma unroll
    for (int nn = n0; nn < n1; ++nn) {
      const int n = nn < 4 ? nn + 2 : nn & 1, p = (nn >> 1) != 2;
      acc[m][n] = n < 4 ? MFMA(bf[p][n], af[p][m], acc[m][n]) : MFMA(af[p][m], bf[p][n], acc[m][n]);
    }
  };
  if (!isb) {
    fin(std::false_type{}, acc, mm6);
#pragma unroll
    for (int i = 0; i < 24; ++i) asm volatile("" : "=v"(acc[i / 6][i % 6]));
#pragma unroll
    for (int i = 0; i < 6; ++i) asm volatile("" : "=v"(bf[1][i]));
#pragma unroll
    for (int i = 0; i < 8; ++i) asm volatile("" : "=v"(af[i >> 2][i & 3]));
    asm volatile("" : "=v"(bf[0][0])); asm volatile("" : "=v"(bf[0][1]));
  }
  OPAQUE_S(isb);
  if (isb) fin(std::true_type{}, acc, mm6);
}
__device__ __forceinline__ u32x4 pack(const f32x4& a, const f32x4& b) { return u32x4{pk2(a[0], a[1]), pk2(a[2], a[3]), pk2(b[0], b[1]), pk2(b[2], b[3])}; }

template <int N> __device__ __forceinline__ void hedgehog(const f32x4 (&r)[4][2], const float (&c)[4], float (&ep)[4][8], float (&en)[4][8]) {
  float mx[4], nm[4];
#pragma unroll
  for (int m = 0; m < N; ++m) {
    const f32x4 a = r[m][0], b = r[m][1];
    mx[m] = vmax2(fmaxf(fmaxf(a[3], b[0]), b[1]), fmaxf(fmaxf(fmaxf(fmaxf(a[0], a[1]), a[2]), b[2]), b[3]));
    nm[m] = vmax2(fmaxf(fmaxf(-a[3], -b[0]), -b[1]), fmaxf(fmaxf(fmaxf(fmaxf(-a[0], -a[1]), -a[2]), -b[2]), -b[3]));
  }
  SCHED;
  red4<true>(mx[0], mx[1], nm[0], nm[1], [](float x) { return x; });
  if (N == 4) red4<true>(mx[2], mx[3], nm[2], nm[3], [](float x) { return x; });
#pragma unroll
  for (int m = 0; m < N; ++m) { mx[m] *= -c[m]; nm[m] *= -c[m]; }
  SCHED;
#pragma unroll
  for (int m = 0; m < N; ++m) {
#pragma unroll
    for (int i = 0; i < 8; ++i) { ep[m][i] = __builtin_fmaf(r[m][i >> 2][i & 3], c[m], mx[m]); en[m][i] = __builtin_fmaf(r[m][i >> 2][i & 3], -c[m], nm[m]); }
    SCHED;
#pragma unroll
    for (int i = 0; i < 8; ++i) { ep[m][i] = __builtin_amdgcn_exp2f(ep[m][i]); en[m][i] = __builtin_amdgcn_exp2f(en[m][i]); }
    SCHED;
  }
}
__device__ __forceinline__ u32x4 pack8(const float (&e)[8], float f) {
  return u32x4{pk2(e[0] * f, e[1] * f), pk2(e[2] * f, e[3] * f), pk2(e[4] * f, e[5] * f), pk2(e[6] * f, e[7] * f)};
}
__device__ __forceinline__ u32x4 pack8(const float (&e)[8]) { return u32x4{pk2(e[0], e[1]), pk2(e[2], e[3]), pk2(e[4], e[5]), pk2(e[6], e[7])}; }

constexpr u64 window_mask(int t, int e) {
  u64 mk = 0;
  for (int ln = 0; ln < 64; ++ln) {
    const int delta = (ln & 15) + 16 - t * 16 - (ln >> 4) * 4 - e;
    if (delta >= 0 && delta <= 14 && !(delta & 1)) mk |= 1ull << ln;
  }
  return mk;
}
__device__ __forceinline__ void phik2(const f32x4 (&r)[4][2], const float (&c)[4], unsigned pw) {
  const f32x4 zero = {0.f, 0.f, 0.f, 0.f};
  const bf16x8 ones = {0x3f80, 0x3f80, 0x3f80, 0x3f80, 0x3f80, 0x3f80, 0x3f80, 0x3f80};
  float ep[4][8], en[4][8];
  f32x4 sp[2], sn[2];
  hedgehog<2>(r, c, ep, en);
#pragma unroll
  for (int m = 0; m < 2; ++m) { sp[m] = MFMA(ones, as_frag(pack8(ep[m])), zero); sn[m] = MFMA(ones, as_frag(pack8(en[m])), zero); }
#pragma unroll
  for (int m = 0; m < 2; ++m) { asm("" : "+v"(sp[m])); asm("" : "+v"(sn[m])); }
  SCHED;
#pragma unroll
  for (int m = 0; m < 2; ++m) {
    const float fp = __builtin_amdgcn_rcpf(sp[m][0]), fn = __builtin_amdgcn_rcpf(sn[m][0]);
#pragma unroll
    for (int i = 0; i < 8; ++i) { ep[m][i] *= fp; en[m][i] *= fn; }
    SCHED;
    const u32x4 pp = pack8(ep[m]), pn = pack8(en[m]);
    const unsigned d = pw + m * (16 * 160);
    lds_st<u32x2>(d, u32x2{pp[0], pp[1]});
    lds_st<u32x2>(d + 32, u32x2{pp[2], pp[3]});
    lds_st<u32x2>(d + 64, u32x2{pn[0], pn[1]});
    lds_st<u32x2>(d + 96, u32x2{pn[2], pn[3]});
    SCHED;
  }
}
__device__ __forceinline__ bf16x8 rowsum_sel(int lane) {
  const int fr = lane & 15;
  const unsigned one = (fr < 8) ? (0x3f80u << (16 * (fr & 1))) : 0u;
  const int e = (fr >> 1) & 3;
  return as_frag(u32x4{e == 0 ? one : 0u, e == 1 ? one : 0u, e == 2 ? one : 0u, e == 3 ? one : 0u});
}
__device__ __forceinline__ f32x4 rowsum_fin(f32x4 c) {
#pragma unroll
  for (int k = 0; k < 4; ++k) {
    const float a = c[k];
    c[k] = a + __int_as_float(__builtin_amdgcn_update_dpp(0, __float_as_int(a), 0x128  , 0xf, 0xf, false));
  }
  return c;
}
__device__ __forceinline__ void job_colsum(rsrc_t rs, const u32x4 (&w)[8], int mat, int g, int jc, int hw, int lane) {
  const bf16x8 sel = rowsum_sel(lane);
  f32x4 c = {0.f, 0.f, 0.f, 0.f};
#pragma unroll
  for (int i = 0; i < 8; ++i) c = MFMA(sel, as_frag(w[i]), c);
  const u32x4 w4 = __builtin_bit_cast(u32x4, rowsum_fin(c));
  unsigned so = WS_CS + (unsigned)(((mat * 16 + jc) * 16 + g) * 768);
  OPAQUE_S(so);
  if ((lane & 40) == 0) st16_sc1(rs, (unsigned)(hw * 64 + (lane & 7) * 8 + (lane >> 4) * 4) * 4, so, w4);
}
__device__ __forceinline__ unsigned img_off(int i, int lane) { return (lane >> 3) * 128 + (((lane & 7) ^ ((((lane >> 4) & 1) | ((i & 1) << 1)) << 1)) * 16); }
__device__ __forceinline__ void job_pack(rsrc_t rs, u32x4 w, int i, int mat, int g, int jc, int hw, int lane) {
  st16_sc1(rs, img_off(i, lane),
           WS_WP + (unsigned)(((mat * 16 + jc) * 16 + g) * 24576 + hw * 8192 + i * 1024), w);
}

__global__ __launch_bounds__(512, 2) void mega_kernel(Params p) {
  extern __shared__ __attribute__((aligned(16))) char smem[];
  const int tid = threadIdx.x;
  const int wave0 = __builtin_amdgcn_readfirstlane(tid >> 6);
  const int xx = blockIdx.x & 7, rr = blockIdx.x >> 3;
  const int g0 = (rr / NJ) * NX + (xx & (NX - 1)), j0 = (xx / NX) * NJ + (rr & (NJ - 1));
  constexpr unsigned lds0 = 0, lds00 = 0;
  if ((unsigned)(size_t)(LAS char*)smem != 0u) return;
#define LOCALS0 int g = g0, j = j0, l = l0, wave = wave0;                                              \
  OPAQUE_S(g); OPAQUE_S(j); OPAQUE_S(l); OPAQUE_S(wave);                                               \
  const int hw = wave & 3, wm = wave & 1, h = (wave >> 1) & 1;                                         \
  const unsigned myrep = (unsigned)(JROT(j) / 2 + (g & (NX - 1))) * REP_STRIDE;                              \
  (void)hw; (void)wm; (void)h; (void)myrep;                                                            \
  int lane = tid; OPAQUE_V(lane); lane &= 63
#define LOCALS LOCALS0; const bool isb = l & 1, lead = (wave == 4); const int half = wave >> 2; (void)isb; (void)lead; (void)half
  const rsrc_t rs = mk_rsrc(p.ws, WS_SPAN);
  const bool cw = wave0 < 4;
  int trv = 0, trA = 0, trB = 0, trC = 0;
  STAMP_INIT;
  TRC(0); TRR(62);
  if (wave0 == 3) {
    const char* pc;
    unsigned len;
    asm volatile("s_getpc_b64 %0\n.Lpf_base:\n\ts_mov_b32 %1, .Lpf_end-.Lpf_base" : "=s"(pc), "=s"(len));
    const unsigned off = ((unsigned)rr * 16 + (tid & 15)) * 128;
    touch(mk_rsrc(pc, len), ((int)len > 0 && off + 4 <= len) ? off : 0u, lds00 + L_SINK);
  }
  auto slowbit = [](const void* q) { return 1u << ((3 - ((unsigned)(size_t)q >> 7)) & 7); };
  unsigned slowm = slowbit(p.wqkvA) | (slowbit(p.wqkvB) << 8) | (slowbit(p.woA) << 16) | (slowbit(p.woB) << 24);
  slowm |= PKALL;
  OPAQUE_S(slowm);
  u32x2 epoch = {0u, 0u};
  bool dead = false;
      if (cw) {
        const int l0 = 0;
        LOCALS;
        u32x4 v[2][8];
        if (wave < 3) {
          const int jc = JOBC(j, wave), kt = (KB0(g, j) + (unsigned)g / NX) & 15;
          const rsrc_t ra = mk_rsrc(p.wqkvA, 1024 * 6144);
#pragma unroll
          for (int i = 0; i < 8; ++i)
            v[0][i] = __builtin_amdgcn_raw_buffer_load_b128(ra, (lane >> 3) * 6144 + (lane & 7) * 16, (kt * 64 + i * 8) * 6144 + wave * 2048 + jc * 128, 0);
          SCHED;
          TRC(44); BAR; SCHED;
          job_colsum(rs, v[0], 0, g, jc, wave, lane);
          if (SLOWC(0, jc)) {
#pragma unroll
            for (int i = 0; i < 8; ++i) job_pack(rs, v[0][i], i, 0, kt, jc, wave, lane);
          }
        } else {
          const int fr = lane & 15, fq = lane >> 4;
          const rsrc_t rx = mk_rsrc(p.x, 2048 * 2048);
#pragma unroll
          for (int i = 0; i < 16; ++i)
            v[i & 1][i >> 1] = __builtin_amdgcn_raw_buffer_load_b128(rx, fr * 2048 + fq * 16, (g * 128 + (j & (NJ - 1)) * (128 / NJ) + ((i >> 1) % (8 / NJ)) * 16) * 2048 + ((KB0(g, j) + (i >> 1) / (8 / NJ)) & 15) * 128 + (i & 1) * 64, 0);
          SCHED;
          TRC(44); BAR; SCHED;
          const f32x4 zero = {0.f, 0.f, 0.f, 0.f};
          const bf16x8 ones = {0x3f80, 0x3f80, 0x3f80, 0x3f80, 0x3f80, 0x3f80, 0x3f80, 0x3f80};
          float sm[8], q[8];
#pragma unroll
          for (int m = 0; m < 8; ++m) {
            const f32x4 a = MFMA(ones, as_frag(v[1][m]), MFMA(ones, as_frag(v[0][m]), zero));
            const f32x4 b = MFMA(as_frag(v[1][m]), as_frag(v[1][m]), MFMA(as_frag(v[0][m]), as_frag(v[0][m]), zero));
            sm[m] = a[0];
            q[m] = (fr & 2) ? ((fr & 1) ? b[3] : b[2]) : ((fr & 1) ? b[1] : b[0]);
          }
          if (fq == (fr >> 2)) {
#pragma unroll
            for (int m = 0; m < 8; ++m) st8_sc1(rs, ((m % (8 / NJ)) * 16 + fr) * 8 + (m / (8 / NJ)) * 1024, st_off(g, 0) + JROT(j) * 1024 + (j & (NJ - 1)) * (1024 / NJ), u32x2{__float_as_uint(sm[m]), __float_as_uint(q[m])});
          }
        }
      }
  const char *pA = (const char*)p.x, *pW = (const char*)p.wqkvA;
  unsigned zA = 2048 * 2048, bA = g0 * (128 * 2048), kA = 128, pcA = 32 * 2048, vA;
  unsigned zW = 1024 * 6144, bW = j0 * 128, kW = 64 * 6144, pcW = 2048, hW = 32 * 6144, vW;
  int kb = KB0(g0, j0), dj = -1;
  {
    const int lane = tid & 63, hw = wave0 & 3;
    vA = (hw * 8 + (lane >> 3)) * 2048 + (((lane & 7) ^ (((hw & 1) * 4 + (lane >> 4)) & 7)) * 16);
    vW = (hw * 8 + (lane >> 3)) * 6144 + (((lane & 7) ^ ((((lane >> 4) & 1) | ((hw & 1) << 1)) << 1)) * 16);
  }

#pragma unroll 1
  for (int l0 = 0; l0 < 4; ++l0) {
    if (l0) { TRC(0); TRR(62); }

    f32x4 acc[2][4];
    auto kvpart = [&](unsigned va, unsigned pa, unsigned kvb, int lane, int wave) {
      const f32x4 zero = {0.f, 0.f, 0.f, 0.f};
      const bf16x8 ones = {0x3f80, 0x3f80, 0x3f80, 0x3f80, 0x3f80, 0x3f80, 0x3f80, 0x3f80};
      f32x4 c0 = zero, c1 = zero, z0 = zero, z1 = zero;
#pragma unroll
      for (int ks = 0; ks < 4; ++ks) {
        const bf16x8 v = lds_ld<bf16x8>(va + ks * 2048);
        const bf16x8 a0 = tr2(pa + ks * 32 * 160, pa + (ks * 32 + 16) * 160);
        const bf16x8 a1 = tr2(pa + 32 + ks * 32 * 160, pa + 32 + (ks * 32 + 16) * 160);
        c0 = MFMA(a0, v, c0);
        c1 = MFMA(a1, v, c1);
        z0 = MFMA(a0, ones, z0);
        z1 = MFMA(a1, ones, z1);
      }
      st16_sc1(rs, lane * 16, kvb + (unsigned)wave * 1024, nosent(pack(c0, c1)));
      if (!(wave & 2) && (lane & 15) == 0) st16_sc1(rs, lane, kvb + KVP_Z + (wave >> 2) * 128 + (wave & 1) * 64, nosent(pack(z0, z1)));
    };
#define KV_ADDR                                                                                                            \
    unsigned va = lds0 + SC_VF + (wave >> 2) * 8192 + ((wave >> 1) & 1) * 1024 + lane * 16;                                \
    unsigned pa = lds0 + SC_PHIK + ((wave >> 2) * 128 + fq * 4 + (fr >> 2)) * 160 + (wave & 1) * 64 + (lane & 3) * 8;      \
    unsigned kvj = WS_KVP + (unsigned)j * KVP_J + (unsigned)(l >> 1) * KVP_L;                                              \
    OPAQUE_V(va); OPAQUE_V(pa); OPAQUE_S(kvj)
    if (cw) {
      LOCALS;
      unsigned otile, kvo, l16, pr1, pr4;
      int g2, j2, l2, ln2;
      qkv_loop(l & 1, wm, h, lane, l == 0, trv, [&](int st) {
        if (st == 0) {
          g2 = g0; j2 = j0; l2 = l0; ln2 = tid;
          OPAQUE_S(g2); OPAQUE_S(j2); OPAQUE_S(l2); OPAQUE_V(ln2);
          return;
        }
        otile = ob_off(g2, j2, l2) + (unsigned)(wm * 8 + h) * 1024;
        kvo = WS_KVP + (unsigned)j2 * KVP_J + (unsigned)g2 * KVP_G + (unsigned)(l2 >> 1) * KVP_L;
        l16 = (ln2 & 63) * 16;
        pr1 = lds0 + L_RSTD + (16 + wm * 64 + (ln2 & 15)) * 4;
        pr4 = lds0 + L_RSTD + wm * 256 + (ln2 & 48);
        asm("" : "+s"(otile)); asm("" : "+s"(kvo)); asm("" : "+v"(l16)); asm("" : "+v"(pr1)); asm("" : "+v"(pr4));
      }, [&](auto isb_c, f32x4 (&a)[4][6], auto& mm6) {
        const f32x4 zero = {0.f, 0.f, 0.f, 0.f};
        constexpr float L2E = 1.4426950408889634f;
        float rq[4];
#define ilv8(nv) do { _Pragma("unroll") for (int i_ = 0; i_ < 8; ++i_) { SGB(0x008, 1); SGB(0x100, 2); SGB(0x002, nv); } SCHED; } while (0)
        if constexpr (decltype(isb_c)::value) {
          constexpr float QS = 0.17677669529663687f * L2E, NEG = -3.0e38f;
          const bf16x8 ones = {0x3f80, 0x3f80, 0x3f80, 0x3f80, 0x3f80, 0x3f80, 0x3f80, 0x3f80};
          u32x4 kf[5], vf[5], qf[4], pf[4];
          f32x4 rs4[4], bias[2], s0[4], s1[4], dn[4];
          float x[4][4], mx[4];
          unsigned psel0, psel1;
          auto pkkv = [&](int m) { kf[m + 1] = pack(a[m][2] * rq[m], a[m][3] * rq[m]); vf[m + 1] = pack(a[m][4] * rs4[m], a[m][5] * rs4[m]); };
          auto pkq = [&](int m) { qf[m] = pack(a[m][0], a[m][1]); };
          auto st1 = [&](int m) {
            x[m][0] = vmax2(s0[m][0], s0[m][1]); x[m][1] = vmax2(s0[m][2], s0[m][3]); x[m][2] = vmax2(s1[m][0], s1[m][1]); x[m][3] = vmax2(s1[m][2], s1[m][3]);
            mx[m] = vmax2(vmax2(x[m][0], x[m][1]), vmax2(x[m][2], x[m][3]));
          };
          auto st2 = [&](int m) {
            const float mxl = mx[m] * -rq[m];
#pragma unroll
            for (int e = 0; e < 4; ++e) x[m][e] = __builtin_fmaf(x[m][e], rq[m], mxl);
          };
          auto st3 = [&](int m) {
#pragma unroll
            for (int e = 0; e < 4; ++e) x[m][e] = __builtin_amdgcn_exp2f(x[m][e]);
          };
          auto st5 = [&](int m) {
            const unsigned p0 = pk2(x[m][0], x[m][1]), p1 = pk2(x[m][2], x[m][3]);
            pf[m] = u32x4{__builtin_amdgcn_perm(0u, p0, psel0), __builtin_amdgcn_perm(0u, p0, psel1), __builtin_amdgcn_perm(0u, p1, psel0), __builtin_amdgcn_perm(0u, p1, psel1)};
          };
          auto st6 = [&](int m) {
            dn[m] = MFMA(ones, as_frag(pf[m]), zero);
            s0[m] = MFMA(as_frag(u32x4{vf[m][0], vf[m][1], vf[m + 1][0], vf[m + 1][1]}), as_frag(pf[m]), zero);
            s1[m] = MFMA(as_frag(u32x4{vf[m][2], vf[m][3], vf[m + 1][2], vf[m + 1][3]}), as_frag(pf[m]), zero);
            asm("" : "+v"(dn[m]));
          };
          auto st7 = [&](int m) { const float inv = __builtin_amdgcn_rcpf(dn[m][0]); pf[m] = nosent(pack(s0[m] * inv, s1[m] * inv)); };
          mm6(3);
#pragma unroll
          for (int m = 0; m < 4; ++m) rq[m] = lds_ld<float>(pr1 + m * 64);
#pragma unroll
          for (int m = 3; m >= 0; --m) rs4[m] = lds_ld<f32x4>(pr4 + 64 + m * 64);
          unsigned pv_ = l16 + lds0 + L_PREV + h * 2048;
          asm("" : "+v"(pv_));
          static_for<0, 8>([&](auto i_c) __attribute__((always_inline)) {
            constexpr int i = decltype(i_c)::value;
            constexpr u64 mk = window_mask(i >> 2, i & 3);
            bias[i >> 2][i & 3] = __builtin_amdgcn_inverse_ballot_w64(mk) ? 0.f : NEG;
          });
          asm("" : "+v"(bias[0])); asm("" : "+v"(bias[1]));
          const unsigned hso = kvo + KVP_HALO + h * 2048, hsi = hso - (g > 0 ? KVP_G : 0u);
          ilv8(2);
          mm6(2);
          pkkv(3);
          ilv8(2);
          OPAQUE_V(kf[4]); OPAQUE_V(vf[4]);
          if (wm) {
            st16_sc1(rs, l16, hso, nosent(kf[4]));
            st16_sc1(rs, l16, hso + 1024, nosent(vf[4]));
          } else {
            lds_st<u32x4>(pv_, kf[4]);
            lds_st<u32x4>(pv_ + 1024, vf[4]);
          }
          SCHED;
          mm6(1);
          pkq(3); pkkv(2);
          ilv8(3);
          WAITL0;
          TRQ(17); BAR;
          SCHED;
          mm6(0);
          pkq(2); pkkv(1);
          ilv8(3);
          s0[3] = MFMA(as_frag(kf[3]), as_frag(qf[3]), bias[0]); s1[3] = MFMA(as_frag(kf[4]), as_frag(qf[3]), bias[1]);
          s0[2] = MFMA(as_frag(kf[2]), as_frag(qf[2]), bias[0]); s1[2] = MFMA(as_frag(kf[3]), as_frag(qf[2]), bias[1]);
          pkq(1); pkkv(0);
#pragma unroll
          for (int i = 0; i < 4; ++i) { SGB(0x008, 1); SGB(0x002, 5); }
          SCHED;
          const bool halo = wm == 0 && g > 0;
          const u32x4 hk = ld16p<PA1>(rs, l16, hsi), hv = ld16p<PA1>(rs, l16, hsi + 1024);
          SCHED;
          s0[1] = MFMA(as_frag(kf[1]), as_frag(qf[1]), bias[0]); s1[1] = MFMA(as_frag(kf[2]), as_frag(qf[1]), bias[1]);
          st1(3); st1(2);
#pragma unroll
          for (int m = 0; m < 4; ++m) rq[m] *= QS;
          SCHED;
          st1(1);
          pkq(0);
          psel0 = __builtin_amdgcn_inverse_ballot_w64(0xaaaaaaaaaaaaaaaaull) ? 0x01000c0cu : 0x0c0c0100u;
          psel1 = __builtin_amdgcn_inverse_ballot_w64(0xaaaaaaaaaaaaaaaaull) ? 0x03020c0cu : 0x0c0c0302u;
          { float d = mx[1]; red4<true>(mx[3], mx[2], mx[1], d, [](float v) { return v; }); }
          s1[0] = MFMA(as_frag(kf[1]), as_frag(qf[0]), bias[1]);
          SCHED;
          st2(3); st2(2); st2(1);
          SCHED;
          st3(3); st3(2); st3(1);
          SCHED;
          st5(3); st6(3); st5(2); st6(2); st5(1); st6(1);
          SCHED;
          TRR(60);
          TRC(18);
          u32x4 pk = lds_ld<u32x4>(pv_), pv = lds_ld<u32x4>(pv_ + 1024);
          st7(3); st7(2); st7(1);
          SCHED;
#pragma unroll
          for (int m = 3; m > 0; --m) st16_sc1(rs, l16, otile + m * 2048, pf[m]);
          if (!wm && g == 0) bias[0] = f32x4{NEG, NEG, NEG, NEG};
          SCHED;
          TRV(55, 0); TRC(21);
          if (halo) {
            pk = hk; pv = hv;
            for (unsigned spins = 0; !dead && __any(umax4v(umax4v(0u, pk), pv) == SENT); ++spins) {
              if (spins > SPIN_CAP) { dead = true; break; }
              CLOBBER;
              TRV(55, spins + 1);
              pk = ld16p<PA1>(rs, l16, hsi); pv = ld16p<PA1>(rs, l16, hsi + 1024);
              OPAQUE_V(pk); OPAQUE_V(pv);
            }
          }
          vf[0] = pv;
          s0[0] = MFMA(as_frag(pk), as_frag(qf[0]), bias[0]);
          st1(0);
          mx[0] = red_max4(mx[0]);
          st2(0); st3(0); st5(0); st6(0); st7(0);
          st16_sc1(rs, l16, otile, pf[0]);
        } else {
          LOCALS;
          const int fr = lane & 15, fq = lane >> 4;
          f32x4 rs4[4], r[4][2];
          const bf16x8 ones = {0x3f80, 0x3f80, 0x3f80, 0x3f80, 0x3f80, 0x3f80, 0x3f80, 0x3f80};
          auto proj = [&](int m, int n, const bf16x8& w0, const bf16x8& w1) {
            const u32x4 f = pack(a[m][n], a[m][n + 1]);
            r[m][0] = MFMA(w0, as_frag(f), zero); r[m][1] = MFMA(w1, as_frag(f), zero);
          };
          mm6(3, 0, 2); mm6(2, 0, 2); mm6(1, 0, 2); mm6(0, 0, 2);
          unsigned wa = lds0 + L_WPHI + h * 2048 + (fq * 4 + (fr >> 2)) * 64 + (lane & 3) * 8, kw = lds0 + L_PREV + hw * 2048 + l16;
          asm("" : "+v"(wa)); asm("" : "+v"(kw));
#pragma unroll
          for (int m = 0; m < 4; ++m) rq[m] = lds_ld<float>(pr1 + m * 64);
#pragma unroll
          for (int m = 0; m < 4; ++m) rs4[m] = lds_ld<f32x4>(pr4 + 64 + m * 64);
          const bf16x8 w0 = tr2(wa, wa + 1024);
          const bf16x8 w1 = tr2(wa + 32, wa + 32 + 1024);
#pragma unroll
          for (int i = 0; i < 8; ++i) { SGB(0x008, 1); SGB(0x100, 2); SGB(0x002, 2); }
          SCHED;
          mm6(3, 2, 4); mm6(2, 2, 4);
          lds_st<u32x4>(kw + 1024, pack(a[3][2], a[3][3]));
          lds_st<u32x4>(kw, pack(a[2][2], a[2][3]));
#pragma unroll
          for (int i = 0; i < 2; ++i) { SGB(0x008, 1); SGB(0x002, 2); SGB(0x008, 1); SGB(0x002, 2); SGB(0x200, 1); }
          SCHED;
          mm6(1, 2, 4); mm6(0, 2, 4);
          proj(0, 2, w0, w1); proj(1, 2, w0, w1);
#pragma unroll
          for (int i = 0; i < 4; ++i) { SGB(0x008, 1); SGB(0x002, 2); }
          SCHED;
          WAITL0;
          TRQ(17); BAR;
          SCHED;
          unsigned pw = lds0 + SC_PHIK + (h * 128 + wm * 64 + fr) * 160 + fq * 8, vw = lds0 + SC_VF + h * 8192 + wm * 4096 + l16;
          asm("" : "+v"(pw)); asm("" : "+v"(vw));
#pragma unroll
          for (int m = 0; m < 4; ++m) mm6(m, 4, 8);
#pragma unroll
          for (int m = 0; m < 4; ++m) rq[m] *= L2E;
#pragma unroll
          for (int i = 0; i < 4; ++i)
            lds_st<u32x4>(vw + i * 1024, pack(a[i & 2][4 + (i & 1)] * rs4[i & 2], a[(i & 2) + 1][4 + (i & 1)] * rs4[(i & 2) + 1]));
#pragma unroll
          for (int i = 0; i < 16; ++i) { SGB(0x008, 1); SGB(0x002, 3); }
          SCHED;
          phik2(r, rq, pw);
          KV_ADDR;
          WAITL0;
          TRR(60);
          TRC(19); BAR;
          kvpart(va, pa, kvo, lane, wave);
          CLOBBER;
          TRC(20); BAR;
          u32x4 qp[4], qn[4];
          float sqp[4], sqn[4];
#pragma unroll
          for (int m = 0; m < 4; ++m) proj(m, 0, w0, w1);
          SCHED;
          float ep[4][8], en[4][8];
          hedgehog<4>(r, rq, ep, en);
          const unsigned zo = (lane >> 3) * KVP_G + (lane & 7) * 16, zs = kvj + KVP_Z + h * 128;
          u32x4 zv[2] = {ld16p<PA1>(rs, zo, zs), ld16p<PA1>(rs, zo, zs + 8 * KVP_G)};
          SCHED;
#pragma unroll
          for (int m = 0; m < 4; ++m) {
            qp[m] = pack8(ep[m]); qn[m] = pack8(en[m]);
            sqp[m] = MFMA(ones, as_frag(qp[m]), zero)[0];
            sqn[m] = MFMA(ones, as_frag(qn[m]), zero)[0];
          }
          SCHED;
#pragma unroll
          for (int m = 0; m < 4; ++m) { sqp[m] = __builtin_amdgcn_rcpf(sqp[m]); sqn[m] = __builtin_amdgcn_rcpf(sqn[m]); }
#pragma unroll
          for (int m = 0; m < 4; ++m) { OPAQUE_V(qp[m]); OPAQUE_V(qn[m]); OPAQUE_V(sqp[m]); OPAQUE_V(sqn[m]); }
          unsigned ko = (lane >> 5) * KVP_G + (lane & 31) * 16, ks = kvj + (unsigned)((j ^ RX) - j) * KVP_J + KVP_SUM + h * (8 * KVP_G);
          bf16x8 sel = rowsum_sel(lane);
          OPAQUE_V(ko); OPAQUE_S(ks); OPAQUE_V(sel);
          TRC(21); TRV(50, 0);
          for (unsigned spins = 0; !dead && __any(umax4v(umax4v(0u, zv[0]), zv[1]) == SENT); ++spins) {
            if (spins > SPIN_CAP) { dead = true; break; }
            CLOBBER;
            TRV(50, spins + 1);
            zv[0] = ld16p<PA1>(rs, zo, zs); zv[1] = ld16p<PA1>(rs, zo, zs + 8 * KVP_G);
          }
          TRC(22);
          {
            const f32x4 z = rowsum_fin(MFMA(sel, as_frag(zv[1]), MFMA(sel, as_frag(zv[0]), zero)));
            if ((lane & 40) == 0) lds_st<u32x2>(lds0 + L_ZF + hw * 128 + fr * 16 + fq * 8, u32x2{pk2(z[0], z[1]), pk2(z[2], z[3])});
            WAITL0;
            const bf16x8 zf0 = lds_ld<bf16x8>(lds0 + L_ZF + hw * 128 + fq * 16), zf1 = lds_ld<bf16x8>(lds0 + L_ZF + hw * 128 + 64 + fq * 16);
            float dp[4], dn[4];
#pragma unroll
            for (int m = 0; m < 4; ++m) { dp[m] = MFMA(zf0, as_frag(qp[m]), zero)[0]; dn[m] = MFMA(zf1, as_frag(qn[m]), zero)[0]; }
            SCHED;
#pragma unroll
            for (int m = 0; m < 4; ++m) {
              const float inv = __builtin_amdgcn_rcpf(__builtin_fmaf(dp[m], sqp[m], __builtin_fmaf(dn[m], sqn[m], 1e-6f)));
              sqp[m] *= inv; sqn[m] *= inv;
              OPAQUE_V(sqp[m]); OPAQUE_V(sqn[m]);
            }
          }
          TRV(51, 0);
          TRC(43); BAR;
          if (H2D) __builtin_amdgcn_s_sleep(H2D);
          u32x4 kv[4];
#pragma unroll
          for (int i = 0; i < 4; ++i) kv[i] = ld16p<PA2>(rs, ko, ks + i * (2 * KVP_G));
          SCHED;
#pragma unroll
          for (int m = 0; m < 4; ++m) { qp[m] = pack8(ep[m], sqp[m]); qn[m] = pack8(en[m], sqn[m]); }
#pragma unroll
          for (int m = 0; m < 4; ++m) { OPAQUE_V(qp[m]); OPAQUE_V(qn[m]); }
          SCHED;
          for (unsigned spins = 0; !dead && __any(max(umax4v(umax4v(0u, kv[0]), kv[1]), umax4v(umax4v(0u, kv[2]), kv[3])) == SENT); ++spins) {
            if (spins > SPIN_CAP) { dead = true; break; }
            CLOBBER;
            TRV(51, spins + 1);
#pragma unroll
            for (int i = 0; i < 4; ++i) kv[i] = ld16p<PA2>(rs, ko, ks + i * (2 * KVP_G));
          }
          TRC(23);
          f32x4 o[4][2];
#pragma unroll
          for (int m = 0; m < 4; ++m)
#pragma unroll
            for (int i = 0; i < 2; ++i) o[m][i] = MFMA(as_frag(kv[2 * i + 1]), as_frag(qn[m]), MFMA(as_frag(kv[2 * i]), as_frag(qp[m]), zero));
#pragma unroll
          for (int m = 0; m < 4; ++m) st16_sc1(rs, l16, otile + m * 2048, nosent(pack(o[m][0], o[m][1])));
        }
      });
#pragma unroll
      for (int i = 0; i < 8; ++i) asm volatile("" : "=v"(acc[i >> 2][i & 3]));
    } else {
      __builtin_amdgcn_s_setprio(3);
      if (l0) {
        WAITV(3);
        WAITL0;
        TRC(1); BAR;
      }
      LOCALS;
      u32x4 xr[2][2];
      const unsigned lin = lane * 16, vlin = lin + hw * 1024;
      const int jr = j & ~3;
      u32x4 sent4 = {SENT, SENT, SENT, SENT};
      OPAQUE_V(sent4);
      const unsigned cb0 = ob_off(g, j, l), cb2 = xb_off(g, l) + j * TS;
      const unsigned cb1 = WS_KVP + (unsigned)j * KVP_J + (unsigned)g * KVP_G + (unsigned)(l >> 1) * KVP_L;
      unsigned ra0;
      int tr = 0;
      auto ldres = [&](int t) {
        if (tr == t) {
#pragma unroll
          for (int i = 0; i < 4; ++i) xr[i >> 1][i & 1] = lds_ld<u32x4>((ra0 ^ ((i & 1) * 64)) + (t % 3) * XS + ((i >> 1) ^ 1) * 2048);
        }
      };
      auto aslot = [&](int t) { return lds0 + L_R2 + (t % 3) * XS + hw * 1024; };
      auto wslot = [&](int t) { return lds0 + L_R1 + (t & 3) * 24576 + hw * 1024; };
      const bool l0f = l == 0;
      const rsrc_t rA = mk_rsrc(pA, zA);
      auto kseq = [&](int t) { return (kb + t - (((KX || t < NJ) && t <= dj) ? 1 : 0)) & 15; };
      auto offA = [&](int t) { return bA + (unsigned)kseq(t) * kA; };
      auto offW = [&](int t) { return bW + (unsigned)kseq(t) * kW; };
      auto ldA = [&](int t) { dma4<4096>(rA, vA, offA(t), pcA, aslot(t)); };
      auto ldWh = [&](int t, unsigned o, int hf) { dma3<8192>(mk_rsrc(pW, zW), vW, o + (hf ? hW : 0u), pcW, wslot(t) + hf * 4096); };
      const bool slowj = l0f && SLOWC(0, j);
      auto packedW = [&]() {
        pW = p.ws; zW = WS_SPAN; bW = WS_WP + (unsigned)j * (16 * 24576); kW = 24576; pcW = 8192; hW = 4096; vW = vlin;
      };
      auto ldW = [&](int t) { const unsigned o = offW(t); ldWh(t, o, 0); ldWh(t, o, 1); };
      const unsigned cmask = 0xfu | (isb ? 0u : 0xf0u) | (l == 3 ? 0u : 0xf00u);
      constexpr auto spread = [](int n) { unsigned m = 0; for (int t = 0; t < 26; ++t) if ((t + 1) * n / 13 > t * n / 13) m |= 1u << t; return m; };
      constexpr unsigned sp4 = spread(4), sp8 = spread(8), sp12 = spread(12);
      const unsigned smask = (isb ? (l == 3 ? sp4 : sp8) : sp12) >> ((((g + j * 5) & 15) * 13) >> 4);
      const unsigned cbm = isb ? cb2 : cb1;
      auto clean = [&](int t) __attribute__((always_inline)) {
        if (STG) {
          if (t < 13 && ((smask >> t) & 1) && (!NORS || l0f)) {
            const unsigned cc = __builtin_popcount(smask & ((1u << t) - 1));
            const unsigned cb = cb0 + (cc >= 4 ? cbm - cb0 : 0u) + (cc >= 8 ? cb2 - cbm : 0u), pc = (cc + g + j) & 3;
            st16_sc1(rs, (lane >> 4) * 4096 + (lane & 15) * 16, cb + hw * 1024 + pc * 256, sent4);
          }
        } else if (t < 4 || (t < 12 && ((cmask >> t) & 1))) {
          const unsigned cb = t < 4 ? cb0 : (t < 8 ? cb1 : cb2), pc = (unsigned)((t + g + j) & 3);
          st16_sc1(rs, (lane >> 4) * 4096 + (lane & 15) * 16, cb + hw * 1024 + pc * 256, sent4);
        }
      };
      unsigned flo;
      unsigned ready = ~0u;
      u32x4 cv[4];
#pragma unroll
      for (int ii = 0; ii < 4; ++ii) asm volatile("" : "=v"(cv[ii]));
      auto chkrd = [&](int t) {
#pragma unroll
        for (int ii = 0; ii < 4; ++ii) cv[ii] = lds_ld<u32x4>(aslot(t) + lin + ii * 4096);
      };
      auto chkbad = [&]() { return __any(max(umax4v(umax4v(0u, cv[0]), cv[1]), umax4v(umax4v(0u, cv[2]), cv[3])) == SENT) && !l0f; };
      auto refetch = [&](int t) __attribute__((always_inline)) {
        for (unsigned spins = 0; !dead; ++spins) {
          if (spins > SPIN_CAP) { dead = true; break; }
          WAITV(0);
          TRV(t == 1 ? 56 : 57, spins + 1);
          ldA(t);
          ldA(t + 1);
          WAITV(0);
          chkrd(t);
          if (!chkbad()) break;
          __builtin_amdgcn_s_sleep(1);
        }
      };
      if (l0f) {
        u32x2 gw2;
        ld8_asm(gw2, rs, WS_EPOCH + 0u * lin);
        TRC(44); BAR;
        ldW(0); ldA(0);
        dma1(mk_rsrc(p.wphi, 32 * 2048), vlin, j * 4096, lds0 + L_WPHI + hw * 1024);
        ldW(1);
        if (A1E) ldA(1);
        WAITV(A1E ? 21 : 17);
        OPAQUE_V(gw2);
        const u64 gword = ((u64)(unsigned)UNI(gw2[1]) << 32) | (unsigned)UNI(gw2[0]);
        u64 z = gword + 0x9e3779b97f4a7c15ull;
        z = (z ^ (z >> 30)) * 0xbf58476d1ce4e5b9ull;
        z = (z ^ (z >> 27)) * 0x94d049bb133111ebull;
        z = z ^ (z >> 31);
        z = (z == gword) ? ~z : z;
        epoch = u32x2{(unsigned)z, (unsigned)(z >> 32)};
        tr = (j - kb) & 15;
        WAITV(A1E ? 11 : 7);
        TRC(1); BAR;
        if (!A1E) ldA(1);
        if (W2F) ldW(2);
      } else {
        if (AF == 0) ldW(1);
        if (W2F == 1) ldW(2);
        if (W2F == 2) ldWh(2, offW(2), 0);
        if (AF == 2) ldWh(1, offW(1), 0);
        if (XD) __builtin_amdgcn_s_sleep(XD);
        ldA(1);
        if (AF == 1) ldW(1);
        if (AF == 2) ldWh(1, offW(1), 1);
        if (W2F == 2) ldWh(2, offW(2), 1);
      }
      ldA(2);
      TRC(40);
      if (!W2F) ldW(2);
#pragma unroll
      for (int i = 0; i < 4; ++i) asm volatile("" : "=v"(xr[i >> 1][i & 1]));
      unsigned oA = offA(3), oW = 0;
      OPAQUE_S(oA);
      TRV(56, 0); TRV(57, 0);
      if (LAW == 3) ldW(3);
      ra0 = lds0 + L_R2 + (hw * 32 + (lane & 15)) * 128 + (((((lane >> 4) & 1) * 2 + (lane >> 5)) ^ ((lane >> 1) & 7)) * 16);
      OPAQUE_V(ra0);
      ldres(0);
      clean(0);
      WAITV(4 + !STG + (W2F ? 0 : 6) + (LAW == 3 ? 6 : 0));
      chkrd(1);
      if (__builtin_expect(chkbad(), 0)) refetch(1);
      const int fr = lane & 15, fq = lane >> 4;
      u32x2 fl;
      f32x4 js[4], jc[4];
      float ra[4], rb[4];
      const bool slowo = SLOWC(2 + (l & 1), j);
      const char* pO;
      unsigned zO, bO, pcO, vO, clo;
      u32x4 wo2[2];
      asm volatile("" : "=v"(wo2[0])); asm volatile("" : "=v"(wo2[1]));
      const unsigned fwo = myrep + fl_line(FL_WO + (isb ? 1 : 0), jr + (lane >> 4)) + (lane & 15) * 8;
      static_for<1, 14>([&](auto t_c) __attribute__((always_inline)) {
        constexpr int t = decltype(t_c)::value;
        constexpr auto rest = [](int s) {
          return s == 0 ? (W2F ? 0 : 6) + (LAW == 3 ? 6 : 0) + !STG
                        : (s + LAW <= 15 ? 6 : 0) + (s < 4 && !STG) + (s == NVAL - 2 || s == 5 || s == 11) + ((s == 7 || s == 8) ? 4 : 0);
        };
        constexpr int NW = t == 1 ? 63 : 4 + rest(t - 1) + ((LAW == 3 && (t > 2 || W2F)) ? rest(t - 2) : 0);
        TRC(1 + t);
        if (t >= NVAL && t <= 6 && __builtin_expect((ready & (1u << kseq(t + 2))) == 0, 0)) {
          WAITV(NW); WAITL0; BAR;
          ready = poll_need(rs, flo, epoch, ready, 1u << kseq(t + 2), dead);
          dma4<4096>(rA, vA, oA, pcA, aslot(t + 2));
        } else if (t == 7 && __builtin_expect(ready != ~0u, 0)) {
          WAITV(NW); WAITL0; BAR;
          poll_all(rs, flo, epoch, dead);
          dma4<4096>(rA, vA, oA, pcA, aslot(t + 2));
        } else {
          bar_dma4<4096, NW>(rA, vA, oA, pcA, aslot(t + 2));
        }
        if (t == 1 && lead) set_flags(rs, fl_line(FL_X + l, g), j, l0f ? fl_line(FL_CS, j) : fl_line(FL_X + l, g), l0f ? g : j, epoch, lane);
        if (t == 7 && slowj) packedW();
        if (t + LAW <= 15) oW = offW(t + LAW);
        if (t < 13) oA = offA(t + 3);
        OPAQUE_S(oA); OPAQUE_S(oW);
        clean(t);
        if (t <= NVAL && !l0f) { WAITV(rest(t - 1) + 4 + (t < 4 && !STG)); chkrd(t + 1); }
        if (t == WT + 3 && l0f && SLOWC(2, jr + hw)) {
          OPAQUE_V(wo2[0]); OPAQUE_V(wo2[1]);
#pragma unroll
          for (int i = 0; i < 2; ++i) st16_sc1(rs, lin, WS_WO + (unsigned)((jr + hw) * 131072 + g * 8192 + ((j & 3) * 2 + i) * 1024), wo2[i]);
        }
        if (t == WT + 6 && l0f && lead) set_flags(rs, fl_line(FL_WO, j), g, fl_line(FL_WO, j), g, epoch, lane);
        ldres(t);
        if (t == 7) {
#pragma unroll
          for (int i = 0; i < 4; ++i) ld16_asm(jc[i], rs, fq * 768 + hw * 192 + (fr < 12 ? fr : 0) * 16, WS_CS + (unsigned)(((isb ? 16 : 0) + j) * 16 + i * 4) * 768);
        }
        if (t == 8) {
#pragma unroll
          for (int i = 0; i < 4; ++i) ld16_asm(js[i], rs, fq * 1024 + hw * 256 + fr * 16, st_off(g, l) + i * 4096);
        }
        SCHED;
        if (t + LAW <= 15) ldWh(t + LAW, oW, 0);
        SCHED;
        bool bad = false;
        if (t <= NVAL) bad = chkbad();
        if (t == 1) {
          const unsigned fx = fl_line(FL_X + l, g), fc = fl_line(l0f ? FL_CS : FL_PK, jr) + ((j + fq) & 3) * 128;
          flo = myrep + ((l < 2 && lane >= 16) ? fc : fx) + fr * 8;
          OPAQUE_V(flo);
        }
        if (t == 9) {
          pO = slowo ? p.ws : (const char*)(isb ? p.woB : p.woA);
          zO = slowo ? WS_SPAN : 1024 * 2048; bO = slowo ? WS_WO + (unsigned)((isb ? 16 : 0) + j) * 131072 : j * 128; pcO = slowo ? 4096 : 32 * 2048;
          vO = slowo ? vlin : (hw * 8 + (lane >> 3)) * 2048 + wo_swz(lane);
          clo = myrep + ((lane & 16) ? fl_line(FL_CLC + l, (lane & 32) ? j ^ RX : j) : fl_line(FL_CLR + l, g)) + fr * 8;
          OPAQUE_S(pO); OPAQUE_S(zO); OPAQUE_S(bO); OPAQUE_S(pcO); OPAQUE_V(vO); OPAQUE_V(clo);
        }
        if (t == 10) {
#pragma unroll
          for (int i = 0; i < 4; ++i) OPAQUE_V(jc[i]);
          const f32x4 s = (jc[0] + jc[1]) + (jc[2] + jc[3]);
#pragma unroll
          for (int i = 0; i < 4; ++i) { ra[i] = s[i]; OPAQUE_V(ra[i]); }
        }
        if (t == 11) {
#pragma unroll
          for (int i = 0; i < 4; ++i) OPAQUE_V(js[i]);
          const f32x4 s = (js[0] + js[1]) + (js[2] + js[3]);
#pragma unroll
          for (int i = 0; i < 4; ++i) { rb[i] = s[i]; OPAQUE_V(rb[i]); }
        }
        if (t == 12) {
          red2_add(ra[0], ra[1]); red2_add(ra[2], ra[3]); red2_add(rb[0], rb[1]); red2_add(rb[2], rb[3]);
          if (lane < 12) lds_st<f32x4>(lds0 + L_CS + hw * 192 + lane * 16, f32x4{ra[0], ra[1], ra[2], ra[3]});
#pragma unroll
          for (int i = 0; i < 4; ++i) OPAQUE_V(rb[i]);
        }
        if (t == 13) {
          const float m0 = rb[0] * (1.0f / 1024), m1 = rb[2] * (1.0f / 1024);
          const float r0 = __builtin_amdgcn_rsqf(fmaxf(__builtin_fmaf(rb[1], 1.0f / 1024, -m0 * m0), 0.f) + 1e-5f);
          const float r1 = __builtin_amdgcn_rsqf(fmaxf(__builtin_fmaf(rb[3], 1.0f / 1024, -m1 * m1), 0.f) + 1e-5f);
          if (lane < 16) {
            lds_st<f32x2>(lds0 + L_MEAN + 64 + hw * 128 + fr * 8, f32x2{-m0, -m1});
            lds_st<f32x2>(lds0 + L_RSTD + 64 + hw * 128 + fr * 8, f32x2{r0, r1});
          }
        }
        SCHED;
        if (t + LAW <= 15) ldWh(t + LAW, oW, 1);
        if (t == NVAL - 1 || t == 6) {
          WAITV(10);
          OPAQUE_V(fl);
          if (t == 6) ready = __all(is_set(fl, epoch)) ? ~0u : 0u;
          else {
            ready = l0f ? ~0u : (unsigned)__ballot(is_set(fl, epoch));
            if (slowj && __all(is_set(fl, epoch))) packedW();
          }
        }
        if (t == 12) { WAITV(10); OPAQUE_V(fl); }
        if (t == 13 && slowo && __builtin_expect(!__all(is_set(fl, epoch)), 0)) poll_all(rs, fwo, epoch, dead);
        if (t == NVAL - 2 || t == 5) ld8_asm(fl, rs, flo);
        if (t == 11) ld8_asm(fl, rs, fwo);
        if (t == WT && l0f) {
#pragma unroll
          for (int i = 0; i < 2; ++i) ld16in_asm(wo2[i], mk_rsrc(p.woA, 1024 * 2048), (lane >> 3) * 2048 + wo_swz(lane), (g * 64 + (j & 3) * 16 + i * 8) * 2048 + (jr + hw) * 128);
        }
        if constexpr (t <= NVAL) { if (__builtin_expect(bad, 0)) refetch(t + 1); }
      });
      auto stO = [&](int i) { dma4<4096>(mk_rsrc(pO, zO), vO, bO + wo_src(i, j) * (4 * pcO), pcO, lds0 + wo_slot(i) + hw * 1024); };
      auto clcheck = [&]() {
        OPAQUE_V(fl);
        TRV(58, 0);
        if (__builtin_expect(!__all(is_set(fl, epoch)), 0)) { TRV(58, 1); poll_all(rs, clo, epoch, dead); }
      };
      auto accinit = [&]() {
#pragma unroll
        for (int m = 0; m < 2; ++m)
#pragma unroll
          for (int pp = 0; pp < 2; ++pp) {
            auto r0_ = __builtin_amdgcn_permlane16_swap(xr[m][pp][0], xr[m][pp][2], false, false);
            auto r1_ = __builtin_amdgcn_permlane16_swap(xr[m][pp][1], xr[m][pp][3], false, false);
            acc[m][2 * pp] = f32x4{lo16(r0_[0]), hi16(r0_[0]), lo16(r1_[0]), hi16(r1_[0])};
            acc[m][2 * pp + 1] = f32x4{lo16(r0_[1]), hi16(r0_[1]), lo16(r1_[1]), hi16(r1_[1])};
          }
#pragma unroll
        for (int i = 0; i < 8; ++i) OPAQUE_V(acc[i >> 2][i & 3]);
      };
      WAITV(10);
      WAITL0;
      TRC(15); BAR;
      if (lead) set_flags(rs, fl_line(FL_CLR + l, g), j, fl_line(FL_CLC + l, j), g, epoch, lane);
      if (isb) {
        stO(0); stO(2); stO(3);
        WAITV(12);
        TRC(16); BAR;
        stO(1); stO(4); stO(5);
        __builtin_amdgcn_s_setprio(0);
        TRC(17); BAR;
        __builtin_amdgcn_s_sleep(BD);
        stO(7); stO(6);
        ld8_asm(fl, rs, clo);
        accinit();
        WAITV(0);
        clcheck();
      } else {
        u32x4 vb[8];
#pragma unroll
        for (int i = 0; i < 8; ++i) asm volatile("" : "=v"(vb[i]));
        stO(0); stO(2); stO(3);
        ldres(14);
        WAITV(12);
        WAITL0;
        TRC(16); BAR;
        stO(1);
        ldres(15);
        if (l0f) {
          const bool wj = hw != 0;
          const char* bp = (const char*)(wj ? p.wqkvB : p.woB);
          unsigned rs_ = wj ? 6144 : 2048, sz = rs_ * 1024, ss = rs_ * 8, s2 = wj ? 16 * 6144 : 128;
          unsigned sb = g * 64 * rs_ + (wj ? (hw - 1) * 2048 + JOBC(j, hw - 1) * 128 : (j & 3) * (16 * 2048) + jr * 128);
          OPAQUE_S(bp); OPAQUE_S(rs_); OPAQUE_S(sz); OPAQUE_S(sb); OPAQUE_S(ss); OPAQUE_S(s2);
          const rsrc_t rb = mk_rsrc(bp, sz);
#pragma unroll
          for (int i = 0; i < 8; ++i) ld16in_asm(vb[i], rb, (lane >> 3) * rs_ + (lane & 7) * 16, sb + (i & 1) * ss + (i >> 1) * s2);
        }
        __builtin_amdgcn_s_setprio(0);
        KV_ADDR;
        bf16x8 sel = rowsum_sel(lane);
        unsigned h1 = (lane >> 3) * KVP_G + (lane & 7) * 16;
        unsigned pw = lds0 + SC_PHIK + (h * 128 + wm * 64 + 32 + fr) * 160 + fq * 8, kw = lds0 + L_PREV + hw * 2048 + lin;
        const unsigned wa = lds0 + L_WPHI + h * 2048 + (fq * 4 + (fr >> 2)) * 64 + (lane & 3) * 8;
        bf16x8 w0 = tr2(wa, wa + 1024), w1 = tr2(wa + 32, wa + 32 + 1024);
        float cq[4];
#pragma unroll
        for (int m = 0; m < 2; ++m) { cq[m] = lds_ld<float>(lds0 + L_RSTD + (16 + wm * 64 + 32 + m * 16 + fr) * 4) * 1.4426950408889634f; OPAQUE_V(cq[m]); }
        OPAQUE_V(sel); OPAQUE_V(h1); OPAQUE_V(pw); OPAQUE_V(kw); OPAQUE_V(w0); OPAQUE_V(w1);
        TRC(17); BAR;
        ld8_asm(fl, rs, clo);
        {
          const f32x4 zero = {0.f, 0.f, 0.f, 0.f};
          f32x4 r[4][2];
#pragma unroll
          for (int m = 0; m < 2; ++m) {
            const bf16x8 f = lds_ld<bf16x8>(kw + m * 1024);
            r[m][0] = MFMA(w0, f, zero); r[m][1] = MFMA(w1, f, zero);
          }
          phik2(r, cq, pw);
        }
        WAITV(0);
#pragma unroll
        for (int i = 0; i < 8; ++i) OPAQUE_V(vb[i]);
        clcheck();
        WAITL0;
        TRC(19); BAR;
        kvpart(va, pa, kvj + (unsigned)g * KVP_G, lane, wave);
        CLOBBER;
        TRC(20); BAR;
        auto job = [&]() __attribute__((always_inline)) {
        if (l0f) {
          if (hw) {
            job_colsum(rs, vb, 1, g, JOBC(j, hw - 1), hw - 1, lane);
            if (SLOWC(1, JOBC(j, hw - 1))) {
#pragma unroll
              for (int i = 0; i < 8; ++i) job_pack(rs, vb[i], i, 1, g, JOBC(j, hw - 1), hw - 1, lane);
            }
          } else {
#pragma unroll
            for (int i = 0; i < 8; ++i)
              if (SLOWC(3, jr + (i >> 1))) st16_sc1(rs, (lane >> 3) * 128 + wo_swz(lane), WS_WO + (unsigned)((16 + jr + (i >> 1)) * 131072 + g * 8192 + ((j & 3) * 2 + (i & 1)) * 1024), vb[i]);
          }
        }
        };
        if (JOBP) job();
        if (H1D > 0) stO(4);
        if (H1D > 1) stO(5);
        const f32x4 zero = {0.f, 0.f, 0.f, 0.f};
        const unsigned s1 = kvj + (unsigned)((j ^ RX) - j) * KVP_J + g * 512 + hw * 128;
        if (H1S) __builtin_amdgcn_s_sleep(H1S);
        u32x4 v[2] = {ld16p<PA1>(rs, h1, s1), ld16p<PA1>(rs, h1, s1 + 8 * KVP_G)};
        TRC(21); TRV(50, 0);
        for (unsigned spins = 0; !dead && __any(umax4v(umax4v(0u, v[0]), v[1]) == SENT); ++spins) {
          if (spins > SPIN_CAP) { dead = true; break; }
          CLOBBER;
          TRV(50, spins + 1);
          if (H1P) __builtin_amdgcn_s_sleep(H1P);
          v[0] = ld16p<PA1>(rs, h1, s1); v[1] = ld16p<PA1>(rs, h1, s1 + 8 * KVP_G);
        }
        TRC(22);
        const f32x4 c = rowsum_fin(MFMA(sel, as_frag(v[1]), MFMA(sel, as_frag(v[0]), zero)));
        if ((lane & 40) == 0)
          st8_sc1(rs, fr * 16 + fq * 8, kvj + (unsigned)g * KVP_G + KVP_SUM + hw * 128, u32x2{min(pk2(c[0], c[1]), SENT - 1), min(pk2(c[2], c[3]), SENT - 1)});
        CLOBBER;
        TRC(43); BAR;
        if (JOBP && l0f && lead) set_flags(rs, fl_line(FL_PK, j), g, fl_line(FL_WO + 1, j), g, epoch, lane);
        accinit();
        if (H2D) __builtin_amdgcn_s_sleep(H2D);
        if (H1D < 1) stO(4);
        if (H1D < 2) stO(5);
        stO(6); stO(7);
        if (!JOBP) job();
        WAITV(0);
      }
    }
    {
      LOCALS;
      const int fr = lane & 15, fq = lane >> 4;
      const int kh = half;
      unsigned v0 = lane * 16 + hw * 4096 + kh * 2048, v1 = lane * 16 + hw * 4096 + (kh ^ 1) * 2048;
      const int r0 = (JROT(j) >> 1) + NJ / 2;
      const unsigned ob0 = ob_off(g, kh, l);
      const unsigned pko = myrep + fl_line(FL_PK, JOBC(j, fq - 1)) + fr * 8;
      u32x4 R[8][4];
      u32x2 pkf;
      auto issue_ = [&](int i) {
        const unsigned so = ob0 + (unsigned)((r0 + i) & 7) * (2 * TS);
        R[i][0] = ld16_sc1(rs, v0, so); R[i][1] = ld16_sc1(rs, v1, so);
        R[i][2] = ld16_sc1(rs, v0 + 1024, so); R[i][3] = ld16_sc1(rs, v1 + 1024, so);
      };
      auto issue = [&](int i) { issue_(i); SCHED; };
      constexpr int LA = LOOKAHEAD;
      TRV(52, 0); TRV(53, 0); TRV(54, 0);
      TRR(61);
      __builtin_amdgcn_s_setprio(3);
      unsigned so0 = ob0 + (unsigned)(r0 & 7) * (2 * TS);
      OPAQUE_V(v0); OPAQUE_V(v1); OPAQUE_S(so0);
      TRC(25); BAR;
      if (OD) __builtin_amdgcn_s_sleep(OD);
      if (DLY && !isb) { for (int d = (g >> 1) * DLY; d > 0; --d) __builtin_amdgcn_s_sleep(1); }
      SCHED;
      R[0][0] = ld16p<PA3>(rs, v0, so0); R[0][1] = ld16p<PA3>(rs, v1, so0);
      R[0][2] = ld16p<PA3>(rs, v0 + 1024, so0); R[0][3] = ld16p<PA3>(rs, v1 + 1024, so0);
      SCHED;
      __builtin_amdgcn_s_setprio(2);
      if (!JOBP && l == 0 && lead) set_flags(rs, fl_line(FL_PK, j), g, fl_line(FL_WO + 1, j), g, epoch, lane);

      auto vmax = [&](int i) { return umax4v(umax4v(umax4v(umax4v(0u, R[i][0]), R[i][1]), R[i][2]), R[i][3]); };
      auto bad = [&](int i) { return __any(umax4v(umax4v(umax4v(umax4v(0u, R[i][0]), R[i][1]), R[i][2]), R[i][3]) == SENT); };
      auto poll = [&](int i, int nfl) {
        for (unsigned spins = 0; !dead; ++spins) {
          if (spins > SPIN_CAP) { dead = true; break; }
          __builtin_amdgcn_s_sleep(PS);
          CLOBBER;
          TRV(52 + (i > 2 ? 2 : i), spins + 1);
          issue(i);
#pragma unroll
          for (int k = i + 1; k < i + nfl && k < 8; ++k)
            if (bad(k)) issue(k);
          WAITV(0);
#pragma unroll
          for (int k = i + 1; k < i + nfl && k < 8; ++k)
#pragma unroll
            for (int f = 0; f < 4; ++f) OPAQUE_V(R[k][f]);
          if (!bad(i)) break;
        }
      };
      unsigned bb[4];
      {
        const int kr = fq * 4 + (fr >> 2);
        const int gs = (kr >> 1) & 3;
#pragma unroll
        for (int c = 0; c < 4; ++c) bb[c] = lds0 + kh * 8192 + kr * 128 + ((c ^ gs) * 32) + (lane & 3) * 8;
      }
      bf16x8 bf[2][4];
      auto rdB = [&](int i, int hh) {
        if (hh == 0) {
#pragma unroll
          for (int c = 0; c < 4; ++c) { bb[c] += wo_slot(i) - (i ? wo_slot(i - 1) : 0u); asm("" : "+v"(bb[c])); }
        }
#pragma unroll
        for (int c = 0; c < 4; ++c) bf[hh][c] = tr2(bb[c] + hh * 4096, bb[c] + hh * 4096 + 2048);
      };
      auto mms = [&](int i, int hh) {
#pragma unroll
        for (int n = 0; n < 4; ++n)
#pragma unroll
          for (int m = 0; m < 2; ++m) acc[m][n] = MFMA(bf[hh][n], as_frag(R[i][hh * 2 + m]), acc[m][n]);
      };
      if (!half) {
#pragma unroll
        for (int i = 0; i < 8; ++i) acc[i >> 2][i & 3] = f32x4{0.f, 0.f, 0.f, 0.f};
      }
      const char* obase = (l == 3) ? (const char*)p.out : (const char*)p.ws;
      unsigned osize = (l == 3) ? 2048 * 2048 : WS_SPAN;
      const bool slown = SLOWC((l & 1) ^ 1, j);
      pW = slown ? p.ws : (const char*)(isb ? p.wqkvA : p.wqkvB);
      bW = slown ? WS_WP + (unsigned)((isb ? 0 : 16) + j) * (16 * 24576) : j * 128;
      kW = slown ? 24576 : 64 * 6144; zW = slown ? WS_SPAN : 1024 * 6144; pcW = slown ? 8192 : 2048; hW = slown ? 4096 : 32 * 6144;
      vA = hw * 1024 + lane * 16;
      vW = slown ? vA : (hw * 8 + (lane >> 3)) * 6144 + (((lane & 7) ^ ((((lane >> 4) & 1) | ((hw & 1) << 1)) << 1)) * 16);
      pA = p.ws; zA = WS_SPAN; bA = xb_off(g, l); kA = TS; pcA = 4096; kb = JROT(j) + KX; dj = (j - kb) & 15;
      OPAQUE_S(pW); OPAQUE_S(bW); OPAQUE_S(kW); OPAQUE_S(zW); OPAQUE_S(pcW); OPAQUE_S(hW); OPAQUE_V(vA); OPAQUE_V(vW); OPAQUE_S(bA);
      unsigned sq = bW + j * kW + kh * hW, dst = lds0 + L_R1 + kh * 4096 + hw * 1024;
      OPAQUE_S(obase); OPAQUE_S(osize); OPAQUE_S(sq); OPAQUE_S(dst);
      const rsrc_t ro = mk_rsrc(obase, osize);
      unsigned so[2];
      unsigned xw = lds0 + SC_XCH + wave * 4096 + lane * 16;
      unsigned xa = lds0 + SC_XCH + (wave ^ 4) * 4096 + lane * 16;
      unsigned sb0 = (l == 3) ? g * (128 * 2048) + j * 128 : xb_off(g, l) + j * TS;
      {
        const int row = (hw * 2 + kh) * 16 + fr, ch = (fq & 1) * 2 + (fq >> 1);
        const unsigned sw = (l == 3) ? 0u : (unsigned)((row >> 1) & 7), rb = (unsigned)row << ((l == 3) ? 11 : 7);
#pragma unroll
        for (int pp = 0; pp < 2; ++pp) { so[pp] = rb + (((ch + 4 * pp) ^ sw) * 16); OPAQUE_V(so[pp]); }
        OPAQUE_V(xa); OPAQUE_V(xw); OPAQUE_S(sb0);
      }
      SCHED;
#pragma unroll
      for (int i = 1; i < LA; ++i) issue(i);
      rdB(0, 0);
      if (PRE) {
        f32x4 dm = {0.f, 0.f, 0.f, 0.f};
#pragma unroll 1
        for (int d = 0; d < PRE; ++d) {
#pragma unroll
          for (int c = 0; c < 4; ++c) dm = MFMA(bf[0][c], bf[0][(c + 1) & 3], dm);
        }
        asm volatile("" ::"v"(dm));
        SCHED;
      }
      unsigned mx = vmax(0);
#pragma unroll
      for (int i = 0; i < 8; ++i) {
        SCHED;
        if (__builtin_expect(__any(mx == SENT), 0)) poll(i, LA);
        if (i == 0) TRC(27); else TRQ(27 + i);
        if (i == W1S - 2) { pkf = ld8_sc1(rs, pko, 0); SCHED; }
        if (i == W1S) {
          OPAQUE_V(pkf);
          if (l == 0 && lead && slown && !__all(is_set(pkf, epoch))) poll_all(rs, pko, epoch, dead);
          TRC(35); BAR;
        }
        if (i == 7 && l < 3) dma3<8192>(mk_rsrc(pW, zW), vW, sq, pcW, dst);
        if (i < 4 && (i < PM ? !isb : l == 0)) __builtin_amdgcn_s_sleep(PACE);
        if (i == 2) __builtin_amdgcn_s_setprio(1);
        if (i == 5) __builtin_amdgcn_s_setprio(0);
        SCHED;
        rdB(i, 1);
        if (i == 7) break;
        if (i + LA < 8) issue_(i + LA);
        mms(i, 0);
#pragma unroll
        for (int k = 0; k < 4; ++k) { SGB(0x008, 1); SGB(0x100, 1); SGB(0x020, 1); SGB(0x008, 1); SGB(0x100, 1); }
        SCHED;
        rdB(i + 1, 0); mx = vmax(i + 1);
        mms(i, 1);
#pragma unroll
        for (int k = 0; k < 4; ++k) { SGB(0x008, 1); SGB(0x100, 2); }
#pragma unroll
        for (int k = 4; k < 8; ++k) { SGB(0x008, 1); SGB(0x002, 8 / (8 - 4)); }
      }
#pragma unroll
      for (int n = 0; n < 4; ++n) acc[1][n] = MFMA(bf[0][n], as_frag(R[7][1]), acc[1][n]);
#pragma unroll
      for (int n = 0; n < 4; ++n) acc[0][n] = MFMA(bf[0][n], as_frag(R[7][0]), acc[0][n]);
#pragma unroll
      for (int k = 0; k < 8; ++k) { SGB(0x008, 1); SGB(0x100, 1); }
      SCHED;
#pragma unroll
      for (int n = 0; n < 4; ++n) acc[1][n] = MFMA(bf[1][n], as_frag(R[7][3]), acc[1][n]);
#pragma unroll
      for (int n = 0; n < 4; ++n) acc[0][n] = MFMA(bf[1][n], as_frag(R[7][2]), acc[0][n]);
#pragma unroll
      for (int n = 0; n < 4; ++n) lds_st<f32x4>(xw + n * 1024, acc[1][n]);
      SGB(0x008, 4);
#pragma unroll
      for (int k = 0; k < 4; ++k) { SGB(0x008, 1); SGB(0x200, 1); }
      SCHED;
      TRC(36);
      WAITL0;
      TRC(37); BAR;
      SCHED;
      f32x4 pa[4];
#pragma unroll
      for (int n = 0; n < 4; ++n) pa[n] = lds_ld<f32x4>(xa + n * 1024);
      SCHED;
      u32x4 o[2];
#pragma unroll
      for (int pp = 0; pp < 2; ++pp) {
        const f32x4 a = acc[0][2 * pp] + pa[2 * pp], b = acc[0][2 * pp + 1] + pa[2 * pp + 1];
        auto r0_ = __builtin_amdgcn_permlane16_swap(pk2(a[0], a[1]), pk2(b[0], b[1]), false, false);
        auto r1_ = __builtin_amdgcn_permlane16_swap(pk2(a[2], a[3]), pk2(b[2], b[3]), false, false);
        o[pp] = nosent(u32x4{r0_[0], r1_[0], r0_[1], r1_[1]});
        st16_sc1(ro, so[pp], sb0, o[pp]);
      }
      if (l < 3) {
        lds_st<u32x4>(lds0 + L_R2 + so[0], o[0]);
        lds_st<u32x4>(lds0 + L_R2 + so[1], o[1]);
        const f32x4 zero = {0.f, 0.f, 0.f, 0.f};
        const bf16x8 ones = {0x3f80, 0x3f80, 0x3f80, 0x3f80, 0x3f80, 0x3f80, 0x3f80, 0x3f80};
        const f32x4 a = MFMA(ones, as_frag(o[1]), MFMA(ones, as_frag(o[0]), zero));
        const f32x4 b = MFMA(as_frag(o[1]), as_frag(o[1]), MFMA(as_frag(o[0]), as_frag(o[0]), zero));
        const float q = (fr & 2) ? ((fr & 1) ? b[3] : b[2]) : ((fr & 1) ? b[1] : b[0]);
        if (fq == (fr >> 2)) st8_sc1(rs, ((hw * 2 + kh) * 16 + fr) * 8, st_off(g, l + 1) + j * 1024, u32x2{__float_as_uint(a[0]), __float_as_uint(q)});
      } else if (lead && blockIdx.x == 0 && lane == 0) {
        st8_sc1(rs, WS_EPOCH, 0, epoch);
      }
    }
    TRC(38); TRR(63);
  }
  STAMP_DUMP;
  asm volatile(".Lpf_end:");
}

extern "C" void kernel_launch(void* const* d_in, const int* in_sizes, int n_in, void* d_out, int out_size,
                              void* d_ws, size_t ws_size, hipStream_t stream) {
  Params p;
  p.x = (const u16*)d_in[0];
  p.wqkvA = (const u16*)d_in[1];
  p.wphi = (const u16*)d_in[2];
  p.woA = (const u16*)d_in[3];
  p.wqkvB = (const u16*)d_in[5];
  p.woB = (const u16*)d_in[6];
  p.out = (u16*)d_out;
  p.ws = (char*)d_ws;
  (void)hipFuncSetAttribute((const void*)mega_kernel, hipFuncAttributeMaxDynamicSharedMemorySize, L_TOTAL);
  mega_kernel<<<256, 512, L_TOTAL, stream>>>(p);
}
```

```cpp
#include <hip/hip_runtime.h>
#include <stdint.h>
#include <type_traits>
constexpr int LOOKAHEAD = 2;
constexpr int NVAL = 4;
constexpr int WT = 1;
constexpr int W1S = 6;
constexpr int NJ = 8;
constexpr int KX = 8;
constexpr int RX = 8;
constexpr unsigned TS = 17664;
#define XD 3
#define H1S 6
#define H2D 5
#define BD 8
#define PACE 4

typedef unsigned short u16;
typedef unsigned long long u64;
typedef __attribute__((ext_vector_type(8))) short bf16x8;
typedef __attribute__((ext_vector_type(4))) short s16x4;
typedef __attribute__((ext_vector_type(4))) float f32x4;
typedef __attribute__((ext_vector_type(2))) float f32x2;
typedef __attribute__((ext_vector_type(4))) unsigned u32x4;
typedef __attribute__((ext_vector_type(2))) unsigned u32x2;
typedef __attribute__((ext_vector_type(2))) __bf16 bf16x2_t;
#define LAS __attribute__((address_space(3)))
typedef LAS s16x4* lds_s16x4_ptr;

constexpr unsigned NREP = 8, REP_STRIDE = 17 * 2048;
enum { FL_X = 0, FL_CS = 4, FL_CLR = 5, FL_CLC = 9, FL_PK = 13, FL_WO = 14 };
__device__ __forceinline__ unsigned fl_line(int set, int a) { return (unsigned)(set * 16 + a) * 128; }
constexpr unsigned WS_EPOCH = 512u << 10;
constexpr unsigned WS_CS = 1u << 20;
constexpr unsigned WS_KVP = 4u << 20;
constexpr unsigned KVP_G = 32768, KVP_J = 16 * KVP_G, KVP_L = 16384, KVP_Z = 8192, KVP_SUM = 8704, KVP_HALO = 12u << 10;
constexpr unsigned SENT = 0xffffffffu;
constexpr unsigned WS_X = 14u << 20, WS_O = 29u << 20, WS_ST = 2u << 20;
__device__ __forceinline__ unsigned xb_off(int g, int l) { return WS_X + (unsigned)(l * 16 + g) * (16 * TS); }
__device__ __forceinline__ unsigned ob_off(int g, int j, int l) { return WS_O + (unsigned)((l * 16 + g) * 16 + j) * TS; }
__device__ __forceinline__ unsigned st_off(int g, int l) { return WS_ST + (unsigned)(l * 16 + g) * (16u << 10); }
constexpr unsigned WS_WP = 48u << 20;
constexpr unsigned WS_WO = 60u << 20;
constexpr unsigned WS_SPAN = 64u << 20;

constexpr unsigned L_R1 = 0;
constexpr unsigned L_R2 = 98304, XS = 16384;
constexpr unsigned L_MISC = 147456;
constexpr unsigned L_MEAN = L_MISC;
constexpr unsigned L_RSTD = L_MISC + 576;
constexpr unsigned L_CS = L_MISC + 1152;
constexpr unsigned L_WPHI = L_MISC + 2688;
constexpr unsigned L_SINK = L_MISC + 6912;
constexpr unsigned L_ZF = L_MISC + 1920;
constexpr unsigned L_PREV = L_MISC + 8192;
constexpr unsigned L_TOTAL = 163840;

#define JROT(j) ((j) & ~(NJ - 1))
constexpr int NX = NJ / 2;
#define KB0(g, j) (JROT(j) + (16 / NX) * ((g) & (NX - 1)))
#define JOBC(j, w) (((j) & ~3) + (((j) + (w) + 1) & 3))

#define SLOWC(m, jc) ((slowm >> ((m) * 8 + ((jc) & 7))) & 1)

struct Params {
  const u16* x;
  const u16* wqkvA;
  const u16* wphi;
  const u16* woA;
  const u16* wqkvB;
  const u16* woB;
  u16* out;
  char* ws;
};

#define SCHED __builtin_amdgcn_sched_barrier(0)
#define BAR __builtin_amdgcn_s_barrier()
#define SGB(m, n) __builtin_amdgcn_sched_group_barrier(m, n, 0)
#define WAITV(n) asm volatile("s_waitcnt vmcnt(%0)" ::"n"(n) : "memory")
#define WAITL0 asm volatile("s_waitcnt lgkmcnt(0)" ::: "memory")

__device__ __forceinline__ unsigned pk2(float a, float b) {
  f32x2 v = {a, b};
  return __builtin_bit_cast(unsigned, __builtin_convertvector(v, bf16x2_t));
}
__device__ __forceinline__ float lo16(unsigned u) { return __uint_as_float(u << 16); }
__device__ __forceinline__ float hi16(unsigned u) { return __uint_as_float(u & 0xffff0000u); }

template <int A, int B, typename F> __device__ __forceinline__ void static_for(F f) {
  if constexpr (A < B) { f(std::integral_constant<int, A>{}); static_for<A + 1, B>(f); }
}
typedef __amdgpu_buffer_rsrc_t rsrc_t;
__device__ __forceinline__ rsrc_t mk_rsrc(const void* p, unsigned bytes) { return __builtin_amdgcn_make_buffer_rsrc((void*)p, 0, bytes, 0x00020000); }
#define UNI(x) __builtin_amdgcn_readfirstlane(x)
#define DMA_HEAD "s_mov_b32 m0, %4\n\ts_mov_b32 %0, %3\n\ts_nop 2\n\tbuffer_load_dwordx4 %1, %2, %0 offen sc1 lds\n\t"
#define DMA_NEXT "s_add_u32 m0, m0, %6\n\ts_add_u32 %0, %0, %5\n\tbuffer_load_dwordx4 %1, %2, %0 offen sc1 lds\n\t"
#define DMA_OPS : "=&s"(t) : "v"(voff), "s"(r), "s"(UNI(soff)), "s"(UNI(lds)), "s"(UNI(sstep)), "i"(LSTEP) : "memory", "scc", "m0"
template <int LSTEP> __device__ __forceinline__ void dma4(rsrc_t r, unsigned voff, unsigned soff, unsigned sstep, unsigned lds) {
  unsigned t;
  asm volatile(DMA_HEAD DMA_NEXT DMA_NEXT DMA_NEXT DMA_OPS);
}
template <int LSTEP> __device__ __forceinline__ void dma3(rsrc_t r, unsigned voff, unsigned soff, unsigned sstep, unsigned lds) {
  unsigned t;
  asm volatile(DMA_HEAD DMA_NEXT DMA_NEXT DMA_OPS);
}
template <int LSTEP, int N> __device__ __forceinline__ void bar_dma4(rsrc_t r, unsigned voff, unsigned soff, unsigned sstep, unsigned lds) {
  unsigned t;
  asm volatile("s_mov_b32 m0, %4\n\ts_mov_b32 %0, %3\n\ts_nop 0\n\ts_waitcnt vmcnt(%7) lgkmcnt(0)\n\ts_barrier\n\t"
               "buffer_load_dwordx4 %1, %2, %0 offen sc1 lds\n\t" DMA_NEXT DMA_NEXT DMA_NEXT
               : "=&s"(t) : "v"(voff), "s"(r), "s"(UNI(soff)), "s"(UNI(lds)), "s"(UNI(sstep)), "i"(LSTEP), "n"(N) : "memory", "scc", "m0");
}
__device__ __forceinline__ void dma1(rsrc_t r, unsigned voff, unsigned soff, unsigned lds) {
  asm volatile("s_mov_b32 m0, %3\n\ts_nop 3\n\tbuffer_load_dwordx4 %0, %1, %2 offen sc1 lds" ::"v"(voff), "s"(r), "s"(UNI(soff)), "s"(UNI(lds)) : "memory", "m0");
}
__device__ __forceinline__ void touch(rsrc_t r, unsigned voff, unsigned lds) {
  asm volatile("s_mov_b32 m0, %2\n\ts_nop 3\n\tbuffer_load_dword %0, %1, 0 offen lds" ::"v"(voff), "s"(r), "s"(UNI(lds)) : "memory", "m0");
}
__device__ __forceinline__ void ld8_asm(u32x2& d, rsrc_t r, unsigned voff) {
  asm volatile("s_nop 4\n\tbuffer_load_dwordx2 %0, %1, %2, 0 offen sc1" : "=v"(d) : "v"(voff), "s"(r) : "memory");
}

__device__ __forceinline__ void ld16in_asm(u32x4& d, rsrc_t r, unsigned voff, unsigned soff) {
  asm volatile("s_nop 4\n\tbuffer_load_dwordx4 %0, %1, %2, %3 offen" : "=v"(d) : "v"(voff), "s"(r), "s"(UNI(soff)) : "memory");
}
__device__ __forceinline__ void ld16_asm(f32x4& d, rsrc_t r, unsigned voff, unsigned soff) {
  asm volatile("s_nop 4\n\tbuffer_load_dwordx4 %0, %1, %2, %3 offen sc1" : "=v"(d) : "v"(voff), "s"(r), "s"(UNI(soff)) : "memory");
}

template <typename T> __device__ __forceinline__ T lds_ld(unsigned a) { return *(LAS T*)(a); }
template <typename T> __device__ __forceinline__ void lds_st(unsigned a, T v) { *(LAS T*)(a) = v; }
__device__ __forceinline__ bf16x8 tr2(unsigned a0, unsigned a1) {
  s16x4 v0 = __builtin_amdgcn_ds_read_tr16_b64_v4i16((lds_s16x4_ptr)(a0));
  s16x4 v1 = __builtin_amdgcn_ds_read_tr16_b64_v4i16((lds_s16x4_ptr)(a1));
  return __builtin_shufflevector(v0, v1, 0, 1, 2, 3, 4, 5, 6, 7);
}
__device__ __forceinline__ bf16x8 as_frag(u32x4 v) { return __builtin_bit_cast(bf16x8, v); }
#define MFMA(a, b, c) __builtin_amdgcn_mfma_f32_16x16x32_bf16(a, b, c, 0, 0, 0)

__device__ __forceinline__ float vmax2(float a, float b) { float inf = __builtin_inff(); asm("" : "+s"(inf)); return __builtin_amdgcn_fmed3f(a, b, inf); }
#define SWAP16(a, b) __builtin_amdgcn_permlane16_swap(__float_as_uint(a), __float_as_uint(b), false, false)
#define SWAP32(a, b) __builtin_amdgcn_permlane32_swap(__float_as_uint(a), __float_as_uint(b), false, false)
__device__ __forceinline__ void red2_max(float& a, float& b) {
  auto r = SWAP16(a, b);
  float x = vmax2(__uint_as_float(r[0]), __uint_as_float(r[1]));
  r = SWAP32(x, x);
  x = vmax2(__uint_as_float(r[0]), __uint_as_float(r[1]));
  r = SWAP16(x, x);
  a = __uint_as_float(r[0]); b = __uint_as_float(r[1]);
}
__device__ __forceinline__ void red2_add(float& a, float& b) {
  auto r = SWAP16(a, b);
  float x = __uint_as_float(r[0]) + __uint_as_float(r[1]);
  r = SWAP32(x, x);
  x = __uint_as_float(r[0]) + __uint_as_float(r[1]);
  r = SWAP16(x, x);
  a = __uint_as_float(r[0]); b = __uint_as_float(r[1]);
}
template <bool MAX, typename M> __device__ __forceinline__ void red4(float& a, float& b, float& c, float& d, M mid) {
  auto op = [](unsigned x, unsigned y) { return MAX ? vmax2(__uint_as_float(x), __uint_as_float(y)) : __uint_as_float(x) + __uint_as_float(y); };
  auto r = SWAP16(a, b), q = SWAP16(c, d);
  const float x = op(r[0], r[1]), y = op(q[0], q[1]);
  r = SWAP32(x, y);
  const float w = mid(op(r[0], r[1]));
  r = SWAP32(w, w);
  q = __builtin_amdgcn_permlane16_swap(r[1], r[1], false, false);
  r = __builtin_amdgcn_permlane16_swap(r[0], r[0], false, false);
  a = __uint_as_float(r[0]); b = __uint_as_float(r[1]); c = __uint_as_float(q[0]); d = __uint_as_float(q[1]);
}
__device__ __forceinline__ float red_max4(float x) {
  auto r = __builtin_amdgcn_permlane16_swap(__float_as_uint(x), __float_as_uint(x), false, false);
  x = vmax2(__uint_as_float(r[0]), __uint_as_float(r[1]));
  r = __builtin_amdgcn_permlane32_swap(__float_as_uint(x), __float_as_uint(x), false, false);
  return vmax2(__uint_as_float(r[0]), __uint_as_float(r[1]));
}
__device__ __forceinline__ float red_add4(float x) {
  auto r = __builtin_amdgcn_permlane16_swap(__float_as_uint(x), __float_as_uint(x), false, false);
  x = __uint_as_float(r[0]) + __uint_as_float(r[1]);
  r = __builtin_amdgcn_permlane32_swap(__float_as_uint(x), __float_as_uint(x), false, false);
  return __uint_as_float(r[0]) + __uint_as_float(r[1]);
}

__device__ __forceinline__ void st16_sc1(rsrc_t r, unsigned voff, unsigned soff, u32x4 v) {
  __builtin_amdgcn_raw_buffer_store_b128(v, r, voff, soff, 16);
  asm volatile("s_nop 1" ::"v"(v) : "memory");
}
__device__ __forceinline__ void st8_sc1(rsrc_t r, unsigned voff, unsigned soff, u32x2 v) { __builtin_amdgcn_raw_buffer_store_b64(v, r, voff, soff, 16); }
__device__ __forceinline__ u32x4 ld16_sc1(rsrc_t r, unsigned voff, unsigned soff) { return __builtin_amdgcn_raw_buffer_load_b128(r, voff, soff, 16); }
__device__ __forceinline__ u32x2 ld8_sc1(rsrc_t r, unsigned voff, unsigned soff) { return __builtin_amdgcn_raw_buffer_load_b64(r, voff, soff, 16); }
__device__ __forceinline__ f32x4 ld4f_sc1(rsrc_t r, unsigned voff, unsigned soff) { return __builtin_bit_cast(f32x4, __builtin_amdgcn_raw_buffer_load_b128(r, voff, soff, 16)); }
#define OPAQUE_V(x) asm volatile("" : "+v"(x))
#define OPAQUE_S(x) asm volatile("" : "+s"(x))

#define CLOBBER asm volatile("" ::: "memory")
constexpr unsigned SPIN_CAP = 1u << 17;
__device__ __forceinline__ bool is_set(u32x2 v, u32x2 epoch) { return v[0] == epoch[0] && v[1] == epoch[1]; }
__device__ __forceinline__ void set_flags(rsrc_t rs, unsigned lineA, int colA, unsigned lineB, int colB, u32x2 epoch, int lane) {
  const unsigned off = (lane & 7) * REP_STRIDE + ((lane & 8) ? lineB + colB * 8 : lineA + colA * 8);
  if (lane < 16) st8_sc1(rs, off, 0, epoch);
}
__device__ __forceinline__ unsigned poll_need(rsrc_t rs, unsigned voff, u32x2 epoch, unsigned ready, unsigned need, bool& dead) {
  for (unsigned spins = 0; !dead; ++spins) {
    CLOBBER;
    ready |= (unsigned)__ballot(is_set(ld8_sc1(rs, voff, 0), epoch));
    if ((ready & need) == need) break;
    if (spins > SPIN_CAP) dead = true;
    __builtin_amdgcn_s_sleep(1);
  }
  CLOBBER;
  return dead ? ~0u : ready;
}

__device__ __forceinline__ void poll_all(rsrc_t rs, unsigned voff, u32x2 epoch, bool& dead) {
  for (unsigned spins = 0; !dead; ++spins) {
    CLOBBER;
    if (__all(is_set(ld8_sc1(rs, voff, 0), epoch))) break;
    if (spins > SPIN_CAP) dead = true;
    __builtin_amdgcn_s_sleep(1);
  }
  CLOBBER;
}

constexpr unsigned SC_VF = L_R1 + 32768;
constexpr unsigned SC_PHIK = L_R1 + 49152;
constexpr unsigned SC_XCH = L_R2 + XS;
constexpr unsigned wo_slot(int s) {
  return s < 2 ? L_R2 + (unsigned)(s + 1) * XS : (s < 6 ? L_R1 + (unsigned)(s - 2) * 16384 : (s == 6 ? L_R1 + 81920 : L_R2));
}
__device__ __forceinline__ unsigned wo_src(int s, int j) { return (unsigned)(((JROT(j) >> 1) + NJ / 2 + s) & 7); }
__device__ __forceinline__ unsigned wo_swz(int lane) { return (unsigned)(((lane & 7) ^ ((lane >> 3) & 6)) * 16); }
__device__ __forceinline__ unsigned umax3(unsigned a, unsigned b, unsigned c) { return max(max(a, b), c); }
__device__ __forceinline__ unsigned umax4v(unsigned m, const u32x4& v) { return umax3(umax3(m, v[0], v[1]), v[2], v[3]); }
__device__ __forceinline__ u32x4 nosent(u32x4 v) { return u32x4{min(v[0], SENT - 1), min(v[1], SENT - 1), min(v[2], SENT - 1), min(v[3], SENT - 1)}; }

template <typename P, typename F>
__device__ __forceinline__ void qkv_loop(int isb, int wm, int h, int lane, bool first, P pre, F fin) {
  constexpr unsigned lds0 = 0;
  f32x4 acc[4][6];
  const int fr = lane & 15, fq = lane >> 4;
  unsigned ab[2], bb[2];
  {
    const int r = wm * 64 + fr;
#pragma unroll
    for (int ks = 0; ks < 2; ++ks) ab[ks] = lds0 + L_R2 + r * 128 + (((ks * 4 + fq) ^ ((fr >> 1) & 7)) * 16);
    const int kr = fq * 8 + (fr >> 2);
    const int gs = ((kr >> 1) & 1) | (((kr >> 3) & 1) << 1);
#pragma unroll
    for (int c = 0; c < 2; ++c) bb[c] = lds0 + L_R1 + kr * 128 + (((h * 2 + c) ^ gs) * 32) + (lane & 3) * 8;
  }
#pragma unroll
  for (int m = 0; m < 4; ++m)
#pragma unroll
    for (int n = 0; n < 6; ++n) acc[m][n] = f32x4{0.f, 0.f, 0.f, 0.f};
  bf16x8 af[2][4], bf[2][6];
  auto rds = [&](int ks, int p, int rot = 0) {
#pragma unroll
    for (int m = 0; m < 4; ++m) af[p][m] = lds_ld<bf16x8>(ab[ks] + m * 2048);
#pragma unroll
    for (int nn = 0; nn < 6; ++nn) {
      const int n = (nn + rot) % 6;
      const unsigned a = bb[n & 1] + (n >> 1) * 8192 + ks * 4096;
      bf[p][n] = tr2(a, a + 512);
    }
  };
  auto mms = [&](int p, int rot = 0, int cnt = 6) {
#pragma unroll
    for (int nn = 0; nn < cnt; ++nn)
#pragma unroll
      for (int mm = 0; mm < 4; ++mm) { const int n = (nn + rot) % 6, m = (n & 1) ? 3 - mm : mm; acc[m][n] = n < 4 ? MFMA(bf[p][n], af[p][m], acc[m][n]) : MFMA(af[p][m], bf[p][n], acc[m][n]); }
  };
  auto ilv2 = [&]() {
#pragma unroll
    for (int i = 0; i < 16; ++i) { SGB(0x008, 1); SGB(0x100, 1); }
#pragma unroll
    for (int i = 0; i < 4; ++i) { SGB(0x008, 1); SGB(0x002, 1); }
    SGB(0x008, 4);
  };
  auto ilv3 = [&]() {
#pragma unroll
    for (int i = 0; i < 16; ++i) { SGB(0x008, 1); SGB(0x100, 1); }
#pragma unroll
    for (int i = 0; i < 7; ++i) { SGB(0x008, 1); SGB(0x004, 2); }
    SGB(0x008, 1);
  };
#pragma unroll
  for (int i = 0; i < 24; ++i) OPAQUE_V(acc[i / 6][i % 6]);
  OPAQUE_V(ab[0]); OPAQUE_V(ab[1]); OPAQUE_V(bb[0]); OPAQUE_V(bb[1]);
  if (!first) { WAITV(3); WAITL0; }
  BAR;
  rds(0, 0);
  int sa = 0, sb = 0;
  int da = XS, db = 24576;
  auto step = [&]() {
    ab[0] += da; ab[1] += da;
    bb[0] += db; bb[1] += db;
  };
  auto ring = [&]() {
    sa = (sa == 2) ? 0 : sa + 1;
    sb = (sb == 3) ? 0 : sb + 1;
    da = (sa == 2) ? -2 * (int)XS : (int)XS;
    db = (sb == 3) ? -3 * 24576 : 24576;
  };
#pragma unroll 1
  for (int t = 14; t > 0; --t) {
    SCHED;
    rds(1, 1); mms(0);
    step();
    ilv2();
    SCHED;
    __builtin_amdgcn_s_waitcnt(0x0070);
    BAR;
    SCHED;
    rds(0, 0);
    ring();
    mms(1); ilv3();
  }
  SCHED;
  f32x4 cs[4], nm4[4];
  float nmean[4], csv[2];
  auto fold = [&](int n) {
#pragma unroll
    for (int mm = 0; mm < 4; ++mm) {
      const int m = (n & 1) ? 3 - mm : mm;
#pragma unroll
      for (int e = 0; e < 4; ++e) {
        float t = n < 4 ? __builtin_fmaf(nmean[m], cs[n & 3][e], acc[m][n][e]) : __builtin_fmaf(nm4[m][e], csv[n & 1], acc[m][n][e]);
        asm("" : "+v"(t));
        acc[m][n][e] = t;
      }
    }
  };
  {
    unsigned pm = lds0 + L_MEAN + (16 + wm * 64 + fr) * 4, pc = lds0 + L_CS + (h * 32 + fq * 4) * 4;
    unsigned pm4 = lds0 + L_MEAN + (16 + wm * 64 + fq * 4) * 4, pcv = lds0 + L_CS + (128 + h * 32 + fr) * 4;
    asm("" : "+v"(pm)); asm("" : "+v"(pc)); asm("" : "+v"(pm4)); asm("" : "+v"(pcv));
#pragma unroll
    for (int m = 0; m < 4; ++m) nmean[m] = lds_ld<float>(pm + m * 64);
#pragma unroll
    for (int n = 0; n < 4; ++n) cs[n ^ 2] = lds_ld<f32x4>(pc + (((n ^ 2) >> 1) * 64 + (n & 1) * 16) * 4);
#pragma unroll
    for (int m = 0; m < 4; ++m) nm4[m] = lds_ld<f32x4>(pm4 + m * 64);
#pragma unroll
    for (int n = 0; n < 2; ++n) csv[n] = lds_ld<float>(pcv + n * 64);
    rds(1, 1, 4); mms(0, 2);
    step();
    fold(2); fold(3);
    SGB(0x002, 8);
#pragma unroll
    for (int i = 0; i < 8; ++i) { SGB(0x008, 1); SGB(0x100, 2); }
#pragma unroll
    for (int i = 0; i < 11; ++i) { SGB(0x008, 1); SGB(0x100, 1); SGB(0x002, 2); }
#pragma unroll
    for (int i = 0; i < 5; ++i) { SGB(0x008, 1); SGB(0x002, 3); }
    SCHED;
    __builtin_amdgcn_s_waitcnt(0xC07F);
    BAR;
    SCHED;
    rds(0, 0, 2);
    mms(1, 4); fold(4); fold(5);
#pragma unroll
    for (int i = 0; i < 8; ++i) { SGB(0x008, 1); SGB(0x100, 1); SGB(0x002, 1); }
#pragma unroll
    for (int i = 0; i < 8; ++i) { SGB(0x008, 1); SGB(0x100, 1); SGB(0x002, 2); }
#pragma unroll
    for (int i = 0; i < 8; ++i) { SGB(0x008, 1); SGB(0x002, 2); }
    SCHED;
    pre(0);
    SCHED;
    rds(1, 1, 2);
    mms(0, 2, 4); fold(0); fold(1);
    pre(1);
#pragma unroll
    for (int i = 0; i < 16; ++i) { SGB(0x008, 1); SGB(0x100, 1); SGB(0x002, 2); SGB(0x004, 1); }
  }
  SCHED;
  auto mm6 = [&](int m, int n0 = 0, int n1 = 8) {
#pragma unroll
    for (int nn = n0; nn < n1; ++nn) {
      const int n = nn < 4 ? nn + 2 : nn & 1, p = (nn >> 1) != 2;
      acc[m][n] = n < 4 ? MFMA(bf[p][n], af[p][m], acc[m][n]) : MFMA(af[p][m], bf[p][n], acc[m][n]);
    }
  };
  if (!isb) {
    fin(std::false_type{}, acc, mm6);
#pragma unroll
    for (int i = 0; i < 24; ++i) asm volatile("" : "=v"(acc[i / 6][i % 6]));
#pragma unroll
    for (int i = 0; i < 6; ++i) asm volatile("" : "=v"(bf[1][i]));
#pragma unroll
    for (int i = 0; i < 8; ++i) asm volatile("" : "=v"(af[i >> 2][i & 3]));
    asm volatile("" : "=v"(bf[0][0])); asm volatile("" : "=v"(bf[0][1]));
  }
  OPAQUE_S(isb);
  if (isb) fin(std::true_type{}, acc, mm6);
}
__device__ __forceinline__ u32x4 pack(const f32x4& a, const f32x4& b) { return u32x4{pk2(a[0], a[1]), pk2(a[2], a[3]), pk2(b[0], b[1]), pk2(b[2], b[3])}; }

template <int N> __device__ __forceinline__ void hedgehog(const f32x4 (&r)[4][2], const float (&c)[4], float (&ep)[4][8], float (&en)[4][8]) {
  float mx[4], nm[4];
#pragma unroll
  for (int m = 0; m < N; ++m) {
    const f32x4 a = r[m][0], b = r[m][1];
    mx[m] = vmax2(fmaxf(fmaxf(a[3], b[0]), b[1]), fmaxf(fmaxf(fmaxf(fmaxf(a[0], a[1]), a[2]), b[2]), b[3]));
    nm[m] = vmax2(fmaxf(fmaxf(-a[3], -b[0]), -b[1]), fmaxf(fmaxf(fmaxf(fmaxf(-a[0], -a[1]), -a[2]), -b[2]), -b[3]));
  }
  SCHED;
  red4<true>(mx[0], mx[1], nm[0], nm[1], [](float x) { return x; });
  if (N == 4) red4<true>(mx[2], mx[3], nm[2], nm[3], [](float x) { return x; });
#pragma unroll
  for (int m = 0; m < N; ++m) { mx[m] *= -c[m]; nm[m] *= -c[m]; }
  SCHED;
#pragma unroll
  for (int m = 0; m < N; ++m) {
#pragma unroll
    for (int i = 0; i < 8; ++i) { ep[m][i] = __builtin_fmaf(r[m][i >> 2][i & 3], c[m], mx[m]); en[m][i] = __builtin_fmaf(r[m][i >> 2][i & 3], -c[m], nm[m]); }
    SCHED;
#pragma unroll
    for (int i = 0; i < 8; ++i) { ep[m][i] = __builtin_amdgcn_exp2f(ep[m][i]); en[m][i] = __builtin_amdgcn_exp2f(en[m][i]); }
    SCHED;
  }
}
__device__ __forceinline__ u32x4 pack8(const float (&e)[8], float f) {
  return u32x4{pk2(e[0] * f, e[1] * f), pk2(e[2] * f, e[3] * f), pk2(e[4] * f, e[5] * f), pk2(e[6] * f, e[7] * f)};
}
__device__ __forceinline__ u32x4 pack8(const float (&e)[8]) { return u32x4{pk2(e[0], e[1]), pk2(e[2], e[3]), pk2(e[4], e[5]), pk2(e[6], e[7])}; }

constexpr u64 window_mask(int t, int e) {
  u64 mk = 0;
  for (int ln = 0; ln < 64; ++ln) {
    const int delta = (ln & 15) + 16 - t * 16 - (ln >> 4) * 4 - e;
    if (delta >= 0 && delta <= 14 && !(delta & 1)) mk |= 1ull << ln;
  }
  return mk;
}
__device__ __forceinline__ void phik2(const f32x4 (&r)[4][2], const float (&c)[4], unsigned pw) {
  const f32x4 zero = {0.f, 0.f, 0.f, 0.f};
  const bf16x8 ones = {0x3f80, 0x3f80, 0x3f80, 0x3f80, 0x3f80, 0x3f80, 0x3f80, 0x3f80};
  float ep[4][8], en[4][8];
  f32x4 sp[2], sn[2];
  hedgehog<2>(r, c, ep, en);
#pragma unroll
  for (int m = 0; m < 2; ++m) { sp[m] = MFMA(ones, as_frag(pack8(ep[m])), zero); sn[m] = MFMA(ones, as_frag(pack8(en[m])), zero); }
#pragma unroll
  for (int m = 0; m < 2; ++m) { asm("" : "+v"(sp[m])); asm("" : "+v"(sn[m])); }
  SCHED;
#pragma unroll
  for (int m = 0; m < 2; ++m) {
    const float fp = __builtin_amdgcn_rcpf(sp[m][0]), fn = __builtin_amdgcn_rcpf(sn[m][0]);
#pragma unroll
    for (int i = 0; i < 8; ++i) { ep[m][i] *= fp; en[m][i] *= fn; }
    SCHED;
    const u32x4 pp = pack8(ep[m]), pn = pack8(en[m]);
    const unsigned d = pw + m * (16 * 160);
    lds_st<u32x2>(d, u32x2{pp[0], pp[1]});
    lds_st<u32x2>(d + 32, u32x2{pp[2], pp[3]});
    lds_st<u32x2>(d + 64, u32x2{pn[0], pn[1]});
    lds_st<u32x2>(d + 96, u32x2{pn[2], pn[3]});
    SCHED;
  }
}
__device__ __forceinline__ bf16x8 rowsum_sel(int lane) {
  const int fr = lane & 15;
  const unsigned one = (fr < 8) ? (0x3f80u << (16 * (fr & 1))) : 0u;
  const int e = (fr >> 1) & 3;
  return as_frag(u32x4{e == 0 ? one : 0u, e == 1 ? one : 0u, e == 2 ? one : 0u, e == 3 ? one : 0u});
}
__device__ __forceinline__ f32x4 rowsum_fin(f32x4 c) {
#pragma unroll
  for (int k = 0; k < 4; ++k) {
    const float a = c[k];
    c[k] = a + __int_as_float(__builtin_amdgcn_update_dpp(0, __float_as_int(a), 0x128  , 0xf, 0xf, false));
  }
  return c;
}
__device__ __forceinline__ void job_colsum(rsrc_t rs, const u32x4 (&w)[8], int mat, int g, int jc, int hw, int lane) {
  const bf16x8 sel = rowsum_sel(lane);
  f32x4 c = {0.f, 0.f, 0.f, 0.f};
#pragma unroll
  for (int i = 0; i < 8; ++i) c = MFMA(sel, as_frag(w[i]), c);
  const u32x4 w4 = __builtin_bit_cast(u32x4, rowsum_fin(c));
  unsigned so = WS_CS + (unsigned)(((mat * 16 + jc) * 16 + g) * 768);
  OPAQUE_S(so);
  if ((lane & 40) == 0) st16_sc1(rs, (unsigned)(hw * 64 + (lane & 7) * 8 + (lane >> 4) * 4) * 4, so, w4);
}
__device__ __forceinline__ unsigned img_off(int i, int lane) { return (lane >> 3) * 128 + (((lane & 7) ^ ((((lane >> 4) & 1) | ((i & 1) << 1)) << 1)) * 16); }
__device__ __forceinline__ void job_pack(rsrc_t rs, u32x4 w, int i, int mat, int g, int jc, int hw, int lane) {
  st16_sc1(rs, img_off(i, lane),
           WS_WP + (unsigned)(((mat * 16 + jc) * 16 + g) * 24576 + hw * 8192 + i * 1024), w);
}

__global__ __launch_bounds__(512, 2) void mega_kernel(Params p) {
  extern __shared__ __attribute__((aligned(16))) char smem[];
  const int tid = threadIdx.x;
  const int wave0 = __builtin_amdgcn_readfirstlane(tid >> 6);
  const int xx = blockIdx.x & 7, rr = blockIdx.x >> 3;
  const int g0 = (rr / NJ) * NX + (xx & (NX - 1)), j0 = (xx / NX) * NJ + (rr & (NJ - 1));
  constexpr unsigned lds0 = 0, lds00 = 0;
  if ((unsigned)(size_t)(LAS char*)smem != 0u) return;
#define LOCALS0 int g = g0, j = j0, l = l0, wave = wave0;                                              \
  OPAQUE_S(g); OPAQUE_S(j); OPAQUE_S(l); OPAQUE_S(wave);                                               \
  const int hw = wave & 3, wm = wave & 1, h = (wave >> 1) & 1;                                         \
  const unsigned myrep = (unsigned)(JROT(j) / 2 + (g & (NX - 1))) * REP_STRIDE;                              \
  (void)hw; (void)wm; (void)h; (void)myrep;                                                            \
  int lane = tid; OPAQUE_V(lane); lane &= 63
#define LOCALS LOCALS0; const bool isb = l & 1, lead = (wave == 4); const int half = wave >> 2; (void)isb; (void)lead; (void)half
  const rsrc_t rs = mk_rsrc(p.ws, WS_SPAN);
  const bool cw = wave0 < 4;
  if (wave0 == 3) {
    const char* pc;
    unsigned len;
    asm volatile("s_getpc_b64 %0\n.Lpf_base:\n\ts_mov_b32 %1, .Lpf_end-.Lpf_base" : "=s"(pc), "=s"(len));
    const unsigned off = ((unsigned)rr * 16 + (tid & 15)) * 128;
    touch(mk_rsrc(pc, len), ((int)len > 0 && off + 4 <= len) ? off : 0u, lds00 + L_SINK);
  }
  auto slowbit = [](const void* q) { return 1u << ((3 - ((unsigned)(size_t)q >> 7)) & 7); };
  unsigned slowm = slowbit(p.wqkvA) | (slowbit(p.wqkvB) << 8) | (slowbit(p.woA) << 16) | (slowbit(p.woB) << 24);
  OPAQUE_S(slowm);
  u32x2 epoch = {0u, 0u};
  bool dead = false;
      if (cw) {
        const int l0 = 0;
        LOCALS;
        u32x4 v[2][8];
        if (wave < 3) {
          const int jc = JOBC(j, wave), kt = (KB0(g, j) + (unsigned)g / NX) & 15;
          const rsrc_t ra = mk_rsrc(p.wqkvA, 1024 * 6144);
#pragma unroll
          for (int i = 0; i < 8; ++i)
            v[0][i] = __builtin_amdgcn_raw_buffer_load_b128(ra, (lane >> 3) * 6144 + (lane & 7) * 16, (kt * 64 + i * 8) * 6144 + wave * 2048 + jc * 128, 0);
          SCHED;
          BAR; SCHED;
          job_colsum(rs, v[0], 0, g, jc, wave, lane);
          if (SLOWC(0, jc)) {
#pragma unroll
            for (int i = 0; i < 8; ++i) job_pack(rs, v[0][i], i, 0, kt, jc, wave, lane);
          }
        } else {
          const int fr = lane & 15, fq = lane >> 4;
          const rsrc_t rx = mk_rsrc(p.x, 2048 * 2048);
#pragma unroll
          for (int i = 0; i < 16; ++i)
            v[i & 1][i >> 1] = __builtin_amdgcn_raw_buffer_load_b128(rx, fr * 2048 + fq * 16, (g * 128 + (j & (NJ - 1)) * (128 / NJ) + ((i >> 1) % (8 / NJ)) * 16) * 2048 + ((KB0(g, j) + (i >> 1) / (8 / NJ)) & 15) * 128 + (i & 1) * 64, 0);
          SCHED;
          BAR; SCHED;
          const f32x4 zero = {0.f, 0.f, 0.f, 0.f};
          const bf16x8 ones = {0x3f80, 0x3f80, 0x3f80, 0x3f80, 0x3f80, 0x3f80, 0x3f80, 0x3f80};
          float sm[8], q[8];
#pragma unroll
          for (int m = 0; m < 8; ++m) {
            const f32x4 a = MFMA(ones, as_frag(v[1][m]), MFMA(ones, as_frag(v[0][m]), zero));
            const f32x4 b = MFMA(as_frag(v[1][m]), as_frag(v[1][m]), MFMA(as_frag(v[0][m]), as_frag(v[0][m]), zero));
            sm[m] = a[0];
            q[m] = (fr & 2) ? ((fr & 1) ? b[3] : b[2]) : ((fr & 1) ? b[1] : b[0]);
          }
          if (fq == (fr >> 2)) {
#pragma unroll
            for (int m = 0; m < 8; ++m) st8_sc1(rs, ((m % (8 / NJ)) * 16 + fr) * 8 + (m / (8 / NJ)) * 1024, st_off(g, 0) + JROT(j) * 1024 + (j & (NJ - 1)) * (1024 / NJ), u32x2{__float_as_uint(sm[m]), __float_as_uint(q[m])});
          }
        }
      }
  const char *pA = (const char*)p.x, *pW = (const char*)p.wqkvA;
  unsigned zA = 2048 * 2048, bA = g0 * (128 * 2048), kA = 128, pcA = 32 * 2048, vA;
  unsigned zW = 1024 * 6144, bW = j0 * 128, kW = 64 * 6144, pcW = 2048, hW = 32 * 6144, vW;
  int kb = KB0(g0, j0), dj = -1;
  {
    const int lane = tid & 63, hw = wave0 & 3;
    vA = (hw * 8 + (lane >> 3)) * 2048 + (((lane & 7) ^ (((hw & 1) * 4 + (lane >> 4)) & 7)) * 16);
    vW = (hw * 8 + (lane >> 3)) * 6144 + (((lane & 7) ^ ((((lane >> 4) & 1) | ((hw & 1) << 1)) << 1)) * 16);
  }

#pragma unroll 1
  for (int l0 = 0; l0 < 4; ++l0) {
    f32x4 acc[2][4];
    auto kvpart = [&](unsigned va, unsigned pa, unsigned kvb, int lane, int wave) {
      const f32x4 zero = {0.f, 0.f, 0.f, 0.f};
      const bf16x8 ones = {0x3f80, 0x3f80, 0x3f80, 0x3f80, 0x3f80, 0x3f80, 0x3f80, 0x3f80};
      f32x4 c0 = zero, c1 = zero, z0 = zero, z1 = zero;
#pragma unroll
      for (int ks = 0; ks < 4; ++ks) {
        const bf16x8 v = lds_ld<bf16x8>(va + ks * 2048);
        const bf16x8 a0 = tr2(pa + ks * 32 * 160, pa + (ks * 32 + 16) * 160);
        const bf16x8 a1 = tr2(pa + 32 + ks * 32 * 160, pa + 32 + (ks * 32 + 16) * 160);
        c0 = MFMA(a0, v, c0);
        c1 = MFMA(a1, v, c1);
        z0 = MFMA(a0, ones, z0);
        z1 = MFMA(a1, ones, z1);
      }
      st16_sc1(rs, lane * 16, kvb + (unsigned)wave * 1024, nosent(pack(c0, c1)));
      if (!(wave & 2) && (lane & 15) == 0) st16_sc1(rs, lane, kvb + KVP_Z + (wave >> 2) * 128 + (wave & 1) * 64, nosent(pack(z0, z1)));
    };
#define KV_ADDR                                                                                                            \
    unsigned va = lds0 + SC_VF + (wave >> 2) * 8192 + ((wave >> 1) & 1) * 1024 + lane * 16;                                \
    unsigned pa = lds0 + SC_PHIK + ((wave >> 2) * 128 + fq * 4 + (fr >> 2)) * 160 + (wave & 1) * 64 + (lane & 3) * 8;      \
    unsigned kvj = WS_KVP + (unsigned)j * KVP_J + (unsigned)(l >> 1) * KVP_L;                                              \
    OPAQUE_V(va); OPAQUE_V(pa); OPAQUE_S(kvj)
    if (cw) {
      LOCALS;
      unsigned otile, kvo, l16, pr1, pr4;
      int g2, j2, l2, ln2;
      qkv_loop(l & 1, wm, h, lane, l == 0, [&](int st) {
        if (st == 0) {
          g2 = g0; j2 = j0; l2 = l0; ln2 = tid;
          OPAQUE_S(g2); OPAQUE_S(j2); OPAQUE_S(l2); OPAQUE_V(ln2);
          return;
        }
        otile = ob_off(g2, j2, l2) + (unsigned)(wm * 8 + h) * 1024;
        kvo = WS_KVP + (unsigned)j2 * KVP_J + (unsigned)g2 * KVP_G + (unsigned)(l2 >> 1) * KVP_L;
        l16 = (ln2 & 63) * 16;
        pr1 = lds0 + L_RSTD + (16 + wm * 64 + (ln2 & 15)) * 4;
        pr4 = lds0 + L_RSTD + wm * 256 + (ln2 & 48);
        asm("" : "+s"(otile)); asm("" : "+s"(kvo)); asm("" : "+v"(l16)); asm("" : "+v"(pr1)); asm("" : "+v"(pr4));
      }, [&](auto isb_c, f32x4 (&a)[4][6], auto& mm6) {
        const f32x4 zero = {0.f, 0.f, 0.f, 0.f};
        constexpr float L2E = 1.4426950408889634f;
        float rq[4];
#define ilv8(nv) do { _Pragma("unroll") for (int i_ = 0; i_ < 8; ++i_) { SGB(0x008, 1); SGB(0x100, 2); SGB(0x002, nv); } SCHED; } while (0)
        if constexpr (decltype(isb_c)::value) {
          constexpr float QS = 0.17677669529663687f * L2E, NEG = -3.0e38f;
          const bf16x8 ones = {0x3f80, 0x3f80, 0x3f80, 0x3f80, 0x3f80, 0x3f80, 0x3f80, 0x3f80};
          u32x4 kf[5], vf[5], qf[4], pf[4];
          f32x4 rs4[4], bias[2], s0[4], s1[4], dn[4];
          float x[4][4], mx[4];
          unsigned psel0, psel1;
          auto pkkv = [&](int m) { kf[m + 1] = pack(a[m][2] * rq[m], a[m][3] * rq[m]); vf[m + 1] = pack(a[m][4] * rs4[m], a[m][5] * rs4[m]); };
          auto pkq = [&](int m) { qf[m] = pack(a[m][0], a[m][1]); };
          auto st1 = [&](int m) {
            x[m][0] = vmax2(s0[m][0], s0[m][1]); x[m][1] = vmax2(s0[m][2], s0[m][3]); x[m][2] = vmax2(s1[m][0], s1[m][1]); x[m][3] = vmax2(s1[m][2], s1[m][3]);
            mx[m] = vmax2(vmax2(x[m][0], x[m][1]), vmax2(x[m][2], x[m][3]));
          };
          auto st2 = [&](int m) {
            const float mxl = mx[m] * -rq[m];
#pragma unroll
            for (int e = 0; e < 4; ++e) x[m][e] = __builtin_fmaf(x[m][e], rq[m], mxl);
          };
          auto st3 = [&](int m) {
#pragma unroll
            for (int e = 0; e < 4; ++e) x[m][e] = __builtin_amdgcn_exp2f(x[m][e]);
          };
          auto st5 = [&](int m) {
            const unsigned p0 = pk2(x[m][0], x[m][1]), p1 = pk2(x[m][2], x[m][3]);
            pf[m] = u32x4{__builtin_amdgcn_perm(0u, p0, psel0), __builtin_amdgcn_perm(0u, p0, psel1), __builtin_amdgcn_perm(0u, p1, psel0), __builtin_amdgcn_perm(0u, p1, psel1)};
          };
          auto st6 = [&](int m) {
            dn[m] = MFMA(ones, as_frag(pf[m]), zero);
            s0[m] = MFMA(as_frag(u32x4{vf[m][0], vf[m][1], vf[m + 1][0], vf[m + 1][1]}), as_frag(pf[m]), zero);
            s1[m] = MFMA(as_frag(u32x4{vf[m][2], vf[m][3], vf[m + 1][2], vf[m + 1][3]}), as_frag(pf[m]), zero);
            asm("" : "+v"(dn[m]));
          };
          auto st7 = [&](int m) { const float inv = __builtin_amdgcn_rcpf(dn[m][0]); pf[m] = nosent(pack(s0[m] * inv, s1[m] * inv)); };
          mm6(3);
#pragma unroll
          for (int m = 0; m < 4; ++m) rq[m] = lds_ld<float>(pr1 + m * 64);
#pragma unroll
          for (int m = 3; m >= 0; --m) rs4[m] = lds_ld<f32x4>(pr4 + 64 + m * 64);
          unsigned pv_ = l16 + lds0 + L_PREV + h * 2048;
          asm("" : "+v"(pv_));
          static_for<0, 8>([&](auto i_c) __attribute__((always_inline)) {
            constexpr int i = decltype(i_c)::value;
            constexpr u64 mk = window_mask(i >> 2, i & 3);
            bias[i >> 2][i & 3] = __builtin_amdgcn_inverse_ballot_w64(mk) ? 0.f : NEG;
          });
          asm("" : "+v"(bias[0])); asm("" : "+v"(bias[1]));
          const unsigned hso = kvo + KVP_HALO + h * 2048, hsi = hso - (g > 0 ? KVP_G : 0u);
          ilv8(2);
          mm6(2);
          pkkv(3);
          ilv8(2);
          OPAQUE_V(kf[4]); OPAQUE_V(vf[4]);
          if (wm) {
            st16_sc1(rs, l16, hso, nosent(kf[4]));
            st16_sc1(rs, l16, hso + 1024, nosent(vf[4]));
          } else {
            lds_st<u32x4>(pv_, kf[4]);
            lds_st<u32x4>(pv_ + 1024, vf[4]);
          }
          SCHED;
          mm6(1);
          pkq(3); pkkv(2);
          ilv8(3);
          WAITL0;
          BAR;
          SCHED;
          mm6(0);
          pkq(2); pkkv(1);
          ilv8(3);
          s0[3] = MFMA(as_frag(kf[3]), as_frag(qf[3]), bias[0]); s1[3] = MFMA(as_frag(kf[4]), as_frag(qf[3]), bias[1]);
          s0[2] = MFMA(as_frag(kf[2]), as_frag(qf[2]), bias[0]); s1[2] = MFMA(as_frag(kf[3]), as_frag(qf[2]), bias[1]);
          pkq(1); pkkv(0);
#pragma unroll
          for (int i = 0; i < 4; ++i) { SGB(0x008, 1); SGB(0x002, 5); }
          SCHED;
          const bool halo = wm == 0 && g > 0;
          const u32x4 hk = ld16_sc1(rs, l16, hsi), hv = ld16_sc1(rs, l16, hsi + 1024);
          SCHED;
          s0[1] = MFMA(as_frag(kf[1]), as_frag(qf[1]), bias[0]); s1[1] = MFMA(as_frag(kf[2]), as_frag(qf[1]), bias[1]);
          st1(3); st1(2);
#pragma unroll
          for (int m = 0; m < 4; ++m) rq[m] *= QS;
          SCHED;
          st1(1);
          pkq(0);
          psel0 = __builtin_amdgcn_inverse_ballot_w64(0xaaaaaaaaaaaaaaaaull) ? 0x01000c0cu : 0x0c0c0100u;
          psel1 = __builtin_amdgcn_inverse_ballot_w64(0xaaaaaaaaaaaaaaaaull) ? 0x03020c0cu : 0x0c0c0302u;
          { float d = mx[1]; red4<true>(mx[3], mx[2], mx[1], d, [](float v) { return v; }); }
          s1[0] = MFMA(as_frag(kf[1]), as_frag(qf[0]), bias[1]);
          SCHED;
          st2(3); st2(2); st2(1);
          SCHED;
          st3(3); st3(2); st3(1);
          SCHED;
          st5(3); st6(3); st5(2); st6(2); st5(1); st6(1);
          SCHED;
          u32x4 pk = lds_ld<u32x4>(pv_), pv = lds_ld<u32x4>(pv_ + 1024);
          st7(3); st7(2); st7(1);
          SCHED;
#pragma unroll
          for (int m = 3; m > 0; --m) st16_sc1(rs, l16, otile + m * 2048, pf[m]);
          if (!wm && g == 0) bias[0] = f32x4{NEG, NEG, NEG, NEG};
          SCHED;
          if (halo) {
            pk = hk; pv = hv;
            for (unsigned spins = 0; !dead && __any(umax4v(umax4v(0u, pk), pv) == SENT); ++spins) {
              if (spins > SPIN_CAP) { dead = true; break; }
              CLOBBER;
              pk = ld16_sc1(rs, l16, hsi); pv = ld16_sc1(rs, l16, hsi + 1024);
              OPAQUE_V(pk); OPAQUE_V(pv);
            }
          }
          vf[0] = pv;
          s0[0] = MFMA(as_frag(pk), as_frag(qf[0]), bias[0]);
          st1(0);
          mx[0] = red_max4(mx[0]);
          st2(0); st3(0); st5(0); st6(0); st7(0);
          st16_sc1(rs, l16, otile, pf[0]);
        } else {
          LOCALS;
          const int fr = lane & 15, fq = lane >> 4;
          f32x4 rs4[4], r[4][2];
          const bf16x8 ones = {0x3f80, 0x3f80, 0x3f80, 0x3f80, 0x3f80, 0x3f80, 0x3f80, 0x3f80};
          auto proj = [&](int m, int n, const bf16x8& w0, const bf16x8& w1) {
            const u32x4 f = pack(a[m][n], a[m][n + 1]);
            r[m][0] = MFMA(w0, as_frag(f), zero); r[m][1] = MFMA(w1, as_frag(f), zero);
          };
          mm6(3, 0, 2); mm6(2, 0, 2); mm6(1, 0, 2); mm6(0, 0, 2);
          unsigned wa = lds0 + L_WPHI + h * 2048 + (fq * 4 + (fr >> 2)) * 64 + (lane & 3) * 8, kw = lds0 + L_PREV + hw * 2048 + l16;
          asm("" : "+v"(wa)); asm("" : "+v"(kw));
#pragma unroll
          for (int m = 0; m < 4; ++m) rq[m] = lds_ld<float>(pr1 + m * 64);
#pragma unroll
          for (int m = 0; m < 4; ++m) rs4[m] = lds_ld<f32x4>(pr4 + 64 + m * 64);
          const bf16x8 w0 = tr2(wa, wa + 1024);
          const bf16x8 w1 = tr2(wa + 32, wa + 32 + 1024);
#pragma unroll
          for (int i = 0; i < 8; ++i) { SGB(0x008, 1); SGB(0x100, 2); SGB(0x002, 2); }
          SCHED;
          mm6(3, 2, 4); mm6(2, 2, 4);
          lds_st<u32x4>(kw + 1024, pack(a[3][2], a[3][3]));
          lds_st<u32x4>(kw, pack(a[2][2], a[2][3]));
#pragma unroll
          for (int i = 0; i < 2; ++i) { SGB(0x008, 1); SGB(0x002, 2); SGB(0x008, 1); SGB(0x002, 2); SGB(0x200, 1); }
          SCHED;
          mm6(1, 2, 4); mm6(0, 2, 4);
          proj(0, 2, w0, w1); proj(1, 2, w0, w1);
#pragma unroll
          for (int i = 0; i < 4; ++i) { SGB(0x008, 1); SGB(0x002, 2); }
          SCHED;
          WAITL0;
          BAR;
          SCHED;
          unsigned pw = lds0 + SC_PHIK + (h * 128 + wm * 64 + fr) * 160 + fq * 8, vw = lds0 + SC_VF + h * 8192 + wm * 4096 + l16;
          asm("" : "+v"(pw)); asm("" : "+v"(vw));
#pragma unroll
          for (int m = 0; m < 4; ++m) mm6(m, 4, 8);
#pragma unroll
          for (int m = 0; m < 4; ++m) rq[m] *= L2E;
#pragma unroll
          for (int i = 0; i < 4; ++i)
            lds_st<u32x4>(vw + i * 1024, pack(a[i & 2][4 + (i & 1)] * rs4[i & 2], a[(i & 2) + 1][4 + (i & 1)] * rs4[(i & 2) + 1]));
#pragma unroll
          for (int i = 0; i < 16; ++i) { SGB(0x008, 1); SGB(0x002, 3); }
          SCHED;
          phik2(r, rq, pw);
          KV_ADDR;
          WAITL0;
          BAR;
          kvpart(va, pa, kvo, lane, wave);
          CLOBBER;
          BAR;
          u32x4 qp[4], qn[4];
          float sqp[4], sqn[4];
#pragma unroll
          for (int m = 0; m < 4; ++m) proj(m, 0, w0, w1);
          SCHED;
          float ep[4][8], en[4][8];
          hedgehog<4>(r, rq, ep, en);
          const unsigned zo = (lane >> 3) * KVP_G + (lane & 7) * 16, zs = kvj + KVP_Z + h * 128;
          u32x4 zv[2] = {ld16_sc1(rs, zo, zs), ld16_sc1(rs, zo, zs + 8 * KVP_G)};
          SCHED;
#pragma unroll
          for (int m = 0; m < 4; ++m) {
            qp[m] = pack8(ep[m]); qn[m] = pack8(en[m]);
            sqp[m] = MFMA(ones, as_frag(qp[m]), zero)[0];
            sqn[m] = MFMA(ones, as_frag(qn[m]), zero)[0];
          }
          SCHED;
#pragma unroll
          for (int m = 0; m < 4; ++m) { sqp[m] = __builtin_amdgcn_rcpf(sqp[m]); sqn[m] = __builtin_amdgcn_rcpf(sqn[m]); }
#pragma unroll
          for (int m = 0; m < 4; ++m) { OPAQUE_V(qp[m]); OPAQUE_V(qn[m]); OPAQUE_V(sqp[m]); OPAQUE_V(sqn[m]); }
          unsigned ko = (lane >> 5) * KVP_G + (lane & 31) * 16, ks = kvj + (unsigned)((j ^ RX) - j) * KVP_J + KVP_SUM + h * (8 * KVP_G);
          bf16x8 sel = rowsum_sel(lane);
          OPAQUE_V(ko); OPAQUE_S(ks); OPAQUE_V(sel);
          for (unsigned spins = 0; !dead && __any(umax4v(umax4v(0u, zv[0]), zv[1]) == SENT); ++spins) {
            if (spins > SPIN_CAP) { dead = true; break; }
            CLOBBER;
            zv[0] = ld16_sc1(rs, zo, zs); zv[1] = ld16_sc1(rs, zo, zs + 8 * KVP_G);
          }
          {
            const f32x4 z = rowsum_fin(MFMA(sel, as_frag(zv[1]), MFMA(sel, as_frag(zv[0]), zero)));
            if ((lane & 40) == 0) lds_st<u32x2>(lds0 + L_ZF + hw * 128 + fr * 16 + fq * 8, u32x2{pk2(z[0], z[1]), pk2(z[2], z[3])});
            WAITL0;
            const bf16x8 zf0 = lds_ld<bf16x8>(lds0 + L_ZF + hw * 128 + fq * 16), zf1 = lds_ld<bf16x8>(lds0 + L_ZF + hw * 128 + 64 + fq * 16);
            float dp[4], dn[4];
#pragma unroll
            for (int m = 0; m < 4; ++m) { dp[m] = MFMA(zf0, as_frag(qp[m]), zero)[0]; dn[m] = MFMA(zf1, as_frag(qn[m]), zero)[0]; }
            SCHED;
#pragma unroll
            for (int m = 0; m < 4; ++m) {
              const float inv = __builtin_amdgcn_rcpf(__builtin_fmaf(dp[m], sqp[m], __builtin_fmaf(dn[m], sqn[m], 1e-6f)));
              sqp[m] *= inv; sqn[m] *= inv;
              OPAQUE_V(sqp[m]); OPAQUE_V(sqn[m]);
            }
          }
          BAR;
          __builtin_amdgcn_s_sleep(H2D);
          u32x4 kv[4];
#pragma unroll
          for (int i = 0; i < 4; ++i) kv[i] = ld16_sc1(rs, ko, ks + i * (2 * KVP_G));
          SCHED;
#pragma unroll
          for (int m = 0; m < 4; ++m) { qp[m] = pack8(ep[m], sqp[m]); qn[m] = pack8(en[m], sqn[m]); }
#pragma unroll
          for (int m = 0; m < 4; ++m) { OPAQUE_V(qp[m]); OPAQUE_V(qn[m]); }
          SCHED;
          for (unsigned spins = 0; !dead && __any(max(umax4v(umax4v(0u, kv[0]), kv[1]), umax4v(umax4v(0u, kv[2]), kv[3])) == SENT); ++spins) {
            if (spins > SPIN_CAP) { dead = true; break; }
            CLOBBER;
#pragma unroll
            for (int i = 0; i < 4; ++i) kv[i] = ld16_sc1(rs, ko, ks + i * (2 * KVP_G));
          }
          f32x4 o[4][2];
#pragma unroll
          for (int m = 0; m < 4; ++m)
#pragma unroll
            for (int i = 0; i < 2; ++i) o[m][i] = MFMA(as_frag(kv[2 * i + 1]), as_frag(qn[m]), MFMA(as_frag(kv[2 * i]), as_frag(qp[m]), zero));
#pragma unroll
          for (int m = 0; m < 4; ++m) st16_sc1(rs, l16, otile + m * 2048, nosent(pack(o[m][0], o[m][1])));
        }
      });
#pragma unroll
      for (int i = 0; i < 8; ++i) asm volatile("" : "=v"(acc[i >> 2][i & 3]));
    } else {
      __builtin_amdgcn_s_setprio(3);
      if (l0) {
        WAITV(3);
        WAITL0;
        BAR;
      }
      LOCALS;
      u32x4 xr[2][2];
      const unsigned lin = lane * 16, vlin = lin + hw * 1024;
      const int jr = j & ~3;
      u32x4 sent4 = {SENT, SENT, SENT, SENT};
      OPAQUE_V(sent4);
      const unsigned cb0 = ob_off(g, j, l), cb2 = xb_off(g, l) + j * TS;
      const unsigned cb1 = WS_KVP + (unsigned)j * KVP_J + (unsigned)g * KVP_G + (unsigned)(l >> 1) * KVP_L;
      unsigned ra0;
      int tr = 0;
      auto ldres = [&](int t) {
        if (tr == t) {
#pragma unroll
          for (int i = 0; i < 4; ++i) xr[i >> 1][i & 1] = lds_ld<u32x4>((ra0 ^ ((i & 1) * 64)) + (t % 3) * XS + ((i >> 1) ^ 1) * 2048);
        }
      };
      auto aslot = [&](int t) { return lds0 + L_R2 + (t % 3) * XS + hw * 1024; };
      auto wslot = [&](int t) { return lds0 + L_R1 + (t & 3) * 24576 + hw * 1024; };
      const bool l0f = l == 0;
      const rsrc_t rA = mk_rsrc(pA, zA);
      auto kseq = [&](int t) { return (kb + t - (t <= dj ? 1 : 0)) & 15; };
      auto offA = [&](int t) { return bA + (unsigned)kseq(t) * kA; };
      auto offW = [&](int t) { return bW + (unsigned)kseq(t) * kW; };
      auto ldA = [&](int t) { dma4<4096>(rA, vA, offA(t), pcA, aslot(t)); };
      auto ldWh = [&](int t, unsigned o, int hf) { dma3<8192>(mk_rsrc(pW, zW), vW, o + (hf ? hW : 0u), pcW, wslot(t) + hf * 4096); };
      const bool slowj = l0f && SLOWC(0, j);
      auto packedW = [&]() {
        pW = p.ws; zW = WS_SPAN; bW = WS_WP + (unsigned)j * (16 * 24576); kW = 24576; pcW = 8192; hW = 4096; vW = vlin;
      };
      auto ldW = [&](int t) { const unsigned o = offW(t); ldWh(t, o, 0); ldWh(t, o, 1); };
      constexpr auto spread = [](int n) { unsigned m = 0; for (int t = 0; t < 26; ++t) if ((t + 1) * n / 13 > t * n / 13) m |= 1u << t; return m; };
      constexpr unsigned sp4 = spread(4), sp8 = spread(8), sp12 = spread(12);
      const unsigned smask = (isb ? (l == 3 ? sp4 : sp8) : sp12) >> ((((g + j * 5) & 15) * 13) >> 4);
      const unsigned cbm = isb ? cb2 : cb1;
      auto clean = [&](int t) __attribute__((always_inline)) {
        if (t < 13 && ((smask >> t) & 1)) {
          const unsigned cc = __builtin_popcount(smask & ((1u << t) - 1));
          const unsigned cb = cb0 + (cc >= 4 ? cbm - cb0 : 0u) + (cc >= 8 ? cb2 - cbm : 0u), pc = (cc + g + j) & 3;
          st16_sc1(rs, (lane >> 4) * 4096 + (lane & 15) * 16, cb + hw * 1024 + pc * 256, sent4);
        }
      };
      unsigned flo;
      unsigned ready = ~0u;
      u32x4 cv[4];
#pragma unroll
      for (int ii = 0; ii < 4; ++ii) asm volatile("" : "=v"(cv[ii]));
      auto chkrd = [&](int t) {
#pragma unroll
        for (int ii = 0; ii < 4; ++ii) cv[ii] = lds_ld<u32x4>(aslot(t) + lin + ii * 4096);
      };
      auto chkbad = [&]() { return __any(max(umax4v(umax4v(0u, cv[0]), cv[1]), umax4v(umax4v(0u, cv[2]), cv[3])) == SENT) && !l0f; };
      auto refetch = [&](int t) __attribute__((always_inline)) {
        for (unsigned spins = 0; !dead; ++spins) {
          if (spins > SPIN_CAP) { dead = true; break; }
          WAITV(0);
          ldA(t);
          ldA(t + 1);
          WAITV(0);
          chkrd(t);
          if (!chkbad()) break;
          __builtin_amdgcn_s_sleep(1);
        }
      };
      if (l0f) {
        u32x2 gw2;
        ld8_asm(gw2, rs, WS_EPOCH + 0u * lin);
        BAR;
        ldW(0); ldA(0);
        dma1(mk_rsrc(p.wphi, 32 * 2048), vlin, j * 4096, lds0 + L_WPHI + hw * 1024);
        ldW(1);
        WAITV(17);
        OPAQUE_V(gw2);
        const u64 gword = ((u64)(unsigned)UNI(gw2[1]) << 32) | (unsigned)UNI(gw2[0]);
        u64 z = gword + 0x9e3779b97f4a7c15ull;
        z = (z ^ (z >> 30)) * 0xbf58476d1ce4e5b9ull;
        z = (z ^ (z >> 27)) * 0x94d049bb133111ebull;
        z = z ^ (z >> 31);
        z = (z == gword) ? ~z : z;
        epoch = u32x2{(unsigned)z, (unsigned)(z >> 32)};
        tr = (j - kb) & 15;
        WAITV(7);
        BAR;
        ldA(1);
      } else {
        ldW(1);
        __builtin_amdgcn_s_sleep(XD);
        ldA(1);
      }
      ldA(2);
      ldW(2);
#pragma unroll
      for (int i = 0; i < 4; ++i) asm volatile("" : "=v"(xr[i >> 1][i & 1]));
      unsigned oA = offA(3), oW = 0;
      OPAQUE_S(oA);
      ra0 = lds0 + L_R2 + (hw * 32 + (lane & 15)) * 128 + (((((lane >> 4) & 1) * 2 + (lane >> 5)) ^ ((lane >> 1) & 7)) * 16);
      OPAQUE_V(ra0);
      ldres(0);
      clean(0);
      WAITV(10);
      chkrd(1);
      if (__builtin_expect(chkbad(), 0)) refetch(1);
      const int fr = lane & 15, fq = lane >> 4;
      u32x2 fl;
      f32x4 js[4], jc[4];
      float ra[4], rb[4];
      const bool slowo = SLOWC(2 + (l & 1), j);
      const char* pO;
      unsigned zO, bO, pcO, vO, clo;
      u32x4 wo2[2];
      asm volatile("" : "=v"(wo2[0])); asm volatile("" : "=v"(wo2[1]));
      const unsigned fwo = myrep + fl_line(FL_WO + (isb ? 1 : 0), jr + (lane >> 4)) + (lane & 15) * 8;
      static_for<1, 14>([&](auto t_c) __attribute__((always_inline)) {
        constexpr int t = decltype(t_c)::value;
        constexpr auto rest = [](int s) { return 6 + (s == NVAL - 2 || s == 5 || s == 11) + ((s == 7 || s == 8) ? 4 : 0); };
        constexpr int NW = t == 1 ? 63 : 4 + rest(t - 1);
        if (t >= NVAL && t <= 6 && __builtin_expect((ready & (1u << kseq(t + 2))) == 0, 0)) {
          WAITV(NW); WAITL0; BAR;
          ready = poll_need(rs, flo, epoch, ready, 1u << kseq(t + 2), dead);
          dma4<4096>(rA, vA, oA, pcA, aslot(t + 2));
        } else if (t == 7 && __builtin_expect(ready != ~0u, 0)) {
          WAITV(NW); WAITL0; BAR;
          poll_all(rs, flo, epoch, dead);
          dma4<4096>(rA, vA, oA, pcA, aslot(t + 2));
        } else {
          bar_dma4<4096, NW>(rA, vA, oA, pcA, aslot(t + 2));
        }
        if (t == 1 && lead) set_flags(rs, fl_line(FL_X + l, g), j, l0f ? fl_line(FL_CS, j) : fl_line(FL_X + l, g), l0f ? g : j, epoch, lane);
        if (t == 7 && slowj) packedW();
        oW = offW(t + 2);
        if (t < 13) oA = offA(t + 3);
        OPAQUE_S(oA); OPAQUE_S(oW);
        clean(t);
        if (t <= NVAL && !l0f) { WAITV(rest(t - 1) + 4); chkrd(t + 1); }
        if (t == WT + 3 && l0f && SLOWC(2, jr + hw)) {
          OPAQUE_V(wo2[0]); OPAQUE_V(wo2[1]);
#pragma unroll
          for (int i = 0; i < 2; ++i) st16_sc1(rs, lin, WS_WO + (unsigned)((jr + hw) * 131072 + g * 8192 + ((j & 3) * 2 + i) * 1024), wo2[i]);
        }
        if (t == WT + 6 && l0f && lead) set_flags(rs, fl_line(FL_WO, j), g, fl_line(FL_WO, j), g, epoch, lane);
        ldres(t);
        if (t == 7) {
#pragma unroll
          for (int i = 0; i < 4; ++i) ld16_asm(jc[i], rs, fq * 768 + hw * 192 + (fr < 12 ? fr : 0) * 16, WS_CS + (unsigned)(((isb ? 16 : 0) + j) * 16 + i * 4) * 768);
        }
        if (t == 8) {
#pragma unroll
          for (int i = 0; i < 4; ++i) ld16_asm(js[i], rs, fq * 1024 + hw * 256 + fr * 16, st_off(g, l) + i * 4096);
        }
        SCHED;
        ldWh(t + 2, oW, 0);
        SCHED;
        bool bad = false;
        if (t <= NVAL) bad = chkbad();
        if (t == 1) {
          const unsigned fx = fl_line(FL_X + l, g), fc = fl_line(l0f ? FL_CS : FL_PK, jr) + ((j + fq) & 3) * 128;
          flo = myrep + ((l < 2 && lane >= 16) ? fc : fx) + fr * 8;
          OPAQUE_V(flo);
        }
        if (t == 9) {
          pO = slowo ? p.ws : (const char*)(isb ? p.woB : p.woA);
          zO = slowo ? WS_SPAN : 1024 * 2048; bO = slowo ? WS_WO + (unsigned)((isb ? 16 : 0) + j) * 131072 : j * 128; pcO = slowo ? 4096 : 32 * 2048;
          vO = slowo ? vlin : (hw * 8 + (lane >> 3)) * 2048 + wo_swz(lane);
          clo = myrep + ((lane & 16) ? fl_line(FL_CLC + l, (lane & 32) ? j ^ RX : j) : fl_line(FL_CLR + l, g)) + fr * 8;
          OPAQUE_S(pO); OPAQUE_S(zO); OPAQUE_S(bO); OPAQUE_S(pcO); OPAQUE_V(vO); OPAQUE_V(clo);
        }
        if (t == 10) {
#pragma unroll
          for (int i = 0; i < 4; ++i) OPAQUE_V(jc[i]);
          const f32x4 s = (jc[0] + jc[1]) + (jc[2] + jc[3]);
#pragma unroll
          for (int i = 0; i < 4; ++i) { ra[i] = s[i]; OPAQUE_V(ra[i]); }
        }
        if (t == 11) {
#pragma unroll
          for (int i = 0; i < 4; ++i) OPAQUE_V(js[i]);
          const f32x4 s = (js[0] + js[1]) + (js[2] + js[3]);
#pragma unroll
          for (int i = 0; i < 4; ++i) { rb[i] = s[i]; OPAQUE_V(rb[i]); }
        }
        if (t == 12) {
          red2_add(ra[0], ra[1]); red2_add(ra[2], ra[3]); red2_add(rb[0], rb[1]); red2_add(rb[2], rb[3]);
          if (lane < 12) lds_st<f32x4>(lds0 + L_CS + hw * 192 + lane * 16, f32x4{ra[0], ra[1], ra[2], ra[3]});
#pragma unroll
          for (int i = 0; i < 4; ++i) OPAQUE_V(rb[i]);
        }
        if (t == 13) {
          const float m0 = rb[0] * (1.0f / 1024), m1 = rb[2] * (1.0f / 1024);
          const float r0 = __builtin_amdgcn_rsqf(fmaxf(__builtin_fmaf(rb[1], 1.0f / 1024, -m0 * m0), 0.f) + 1e-5f);
          const float r1 = __builtin_amdgcn_rsqf(fmaxf(__builtin_fmaf(rb[3], 1.0f / 1024, -m1 * m1), 0.f) + 1e-5f);
          if (lane < 16) {
            lds_st<f32x2>(lds0 + L_MEAN + 64 + hw * 128 + fr * 8, f32x2{-m0, -m1});
            lds_st<f32x2>(lds0 + L_RSTD + 64 + hw * 128 + fr * 8, f32x2{r0, r1});
          }
        }
        SCHED;
        ldWh(t + 2, oW, 1);
        if (t == NVAL - 1 || t == 6) {
          WAITV(10);
          OPAQUE_V(fl);
          if (t == 6) ready = __all(is_set(fl, epoch)) ? ~0u : 0u;
          else {
            ready = l0f ? ~0u : (unsigned)__ballot(is_set(fl, epoch));
            if (slowj && __all(is_set(fl, epoch))) packedW();
          }
        }
        if (t == 12) { WAITV(10); OPAQUE_V(fl); }
        if (t == 13 && slowo && __builtin_expect(!__all(is_set(fl, epoch)), 0)) poll_all(rs, fwo, epoch, dead);
        if (t == NVAL - 2 || t == 5) ld8_asm(fl, rs, flo);
        if (t == 11) ld8_asm(fl, rs, fwo);
        if (t == WT && l0f) {
#pragma unroll
          for (int i = 0; i < 2; ++i) ld16in_asm(wo2[i], mk_rsrc(p.woA, 1024 * 2048), (lane >> 3) * 2048 + wo_swz(lane), (g * 64 + (j & 3) * 16 + i * 8) * 2048 + (jr + hw) * 128);
        }
        if constexpr (t <= NVAL) { if (__builtin_expect(bad, 0)) refetch(t + 1); }
      });
      auto stO = [&](int i) { dma4<4096>(mk_rsrc(pO, zO), vO, bO + wo_src(i, j) * (4 * pcO), pcO, lds0 + wo_slot(i) + hw * 1024); };
      auto clcheck = [&]() {
        OPAQUE_V(fl);
        if (__builtin_expect(!__all(is_set(fl, epoch)), 0)) poll_all(rs, clo, epoch, dead);
      };
      auto accinit = [&]() {
#pragma unroll
        for (int m = 0; m < 2; ++m)
#pragma unroll
          for (int pp = 0; pp < 2; ++pp) {
            auto r0_ = __builtin_amdgcn_permlane16_swap(xr[m][pp][0], xr[m][pp][2], false, false);
            auto r1_ = __builtin_amdgcn_permlane16_swap(xr[m][pp][1], xr[m][pp][3], false, false);
            acc[m][2 * pp] = f32x4{lo16(r0_[0]), hi16(r0_[0]), lo16(r1_[0]), hi16(r1_[0])};
            acc[m][2 * pp + 1] = f32x4{lo16(r0_[1]), hi16(r0_[1]), lo16(r1_[1]), hi16(r1_[1])};
          }
#pragma unroll
        for (int i = 0; i < 8; ++i) OPAQUE_V(acc[i >> 2][i & 3]);
      };
      WAITV(10);
      WAITL0;
      BAR;
      if (lead) set_flags(rs, fl_line(FL_CLR + l, g), j, fl_line(FL_CLC + l, j), g, epoch, lane);
      if (isb) {
        stO(0); stO(2); stO(3);
        WAITV(12);
        BAR;
        stO(1); stO(4); stO(5);
        __builtin_amdgcn_s_setprio(0);
        BAR;
        __builtin_amdgcn_s_sleep(BD);
        stO(7); stO(6);
        ld8_asm(fl, rs, clo);
        accinit();
        WAITV(0);
        clcheck();
      } else {
        u32x4 vb[8];
#pragma unroll
        for (int i = 0; i < 8; ++i) asm volatile("" : "=v"(vb[i]));
        stO(0); stO(2); stO(3);
        ldres(14);
        WAITV(12);
        WAITL0;
        BAR;
        stO(1);
        ldres(15);
        if (l0f) {
          const bool wj = hw != 0;
          const char* bp = (const char*)(wj ? p.wqkvB : p.woB);
          unsigned rs_ = wj ? 6144 : 2048, sz = rs_ * 1024, ss = rs_ * 8, s2 = wj ? 16 * 6144 : 128;
          unsigned sb = g * 64 * rs_ + (wj ? (hw - 1) * 2048 + JOBC(j, hw - 1) * 128 : (j & 3) * (16 * 2048) + jr * 128);
          OPAQUE_S(bp); OPAQUE_S(rs_); OPAQUE_S(sz); OPAQUE_S(sb); OPAQUE_S(ss); OPAQUE_S(s2);
          const rsrc_t rb = mk_rsrc(bp, sz);
#pragma unroll
          for (int i = 0; i < 8; ++i) ld16in_asm(vb[i], rb, (lane >> 3) * rs_ + (lane & 7) * 16, sb + (i & 1) * ss + (i >> 1) * s2);
        }
        __builtin_amdgcn_s_setprio(0);
        KV_ADDR;
        bf16x8 sel = rowsum_sel(lane);
        unsigned h1 = (lane >> 3) * KVP_G + (lane & 7) * 16;
        unsigned pw = lds0 + SC_PHIK + (h * 128 + wm * 64 + 32 + fr) * 160 + fq * 8, kw = lds0 + L_PREV + hw * 2048 + lin;
        const unsigned wa = lds0 + L_WPHI + h * 2048 + (fq * 4 + (fr >> 2)) * 64 + (lane & 3) * 8;
        bf16x8 w0 = tr2(wa, wa + 1024), w1 = tr2(wa + 32, wa + 32 + 1024);
        float cq[4];
#pragma unroll
        for (int m = 0; m < 2; ++m) { cq[m] = lds_ld<float>(lds0 + L_RSTD + (16 + wm * 64 + 32 + m * 16 + fr) * 4) * 1.4426950408889634f; OPAQUE_V(cq[m]); }
        OPAQUE_V(sel); OPAQUE_V(h1); OPAQUE_V(pw); OPAQUE_V(kw); OPAQUE_V(w0); OPAQUE_V(w1);
        BAR;
        ld8_asm(fl, rs, clo);
        {
          const f32x4 zero = {0.f, 0.f, 0.f, 0.f};
          f32x4 r[4][2];
#pragma unroll
          for (int m = 0; m < 2; ++m) {
            const bf16x8 f = lds_ld<bf16x8>(kw + m * 1024);
            r[m][0] = MFMA(w0, f, zero); r[m][1] = MFMA(w1, f, zero);
          }
          phik2(r, cq, pw);
        }
        WAITV(0);
#pragma unroll
        for (int i = 0; i < 8; ++i) OPAQUE_V(vb[i]);
        clcheck();
        WAITL0;
        BAR;
        kvpart(va, pa, kvj + (unsigned)g * KVP_G, lane, wave);
        CLOBBER;
        BAR;
        stO(4); stO(5);
        const f32x4 zero = {0.f, 0.f, 0.f, 0.f};
        const unsigned s1 = kvj + (unsigned)((j ^ RX) - j) * KVP_J + g * 512 + hw * 128;
        __builtin_amdgcn_s_sleep(H1S);
        u32x4 v[2] = {ld16_sc1(rs, h1, s1), ld16_sc1(rs, h1, s1 + 8 * KVP_G)};
        for (unsigned spins = 0; !dead && __any(umax4v(umax4v(0u, v[0]), v[1]) == SENT); ++spins) {
          if (spins > SPIN_CAP) { dead = true; break; }
          CLOBBER;
          v[0] = ld16_sc1(rs, h1, s1); v[1] = ld16_sc1(rs, h1, s1 + 8 * KVP_G);
        }
        const f32x4 c = rowsum_fin(MFMA(sel, as_frag(v[1]), MFMA(sel, as_frag(v[0]), zero)));
        if ((lane & 40) == 0)
          st8_sc1(rs, fr * 16 + fq * 8, kvj + (unsigned)g * KVP_G + KVP_SUM + hw * 128, u32x2{min(pk2(c[0], c[1]), SENT - 1), min(pk2(c[2], c[3]), SENT - 1)});
        CLOBBER;
        BAR;
        accinit();
        __builtin_amdgcn_s_sleep(H2D);
        stO(6); stO(7);
        if (l0f) {
          if (hw) {
            job_colsum(rs, vb, 1, g, JOBC(j, hw - 1), hw - 1, lane);
            if (SLOWC(1, JOBC(j, hw - 1))) {
#pragma unroll
              for (int i = 0; i < 8; ++i) job_pack(rs, vb[i], i, 1, g, JOBC(j, hw - 1), hw - 1, lane);
            }
          } else {
#pragma unroll
            for (int i = 0; i < 8; ++i)
              if (SLOWC(3, jr + (i >> 1))) st16_sc1(rs, (lane >> 3) * 128 + wo_swz(lane), WS_WO + (unsigned)((16 + jr + (i >> 1)) * 131072 + g * 8192 + ((j & 3) * 2 + (i & 1)) * 1024), vb[i]);
          }
        }
        WAITV(0);
      }
    }
    {
      LOCALS;
      const int fr = lane & 15, fq = lane >> 4;
      const int kh = half;
      unsigned v0 = lane * 16 + hw * 4096 + kh * 2048, v1 = lane * 16 + hw * 4096 + (kh ^ 1) * 2048;
      const int r0 = (JROT(j) >> 1) + NJ / 2;
      const unsigned ob0 = ob_off(g, kh, l);
      const unsigned pko = myrep + fl_line(FL_PK, JOBC(j, fq - 1)) + fr * 8;
      u32x4 R[8][4];
      u32x2 pkf;
      auto issue_ = [&](int i) {
        const unsigned so = ob0 + (unsigned)((r0 + i) & 7) * (2 * TS);
        R[i][0] = ld16_sc1(rs, v0, so); R[i][1] = ld16_sc1(rs, v1, so);
        R[i][2] = ld16_sc1(rs, v0 + 1024, so); R[i][3] = ld16_sc1(rs, v1 + 1024, so);
      };
      auto issue = [&](int i) { issue_(i); SCHED; };
      constexpr int LA = LOOKAHEAD;
      __builtin_amdgcn_s_setprio(3);
      unsigned so0 = ob0 + (unsigned)(r0 & 7) * (2 * TS);
      OPAQUE_V(v0); OPAQUE_V(v1); OPAQUE_S(so0);
      BAR;
      SCHED;
      R[0][0] = ld16_sc1(rs, v0, so0); R[0][1] = ld16_sc1(rs, v1, so0);
      R[0][2] = ld16_sc1(rs, v0 + 1024, so0); R[0][3] = ld16_sc1(rs, v1 + 1024, so0);
      SCHED;
      __builtin_amdgcn_s_setprio(2);
      if (l == 0 && lead) set_flags(rs, fl_line(FL_PK, j), g, fl_line(FL_WO + 1, j), g, epoch, lane);

      auto vmax = [&](int i) { return umax4v(umax4v(umax4v(umax4v(0u, R[i][0]), R[i][1]), R[i][2]), R[i][3]); };
      auto bad = [&](int i) { return __any(umax4v(umax4v(umax4v(umax4v(0u, R[i][0]), R[i][1]), R[i][2]), R[i][3]) == SENT); };
      auto poll = [&](int i, int nfl) {
        for (unsigned spins = 0; !dead; ++spins) {
          if (spins > SPIN_CAP) { dead = true; break; }
          __builtin_amdgcn_s_sleep(2);
          CLOBBER;
          issue(i);
#pragma unroll
          for (int k = i + 1; k < i + nfl && k < 8; ++k)
            if (bad(k)) issue(k);
          WAITV(0);
#pragma unroll
          for (int k = i + 1; k < i + nfl && k < 8; ++k)
#pragma unroll
            for (int f = 0; f < 4; ++f) OPAQUE_V(R[k][f]);
          if (!bad(i)) break;
        }
      };
      unsigned bb[4];
      {
        const int kr = fq * 4 + (fr >> 2);
        const int gs = (kr >> 1) & 3;
#pragma unroll
        for (int c = 0; c < 4; ++c) bb[c] = lds0 + kh * 8192 + kr * 128 + ((c ^ gs) * 32) + (lane & 3) * 8;
      }
      bf16x8 bf[2][4];
      auto rdB = [&](int i, int hh) {
        if (hh == 0) {
#pragma unroll
          for (int c = 0; c < 4; ++c) { bb[c] += wo_slot(i) - (i ? wo_slot(i - 1) : 0u); asm("" : "+v"(bb[c])); }
        }
#pragma unroll
        for (int c = 0; c < 4; ++c) bf[hh][c] = tr2(bb[c] + hh * 4096, bb[c] + hh * 4096 + 2048);
      };
      auto mms = [&](int i, int hh) {
#pragma unroll
        for (int n = 0; n < 4; ++n)
#pragma unroll
          for (int m = 0; m < 2; ++m) acc[m][n] = MFMA(bf[hh][n], as_frag(R[i][hh * 2 + m]), acc[m][n]);
      };
      if (!half) {
#pragma unroll
        for (int i = 0; i < 8; ++i) acc[i >> 2][i & 3] = f32x4{0.f, 0.f, 0.f, 0.f};
      }
      const char* obase = (l == 3) ? (const char*)p.out : (const char*)p.ws;
      unsigned osize = (l == 3) ? 2048 * 2048 : WS_SPAN;
      const bool slown = SLOWC((l & 1) ^ 1, j);
      pW = slown ? p.ws : (const char*)(isb ? p.wqkvA : p.wqkvB);
      bW = slown ? WS_WP + (unsigned)((isb ? 0 : 16) + j) * (16 * 24576) : j * 128;
      kW = slown ? 24576 : 64 * 6144; zW = slown ? WS_SPAN : 1024 * 6144; pcW = slown ? 8192 : 2048; hW = slown ? 4096 : 32 * 6144;
      vA = hw * 1024 + lane * 16;
      vW = slown ? vA : (hw * 8 + (lane >> 3)) * 6144 + (((lane & 7) ^ ((((lane >> 4) & 1) | ((hw & 1) << 1)) << 1)) * 16);
      pA = p.ws; zA = WS_SPAN; bA = xb_off(g, l); kA = TS; pcA = 4096; kb = JROT(j) + KX; dj = (j - kb) & 15;
      OPAQUE_S(pW); OPAQUE_S(bW); OPAQUE_S(kW); OPAQUE_S(zW); OPAQUE_S(pcW); OPAQUE_S(hW); OPAQUE_V(vA); OPAQUE_V(vW); OPAQUE_S(bA);
      unsigned sq = bW + j * kW + kh * hW, dst = lds0 + L_R1 + kh * 4096 + hw * 1024;
      OPAQUE_S(obase); OPAQUE_S(osize); OPAQUE_S(sq); OPAQUE_S(dst);
      const rsrc_t ro = mk_rsrc(obase, osize);
      unsigned so[2];
      unsigned xw = lds0 + SC_XCH + wave * 4096 + lane * 16;
      unsigned xa = lds0 + SC_XCH + (wave ^ 4) * 4096 + lane * 16;
      unsigned sb0 = (l == 3) ? g * (128 * 2048) + j * 128 : xb_off(g, l) + j * TS;
      {
        const int row = (hw * 2 + kh) * 16 + fr, ch = (fq & 1) * 2 + (fq >> 1);
        const unsigned sw = (l == 3) ? 0u : (unsigned)((row >> 1) & 7), rb = (unsigned)row << ((l == 3) ? 11 : 7);
#pragma unroll
        for (int pp = 0; pp < 2; ++pp) { so[pp] = rb + (((ch + 4 * pp) ^ sw) * 16); OPAQUE_V(so[pp]); }
        OPAQUE_V(xa); OPAQUE_V(xw); OPAQUE_S(sb0);
      }
      SCHED;
#pragma unroll
      for (int i = 1; i < LA; ++i) issue(i);
      rdB(0, 0);
      unsigned mx = vmax(0);
#pragma unroll
      for (int i = 0; i < 8; ++i) {
        SCHED;
        if (__builtin_expect(__any(mx == SENT), 0)) poll(i, LA);
        if (i == W1S - 2) { pkf = ld8_sc1(rs, pko, 0); SCHED; }
        if (i == W1S) {
          OPAQUE_V(pkf);
          if (l == 0 && lead && slown && !__all(is_set(pkf, epoch))) poll_all(rs, pko, epoch, dead);
          BAR;
        }
        if (i == 7 && l < 3) dma3<8192>(mk_rsrc(pW, zW), vW, sq, pcW, dst);
        if (i < 4 && (i < 2 ? !isb : l == 0)) __builtin_amdgcn_s_sleep(PACE);
        if (i == 2) __builtin_amdgcn_s_setprio(1);
        if (i == 5) __builtin_amdgcn_s_setprio(0);
        SCHED;
        rdB(i, 1);
        if (i == 7) break;
        if (i + LA < 8) issue_(i + LA);
        mms(i, 0);
#pragma unroll
        for (int k = 0; k < 4; ++k) { SGB(0x008, 1); SGB(0x100, 1); SGB(0x020, 1); SGB(0x008, 1); SGB(0x100, 1); }
        SCHED;
        rdB(i + 1, 0); mx = vmax(i + 1);
        mms(i, 1);
#pragma unroll
        for (int k = 0; k < 4; ++k) { SGB(0x008, 1); SGB(0x100, 2); }
#pragma unroll
        for (int k = 4; k < 8; ++k) { SGB(0x008, 1); SGB(0x002, 8 / (8 - 4)); }
      }
#pragma unroll
      for (int n = 0; n < 4; ++n) acc[1][n] = MFMA(bf[0][n], as_frag(R[7][1]), acc[1][n]);
#pragma unroll
      for (int n = 0; n < 4; ++n) acc[0][n] = MFMA(bf[0][n], as_frag(R[7][0]), acc[0][n]);
#pragma unroll
      for (int k = 0; k < 8; ++k) { SGB(0x008, 1); SGB(0x100, 1); }
      SCHED;
#pragma unroll
      for (int n = 0; n < 4; ++n) acc[1][n] = MFMA(bf[1][n], as_frag(R[7][3]), acc[1][n]);
#pragma unroll
      for (int n = 0; n < 4; ++n) acc[0][n] = MFMA(bf[1][n], as_frag(R[7][2]), acc[0][n]);
#pragma unroll
      for (int n = 0; n < 4; ++n) lds_st<f32x4>(xw + n * 1024, acc[1][n]);
      SGB(0x008, 4);
#pragma unroll
      for (int k = 0; k < 4; ++k) { SGB(0x008, 1); SGB(0x200, 1); }
      SCHED;
      WAITL0;
      BAR;
      SCHED;
      f32x4 pa[4];
#pragma unroll
      for (int n = 0; n < 4; ++n) pa[n] = lds_ld<f32x4>(xa + n * 1024);
      SCHED;
      u32x4 o[2];
#pragma unroll
      for (int pp = 0; pp < 2; ++pp) {
        const f32x4 a = acc[0][2 * pp] + pa[2 * pp], b = acc[0][2 * pp + 1] + pa[2 * pp + 1];
        auto r0_ = __builtin_amdgcn_permlane16_swap(pk2(a[0], a[1]), pk2(b[0], b[1]), false, false);
        auto r1_ = __builtin_amdgcn_permlane16_swap(pk2(a[2], a[3]), pk2(b[2], b[3]), false, false);
        o[pp] = nosent(u32x4{r0_[0], r1_[0], r0_[1], r1_[1]});
        st16_sc1(ro, so[pp], sb0, o[pp]);
      }
      if (l < 3) {
        lds_st<u32x4>(lds0 + L_R2 + so[0], o[0]);
        lds_st<u32x4>(lds0 + L_R2 + so[1], o[1]);
        const f32x4 zero = {0.f, 0.f, 0.f, 0.f};
        const bf16x8 ones = {0x3f80, 0x3f80, 0x3f80, 0x3f80, 0x3f80, 0x3f80, 0x3f80, 0x3f80};
        const f32x4 a = MFMA(ones, as_frag(o[1]), MFMA(ones, as_frag(o[0]), zero));
        const f32x4 b = MFMA(as_frag(o[1]), as_frag(o[1]), MFMA(as_frag(o[0]), as_frag(o[0]), zero));
        const float q = (fr & 2) ? ((fr & 1) ? b[3] : b[2]) : ((fr & 1) ? b[1] : b[0]);
        if (fq == (fr >> 2)) st8_sc1(rs, ((hw * 2 + kh) * 16 + fr) * 8, st_off(g, l + 1) + j * 1024, u32x2{__float_as_uint(a[0]), __float_as_uint(q)});
      } else if (lead && blockIdx.x == 0 && lane == 0) {
        st8_sc1(rs, WS_EPOCH, 0, epoch);
      }
    }
  }
  asm volatile(".Lpf_end:");
}

extern "C" void kernel_launch(void* const* d_in, const int* in_sizes, int n_in, void* d_out, int out_size,
                              void* d_ws, size_t ws_size, hipStream_t stream) {
  Params p;
  p.x = (const u16*)d_in[0];
  p.wqkvA = (const u16*)d_in[1];
  p.wphi = (const u16*)d_in[2];
  p.woA = (const u16*)d_in[3];
  p.wqkvB = (const u16*)d_in[5];
  p.woB = (const u16*)d_in[6];
  p.out = (u16*)d_out;
  p.ws = (char*)d_ws;
  (void)hipFuncSetAttribute((const void*)mega_kernel, hipFuncAttributeMaxDynamicSharedMemorySize, L_TOTAL);
  mega_kernel<<<256, 512, L_TOTAL, stream>>>(p);
}
```

```cpp
#include <hip/hip_runtime.h>
#include <stdint.h>
#include <type_traits>
constexpr int LOOKAHEAD = 2;
constexpr int NVAL = 4;
constexpr int WT = 1;
constexpr int W1S = 6;
constexpr int NJ = 8;
constexpr int KX = 8;
constexpr int RX = 8;
constexpr unsigned TS = 17664;
#define XD 3
#define H1S 6
#define H2D 5
#define BD 8
#define PACE 4

typedef unsigned short u16;
typedef unsigned long long u64;
typedef __attribute__((ext_vector_type(8))) short bf16x8;
typedef __attribute__((ext_vector_type(4))) short s16x4;
typedef __attribute__((ext_vector_type(4))) float f32x4;
typedef __attribute__((ext_vector_type(2))) float f32x2;
typedef __attribute__((ext_vector_type(4))) unsigned u32x4;
typedef __attribute__((ext_vector_type(2))) unsigned u32x2;
typedef __attribute__((ext_vector_type(2))) __bf16 bf16x2_t;
#define LAS __attribute__((address_space(3)))
typedef LAS s16x4* lds_s16x4_ptr;

constexpr unsigned NREP = 8, REP_STRIDE = 17 * 2048;
enum { FL_X = 0, FL_CS = 4, FL_CLR = 5, FL_CLC = 9, FL_PK = 13, FL_WO = 14 };
__device__ __forceinline__ unsigned fl_line(int set, int a) { return (unsigned)(set * 16 + a) * 128; }
constexpr unsigned WS_EPOCH = 512u << 10;
constexpr unsigned WS_CS = 1u << 20;
constexpr unsigned WS_KVP = 4u << 20;
constexpr unsigned KVP_G = 32768, KVP_J = 16 * KVP_G, KVP_L = 16384, KVP_Z = 8192, KVP_SUM = 8704, KVP_HALO = 12u << 10;
constexpr unsigned SENT = 0xffffffffu;
constexpr unsigned WS_X = 14u << 20, WS_O = 29u << 20, WS_ST = 2u << 20;
__device__ __forceinline__ unsigned xb_off(int g, int l) { return WS_X + (unsigned)(l * 16 + g) * (16 * TS); }
__device__ __forceinline__ unsigned ob_off(int g, int j, int l) { return WS_O + (unsigned)((l * 16 + g) * 16 + j) * TS; }
__device__ __forceinline__ unsigned st_off(int g, int l) { return WS_ST + (unsigned)(l * 16 + g) * (16u << 10); }
constexpr unsigned WS_WP = 48u << 20;
constexpr unsigned WS_WO = 60u << 20;
constexpr unsigned WS_SPAN = 64u << 20;

constexpr unsigned L_R1 = 0;
constexpr unsigned L_R2 = 98304, XS = 16384;
constexpr unsigned L_MISC = 147456;
constexpr unsigned L_MEAN = L_MISC;
constexpr unsigned L_RSTD = L_MISC + 576;
constexpr unsigned L_CS = L_MISC + 1152;
constexpr unsigned L_WPHI = L_MISC + 2688;
constexpr unsigned L_SINK = L_MISC + 6912;
constexpr unsigned L_ZF = L_MISC + 1920;
constexpr unsigned L_PREV = L_MISC + 8192;
constexpr unsigned L_TOTAL = 163840;

#define JROT(j) ((j) & ~(NJ - 1))
constexpr int NX = NJ / 2;
#define KB0(g, j) (JROT(j) + (16 / NX) * ((g) & (NX - 1)))
#define JOBC(j, w) (((j) & ~3) + (((j) + (w) + 1) & 3))

#define SLOWC(m, jc) ((slowm >> ((m) * 8 + ((jc) & 7))) & 1)

struct Params {
  const u16* x;
  const u16* wqkvA;
  const u16* wphi;
  const u16* woA;
  const u16* wqkvB;
  const u16* woB;
  u16* out;
  char* ws;
};

#define SCHED __builtin_amdgcn_sched_barrier(0)
#define BAR __builtin_amdgcn_s_barrier()
#define SGB(m, n) __builtin_amdgcn_sched_group_barrier(m, n, 0)
#define WAITV(n) asm volatile("s_waitcnt vmcnt(%0)" ::"n"(n) : "memory")
#define WAITL0 asm volatile("s_waitcnt lgkmcnt(0)" ::: "memory")

__device__ __forceinline__ unsigned pk2(float a, float b) {
  f32x2 v = {a, b};
  return __builtin_bit_cast(unsigned, __builtin_convertvector(v, bf16x2_t));
}
__device__ __forceinline__ float lo16(unsigned u) { return __uint_as_float(u << 16); }
__device__ __forceinline__ float hi16(unsigned u) { return __uint_as_float(u & 0xffff0000u); }

template <int A, int B, typename F> __device__ __forceinline__ void static_for(F f) {
  if constexpr (A < B) { f(std::integral_constant<int, A>{}); static_for<A + 1, B>(f); }
}
typedef __amdgpu_buffer_rsrc_t rsrc_t;
__device__ __forceinline__ rsrc_t mk_rsrc(const void* p, unsigned bytes) { return __builtin_amdgcn_make_buffer_rsrc((void*)p, 0, bytes, 0x00020000); }
#define UNI(x) __builtin_amdgcn_readfirstlane(x)
#define DMA_HEAD "s_mov_b32 m0, %4\n\ts_mov_b32 %0, %3\n\ts_nop 2\n\tbuffer_load_dwordx4 %1, %2, %0 offen sc1 lds\n\t"
#define DMA_NEXT "s_add_u32 m0, m0, %6\n\ts_add_u32 %0, %0, %5\n\tbuffer_load_dwordx4 %1, %2, %0 offen sc1 lds\n\t"
#define DMA_OPS : "=&s"(t) : "v"(voff), "s"(r), "s"(UNI(soff)), "s"(UNI(lds)), "s"(UNI(sstep)), "i"(LSTEP) : "memory", "scc", "m0"
template <int LSTEP> __device__ __forceinline__ void dma4(rsrc_t r, unsigned voff, unsigned soff, unsigned sstep, unsigned lds) {
  unsigned t;
  asm volatile(DMA_HEAD DMA_NEXT DMA_NEXT DMA_NEXT DMA_OPS);
}
template <int LSTEP> __device__ __forceinline__ void dma3(rsrc_t r, unsigned voff, unsigned soff, unsigned sstep, unsigned lds) {
  unsigned t;
  asm volatile(DMA_HEAD DMA_NEXT DMA_NEXT DMA_OPS);
}
template <int LSTEP, int N> __device__ __forceinline__ void bar_dma4(rsrc_t r, unsigned voff, unsigned soff, unsigned sstep, unsigned lds) {
  unsigned t;
  asm volatile("s_mov_b32 m0, %4\n\ts_mov_b32 %0, %3\n\ts_nop 0\n\ts_waitcnt vmcnt(%7) lgkmcnt(0)\n\ts_barrier\n\t"
               "buffer_load_dwordx4 %1, %2, %0 offen sc1 lds\n\t" DMA_NEXT DMA_NEXT DMA_NEXT
               : "=&s"(t) : "v"(voff), "s"(r), "s"(UNI(soff)), "s"(UNI(lds)), "s"(UNI(sstep)), "i"(LSTEP), "n"(N) : "memory", "scc", "m0");
}
__device__ __forceinline__ void dma1(rsrc_t r, unsigned voff, unsigned soff, unsigned lds) {
  asm volatile("s_mov_b32 m0, %3\n\ts_nop 3\n\tbuffer_load_dwordx4 %0, %1, %2 offen sc1 lds" ::"v"(voff), "s"(r), "s"(UNI(soff)), "s"(UNI(lds)) : "memory", "m0");
}
__device__ __forceinline__ void touch(rsrc_t r, unsigned voff, unsigned lds) {
  asm volatile("s_mov_b32 m0, %2\n\ts_nop 3\n\tbuffer_load_dword %0, %1, 0 offen lds" ::"v"(voff), "s"(r), "s"(UNI(lds)) : "memory", "m0");
}
__device__ __forceinline__ void ld8_asm(u32x2& d, rsrc_t r, unsigned voff) {
  asm volatile("s_nop 4\n\tbuffer_load_dwordx2 %0, %1, %2, 0 offen sc1" : "=v"(d) : "v"(voff), "s"(r) : "memory");
}

__device__ __forceinline__ void ld16in_asm(u32x4& d, rsrc_t r, unsigned voff, unsigned soff) {
  asm volatile("s_nop 4\n\tbuffer_load_dwordx4 %0, %1, %2, %3 offen" : "=v"(d) : "v"(voff), "s"(r), "s"(UNI(soff)) : "memory");
}
__device__ __forceinline__ void ld16_asm(f32x4& d, rsrc_t r, unsigned voff, unsigned soff) {
  asm volatile("s_nop 4\n\tbuffer_load_dwordx4 %0, %1, %2, %3 offen sc1" : "=v"(d) : "v"(voff), "s"(r), "s"(UNI(soff)) : "memory");
}

template <typename T> __device__ __forceinline__ T lds_ld(unsigned a) { return *(LAS T*)(a); }
template <typename T> __device__ __forceinline__ void lds_st(unsigned a, T v) { *(LAS T*)(a) = v; }
__device__ __forceinline__ bf16x8 tr2(unsigned a0, unsigned a1) {
  s16x4 v0 = __builtin_amdgcn_ds_read_tr16_b64_v4i16((lds_s16x4_ptr)(a0));
  s16x4 v1 = __builtin_amdgcn_ds_read_tr16_b64_v4i16((lds_s16x4_ptr)(a1));
  return __builtin_shufflevector(v0, v1, 0, 1, 2, 3, 4, 5, 6, 7);
}
__device__ __forceinline__ bf16x8 as_frag(u32x4 v) { return __builtin_bit_cast(bf16x8, v); }
#define MFMA(a, b, c) __builtin_amdgcn_mfma_f32_16x16x32_bf16(a, b, c, 0, 0, 0)

__device__ __forceinline__ float vmax2(float a, float b) { float inf = __builtin_inff(); asm("" : "+s"(inf)); return __builtin_amdgcn_fmed3f(a, b, inf); }
#define SWAP16(a, b) __builtin_amdgcn_permlane16_swap(__float_as_uint(a), __float_as_uint(b), false, false)
#define SWAP32(a, b) __builtin_amdgcn_permlane32_swap(__float_as_uint(a), __float_as_uint(b), false, false)
__device__ __forceinline__ void red2_max(float& a, float& b) {
  auto r = SWAP16(a, b);
  float x = vmax2(__uint_as_float(r[0]), __uint_as_float(r[1]));
  r = SWAP32(x, x);
  x = vmax2(__uint_as_float(r[0]), __uint_as_float(r[1]));
  r = SWAP16(x, x);
  a = __uint_as_float(r[0]); b = __uint_as_float(r[1]);
}
__device__ __forceinline__ void red2_add(float& a, float& b) {
  auto r = SWAP16(a, b);
  float x = __uint_as_float(r[0]) + __uint_as_float(r[1]);
  r = SWAP32(x, x);
  x = __uint_as_float(r[0]) + __uint_as_float(r[1]);
  r = SWAP16(x, x);
  a = __uint_as_float(r[0]); b = __uint_as_float(r[1]);
}
template <bool MAX, typename M> __device__ __forceinline__ void red4(float& a, float& b, float& c, float& d, M mid) {
  auto op = [](unsigned x, unsigned y) { return MAX ? vmax2(__uint_as_float(x), __uint_as_float(y)) : __uint_as_float(x) + __uint_as_float(y); };
  auto r = SWAP16(a, b), q = SWAP16(c, d);
  const float x = op(r[0], r[1]), y = op(q[0], q[1]);
  r = SWAP32(x, y);
  const float w = mid(op(r[0], r[1]));
  r = SWAP32(w, w);
  q = __builtin_amdgcn_permlane16_swap(r[1], r[1], false, false);
  r = __builtin_amdgcn_permlane16_swap(r[0], r[0], false, false);
  a = __uint_as_float(r[0]); b = __uint_as_float(r[1]); c = __uint_as_float(q[0]); d = __uint_as_float(q[1]);
}
__device__ __forceinline__ float red_max4(float x) {
  auto r = __builtin_amdgcn_permlane16_swap(__float_as_uint(x), __float_as_uint(x), false, false);
  x = vmax2(__uint_as_float(r[0]), __uint_as_float(r[1]));
  r = __builtin_amdgcn_permlane32_swap(__float_as_uint(x), __float_as_uint(x), false, false);
  return vmax2(__uint_as_float(r[0]), __uint_as_float(r[1]));
}
__device__ __forceinline__ float red_add4(float x) {
  auto r = __builtin_amdgcn_permlane16_swap(__float_as_uint(x), __float_as_uint(x), false, false);
  x = __uint_as_float(r[0]) + __uint_as_float(r[1]);
  r = __builtin_amdgcn_permlane32_swap(__float_as_uint(x), __float_as_uint(x), false, false);
  return __uint_as_float(r[0]) + __uint_as_float(r[1]);
}

__device__ __forceinline__ void st16_sc1(rsrc_t r, unsigned voff, unsigned soff, u32x4 v) {
  __builtin_amdgcn_raw_buffer_store_b128(v, r, voff, soff, 16);
  asm volatile("s_nop 1" ::"v"(v) : "memory");
}
__device__ __forceinline__ void st8_sc1(rsrc_t r, unsigned voff, unsigned soff, u32x2 v) { __builtin_amdgcn_raw_buffer_store_b64(v, r, voff, soff, 16); }
__device__ __forceinline__ u32x4 ld16_sc1(rsrc_t r, unsigned voff, unsigned soff) { return __builtin_amdgcn_raw_buffer_load_b128(r, voff, soff, 16); }
__device__ __forceinline__ u32x2 ld8_sc1(rsrc_t r, unsigned voff, unsigned soff) { return __builtin_amdgcn_raw_buffer_load_b64(r, voff, soff, 16); }
__device__ __forceinline__ f32x4 ld4f_sc1(rsrc_t r, unsigned voff, unsigned soff) { return __builtin_bit_cast(f32x4, __builtin_amdgcn_raw_buffer_load_b128(r, voff, soff, 16)); }
#define OPAQUE_V(x) asm volatile("" : "+v"(x))
#define OPAQUE_S(x) asm volatile("" : "+s"(x))

#define CLOBBER asm volatile("" ::: "memory")
constexpr unsigned SPIN_CAP = 1u << 17;
__device__ __forceinline__ bool is_set(u32x2 v, u32x2 epoch) { return v[0] == epoch[0] && v[1] == epoch[1]; }
__device__ __forceinline__ void set_flags(rsrc_t rs, unsigned lineA, int colA, unsigned lineB, int colB, u32x2 epoch, int lane) {
  const unsigned off = (lane & 7) * REP_STRIDE + ((lane & 8) ? lineB + colB * 8 : lineA + colA * 8);
  if (lane < 16) st8_sc1(rs, off, 0, epoch);
}
__device__ __forceinline__ unsigned poll_need(rsrc_t rs, unsigned voff, u32x2 epoch, unsigned ready, unsigned need, bool& dead) {
  for (unsigned spins = 0; !dead; ++spins) {
    CLOBBER;
    ready |= (unsigned)__ballot(is_set(ld8_sc1(rs, voff, 0), epoch));
    if ((ready & need) == need) break;
    if (spins > SPIN_CAP) dead = true;
    __builtin_amdgcn_s_sleep(1);
  }
  CLOBBER;
  return dead ? ~0u : ready;
}

__device__ __forceinline__ void poll_all(rsrc_t rs, unsigned voff, u32x2 epoch, bool& dead) {
  for (unsigned spins = 0; !dead; ++spins) {
    CLOBBER;
    if (__all(is_set(ld8_sc1(rs, voff, 0), epoch))) break;
    if (spins > SPIN_CAP) dead = true;
    __builtin_amdgcn_s_sleep(1);
  }
  CLOBBER;
}

constexpr unsigned SC_VF = L_R1 + 32768;
constexpr unsigned SC_PHIK = L_R1 + 49152;
constexpr unsigned SC_XCH = L_R2 + XS;
constexpr unsigned wo_slot(int s) {
  return s < 2 ? L_R2 + (unsigned)(s + 1) * XS : (s < 6 ? L_R1 + (unsigned)(s - 2) * 16384 : (s == 6 ? L_R1 + 81920 : L_R2));
}
__device__ __forceinline__ unsigned wo_src(int s, int j) { return (unsigned)(((JROT(j) >> 1) + NJ / 2 + s) & 7); }
__device__ __forceinline__ unsigned wo_swz(int lane) { return (unsigned)(((lane & 7) ^ ((lane >> 3) & 6)) * 16); }
__device__ __forceinline__ unsigned umax3(unsigned a, unsigned b, unsigned c) { return max(max(a, b), c); }
__device__ __forceinline__ unsigned umax4v(unsigned m, const u32x4& v) { return umax3(umax3(m, v[0], v[1]), v[2], v[3]); }
__device__ __forceinline__ u32x4 nosent(u32x4 v) { return u32x4{min(v[0], SENT - 1), min(v[1], SENT - 1), min(v[2], SENT - 1), min(v[3], SENT - 1)}; }

template <typename P, typename F>
__device__ __forceinline__ void qkv_loop(int isb, int wm, int h, int lane, bool first, P pre, F fin) {
  constexpr unsigned lds0 = 0;
  f32x4 acc[4][6];
  const int fr = lane & 15, fq = lane >> 4;
  unsigned ab[2], bb[2];
  {
    const int r = wm * 64 + fr;
#pragma unroll
    for (int ks = 0; ks < 2; ++ks) ab[ks] = lds0 + L_R2 + r * 128 + (((ks * 4 + fq) ^ ((fr >> 1) & 7)) * 16);
    const int kr = fq * 8 + (fr >> 2);
    const int gs = ((kr >> 1) & 1) | (((kr >> 3) & 1) << 1);
#pragma unroll
    for (int c = 0; c < 2; ++c) bb[c] = lds0 + L_R1 + kr * 128 + (((h * 2 + c) ^ gs) * 32) + (lane & 3) * 8;
  }
#pragma unroll
  for (int m = 0; m < 4; ++m)
#pragma unroll
    for (int n = 0; n < 6; ++n) acc[m][n] = f32x4{0.f, 0.f, 0.f, 0.f};
  bf16x8 af[2][4], bf[2][6];
  auto rds = [&](int ks, int p, int rot = 0) {
#pragma unroll
    for (int m = 0; m < 4; ++m) af[p][m] = lds_ld<bf16x8>(ab[ks] + m * 2048);
#pragma unroll
    for (int nn = 0; nn < 6; ++nn) {
      const int n = (nn + rot) % 6;
      const unsigned a = bb[n & 1] + (n >> 1) * 8192 + ks * 4096;
      bf[p][n] = tr2(a, a + 512);
    }
  };
  auto mms = [&](int p, int rot = 0, int cnt = 6) {
#pragma unroll
    for (int nn = 0; nn < cnt; ++nn)
#pragma unroll
      for (int mm = 0; mm < 4; ++mm) { const int n = (nn + rot) % 6, m = (n & 1) ? 3 - mm : mm; acc[m][n] = n < 4 ? MFMA(bf[p][n], af[p][m], acc[m][n]) : MFMA(af[p][m], bf[p][n], acc[m][n]); }
  };
  auto ilv2 = [&]() {
#pragma unroll
    for (int i = 0; i < 16; ++i) { SGB(0x008, 1); SGB(0x100, 1); }
#pragma unroll
    for (int i = 0; i < 4; ++i) { SGB(0x008, 1); SGB(0x002, 1); }
    SGB(0x008, 4);
  };
  auto ilv3 = [&]() {
#pragma unroll
    for (int i = 0; i < 16; ++i) { SGB(0x008, 1); SGB(0x100, 1); }
#pragma unroll
    for (int i = 0; i < 7; ++i) { SGB(0x008, 1); SGB(0x004, 2); }
    SGB(0x008, 1);
  };
#pragma unroll
  for (int i = 0; i < 24; ++i) OPAQUE_V(acc[i / 6][i % 6]);
  OPAQUE_V(ab[0]); OPAQUE_V(ab[1]); OPAQUE_V(bb[0]); OPAQUE_V(bb[1]);
  if (!first) { WAITV(3); WAITL0; }
  BAR;
  rds(0, 0);
  int sa = 0, sb = 0;
  int da = XS, db = 24576;
  auto step = [&]() {
    ab[0] += da; ab[1] += da;
    bb[0] += db; bb[1] += db;
  };
  auto ring = [&]() {
    sa = (sa == 2) ? 0 : sa + 1;
    sb = (sb == 3) ? 0 : sb + 1;
    da = (sa == 2) ? -2 * (int)XS : (int)XS;
    db = (sb == 3) ? -3 * 24576 : 24576;
  };
#pragma unroll 1
  for (int t = 14; t > 0; --t) {
    SCHED;
    rds(1, 1); mms(0);
    step();
    ilv2();
    SCHED;
    __builtin_amdgcn_s_waitcnt(0x0070);
    BAR;
    SCHED;
    rds(0, 0);
    ring();
    mms(1); ilv3();
  }
  SCHED;
  f32x4 cs[4], nm4[4];
  float nmean[4], csv[2];
  auto fold = [&](int n) {
#pragma unroll
    for (int mm = 0; mm < 4; ++mm) {
      const int m = (n & 1) ? 3 - mm : mm;
#pragma unroll
      for (int e = 0; e < 4; ++e) {
        float t = n < 4 ? __builtin_fmaf(nmean[m], cs[n & 3][e], acc[m][n][e]) : __builtin_fmaf(nm4[m][e], csv[n & 1], acc[m][n][e]);
        asm("" : "+v"(t));
        acc[m][n][e] = t;
      }
    }
  };
  {
    unsigned pm = lds0 + L_MEAN + (16 + wm * 64 + fr) * 4, pc = lds0 + L_CS + (h * 32 + fq * 4) * 4;
    unsigned pm4 = lds0 + L_MEAN + (16 + wm * 64 + fq * 4) * 4, pcv = lds0 + L_CS + (128 + h * 32 + fr) * 4;
    asm("" : "+v"(pm)); asm("" : "+v"(pc)); asm("" : "+v"(pm4)); asm("" : "+v"(pcv));
#pragma unroll
    for (int m = 0; m < 4; ++m) nmean[m] = lds_ld<float>(pm + m * 64);
#pragma unroll
    for (int n = 0; n < 4; ++n) cs[n ^ 2] = lds_ld<f32x4>(pc + (((n ^ 2) >> 1) * 64 + (n & 1) * 16) * 4);
#pragma unroll
    for (int m = 0; m < 4; ++m) nm4[m] = lds_ld<f32x4>(pm4 + m * 64);
#pragma unroll
    for (int n = 0; n < 2; ++n) csv[n] = lds_ld<float>(pcv + n * 64);
    rds(1, 1, 4); mms(0, 2);
    step();
    fold(2); fold(3);
    SGB(0x002, 8);
#pragma unroll
    for (int i = 0; i < 8; ++i) { SGB(0x008, 1); SGB(0x100, 2); }
#pragma unroll
    for (int i = 0; i < 11; ++i) { SGB(0x008, 1); SGB(0x100, 1); SGB(0x002, 2); }
#pragma unroll
    for (int i = 0; i < 5; ++i) { SGB(0x008, 1); SGB(0x002, 3); }
    SCHED;
    __builtin_amdgcn_s_waitcnt(0xC07F);
    BAR;
    SCHED;
    rds(0, 0, 2);
    mms(1, 4); fold(4); fold(5);
#pragma unroll
    for (int i = 0; i < 8; ++i) { SGB(0x008, 1); SGB(0x100, 1); SGB(0x002, 1); }
#pragma unroll
    for (int i = 0; i < 8; ++i) { SGB(0x008, 1); SGB(0x100, 1); SGB(0x002, 2); }
#pragma unroll
    for (int i = 0; i < 8; ++i) { SGB(0x008, 1); SGB(0x002, 2); }
    SCHED;
    pre(0);
    SCHED;
    rds(1, 1, 2);
    mms(0, 2, 4); fold(0); fold(1);
    pre(1);
#pragma unroll
    for (int i = 0; i < 16; ++i) { SGB(0x008, 1); SGB(0x100, 1); SGB(0x002, 2); SGB(0x004, 1); }
  }
  SCHED;
  auto mm6 = [&](int m, int n0 = 0, int n1 = 8) {
#pragma unroll
    for (int nn = n0; nn < n1; ++nn) {
      const int n = nn < 4 ? nn + 2 : nn & 1, p = (nn >> 1) != 2;
      acc[m][n] = n < 4 ? MFMA(bf[p][n], af[p][m], acc[m][n]) : MFMA(af[p][m], bf[p][n], acc[m][n]);
    }
  };
  if (!isb) {
    fin(std::false_type{}, acc, mm6);
#pragma unroll
    for (int i = 0; i < 24; ++i) asm volatile("" : "=v"(acc[i / 6][i % 6]));
#pragma unroll
    for (int i = 0; i < 6; ++i) asm volatile("" : "=v"(bf[1][i]));
#pragma unroll
    for (int i = 0; i < 8; ++i) asm volatile("" : "=v"(af[i >> 2][i & 3]));
    asm volatile("" : "=v"(bf[0][0])); asm volatile("" : "=v"(bf[0][1]));
  }
  OPAQUE_S(isb);
  if (isb) fin(std::true_type{}, acc, mm6);
}
__device__ __forceinline__ u32x4 pack(const f32x4& a, const f32x4& b) { return u32x4{pk2(a[0], a[1]), pk2(a[2], a[3]), pk2(b[0], b[1]), pk2(b[2], b[3])}; }

__device__ __forceinline__ u32x2 nosent2(const f32x4& a) { return u32x2{min(pk2(a[0], a[1]), SENT - 1), min(pk2(a[2], a[3]), SENT - 1)}; }
template <int N> __device__ __forceinline__ void hedgehog(const f32x4 (&r)[4][2], const float (&c)[4], float (&ep)[4][8], float (&en)[4][8]) {
  float mx[4], nm[4];
#pragma unroll
  for (int m = 0; m < N; ++m) {
    const f32x4 a = r[m][0], b = r[m][1];
    mx[m] = vmax2(fmaxf(fmaxf(a[3], b[0]), b[1]), fmaxf(fmaxf(fmaxf(fmaxf(a[0], a[1]), a[2]), b[2]), b[3]));
    nm[m] = vmax2(fmaxf(fmaxf(-a[3], -b[0]), -b[1]), fmaxf(fmaxf(fmaxf(fmaxf(-a[0], -a[1]), -a[2]), -b[2]), -b[3]));
  }
  SCHED;
  red4<true>(mx[0], mx[1], nm[0], nm[1], [](float x) { return x; });
  if (N == 4) red4<true>(mx[2], mx[3], nm[2], nm[3], [](float x) { return x; });
#pragma unroll
  for (int m = 0; m < N; ++m) { mx[m] *= -c[m]; nm[m] *= -c[m]; }
  SCHED;
#pragma unroll
  for (int m = 0; m < N; ++m) {
#pragma unroll
    for (int i = 0; i < 8; ++i) { ep[m][i] = __builtin_fmaf(r[m][i >> 2][i & 3], c[m], mx[m]); en[m][i] = __builtin_fmaf(r[m][i >> 2][i & 3], -c[m], nm[m]); }
    SCHED;
#pragma unroll
    for (int i = 0; i < 8; ++i) { ep[m][i] = __builtin_amdgcn_exp2f(ep[m][i]); en[m][i] = __builtin_amdgcn_exp2f(en[m][i]); }
    SCHED;
  }
}
__device__ __forceinline__ u32x4 pack8(const float (&e)[8], float f) {
  return u32x4{pk2(e[0] * f, e[1] * f), pk2(e[2] * f, e[3] * f), pk2(e[4] * f, e[5] * f), pk2(e[6] * f, e[7] * f)};
}
__device__ __forceinline__ u32x4 pack8(const float (&e)[8]) { return u32x4{pk2(e[0], e[1]), pk2(e[2], e[3]), pk2(e[4], e[5]), pk2(e[6], e[7])}; }

constexpr u64 window_mask(int t, int e) {
  u64 mk = 0;
  for (int ln = 0; ln < 64; ++ln) {
    const int delta = (ln & 15) + 16 - t * 16 - (ln >> 4) * 4 - e;
    if (delta >= 0 && delta <= 14 && !(delta & 1)) mk |= 1ull << ln;
  }
  return mk;
}
__device__ __forceinline__ void phik2(const f32x4 (&r)[4][2], const float (&c)[4], unsigned pw) {
  const f32x4 zero = {0.f, 0.f, 0.f, 0.f};
  const bf16x8 ones = {0x3f80, 0x3f80, 0x3f80, 0x3f80, 0x3f80, 0x3f80, 0x3f80, 0x3f80};
  float ep[4][8], en[4][8];
  f32x4 sp[2], sn[2];
  hedgehog<2>(r, c, ep, en);
#pragma unroll
  for (int m = 0; m < 2; ++m) { sp[m] = MFMA(ones, as_frag(pack8(ep[m])), zero); sn[m] = MFMA(ones, as_frag(pack8(en[m])), zero); }
#pragma unroll
  for (int m = 0; m < 2; ++m) { asm("" : "+v"(sp[m])); asm("" : "+v"(sn[m])); }
  SCHED;
#pragma unroll
  for (int m = 0; m < 2; ++m) {
    const float fp = __builtin_amdgcn_rcpf(sp[m][0]), fn = __builtin_amdgcn_rcpf(sn[m][0]);
#pragma unroll
    for (int i = 0; i < 8; ++i) { ep[m][i] *= fp; en[m][i] *= fn; }
    SCHED;
    const u32x4 pp = pack8(ep[m]), pn = pack8(en[m]);
    const unsigned d = pw + m * (16 * 160);
    lds_st<u32x2>(d, u32x2{pp[0], pp[1]});
    lds_st<u32x2>(d + 32, u32x2{pp[2], pp[3]});
    lds_st<u32x2>(d + 64, u32x2{pn[0], pn[1]});
    lds_st<u32x2>(d + 96, u32x2{pn[2], pn[3]});
    SCHED;
  }
}
__device__ __forceinline__ bf16x8 rowsum_sel(int lane) {
  const int fr = lane & 15;
  const unsigned one = (fr < 8) ? (0x3f80u << (16 * (fr & 1))) : 0u;
  const int e = (fr >> 1) & 3;
  return as_frag(u32x4{e == 0 ? one : 0u, e == 1 ? one : 0u, e == 2 ? one : 0u, e == 3 ? one : 0u});
}
__device__ __forceinline__ f32x4 rowsum_fin(f32x4 c) {
#pragma unroll
  for (int k = 0; k < 4; ++k) {
    const float a = c[k];
    c[k] = a + __int_as_float(__builtin_amdgcn_update_dpp(0, __float_as_int(a), 0x128  , 0xf, 0xf, false));
  }
  return c;
}
__device__ __forceinline__ void job_colsum(rsrc_t rs, const u32x4 (&w)[8], int mat, int g, int jc, int hw, int lane) {
  const bf16x8 sel = rowsum_sel(lane);
  f32x4 c = {0.f, 0.f, 0.f, 0.f};
#pragma unroll
  for (int i = 0; i < 8; ++i) c = MFMA(sel, as_frag(w[i]), c);
  const u32x4 w4 = __builtin_bit_cast(u32x4, rowsum_fin(c));
  unsigned so = WS_CS + (unsigned)(((mat * 16 + jc) * 16 + g) * 768);
  OPAQUE_S(so);
  if ((lane & 40) == 0) st16_sc1(rs, (unsigned)(hw * 64 + (lane & 7) * 8 + (lane >> 4) * 4) * 4, so, w4);
}
__device__ __forceinline__ unsigned img_off(int i, int lane) { return (lane >> 3) * 128 + (((lane & 7) ^ ((((lane >> 4) & 1) | ((i & 1) << 1)) << 1)) * 16); }
__device__ __forceinline__ void job_pack(rsrc_t rs, u32x4 w, int i, int mat, int g, int jc, int hw, int lane) {
  st16_sc1(rs, img_off(i, lane),
           WS_WP + (unsigned)(((mat * 16 + jc) * 16 + g) * 24576 + hw * 8192 + i * 1024), w);
}

__global__ __launch_bounds__(512, 2) void mega_kernel(Params p) {
  extern __shared__ __attribute__((aligned(16))) char smem[];
  const int tid = threadIdx.x;
  const int wave0 = __builtin_amdgcn_readfirstlane(tid >> 6);
  const int xx = blockIdx.x & 7, rr = blockIdx.x >> 3;
  const int g0 = (rr / NJ) * NX + (xx & (NX - 1)), j0 = (xx / NX) * NJ + (rr & (NJ - 1));
  constexpr unsigned lds0 = 0, lds00 = 0;
  if ((unsigned)(size_t)(LAS char*)smem != 0u) return;
#define LOCALS0 int g = g0, j = j0, l = l0, wave = wave0;                                              \
  OPAQUE_S(g); OPAQUE_S(j); OPAQUE_S(l); OPAQUE_S(wave);                                               \
  const int hw = wave & 3, wm = wave & 1, h = (wave >> 1) & 1;                                         \
  const unsigned myrep = (unsigned)(JROT(j) / 2 + (g & (NX - 1))) * REP_STRIDE;                              \
  (void)hw; (void)wm; (void)h; (void)myrep;                                                            \
  int lane = tid; OPAQUE_V(lane); lane &= 63
#define LOCALS LOCALS0; const bool isb = l & 1, lead = (wave == 4); const int half = wave >> 2; (void)isb; (void)lead; (void)half
  const rsrc_t rs = mk_rsrc(p.ws, WS_SPAN);
  const bool cw = wave0 < 4;
  if (wave0 == 3) {
    const char* pc;
    unsigned len;
    asm volatile("s_getpc_b64 %0\n.Lpf_base:\n\ts_mov_b32 %1, .Lpf_end-.Lpf_base" : "=s"(pc), "=s"(len));
    const unsigned off = ((unsigned)rr * 16 + (tid & 15)) * 128;
    touch(mk_rsrc(pc, len), ((int)len > 0 && off + 4 <= len) ? off : 0u, lds00 + L_SINK);
  }
  auto slowbit = [](const void* q) { return 1u << ((3 - ((unsigned)(size_t)q >> 7)) & 7); };
  unsigned slowm = slowbit(p.wqkvA) | (slowbit(p.wqkvB) << 8) | (slowbit(p.woA) << 16) | (slowbit(p.woB) << 24);
  OPAQUE_S(slowm);
  u32x2 epoch = {0u, 0u};
  bool dead = false;
      if (cw) {
        const int l0 = 0;
        LOCALS;
        u32x4 v[2][8];
        if (wave < 3) {
          const int jc = JOBC(j, wave), kt = (KB0(g, j) + (unsigned)g / NX) & 15;
          const rsrc_t ra = mk_rsrc(p.wqkvA, 1024 * 6144);
#pragma unroll
          for (int i = 0; i < 8; ++i)
            v[0][i] = __builtin_amdgcn_raw_buffer_load_b128(ra, (lane >> 3) * 6144 + (lane & 7) * 16, (kt * 64 + i * 8) * 6144 + wave * 2048 + jc * 128, 0);
          SCHED;
          BAR; SCHED;
          job_colsum(rs, v[0], 0, g, jc, wave, lane);
          if (SLOWC(0, jc)) {
#pragma unroll
            for (int i = 0; i < 8; ++i) job_pack(rs, v[0][i], i, 0, kt, jc, wave, lane);
          }
        } else {
          const int fr = lane & 15, fq = lane >> 4;
          const rsrc_t rx = mk_rsrc(p.x, 2048 * 2048);
#pragma unroll
          for (int i = 0; i < 16; ++i)
            v[i & 1][i >> 1] = __builtin_amdgcn_raw_buffer_load_b128(rx, fr * 2048 + fq * 16, (g * 128 + (j & (NJ - 1)) * (128 / NJ) + ((i >> 1) % (8 / NJ)) * 16) * 2048 + ((KB0(g, j) + (i >> 1) / (8 / NJ)) & 15) * 128 + (i & 1) * 64, 0);
          SCHED;
          BAR; SCHED;
          const f32x4 zero = {0.f, 0.f, 0.f, 0.f};
          const bf16x8 ones = {0x3f80, 0x3f80, 0x3f80, 0x3f80, 0x3f80, 0x3f80, 0x3f80, 0x3f80};
          float sm[8], q[8];
#pragma unroll
          for (int m = 0; m < 8; ++m) {
            const f32x4 a = MFMA(ones, as_frag(v[1][m]), MFMA(ones, as_frag(v[0][m]), zero));
            const f32x4 b = MFMA(as_frag(v[1][m]), as_frag(v[1][m]), MFMA(as_frag(v[0][m]), as_frag(v[0][m]), zero));
            sm[m] = a[0];
            q[m] = (fr & 2) ? ((fr & 1) ? b[3] : b[2]) : ((fr & 1) ? b[1] : b[0]);
          }
          if (fq == (fr >> 2)) {
#pragma unroll
            for (int m = 0; m < 8; ++m) st8_sc1(rs, ((m % (8 / NJ)) * 16 + fr) * 8 + (m / (8 / NJ)) * 1024, st_off(g, 0) + JROT(j) * 1024 + (j & (NJ - 1)) * (1024 / NJ), u32x2{__float_as_uint(sm[m]), __float_as_uint(q[m])});
          }
        }
      }
  const char *pA = (const char*)p.x, *pW = (const char*)p.wqkvA;
  unsigned zA = 2048 * 2048, bA = g0 * (128 * 2048), kA = 128, pcA = 32 * 2048, vA;
  unsigned zW = 1024 * 6144, bW = j0 * 128, kW = 64 * 6144, pcW = 2048, hW = 32 * 6144, vW;
  int kb = KB0(g0, j0), dj = -1;
  {
    const int lane = tid & 63, hw = wave0 & 3;
    vA = (hw * 8 + (lane >> 3)) * 2048 + (((lane & 7) ^ (((hw & 1) * 4 + (lane >> 4)) & 7)) * 16);
    vW = (hw * 8 + (lane >> 3)) * 6144 + (((lane & 7) ^ ((((lane >> 4) & 1) | ((hw & 1) << 1)) << 1)) * 16);
  }

#pragma unroll 1
  for (int l0 = 0; l0 < 4; ++l0) {
    f32x4 acc[2][4];
    auto kvpart = [&](unsigned va, unsigned pa, unsigned pb, unsigned kvb, int lane, int wave) {
      const f32x4 zero = {0.f, 0.f, 0.f, 0.f};
      const bf16x8 ones = {0x3f80, 0x3f80, 0x3f80, 0x3f80, 0x3f80, 0x3f80, 0x3f80, 0x3f80};
      f32x4 c0 = zero, c1 = zero, z0 = zero;
#pragma unroll
      for (int ks = 0; ks < 4; ++ks) {
        const bf16x8 v = lds_ld<bf16x8>(va + ks * 2048);
        const bf16x8 a0 = tr2(pa + ks * 32 * 160, pa + (ks * 32 + 16) * 160);
        const bf16x8 a1 = tr2(pb + ks * 32 * 160, pb + (ks * 32 + 16) * 160);
        c0 = MFMA(a0, v, c0);
        c1 = MFMA(a1, v, c1);
        z0 = MFMA(a0, ones, z0);
      }
      const u32x4 c = nosent(pack(c0, c1));
      const bool dt = wave & 2;
      st16_sc1(rs, lane * 16, kvb + (unsigned)wave * 1024, u32x4{dt ? c[2] : c[0], dt ? c[3] : c[1], dt ? c[0] : c[2], dt ? c[1] : c[3]});
      if ((lane & 15) == 0)
        st8_sc1(rs, lane, kvb + KVP_Z + (wave >> 2) * 128 + (wave & 1) * 64 + (wave & 2) * 4, nosent2(z0));
    };
#define KV_ADDR                                                                                                            \
    unsigned va = lds0 + SC_VF + (wave >> 2) * 8192 + ((wave >> 1) & 1) * 1024 + lane * 16;                                \
    unsigned pa = lds0 + SC_PHIK + ((wave >> 2) * 128 + fq * 4 + (fr >> 2)) * 160 + (wave & 1) * 64 + (lane & 3) * 8;      \
    unsigned kvj = WS_KVP + (unsigned)j * KVP_J + (unsigned)(l >> 1) * KVP_L;                                              \
    unsigned pb = pa + 32 - (wave & 2) * 16;                                                                               \
    pa += (wave & 2) * 16;                                                                                                 \
    OPAQUE_V(va); OPAQUE_V(pa); OPAQUE_V(pb); OPAQUE_S(kvj)
    if (cw) {
      LOCALS;
      unsigned otile, kvo, l16, pr1, pr4;
      int g2, j2, l2, ln2;
      qkv_loop(l & 1, wm, h, lane, l == 0, [&](int st) {
        if (st == 0) {
          g2 = g0; j2 = j0; l2 = l0; ln2 = tid;
          OPAQUE_S(g2); OPAQUE_S(j2); OPAQUE_S(l2); OPAQUE_V(ln2);
          return;
        }
        otile = ob_off(g2, j2, l2) + (unsigned)(wm * 8 + h) * 1024;
        kvo = WS_KVP + (unsigned)j2 * KVP_J + (unsigned)g2 * KVP_G + (unsigned)(l2 >> 1) * KVP_L;
        l16 = (ln2 & 63) * 16;
        pr1 = lds0 + L_RSTD + (16 + wm * 64 + (ln2 & 15)) * 4;
        pr4 = lds0 + L_RSTD + wm * 256 + (ln2 & 48);
        asm("" : "+s"(otile)); asm("" : "+s"(kvo)); asm("" : "+v"(l16)); asm("" : "+v"(pr1)); asm("" : "+v"(pr4));
      }, [&](auto isb_c, f32x4 (&a)[4][6], auto& mm6) {
        const f32x4 zero = {0.f, 0.f, 0.f, 0.f};
        constexpr float L2E = 1.4426950408889634f;
        float rq[4];
#define ilv8(nv) do { _Pragma("unroll") for (int i_ = 0; i_ < 8; ++i_) { SGB(0x008, 1); SGB(0x100, 2); SGB(0x002, nv); } SCHED; } while (0)
        if constexpr (decltype(isb_c)::value) {
          constexpr float QS = 0.17677669529663687f * L2E, NEG = -3.0e38f;
          const bf16x8 ones = {0x3f80, 0x3f80, 0x3f80, 0x3f80, 0x3f80, 0x3f80, 0x3f80, 0x3f80};
          u32x4 kf[5], vf[5], qf[4], pf[4];
          f32x4 rs4[4], bias[2], s0[4], s1[4], dn[4];
          float x[4][4], mx[4];
          unsigned psel0, psel1;
          auto pkkv = [&](int m) { kf[m + 1] = pack(a[m][2] * rq[m], a[m][3] * rq[m]); vf[m + 1] = pack(a[m][4] * rs4[m], a[m][5] * rs4[m]); };
          auto pkq = [&](int m) { qf[m] = pack(a[m][0], a[m][1]); };
          auto st1 = [&](int m) {
            x[m][0] = vmax2(s0[m][0], s0[m][1]); x[m][1] = vmax2(s0[m][2], s0[m][3]); x[m][2] = vmax2(s1[m][0], s1[m][1]); x[m][3] = vmax2(s1[m][2], s1[m][3]);
            mx[m] = vmax2(vmax2(x[m][0], x[m][1]), vmax2(x[m][2], x[m][3]));
          };
          auto st2 = [&](int m) {
            const float mxl = mx[m] * -rq[m];
#pragma unroll
            for (int e = 0; e < 4; ++e) x[m][e] = __builtin_fmaf(x[m][e], rq[m], mxl);
          };
          auto st3 = [&](int m) {
#pragma unroll
            for (int e = 0; e < 4; ++e) x[m][e] = __builtin_amdgcn_exp2f(x[m][e]);
          };
          auto st5 = [&](int m) {
            const unsigned p0 = pk2(x[m][0], x[m][1]), p1 = pk2(x[m][2], x[m][3]);
            pf[m] = u32x4{__builtin_amdgcn_perm(0u, p0, psel0), __builtin_amdgcn_perm(0u, p0, psel1), __builtin_amdgcn_perm(0u, p1, psel0), __builtin_amdgcn_perm(0u, p1, psel1)};
          };
          auto st6 = [&](int m) {
            dn[m] = MFMA(ones, as_frag(pf[m]), zero);
            s0[m] = MFMA(as_frag(u32x4{vf[m][0], vf[m][1], vf[m + 1][0], vf[m + 1][1]}), as_frag(pf[m]), zero);
            s1[m] = MFMA(as_frag(u32x4{vf[m][2], vf[m][3], vf[m + 1][2], vf[m + 1][3]}), as_frag(pf[m]), zero);
            asm("" : "+v"(dn[m]));
          };
          auto st7 = [&](int m) { const float inv = __builtin_amdgcn_rcpf(dn[m][0]); pf[m] = nosent(pack(s0[m] * inv, s1[m] * inv)); };
          mm6(3);
#pragma unroll
          for (int m = 0; m < 4; ++m) rq[m] = lds_ld<float>(pr1 + m * 64);
#pragma unroll
          for (int m = 3; m >= 0; --m) rs4[m] = lds_ld<f32x4>(pr4 + 64 + m * 64);
          unsigned pv_ = l16 + lds0 + L_PREV + h * 2048;
          asm("" : "+v"(pv_));
          static_for<0, 8>([&](auto i_c) __attribute__((always_inline)) {
            constexpr int i = decltype(i_c)::value;
            constexpr u64 mk = window_mask(i >> 2, i & 3);
            bias[i >> 2][i & 3] = __builtin_amdgcn_inverse_ballot_w64(mk) ? 0.f : NEG;
          });
          asm("" : "+v"(bias[0])); asm("" : "+v"(bias[1]));
          const unsigned hso = kvo + KVP_HALO + h * 2048, hsi = hso - (g > 0 ? KVP_G : 0u);
          ilv8(2);
          mm6(2);
          pkkv(3);
          ilv8(2);
          OPAQUE_V(kf[4]); OPAQUE_V(vf[4]);
          if (wm) {
            st16_sc1(rs, l16, hso, nosent(kf[4]));
            st16_sc1(rs, l16, hso + 1024, nosent(vf[4]));
          } else {
            lds_st<u32x4>(pv_, kf[4]);
            lds_st<u32x4>(pv_ + 1024, vf[4]);
          }
          SCHED;
          mm6(1);
          pkq(3); pkkv(2);
          ilv8(3);
          WAITL0;
          BAR;
          SCHED;
          mm6(0);
          pkq(2); pkkv(1);
          ilv8(3);
          s0[3] = MFMA(as_frag(kf[3]), as_frag(qf[3]), bias[0]); s1[3] = MFMA(as_frag(kf[4]), as_frag(qf[3]), bias[1]);
          s0[2] = MFMA(as_frag(kf[2]), as_frag(qf[2]), bias[0]); s1[2] = MFMA(as_frag(kf[3]), as_frag(qf[2]), bias[1]);
          pkq(1); pkkv(0);
#pragma unroll
          for (int i = 0; i < 4; ++i) { SGB(0x008, 1); SGB(0x002, 5); }
          SCHED;
          const bool halo = wm == 0 && g > 0;
          const u32x4 hk = ld16_sc1(rs, l16, hsi), hv = ld16_sc1(rs, l16, hsi + 1024);
          SCHED;
          s0[1] = MFMA(as_frag(kf[1]), as_frag(qf[1]), bias[0]); s1[1] = MFMA(as_frag(kf[2]), as_frag(qf[1]), bias[1]);
          st1(3); st1(2);
#pragma unroll
          for (int m = 0; m < 4; ++m) rq[m] *= QS;
          SCHED;
          st1(1);
          pkq(0);
          psel0 = __builtin_amdgcn_inverse_ballot_w64(0xaaaaaaaaaaaaaaaaull) ? 0x01000c0cu : 0x0c0c0100u;
          psel1 = __builtin_amdgcn_inverse_ballot_w64(0xaaaaaaaaaaaaaaaaull) ? 0x03020c0cu : 0x0c0c0302u;
          { float d = mx[1]; red4<true>(mx[3], mx[2], mx[1], d, [](float v) { return v; }); }
          s1[0] = MFMA(as_frag(kf[1]), as_frag(qf[0]), bias[1]);
          SCHED;
          st2(3); st2(2); st2(1);
          SCHED;
          st3(3); st3(2); st3(1);
          SCHED;
          st5(3); st6(3); st5(2); st6(2); st5(1); st6(1);
          SCHED;
          u32x4 pk = lds_ld<u32x4>(pv_), pv = lds_ld<u32x4>(pv_ + 1024);
          st7(3); st7(2); st7(1);
          SCHED;
#pragma unroll
          for (int m = 3; m > 0; --m) st16_sc1(rs, l16, otile + m * 2048, pf[m]);
          if (!wm && g == 0) bias[0] = f32x4{NEG, NEG, NEG, NEG};
          SCHED;
          if (halo) {
            pk = hk; pv = hv;
            for (unsigned spins = 0; !dead && __any(umax4v(umax4v(0u, pk), pv) == SENT); ++spins) {
              if (spins > SPIN_CAP) { dead = true; break; }
              CLOBBER;
              pk = ld16_sc1(rs, l16, hsi); pv = ld16_sc1(rs, l16, hsi + 1024);
              OPAQUE_V(pk); OPAQUE_V(pv);
            }
          }
          vf[0] = pv;
          s0[0] = MFMA(as_frag(pk), as_frag(qf[0]), bias[0]);
          st1(0);
          mx[0] = red_max4(mx[0]);
          st2(0); st3(0); st5(0); st6(0); st7(0);
          st16_sc1(rs, l16, otile, pf[0]);
        } else {
          LOCALS;
          const int fr = lane & 15, fq = lane >> 4;
          f32x4 rs4[4], r[4][2];
          const bf16x8 ones = {0x3f80, 0x3f80, 0x3f80, 0x3f80, 0x3f80, 0x3f80, 0x3f80, 0x3f80};
          auto proj = [&](int m, int n, const bf16x8& w0, const bf16x8& w1) {
            const u32x4 f = pack(a[m][n], a[m][n + 1]);
            r[m][0] = MFMA(w0, as_frag(f), zero); r[m][1] = MFMA(w1, as_frag(f), zero);
          };
          mm6(3, 0, 2); mm6(2, 0, 2); mm6(1, 0, 2); mm6(0, 0, 2);
          unsigned wa = lds0 + L_WPHI + h * 2048 + (fq * 4 + (fr >> 2)) * 64 + (lane & 3) * 8, kw = lds0 + L_PREV + hw * 2048 + l16;
          asm("" : "+v"(wa)); asm("" : "+v"(kw));
#pragma unroll
          for (int m = 0; m < 4; ++m) rq[m] = lds_ld<float>(pr1 + m * 64);
#pragma unroll
          for (int m = 0; m < 4; ++m) rs4[m] = lds_ld<f32x4>(pr4 + 64 + m * 64);
          const bf16x8 w0 = tr2(wa, wa + 1024);
          const bf16x8 w1 = tr2(wa + 32, wa + 32 + 1024);
#pragma unroll
          for (int i = 0; i < 8; ++i) { SGB(0x008, 1); SGB(0x100, 2); SGB(0x002, 2); }
          SCHED;
          mm6(3, 2, 4); mm6(2, 2, 4);
          lds_st<u32x4>(kw + 1024, pack(a[3][2], a[3][3]));
          lds_st<u32x4>(kw, pack(a[2][2], a[2][3]));
#pragma unroll
          for (int i = 0; i < 2; ++i) { SGB(0x008, 1); SGB(0x002, 2); SGB(0x008, 1); SGB(0x002, 2); SGB(0x200, 1); }
          SCHED;
          mm6(1, 2, 4); mm6(0, 2, 4);
          proj(0, 2, w0, w1); proj(1, 2, w0, w1);
#pragma unroll
          for (int i = 0; i < 4; ++i) { SGB(0x008, 1); SGB(0x002, 2); }
          SCHED;
          WAITL0;
          BAR;
          SCHED;
          unsigned pw = lds0 + SC_PHIK + (h * 128 + wm * 64 + fr) * 160 + fq * 8, vw = lds0 + SC_VF + h * 8192 + wm * 4096 + l16;
          asm("" : "+v"(pw)); asm("" : "+v"(vw));
#pragma unroll
          for (int m = 0; m < 4; ++m) rq[m] *= L2E;
#pragma unroll
          for (int i = 0; i < 4; ++i)
            lds_st<u32x4>(vw + i * 1024, pack(a[i & 2][4 + (i & 1)] * rs4[i & 2], a[(i & 2) + 1][4 + (i & 1)] * rs4[(i & 2) + 1]));
          SCHED;
          phik2(r, rq, pw);
          KV_ADDR;
          WAITL0;
          BAR;
          kvpart(va, pa, pb, kvo, lane, wave);
          SCHED;
#pragma unroll
          for (int m = 0; m < 4; ++m) mm6(m, 4, 8);
          SCHED;
          CLOBBER;
          BAR;
          u32x4 qp[4], qn[4];
          float sqp[4], sqn[4];
#pragma unroll
          for (int m = 0; m < 4; ++m) proj(m, 0, w0, w1);
          SCHED;
          float ep[4][8], en[4][8];
          hedgehog<4>(r, rq, ep, en);
          const unsigned zo = (lane >> 3) * KVP_G + (lane & 7) * 16, zs = kvj + KVP_Z + h * 128;
          u32x4 zv[2] = {ld16_sc1(rs, zo, zs), ld16_sc1(rs, zo, zs + 8 * KVP_G)};
          SCHED;
#pragma unroll
          for (int m = 0; m < 4; ++m) {
            qp[m] = pack8(ep[m]); qn[m] = pack8(en[m]);
            sqp[m] = MFMA(ones, as_frag(qp[m]), zero)[0];
            sqn[m] = MFMA(ones, as_frag(qn[m]), zero)[0];
          }
          SCHED;
#pragma unroll
          for (int m = 0; m < 4; ++m) { sqp[m] = __builtin_amdgcn_rcpf(sqp[m]); sqn[m] = __builtin_amdgcn_rcpf(sqn[m]); }
#pragma unroll
          for (int m = 0; m < 4; ++m) { OPAQUE_V(qp[m]); OPAQUE_V(qn[m]); OPAQUE_V(sqp[m]); OPAQUE_V(sqn[m]); }
          unsigned ko = (lane >> 5) * KVP_G + (lane & 31) * 16, ks = kvj + (unsigned)((j ^ RX) - j) * KVP_J + KVP_SUM + h * (8 * KVP_G);
          bf16x8 sel = rowsum_sel(lane);
          OPAQUE_V(ko); OPAQUE_S(ks); OPAQUE_V(sel);
          for (unsigned spins = 0; !dead && __any(umax4v(umax4v(0u, zv[0]), zv[1]) == SENT); ++spins) {
            if (spins > SPIN_CAP) { dead = true; break; }
            CLOBBER;
            zv[0] = ld16_sc1(rs, zo, zs); zv[1] = ld16_sc1(rs, zo, zs + 8 * KVP_G);
          }
          {
            const f32x4 z = rowsum_fin(MFMA(sel, as_frag(zv[1]), MFMA(sel, as_frag(zv[0]), zero)));
            if ((lane & 40) == 0) lds_st<u32x2>(lds0 + L_ZF + hw * 128 + fr * 16 + fq * 8, u32x2{pk2(z[0], z[1]), pk2(z[2], z[3])});
            WAITL0;
            const bf16x8 zf0 = lds_ld<bf16x8>(lds0 + L_ZF + hw * 128 + fq * 16), zf1 = lds_ld<bf16x8>(lds0 + L_ZF + hw * 128 + 64 + fq * 16);
            float dp[4], dn[4];
#pragma unroll
            for (int m = 0; m < 4; ++m) { dp[m] = MFMA(zf0, as_frag(qp[m]), zero)[0]; dn[m] = MFMA(zf1, as_frag(qn[m]), zero)[0]; }
            SCHED;
#pragma unroll
            for (int m = 0; m < 4; ++m) {
              const float inv = __builtin_amdgcn_rcpf(__builtin_fmaf(dp[m], sqp[m], __builtin_fmaf(dn[m], sqn[m], 1e-6f)));
              sqp[m] *= inv; sqn[m] *= inv;
              OPAQUE_V(sqp[m]); OPAQUE_V(sqn[m]);
            }
          }
          BAR;
          __builtin_amdgcn_s_sleep(H2D);
          u32x4 kv[4];
#pragma unroll
          for (int i = 0; i < 4; ++i) kv[i] = ld16_sc1(rs, ko, ks + i * (2 * KVP_G));
          SCHED;
#pragma unroll
          for (int m = 0; m < 4; ++m) { qp[m] = pack8(ep[m], sqp[m]); qn[m] = pack8(en[m], sqn[m]); }
#pragma unroll
          for (int m = 0; m < 4; ++m) { OPAQUE_V(qp[m]); OPAQUE_V(qn[m]); }
          SCHED;
          for (unsigned spins = 0; !dead && __any(max(umax4v(umax4v(0u, kv[0]), kv[1]), umax4v(umax4v(0u, kv[2]), kv[3])) == SENT); ++spins) {
            if (spins > SPIN_CAP) { dead = true; break; }
            CLOBBER;
#pragma unroll
            for (int i = 0; i < 4; ++i) kv[i] = ld16_sc1(rs, ko, ks + i * (2 * KVP_G));
          }
          f32x4 o[4][2];
#pragma unroll
          for (int m = 0; m < 4; ++m)
#pragma unroll
            for (int i = 0; i < 2; ++i) o[m][i] = MFMA(as_frag(kv[2 * i + 1]), as_frag(qn[m]), MFMA(as_frag(kv[2 * i]), as_frag(qp[m]), zero));
#pragma unroll
          for (int m = 0; m < 4; ++m) st16_sc1(rs, l16, otile + m * 2048, nosent(pack(o[m][0], o[m][1])));
        }
      });
#pragma unroll
      for (int i = 0; i < 8; ++i) asm volatile("" : "=v"(acc[i >> 2][i & 3]));
    } else {
      __builtin_amdgcn_s_setprio(3);
      if (l0) {
        WAITV(3);
        WAITL0;
        BAR;
      }
      LOCALS;
      u32x4 xr[2][2];
      const unsigned lin = lane * 16, vlin = lin + hw * 1024;
      const int jr = j & ~3;
      u32x4 sent4 = {SENT, SENT, SENT, SENT};
      OPAQUE_V(sent4);
      const unsigned cb0 = ob_off(g, j, l), cb2 = xb_off(g, l) + j * TS;
      const unsigned cb1 = WS_KVP + (unsigned)j * KVP_J + (unsigned)g * KVP_G + (unsigned)(l >> 1) * KVP_L;
      unsigned ra0;
      int tr = 0;
      auto ldres = [&](int t) {
        if (tr == t) {
#pragma unroll
          for (int i = 0; i < 4; ++i) xr[i >> 1][i & 1] = lds_ld<u32x4>((ra0 ^ ((i & 1) * 64)) + (t % 3) * XS + ((i >> 1) ^ 1) * 2048);
        }
      };
      auto aslot = [&](int t) { return lds0 + L_R2 + (t % 3) * XS + hw * 1024; };
      auto wslot = [&](int t) { return lds0 + L_R1 + (t & 3) * 24576 + hw * 1024; };
      const bool l0f = l == 0;
      const rsrc_t rA = mk_rsrc(pA, zA);
      auto kseq = [&](int t) { return (kb + t - (t <= dj ? 1 : 0)) & 15; };
      auto offA = [&](int t) { return bA + (unsigned)kseq(t) * kA; };
      auto offW = [&](int t) { return bW + (unsigned)kseq(t) * kW; };
      auto ldA = [&](int t) { dma4<4096>(rA, vA, offA(t), pcA, aslot(t)); };
      auto ldWh = [&](int t, unsigned o, int hf) { dma3<8192>(mk_rsrc(pW, zW), vW, o + (hf ? hW : 0u), pcW, wslot(t) + hf * 4096); };
      const bool slowj = l0f && SLOWC(0, j);
      auto packedW = [&]() {
        pW = p.ws; zW = WS_SPAN; bW = WS_WP + (unsigned)j * (16 * 24576); kW = 24576; pcW = 8192; hW = 4096; vW = vlin;
      };
      auto ldW = [&](int t) { const unsigned o = offW(t); ldWh(t, o, 0); ldWh(t, o, 1); };
      constexpr auto spread = [](int n) { unsigned m = 0; for (int t = 0; t < 26; ++t) if ((t + 1) * n / 13 > t * n / 13) m |= 1u << t; return m; };
      constexpr unsigned sp4 = spread(4), sp8 = spread(8), sp12 = spread(12);
      const unsigned smask = (isb ? (l == 3 ? sp4 : sp8) : sp12) >> ((((g + j * 5) & 15) * 13) >> 4);
      const unsigned cbm = isb ? cb2 : cb1;
      auto clean = [&](int t) __attribute__((always_inline)) {
        if (t < 13 && ((smask >> t) & 1)) {
          const unsigned cc = __builtin_popcount(smask & ((1u << t) - 1));
          const unsigned cb = cb0 + (cc >= 4 ? cbm - cb0 : 0u) + (cc >= 8 ? cb2 - cbm : 0u), pc = (cc + g + j) & 3;
          st16_sc1(rs, (lane >> 4) * 4096 + (lane & 15) * 16, cb + hw * 1024 + pc * 256, sent4);
        }
      };
      unsigned flo;
      unsigned ready = ~0u;
      u32x4 cv[4];
#pragma unroll
      for (int ii = 0; ii < 4; ++ii) asm volatile("" : "=v"(cv[ii]));
      auto chkrd = [&](int t) {
#pragma unroll
        for (int ii = 0; ii < 4; ++ii) cv[ii] = lds_ld<u32x4>(aslot(t) + lin + ii * 4096);
      };
      auto chkbad = [&]() { return __any(max(umax4v(umax4v(0u, cv[0]), cv[1]), umax4v(umax4v(0u, cv[2]), cv[3])) == SENT) && !l0f; };
      auto refetch = [&](int t) __attribute__((always_inline)) {
        for (unsigned spins = 0; !dead; ++spins) {
          if (spins > SPIN_CAP) { dead = true; break; }
          WAITV(0);
          ldA(t);
          ldA(t + 1);
          WAITV(0);
          chkrd(t);
          if (!chkbad()) break;
          __builtin_amdgcn_s_sleep(1);
        }
      };
      if (l0f) {
        u32x2 gw2;
        ld8_asm(gw2, rs, WS_EPOCH + 0u * lin);
        BAR;
        ldW(0); ldA(0);
        dma1(mk_rsrc(p.wphi, 32 * 2048), vlin, j * 4096, lds0 + L_WPHI + hw * 1024);
        ldW(1);
        WAITV(17);
        OPAQUE_V(gw2);
        const u64 gword = ((u64)(unsigned)UNI(gw2[1]) << 32) | (unsigned)UNI(gw2[0]);
        u64 z = gword + 0x9e3779b97f4a7c15ull;
        z = (z ^ (z >> 30)) * 0xbf58476d1ce4e5b9ull;
        z = (z ^ (z >> 27)) * 0x94d049bb133111ebull;
        z = z ^ (z >> 31);
        z = (z == gword) ? ~z : z;
        epoch = u32x2{(unsigned)z, (unsigned)(z >> 32)};
        tr = (j - kb) & 15;
        WAITV(7);
        BAR;
        ldA(1);
      } else {
        ldW(1);
        __builtin_amdgcn_s_sleep(XD);
        ldA(1);
      }
      ldA(2);
      ldW(2);
#pragma unroll
      for (int i = 0; i < 4; ++i) asm volatile("" : "=v"(xr[i >> 1][i & 1]));
      unsigned oA = offA(3), oW = 0;
      OPAQUE_S(oA);
      ra0 = lds0 + L_R2 + (hw * 32 + (lane & 15)) * 128 + (((((lane >> 4) & 1) * 2 + (lane >> 5)) ^ ((lane >> 1) & 7)) * 16);
      OPAQUE_V(ra0);
      ldres(0);
      clean(0);
      WAITV(10);
      chkrd(1);
      if (__builtin_expect(chkbad(), 0)) refetch(1);
      const int fr = lane & 15, fq = lane >> 4;
      u32x2 fl;
      f32x4 js[4], jc[4];
      float ra[4], rb[4];
      const bool slowo = SLOWC(2 + (l & 1), j);
      const char* pO;
      unsigned zO, bO, pcO, vO, clo;
      u32x4 wo2[2];
      asm volatile("" : "=v"(wo2[0])); asm volatile("" : "=v"(wo2[1]));
      const unsigned fwo = myrep + fl_line(FL_WO + (isb ? 1 : 0), jr + (lane >> 4)) + (lane & 15) * 8;
      static_for<1, 14>([&](auto t_c) __attribute__((always_inline)) {
        constexpr int t = decltype(t_c)::value;
        constexpr auto rest = [](int s) { return 6 + (s == NVAL - 2 || s == 5 || s == 11) + ((s == 7 || s == 8) ? 4 : 0); };
        constexpr int NW = t == 1 ? 63 : 4 + rest(t - 1);
        if (t >= NVAL && t <= 6 && __builtin_expect((ready & (1u << kseq(t + 2))) == 0, 0)) {
          WAITV(NW); WAITL0; BAR;
          ready = poll_need(rs, flo, epoch, ready, 1u << kseq(t + 2), dead);
          dma4<4096>(rA, vA, oA, pcA, aslot(t + 2));
        } else if (t == 7 && __builtin_expect(ready != ~0u, 0)) {
          WAITV(NW); WAITL0; BAR;
          poll_all(rs, flo, epoch, dead);
          dma4<4096>(rA, vA, oA, pcA, aslot(t + 2));
        } else {
          bar_dma4<4096, NW>(rA, vA, oA, pcA, aslot(t + 2));
        }
        if (t == 1 && lead) set_flags(rs, fl_line(FL_X + l, g), j, l0f ? fl_line(FL_CS, j) : fl_line(FL_X + l, g), l0f ? g : j, epoch, lane);
        if (t == 7 && slowj) packedW();
        oW = offW(t + 2);
        if (t < 13) oA = offA(t + 3);
        OPAQUE_S(oA); OPAQUE_S(oW);
        clean(t);
        if (t <= NVAL && !l0f) { WAITV(rest(t - 1) + 4); chkrd(t + 1); }
        if (t == WT + 3 && l0f && SLOWC(2, jr + hw)) {
          OPAQUE_V(wo2[0]); OPAQUE_V(wo2[1]);
#pragma unroll
          for (int i = 0; i < 2; ++i) st16_sc1(rs, lin, WS_WO + (unsigned)((jr + hw) * 131072 + g * 8192 + ((j & 3) * 2 + i) * 1024), wo2[i]);
        }
        if (t == WT + 6 && l0f && lead) set_flags(rs, fl_line(FL_WO, j), g, fl_line(FL_WO, j), g, epoch, lane);
        ldres(t);
        if (t == 7) {
#pragma unroll
          for (int i = 0; i < 4; ++i) ld16_asm(jc[i], rs, fq * 768 + hw * 192 + (fr < 12 ? fr : 0) * 16, WS_CS + (unsigned)(((isb ? 16 : 0) + j) * 16 + i * 4) * 768);
        }
        if (t == 8) {
#pragma unroll
          for (int i = 0; i < 4; ++i) ld16_asm(js[i], rs, fq * 1024 + hw * 256 + fr * 16, st_off(g, l) + i * 4096);
        }
        SCHED;
        ldWh(t + 2, oW, 0);
        SCHED;
        bool bad = false;
        if (t <= NVAL) bad = chkbad();
        if (t == 1) {
          const unsigned fx = fl_line(FL_X + l, g), fc = fl_line(l0f ? FL_CS : FL_PK, jr) + ((j + fq) & 3) * 128;
          flo = myrep + ((l < 2 && lane >= 16) ? fc : fx) + fr * 8;
          OPAQUE_V(flo);
        }
        if (t == 9) {
          pO = slowo ? p.ws : (const char*)(isb ? p.woB : p.woA);
          zO = slowo ? WS_SPAN : 1024 * 2048; bO = slowo ? WS_WO + (unsigned)((isb ? 16 : 0) + j) * 131072 : j * 128; pcO = slowo ? 4096 : 32 * 2048;
          vO = slowo ? vlin : (hw * 8 + (lane >> 3)) * 2048 + wo_swz(lane);
          clo = myrep + ((lane & 16) ? fl_line(FL_CLC + l, (lane & 32) ? j ^ RX : j) : fl_line(FL_CLR + l, g)) + fr * 8;
          OPAQUE_S(pO); OPAQUE_S(zO); OPAQUE_S(bO); OPAQUE_S(pcO); OPAQUE_V(vO); OPAQUE_V(clo);
        }
        if (t == 10) {
#pragma unroll
          for (int i = 0; i < 4; ++i) OPAQUE_V(jc[i]);
          const f32x4 s = (jc[0] + jc[1]) + (jc[2] + jc[3]);
#pragma unroll
          for (int i = 0; i < 4; ++i) { ra[i] = s[i]; OPAQUE_V(ra[i]); }
        }
        if (t == 11) {
#pragma unroll
          for (int i = 0; i < 4; ++i) OPAQUE_V(js[i]);
          const f32x4 s = (js[0] + js[1]) + (js[2] + js[3]);
#pragma unroll
          for (int i = 0; i < 4; ++i) { rb[i] = s[i]; OPAQUE_V(rb[i]); }
        }
        if (t == 12) {
          red2_add(ra[0], ra[1]); red2_add(ra[2], ra[3]); red2_add(rb[0], rb[1]); red2_add(rb[2], rb[3]);
          if (lane < 12) lds_st<f32x4>(lds0 + L_CS + hw * 192 + lane * 16, f32x4{ra[0], ra[1], ra[2], ra[3]});
#pragma unroll
          for (int i = 0; i < 4; ++i) OPAQUE_V(rb[i]);
        }
        if (t == 13) {
          const float m0 = rb[0] * (1.0f / 1024), m1 = rb[2] * (1.0f / 1024);
          const float r0 = __builtin_amdgcn_rsqf(fmaxf(__builtin_fmaf(rb[1], 1.0f / 1024, -m0 * m0), 0.f) + 1e-5f);
          const float r1 = __builtin_amdgcn_rsqf(fmaxf(__builtin_fmaf(rb[3], 1.0f / 1024, -m1 * m1), 0.f) + 1e-5f);
          if (lane < 16) {
            lds_st<f32x2>(lds0 + L_MEAN + 64 + hw * 128 + fr * 8, f32x2{-m0, -m1});
            lds_st<f32x2>(lds0 + L_RSTD + 64 + hw * 128 + fr * 8, f32x2{r0, r1});
          }
        }
        SCHED;
        ldWh(t + 2, oW, 1);
        if (t == NVAL - 1 || t == 6) {
          WAITV(10);
          OPAQUE_V(fl);
          if (t == 6) ready = __all(is_set(fl, epoch)) ? ~0u : 0u;
          else {
            ready = l0f ? ~0u : (unsigned)__ballot(is_set(fl, epoch));
            if (slowj && __all(is_set(fl, epoch))) packedW();
          }
        }
        if (t == 12) { WAITV(10); OPAQUE_V(fl); }
        if (t == 13 && slowo && __builtin_expect(!__all(is_set(fl, epoch)), 0)) poll_all(rs, fwo, epoch, dead);
        if (t == NVAL - 2 || t == 5) ld8_asm(fl, rs, flo);
        if (t == 11) ld8_asm(fl, rs, fwo);
        if (t == WT && l0f) {
#pragma unroll
          for (int i = 0; i < 2; ++i) ld16in_asm(wo2[i], mk_rsrc(p.woA, 1024 * 2048), (lane >> 3) * 2048 + wo_swz(lane), (g * 64 + (j & 3) * 16 + i * 8) * 2048 + (jr + hw) * 128);
        }
        if constexpr (t <= NVAL) { if (__builtin_expect(bad, 0)) refetch(t + 1); }
      });
      auto stO = [&](int i) { dma4<4096>(mk_rsrc(pO, zO), vO, bO + wo_src(i, j) * (4 * pcO), pcO, lds0 + wo_slot(i) + hw * 1024); };
      auto clcheck = [&]() {
        OPAQUE_V(fl);
        if (__builtin_expect(!__all(is_set(fl, epoch)), 0)) poll_all(rs, clo, epoch, dead);
      };
      auto accinit = [&]() {
#pragma unroll
        for (int m = 0; m < 2; ++m)
#pragma unroll
          for (int pp = 0; pp < 2; ++pp) {
            auto r0_ = __builtin_amdgcn_permlane16_swap(xr[m][pp][0], xr[m][pp][2], false, false);
            auto r1_ = __builtin_amdgcn_permlane16_swap(xr[m][pp][1], xr[m][pp][3], false, false);
            acc[m][2 * pp] = f32x4{lo16(r0_[0]), hi16(r0_[0]), lo16(r1_[0]), hi16(r1_[0])};
            acc[m][2 * pp + 1] = f32x4{lo16(r0_[1]), hi16(r0_[1]), lo16(r1_[1]), hi16(r1_[1])};
          }
#pragma unroll
        for (int i = 0; i < 8; ++i) OPAQUE_V(acc[i >> 2][i & 3]);
      };
      WAITV(10);
      WAITL0;
      BAR;
      if (lead) set_flags(rs, fl_line(FL_CLR + l, g), j, fl_line(FL_CLC + l, j), g, epoch, lane);
      if (isb) {
        stO(0); stO(2); stO(3);
        WAITV(12);
        BAR;
        stO(1); stO(4); stO(5);
        __builtin_amdgcn_s_setprio(0);
        BAR;
        __builtin_amdgcn_s_sleep(BD);
        stO(7); stO(6);
        ld8_asm(fl, rs, clo);
        accinit();
        WAITV(0);
        clcheck();
      } else {
        u32x4 vb[8];
#pragma unroll
        for (int i = 0; i < 8; ++i) asm volatile("" : "=v"(vb[i]));
        stO(0); stO(2); stO(3);
        ldres(14);
        WAITV(12);
        WAITL0;
        BAR;
        stO(1);
        ldres(15);
        if (l0f) {
          const bool wj = hw != 0;
          const char* bp = (const char*)(wj ? p.wqkvB : p.woB);
          unsigned rs_ = wj ? 6144 : 2048, sz = rs_ * 1024, ss = rs_ * 8, s2 = wj ? 16 * 6144 : 128;
          unsigned sb = g * 64 * rs_ + (wj ? (hw - 1) * 2048 + JOBC(j, hw - 1) * 128 : (j & 3) * (16 * 2048) + jr * 128);
          OPAQUE_S(bp); OPAQUE_S(rs_); OPAQUE_S(sz); OPAQUE_S(sb); OPAQUE_S(ss); OPAQUE_S(s2);
          const rsrc_t rb = mk_rsrc(bp, sz);
#pragma unroll
          for (int i = 0; i < 8; ++i) ld16in_asm(vb[i], rb, (lane >> 3) * rs_ + (lane & 7) * 16, sb + (i & 1) * ss + (i >> 1) * s2);
        }
        __builtin_amdgcn_s_setprio(0);
        KV_ADDR;
        bf16x8 sel = rowsum_sel(lane);
        unsigned h1 = (lane >> 3) * KVP_G + (lane & 7) * 16;
        unsigned pw = lds0 + SC_PHIK + (h * 128 + wm * 64 + 32 + fr) * 160 + fq * 8, kw = lds0 + L_PREV + hw * 2048 + lin;
        const unsigned wa = lds0 + L_WPHI + h * 2048 + (fq * 4 + (fr >> 2)) * 64 + (lane & 3) * 8;
        bf16x8 w0 = tr2(wa, wa + 1024), w1 = tr2(wa + 32, wa + 32 + 1024);
        float cq[4];
#pragma unroll
        for (int m = 0; m < 2; ++m) { cq[m] = lds_ld<float>(lds0 + L_RSTD + (16 + wm * 64 + 32 + m * 16 + fr) * 4) * 1.4426950408889634f; OPAQUE_V(cq[m]); }
        OPAQUE_V(sel); OPAQUE_V(h1); OPAQUE_V(pw); OPAQUE_V(kw); OPAQUE_V(w0); OPAQUE_V(w1);
        BAR;
        ld8_asm(fl, rs, clo);
        {
          const f32x4 zero = {0.f, 0.f, 0.f, 0.f};
          f32x4 r[4][2];
#pragma unroll
          for (int m = 0; m < 2; ++m) {
            const bf16x8 f = lds_ld<bf16x8>(kw + m * 1024);
            r[m][0] = MFMA(w0, f, zero); r[m][1] = MFMA(w1, f, zero);
          }
          phik2(r, cq, pw);
        }
        WAITV(0);
#pragma unroll
        for (int i = 0; i < 8; ++i) OPAQUE_V(vb[i]);
        clcheck();
        WAITL0;
        BAR;
        kvpart(va, pa, pb, kvj + (unsigned)g * KVP_G, lane, wave);
        CLOBBER;
        BAR;
        stO(4); stO(5);
        const f32x4 zero = {0.f, 0.f, 0.f, 0.f};
        const unsigned s1 = kvj + (unsigned)((j ^ RX) - j) * KVP_J + g * 512 + hw * 128;
        __builtin_amdgcn_s_sleep(H1S);
        u32x4 v[2] = {ld16_sc1(rs, h1, s1), ld16_sc1(rs, h1, s1 + 8 * KVP_G)};
        for (unsigned spins = 0; !dead && __any(umax4v(umax4v(0u, v[0]), v[1]) == SENT); ++spins) {
          if (spins > SPIN_CAP) { dead = true; break; }
          CLOBBER;
          v[0] = ld16_sc1(rs, h1, s1); v[1] = ld16_sc1(rs, h1, s1 + 8 * KVP_G);
        }
        const f32x4 c = rowsum_fin(MFMA(sel, as_frag(v[1]), MFMA(sel, as_frag(v[0]), zero)));
        if ((lane & 40) == 0)
          st8_sc1(rs, fr * 16 + fq * 8, kvj + (unsigned)g * KVP_G + KVP_SUM + hw * 128, nosent2(c));
        CLOBBER;
        BAR;
        accinit();
        __builtin_amdgcn_s_sleep(H2D);
        stO(6); stO(7);
        if (l0f) {
          if (hw) {
            job_colsum(rs, vb, 1, g, JOBC(j, hw - 1), hw - 1, lane);
            if (SLOWC(1, JOBC(j, hw - 1))) {
#pragma unroll
              for (int i = 0; i < 8; ++i) job_pack(rs, vb[i], i, 1, g, JOBC(j, hw - 1), hw - 1, lane);
            }
          } else {
#pragma unroll
            for (int i = 0; i < 8; ++i)
              if (SLOWC(3, jr + (i >> 1))) st16_sc1(rs, (lane >> 3) * 128 + wo_swz(lane), WS_WO + (unsigned)((16 + jr + (i >> 1)) * 131072 + g * 8192 + ((j & 3) * 2 + (i & 1)) * 1024), vb[i]);
          }
        }
        WAITV(0);
      }
    }
    {
      LOCALS;
      const int fr = lane & 15, fq = lane >> 4;
      const int kh = half;
      unsigned v0 = lane * 16 + hw * 4096 + kh * 2048, v1 = lane * 16 + hw * 4096 + (kh ^ 1) * 2048;
      const int r0 = (JROT(j) >> 1) + NJ / 2;
      const unsigned ob0 = ob_off(g, kh, l);
      const unsigned pko = myrep + fl_line(FL_PK, JOBC(j, fq - 1)) + fr * 8;
      u32x4 R[8][4];
      u32x2 pkf;
      auto issue_ = [&](int i) {
        const unsigned so = ob0 + (unsigned)((r0 + i) & 7) * (2 * TS);
        R[i][0] = ld16_sc1(rs, v0, so); R[i][1] = ld16_sc1(rs, v1, so);
        R[i][2] = ld16_sc1(rs, v0 + 1024, so); R[i][3] = ld16_sc1(rs, v1 + 1024, so);
      };
      auto issue = [&](int i) { issue_(i); SCHED; };
      constexpr int LA = LOOKAHEAD;
      __builtin_amdgcn_s_setprio(3);
      unsigned so0 = ob0 + (unsigned)(r0 & 7) * (2 * TS);
      OPAQUE_V(v0); OPAQUE_V(v1); OPAQUE_S(so0);
      BAR;
      SCHED;
      R[0][0] = ld16_sc1(rs, v0, so0); R[0][1] = ld16_sc1(rs, v1, so0);
      R[0][2] = ld16_sc1(rs, v0 + 1024, so0); R[0][3] = ld16_sc1(rs, v1 + 1024, so0);
      SCHED;
      __builtin_amdgcn_s_setprio(2);
      if (l == 0 && lead) set_flags(rs, fl_line(FL_PK, j), g, fl_line(FL_WO + 1, j), g, epoch, lane);

      auto vmax = [&](int i) { return umax4v(umax4v(umax4v(umax4v(0u, R[i][0]), R[i][1]), R[i][2]), R[i][3]); };
      auto bad = [&](int i) { return __any(umax4v(umax4v(umax4v(umax4v(0u, R[i][0]), R[i][1]), R[i][2]), R[i][3]) == SENT); };
      auto poll = [&](int i, int nfl) {
        for (unsigned spins = 0; !dead; ++spins) {
          if (spins > SPIN_CAP) { dead = true; break; }
          __builtin_amdgcn_s_sleep(2);
          CLOBBER;
          issue(i);
#pragma unroll
          for (int k = i + 1; k < i + nfl && k < 8; ++k)
            if (bad(k)) issue(k);
          WAITV(0);
#pragma unroll
          for (int k = i + 1; k < i + nfl && k < 8; ++k)
#pragma unroll
            for (int f = 0; f < 4; ++f) OPAQUE_V(R[k][f]);
          if (!bad(i)) break;
        }
      };
      unsigned bb[4];
      {
        const int kr = fq * 4 + (fr >> 2);
        const int gs = (kr >> 1) & 3;
#pragma unroll
        for (int c = 0; c < 4; ++c) bb[c] = lds0 + kh * 8192 + kr * 128 + ((c ^ gs) * 32) + (lane & 3) * 8;
      }
      bf16x8 bf[2][4];
      auto rdB = [&](int i, int hh) {
        if (hh == 0) {
#pragma unroll
          for (int c = 0; c < 4; ++c) { bb[c] += wo_slot(i) - (i ? wo_slot(i - 1) : 0u); asm("" : "+v"(bb[c])); }
        }
#pragma unroll
        for (int c = 0; c < 4; ++c) bf[hh][c] = tr2(bb[c] + hh * 4096, bb[c] + hh * 4096 + 2048);
      };
      auto mms = [&](int i, int hh) {
#pragma unroll
        for (int n = 0; n < 4; ++n)
#pragma unroll
          for (int m = 0; m < 2; ++m) acc[m][n] = MFMA(bf[hh][n], as_frag(R[i][hh * 2 + m]), acc[m][n]);
      };
      if (!half) {
#pragma unroll
        for (int i = 0; i < 8; ++i) acc[i >> 2][i & 3] = f32x4{0.f, 0.f, 0.f, 0.f};
      }
      const char* obase = (l == 3) ? (const char*)p.out : (const char*)p.ws;
      unsigned osize = (l == 3) ? 2048 * 2048 : WS_SPAN;
      const bool slown = SLOWC((l & 1) ^ 1, j);
      pW = slown ? p.ws : (const char*)(isb ? p.wqkvA : p.wqkvB);
      bW = slown ? WS_WP + (unsigned)((isb ? 0 : 16) + j) * (16 * 24576) : j * 128;
      kW = slown ? 24576 : 64 * 6144; zW = slown ? WS_SPAN : 1024 * 6144; pcW = slown ? 8192 : 2048; hW = slown ? 4096 : 32 * 6144;
      vA = hw * 1024 + lane * 16;
      vW = slown ? vA : (hw * 8 + (lane >> 3)) * 6144 + (((lane & 7) ^ ((((lane >> 4) & 1) | ((hw & 1) << 1)) << 1)) * 16);
      pA = p.ws; zA = WS_SPAN; bA = xb_off(g, l); kA = TS; pcA = 4096; kb = JROT(j) + KX; dj = (j - kb) & 15;
      OPAQUE_S(pW); OPAQUE_S(bW); OPAQUE_S(kW); OPAQUE_S(zW); OPAQUE_S(pcW); OPAQUE_S(hW); OPAQUE_V(vA); OPAQUE_V(vW); OPAQUE_S(bA);
      unsigned sq = bW + j * kW + kh * hW, dst = lds0 + L_R1 + kh * 4096 + hw * 1024;
      OPAQUE_S(obase); OPAQUE_S(osize); OPAQUE_S(sq); OPAQUE_S(dst);
      const rsrc_t ro = mk_rsrc(obase, osize);
      unsigned so[2];
      unsigned xw = lds0 + SC_XCH + wave * 4096 + lane * 16;
      unsigned xa = lds0 + SC_XCH + (wave ^ 4) * 4096 + lane * 16;
      unsigned sb0 = (l == 3) ? g * (128 * 2048) + j * 128 : xb_off(g, l) + j * TS;
      {
        const int row = (hw * 2 + kh) * 16 + fr, ch = (fq & 1) * 2 + (fq >> 1);
        const unsigned sw = (l == 3) ? 0u : (unsigned)((row >> 1) & 7), rb = (unsigned)row << ((l == 3) ? 11 : 7);
#pragma unroll
        for (int pp = 0; pp < 2; ++pp) { so[pp] = rb + (((ch + 4 * pp) ^ sw) * 16); OPAQUE_V(so[pp]); }
        OPAQUE_V(xa); OPAQUE_V(xw); OPAQUE_S(sb0);
      }
      SCHED;
#pragma unroll
      for (int i = 1; i < LA; ++i) issue(i);
      rdB(0, 0);
      unsigned mx = vmax(0);
#pragma unroll
      for (int i = 0; i < 8; ++i) {
        SCHED;
        if (__builtin_expect(__any(mx == SENT), 0)) poll(i, LA);
        if (i == W1S - 2) { pkf = ld8_sc1(rs, pko, 0); SCHED; }
        if (i == W1S) {
          OPAQUE_V(pkf);
          if (l == 0 && lead && slown && !__all(is_set(pkf, epoch))) poll_all(rs, pko, epoch, dead);
          BAR;
        }
        if (i == 7 && l < 3) dma3<8192>(mk_rsrc(pW, zW), vW, sq, pcW, dst);
        if (i < 4 && !isb) __builtin_amdgcn_s_sleep(PACE);
        if (i == 2) __builtin_amdgcn_s_setprio(1);
        if (i == 5) __builtin_amdgcn_s_setprio(0);
        SCHED;
        rdB(i, 1);
        if (i == 7) break;
        if (i + LA < 8) issue_(i + LA);
        mms(i, 0);
#pragma unroll
        for (int k = 0; k < 4; ++k) { SGB(0x008, 1); SGB(0x100, 1); SGB(0x020, 1); SGB(0x008, 1); SGB(0x100, 1); }
        SCHED;
        rdB(i + 1, 0); mx = vmax(i + 1);
        mms(i, 1);
#pragma unroll
        for (int k = 0; k < 4; ++k) { SGB(0x008, 1); SGB(0x100, 2); }
#pragma unroll
        for (int k = 4; k < 8; ++k) { SGB(0x008, 1); SGB(0x002, 8 / (8 - 4)); }
      }
#pragma unroll
      for (int n = 0; n < 4; ++n) acc[1][n] = MFMA(bf[0][n], as_frag(R[7][1]), acc[1][n]);
#pragma unroll
      for (int n = 0; n < 4; ++n) acc[0][n] = MFMA(bf[0][n], as_frag(R[7][0]), acc[0][n]);
#pragma unroll
      for (int k = 0; k < 8; ++k) { SGB(0x008, 1); SGB(0x100, 1); }
      SCHED;
#pragma unroll
      for (int n = 0; n < 4; ++n) acc[1][n] = MFMA(bf[1][n], as_frag(R[7][3]), acc[1][n]);
#pragma unroll
      for (int n = 0; n < 4; ++n) acc[0][n] = MFMA(bf[1][n], as_frag(R[7][2]), acc[0][n]);
#pragma unroll
      for (int n = 0; n < 4; ++n) lds_st<f32x4>(xw + n * 1024, acc[1][n]);
      SGB(0x008, 4);
#pragma unroll
      for (int k = 0; k < 4; ++k) { SGB(0x008, 1); SGB(0x200, 1); }
      SCHED;
      WAITL0;
      BAR;
      SCHED;
      f32x4 pa[4];
#pragma unroll
      for (int n = 0; n < 4; ++n) pa[n] = lds_ld<f32x4>(xa + n * 1024);
      SCHED;
      u32x4 o[2];
#pragma unroll
      for (int pp = 0; pp < 2; ++pp) {
        const f32x4 a = acc[0][2 * pp] + pa[2 * pp], b = acc[0][2 * pp + 1] + pa[2 * pp + 1];
        auto r0_ = __builtin_amdgcn_permlane16_swap(pk2(a[0], a[1]), pk2(b[0], b[1]), false, false);
        auto r1_ = __builtin_amdgcn_permlane16_swap(pk2(a[2], a[3]), pk2(b[2], b[3]), false, false);
        o[pp] = nosent(u32x4{r0_[0], r1_[0], r0_[1], r1_[1]});
        st16_sc1(ro, so[pp], sb0, o[pp]);
      }
      if (l < 3) {
        lds_st<u32x4>(lds0 + L_R2 + so[0], o[0]);
        lds_st<u32x4>(lds0 + L_R2 + so[1], o[1]);
        const f32x4 zero = {0.f, 0.f, 0.f, 0.f};
        const bf16x8 ones = {0x3f80, 0x3f80, 0x3f80, 0x3f80, 0x3f80, 0x3f80, 0x3f80, 0x3f80};
        const f32x4 a = MFMA(ones, as_frag(o[1]), MFMA(ones, as_frag(o[0]), zero));
        const f32x4 b = MFMA(as_frag(o[1]), as_frag(o[1]), MFMA(as_frag(o[0]), as_frag(o[0]), zero));
        const float q = (fr & 2) ? ((fr & 1) ? b[3] : b[2]) : ((fr & 1) ? b[1] : b[0]);
        if (fq == (fr >> 2)) st8_sc1(rs, ((hw * 2 + kh) * 16 + fr) * 8, st_off(g, l + 1) + j * 1024, u32x2{__float_as_uint(a[0]), __float_as_uint(q)});
      } else if (lead && blockIdx.x == 0 && lane == 0) {
        st8_sc1(rs, WS_EPOCH, 0, epoch);
      }
    }
  }
  asm volatile(".Lpf_end:");
}

extern "C" void kernel_launch(void* const* d_in, const int* in_sizes, int n_in, void* d_out, int out_size,
                              void* d_ws, size_t ws_size, hipStream_t stream) {
  Params p;
  p.x = (const u16*)d_in[0];
  p.wqkvA = (const u16*)d_in[1];
  p.wphi = (const u16*)d_in[2];
  p.woA = (const u16*)d_in[3];
  p.wqkvB = (const u16*)d_in[5];
  p.woB = (const u16*)d_in[6];
  p.out = (u16*)d_out;
  p.ws = (char*)d_ws;
  (void)hipFuncSetAttribute((const void*)mega_kernel, hipFuncAttributeMaxDynamicSharedMemorySize, L_TOTAL);
  mega_kernel<<<256, 512, L_TOTAL, stream>>>(p);
}
```

```cpp
#include <hip/hip_runtime.h>
#include <stdint.h>
#include <type_traits>
constexpr int LOOKAHEAD = 2;
constexpr int NVAL = 4;
constexpr int WT = 1;
constexpr int W1S = 6;
constexpr int NJ = 8;
constexpr int KX = 8;
constexpr int RX = 8;
constexpr unsigned TS = 17664;
#define XD 3
#define H1S 6
#define H2D 5
#define BD 8
#define PACE 4

typedef unsigned short u16;
typedef unsigned long long u64;
typedef __attribute__((ext_vector_type(8))) short bf16x8;
typedef __attribute__((ext_vector_type(4))) short s16x4;
typedef __attribute__((ext_vector_type(4))) float f32x4;
typedef __attribute__((ext_vector_type(2))) float f32x2;
typedef __attribute__((ext_vector_type(4))) unsigned u32x4;
typedef __attribute__((ext_vector_type(2))) unsigned u32x2;
typedef __attribute__((ext_vector_type(2))) __bf16 bf16x2_t;
#define LAS __attribute__((address_space(3)))
typedef LAS s16x4* lds_s16x4_ptr;

constexpr unsigned NREP = 8, REP_STRIDE = 17 * 2048;
enum { FL_X = 0, FL_CS = 4, FL_CLR = 5, FL_CLC = 9, FL_PK = 13, FL_WO = 14 };
__device__ __forceinline__ unsigned fl_line(int set, int a) { return (unsigned)(set * 16 + a) * 128; }
constexpr unsigned WS_EPOCH = 512u << 10;
constexpr unsigned WS_CS = 1u << 20;
constexpr unsigned WS_KVP = 4u << 20;
constexpr unsigned KVP_G = 32768, KVP_J = 16 * KVP_G, KVP_L = 16384, KVP_Z = 8192, KVP_SUM = 8704, KVP_HALO = 12u << 10;
constexpr unsigned SENT = 0xffffffffu;
constexpr unsigned WS_X = 14u << 20, WS_O = 29u << 20, WS_ST = 2u << 20;
__device__ __forceinline__ unsigned xb_off(int g, int l) { return WS_X + (unsigned)(l * 16 + g) * (16 * TS); }
__device__ __forceinline__ unsigned ob_off(int g, int j, int l) { return WS_O + (unsigned)((l * 16 + g) * 16 + j) * TS; }
__device__ __forceinline__ unsigned st_off(int g, int l) { return WS_ST + (unsigned)(l * 16 + g) * (16u << 10); }
constexpr unsigned WS_WP = 48u << 20;
constexpr unsigned WS_WO = 60u << 20;
constexpr unsigned WS_SPAN = 64u << 20;

constexpr unsigned L_R1 = 0;
constexpr unsigned L_R2 = 98304, XS = 16384;
constexpr unsigned L_MISC = 147456;
constexpr unsigned L_MEAN = L_MISC;
constexpr unsigned L_RSTD = L_MISC + 576;
constexpr unsigned L_CS = L_MISC + 1152;
constexpr unsigned L_WPHI = L_MISC + 2688;
constexpr unsigned L_SINK = L_MISC + 6912;
constexpr unsigned L_ZF = L_MISC + 7168;
constexpr unsigned L_PREV = L_MISC + 8192;
constexpr unsigned L_TOTAL = 163840;

#define JROT(j) ((j) & ~(NJ - 1))
constexpr int NX = NJ / 2;
#define KB0(g, j) (JROT(j) + (16 / NX) * ((g) & (NX - 1)))
#define JOBC(j, w) (((j) & ~3) + (((j) + (w) + 1) & 3))

#define SLOWC(m, jc) ((slowm >> ((m) * 8 + ((jc) & 7))) & 1)

struct Params {
  const u16* x;
  const u16* wqkvA;
  const u16* wphi;
  const u16* woA;
  const u16* wqkvB;
  const u16* woB;
  u16* out;
  char* ws;
};

#define SCHED __builtin_amdgcn_sched_barrier(0)
#define BAR __builtin_amdgcn_s_barrier()
#define SGB(m, n) __builtin_amdgcn_sched_group_barrier(m, n, 0)
#define WAITV(n) asm volatile("s_waitcnt vmcnt(%0)" ::"n"(n) : "memory")
#define WAITL0 asm volatile("s_waitcnt lgkmcnt(0)" ::: "memory")

__device__ __forceinline__ unsigned pk2(float a, float b) {
  f32x2 v = {a, b};
  return __builtin_bit_cast(unsigned, __builtin_convertvector(v, bf16x2_t));
}
__device__ __forceinline__ float lo16(unsigned u) { return __uint_as_float(u << 16); }
__device__ __forceinline__ float hi16(unsigned u) { return __uint_as_float(u & 0xffff0000u); }

template <int A, int B, typename F> __device__ __forceinline__ void static_for(F f) {
  if constexpr (A < B) { f(std::integral_constant<int, A>{}); static_for<A + 1, B>(f); }
}
typedef __amdgpu_buffer_rsrc_t rsrc_t;
__device__ __forceinline__ rsrc_t mk_rsrc(const void* p, unsigned bytes) { return __builtin_amdgcn_make_buffer_rsrc((void*)p, 0, bytes, 0x00020000); }
#define UNI(x) __builtin_amdgcn_readfirstlane(x)
#define DMA_HEAD "s_mov_b32 m0, %4\n\ts_mov_b32 %0, %3\n\ts_nop 2\n\tbuffer_load_dwordx4 %1, %2, %0 offen sc1 lds\n\t"
#define DMA_NEXT "s_add_u32 m0, m0, %6\n\ts_add_u32 %0, %0, %5\n\tbuffer_load_dwordx4 %1, %2, %0 offen sc1 lds\n\t"
#define DMA_OPS : "=&s"(t) : "v"(voff), "s"(r), "s"(UNI(soff)), "s"(UNI(lds)), "s"(UNI(sstep)), "i"(LSTEP) : "memory", "scc", "m0"
template <int LSTEP> __device__ __forceinline__ void dma4(rsrc_t r, unsigned voff, unsigned soff, unsigned sstep, unsigned lds) {
  unsigned t;
  asm volatile(DMA_HEAD DMA_NEXT DMA_NEXT DMA_NEXT DMA_OPS);
}
template <int LSTEP> __device__ __forceinline__ void dma3(rsrc_t r, unsigned voff, unsigned soff, unsigned sstep, unsigned lds) {
  unsigned t;
  asm volatile(DMA_HEAD DMA_NEXT DMA_NEXT DMA_OPS);
}
template <int LSTEP, int N> __device__ __forceinline__ void bar_dma4(rsrc_t r, unsigned voff, unsigned soff, unsigned sstep, unsigned lds) {
  unsigned t;
  asm volatile("s_mov_b32 m0, %4\n\ts_mov_b32 %0, %3\n\ts_nop 0\n\ts_waitcnt vmcnt(%7) lgkmcnt(0)\n\ts_barrier\n\t"
               "buffer_load_dwordx4 %1, %2, %0 offen sc1 lds\n\t" DMA_NEXT DMA_NEXT DMA_NEXT
               : "=&s"(t) : "v"(voff), "s"(r), "s"(UNI(soff)), "s"(UNI(lds)), "s"(UNI(sstep)), "i"(LSTEP), "n"(N) : "memory", "scc", "m0");
}
__device__ __forceinline__ void dma1(rsrc_t r, unsigned voff, unsigned soff, unsigned lds) {
  asm volatile("s_mov_b32 m0, %3\n\ts_nop 3\n\tbuffer_load_dwordx4 %0, %1, %2 offen sc1 lds" ::"v"(voff), "s"(r), "s"(UNI(soff)), "s"(UNI(lds)) : "memory", "m0");
}
__device__ __forceinline__ void touch(rsrc_t r, unsigned voff, unsigned lds) {
  asm volatile("s_mov_b32 m0, %2\n\ts_nop 3\n\tbuffer_load_dword %0, %1, 0 offen lds" ::"v"(voff), "s"(r), "s"(UNI(lds)) : "memory", "m0");
}
__device__ __forceinline__ void ld8_asm(u32x2& d, rsrc_t r, unsigned voff) {
  asm volatile("s_nop 4\n\tbuffer_load_dwordx2 %0, %1, %2, 0 offen sc1" : "=v"(d) : "v"(voff), "s"(r) : "memory");
}

__device__ __forceinline__ void ld16in_asm(u32x4& d, rsrc_t r, unsigned voff, unsigned soff) {
  asm volatile("s_nop 4\n\tbuffer_load_dwordx4 %0, %1, %2, %3 offen" : "=v"(d) : "v"(voff), "s"(r), "s"(UNI(soff)) : "memory");
}
__device__ __forceinline__ void ld16_asm(f32x4& d, rsrc_t r, unsigned voff, unsigned soff) {
  asm volatile("s_nop 4\n\tbuffer_load_dwordx4 %0, %1, %2, %3 offen sc1" : "=v"(d) : "v"(voff), "s"(r), "s"(UNI(soff)) : "memory");
}

template <typename T> __device__ __forceinline__ T lds_ld(unsigned a) { return *(LAS T*)(a); }
template <typename T> __device__ __forceinline__ void lds_st(unsigned a, T v) { *(LAS T*)(a) = v; }
__device__ __forceinline__ bf16x8 tr2(unsigned a0, unsigned a1) {
  s16x4 v0 = __builtin_amdgcn_ds_read_tr16_b64_v4i16((lds_s16x4_ptr)(a0));
  s16x4 v1 = __builtin_amdgcn_ds_read_tr16_b64_v4i16((lds_s16x4_ptr)(a1));
  return __builtin_shufflevector(v0, v1, 0, 1, 2, 3, 4, 5, 6, 7);
}
__device__ __forceinline__ bf16x8 as_frag(u32x4 v) { return __builtin_bit_cast(bf16x8, v); }
#define MFMA(a, b, c) __builtin_amdgcn_mfma_f32_16x16x32_bf16(a, b, c, 0, 0, 0)

__device__ __forceinline__ float vmax2(float a, float b) { float inf = __builtin_inff(); asm("" : "+s"(inf)); return __builtin_amdgcn_fmed3f(a, b, inf); }
#define SWAP16(a, b) __builtin_amdgcn_permlane16_swap(__float_as_uint(a), __float_as_uint(b), false, false)
#define SWAP32(a, b) __builtin_amdgcn_permlane32_swap(__float_as_uint(a), __float_as_uint(b), false, false)
__device__ __forceinline__ void red2_max(float& a, float& b) {
  auto r = SWAP16(a, b);
  float x = vmax2(__uint_as_float(r[0]), __uint_as_float(r[1]));
  r = SWAP32(x, x);
  x = vmax2(__uint_as_float(r[0]), __uint_as_float(r[1]));
  r = SWAP16(x, x);
  a = __uint_as_float(r[0]); b = __uint_as_float(r[1]);
}
__device__ __forceinline__ void red2_add(float& a, float& b) {
  auto r = SWAP16(a, b);
  float x = __uint_as_float(r[0]) + __uint_as_float(r[1]);
  r = SWAP32(x, x);
  x = __uint_as_float(r[0]) + __uint_as_float(r[1]);
  r = SWAP16(x, x);
  a = __uint_as_float(r[0]); b = __uint_as_float(r[1]);
}
template <bool MAX, typename M> __device__ __forceinline__ void red4(float& a, float& b, float& c, float& d, M mid) {
  auto op = [](unsigned x, unsigned y) { return MAX ? vmax2(__uint_as_float(x), __uint_as_float(y)) : __uint_as_float(x) + __uint_as_float(y); };
  auto r = SWAP16(a, b), q = SWAP16(c, d);
  const float x = op(r[0], r[1]), y = op(q[0], q[1]);
  r = SWAP32(x, y);
  const float w = mid(op(r[0], r[1]));
  r = SWAP32(w, w);
  q = __builtin_amdgcn_permlane16_swap(r[1], r[1], false, false);
  r = __builtin_amdgcn_permlane16_swap(r[0], r[0], false, false);
  a = __uint_as_float(r[0]); b = __uint_as_float(r[1]); c = __uint_as_float(q[0]); d = __uint_as_float(q[1]);
}
__device__ __forceinline__ float red_max4(float x) {
  auto r = __builtin_amdgcn_permlane16_swap(__float_as_uint(x), __float_as_uint(x), false, false);
  x = vmax2(__uint_as_float(r[0]), __uint_as_float(r[1]));
  r = __builtin_amdgcn_permlane32_swap(__float_as_uint(x), __float_as_uint(x), false, false);
  return vmax2(__uint_as_float(r[0]), __uint_as_float(r[1]));
}
__device__ __forceinline__ float red_add4(float x) {
  auto r = __builtin_amdgcn_permlane16_swap(__float_as_uint(x), __float_as_uint(x), false, false);
  x = __uint_as_float(r[0]) + __uint_as_float(r[1]);
  r = __builtin_amdgcn_permlane32_swap(__float_as_uint(x), __float_as_uint(x), false, false);
  return __uint_as_float(r[0]) + __uint_as_float(r[1]);
}

__device__ __forceinline__ void st16_sc1(rsrc_t r, unsigned voff, unsigned soff, u32x4 v) {
  __builtin_amdgcn_raw_buffer_store_b128(v, r, voff, soff, 16);
  asm volatile("s_nop 1" ::"v"(v) : "memory");
}
__device__ __forceinline__ void st8_sc1(rsrc_t r, unsigned voff, unsigned soff, u32x2 v) { __builtin_amdgcn_raw_buffer_store_b64(v, r, voff, soff, 16); }
__device__ __forceinline__ u32x4 ld16_sc1(rsrc_t r, unsigned voff, unsigned soff) { return __builtin_amdgcn_raw_buffer_load_b128(r, voff, soff, 16); }
__device__ __forceinline__ u32x2 ld8_sc1(rsrc_t r, unsigned voff, unsigned soff) { return __builtin_amdgcn_raw_buffer_load_b64(r, voff, soff, 16); }
__device__ __forceinline__ f32x4 ld4f_sc1(rsrc_t r, unsigned voff, unsigned soff) { return __builtin_bit_cast(f32x4, __builtin_amdgcn_raw_buffer_load_b128(r, voff, soff, 16)); }
#define OPAQUE_V(x) asm volatile("" : "+v"(x))
#define OPAQUE_S(x) asm volatile("" : "+s"(x))

#define CLOBBER asm volatile("" ::: "memory")
constexpr unsigned SPIN_CAP = 1u << 17;
__device__ __forceinline__ bool is_set(u32x2 v, u32x2 epoch) { return v[0] == epoch[0] && v[1] == epoch[1]; }
__device__ __forceinline__ void set_flags(rsrc_t rs, unsigned lineA, int colA, unsigned lineB, int colB, u32x2 epoch, int lane) {
  const unsigned off = (lane & 7) * REP_STRIDE + ((lane & 8) ? lineB + colB * 8 : lineA + colA * 8);
  if (lane < 16) st8_sc1(rs, off, 0, epoch);
}
__device__ __forceinline__ unsigned poll_need(rsrc_t rs, unsigned voff, u32x2 epoch, unsigned ready, unsigned need, bool& dead) {
  for (unsigned spins = 0; !dead; ++spins) {
    CLOBBER;
    ready |= (unsigned)__ballot(is_set(ld8_sc1(rs, voff, 0), epoch));
    if ((ready & need) == need) break;
    if (spins > SPIN_CAP) dead = true;
    __builtin_amdgcn_s_sleep(1);
  }
  CLOBBER;
  return dead ? ~0u : ready;
}

__device__ __forceinline__ void poll_all(rsrc_t rs, unsigned voff, u32x2 epoch, bool& dead) {
  for (unsigned spins = 0; !dead; ++spins) {
    CLOBBER;
    if (__all(is_set(ld8_sc1(rs, voff, 0), epoch))) break;
    if (spins > SPIN_CAP) dead = true;
    __builtin_amdgcn_s_sleep(1);
  }
  CLOBBER;
}

constexpr unsigned SC_VF = L_R1 + 32768;
constexpr unsigned SC_PHIK = L_R1 + 49152;
constexpr unsigned SC_XCH = L_R2 + XS;
constexpr unsigned wo_slot(int s) {
  return s < 2 ? L_R2 + (unsigned)(s + 1) * XS : (s < 6 ? L_R1 + (unsigned)(s - 2) * 16384 : (s == 6 ? L_R1 + 81920 : L_R2));
}
__device__ __forceinline__ unsigned wo_src(int s, int j) { return (unsigned)(((JROT(j) >> 1) + NJ / 2 + s) & 7); }
__device__ __forceinline__ unsigned wo_swz(int lane) { return (unsigned)(((lane & 7) ^ ((lane >> 3) & 6)) * 16); }
__device__ __forceinline__ unsigned umax3(unsigned a, unsigned b, unsigned c) { return max(max(a, b), c); }
__device__ __forceinline__ unsigned umax4v(unsigned m, const u32x4& v) { return umax3(umax3(m, v[0], v[1]), v[2], v[3]); }
__device__ __forceinline__ u32x4 nosent(u32x4 v) { return u32x4{min(v[0], SENT - 1), min(v[1], SENT - 1), min(v[2], SENT - 1), min(v[3], SENT - 1)}; }

template <typename P, typename F>
__device__ __forceinline__ void qkv_loop(int isb, int wm, int h, int lane, bool first, P pre, F fin) {
  constexpr unsigned lds0 = 0;
  f32x4 acc[4][6];
  const int fr = lane & 15, fq = lane >> 4;
  unsigned ab[2], bb[2];
  {
    const int r = wm * 64 + fr;
#pragma unroll
    for (int ks = 0; ks < 2; ++ks) ab[ks] = lds0 + L_R2 + r * 128 + (((ks * 4 + fq) ^ ((fr >> 1) & 7)) * 16);
    const int kr = fq * 8 + (fr >> 2);
    const int gs = ((kr >> 1) & 1) | (((kr >> 3) & 1) << 1);
#pragma unroll
    for (int c = 0; c < 2; ++c) bb[c] = lds0 + L_R1 + kr * 128 + (((h * 2 + c) ^ gs) * 32) + (lane & 3) * 8;
  }
#pragma unroll
  for (int m = 0; m < 4; ++m)
#pragma unroll
    for (int n = 0; n < 6; ++n) acc[m][n] = f32x4{0.f, 0.f, 0.f, 0.f};
  bf16x8 af[2][4], bf[2][6];
  auto rds = [&](int ks, int p, int rot = 0) {
#pragma unroll
    for (int m = 0; m < 4; ++m) af[p][m] = lds_ld<bf16x8>(ab[ks] + m * 2048);
#pragma unroll
    for (int nn = 0; nn < 6; ++nn) {
      const int n = (nn + rot) % 6;
      const unsigned a = bb[n & 1] + (n >> 1) * 8192 + ks * 4096;
      bf[p][n] = tr2(a, a + 512);
    }
  };
  auto mms = [&](int p, int rot = 0, int cnt = 6) {
#pragma unroll
    for (int nn = 0; nn < cnt; ++nn)
#pragma unroll
      for (int mm = 0; mm < 4; ++mm) { const int n = (nn + rot) % 6, m = (n & 1) ? 3 - mm : mm; acc[m][n] = n < 4 ? MFMA(bf[p][n], af[p][m], acc[m][n]) : MFMA(af[p][m], bf[p][n], acc[m][n]); }
  };
  auto ilv2 = [&]() {
#pragma unroll
    for (int i = 0; i < 16; ++i) { SGB(0x008, 1); SGB(0x100, 1); }
#pragma unroll
    for (int i = 0; i < 4; ++i) { SGB(0x008, 1); SGB(0x002, 1); }
    SGB(0x008, 4);
  };
  auto ilv3 = [&]() {
#pragma unroll
    for (int i = 0; i < 16; ++i) { SGB(0x008, 1); SGB(0x100, 1); }
#pragma unroll
    for (int i = 0; i < 7; ++i) { SGB(0x008, 1); SGB(0x004, 2); }
    SGB(0x008, 1);
  };
#pragma unroll
  for (int i = 0; i < 24; ++i) OPAQUE_V(acc[i / 6][i % 6]);
  OPAQUE_V(ab[0]); OPAQUE_V(ab[1]); OPAQUE_V(bb[0]); OPAQUE_V(bb[1]);
  if (!first) { WAITV(3); WAITL0; }
  BAR;
  rds(0, 0);
  int sa = 0, sb = 0;
  int da = XS, db = 24576;
  auto step = [&]() {
    ab[0] += da; ab[1] += da;
    bb[0] += db; bb[1] += db;
  };
  auto ring = [&]() {
    sa = (sa == 2) ? 0 : sa + 1;
    sb = (sb == 3) ? 0 : sb + 1;
    da = (sa == 2) ? -2 * (int)XS : (int)XS;
    db = (sb == 3) ? -3 * 24576 : 24576;
  };
#pragma unroll 1
  for (int t = 14; t > 0; --t) {
    SCHED;
    rds(1, 1); mms(0);
    step();
    ilv2();
    SCHED;
    __builtin_amdgcn_s_waitcnt(0x0070);
    BAR;
    SCHED;
    rds(0, 0);
    ring();
    mms(1); ilv3();
  }
  SCHED;
  f32x4 cs[4], nm4[4];
  float nmean[4], csv[2];
  auto fold = [&](int n) {
#pragma unroll
    for (int mm = 0; mm < 4; ++mm) {
      const int m = (n & 1) ? 3 - mm : mm;
#pragma unroll
      for (int e = 0; e < 4; ++e) {
        float t = n < 4 ? __builtin_fmaf(nmean[m], cs[n & 3][e], acc[m][n][e]) : __builtin_fmaf(nm4[m][e], csv[n & 1], acc[m][n][e]);
        asm("" : "+v"(t));
        acc[m][n][e] = t;
      }
    }
  };
  {
    unsigned pm = lds0 + L_MEAN + (16 + wm * 64 + fr) * 4, pc = lds0 + L_CS + isb * 768 + (h * 32 + fq * 4) * 4;
    unsigned pm4 = lds0 + L_MEAN + (16 + wm * 64 + fq * 4) * 4, pcv = lds0 + L_CS + isb * 768 + (128 + h * 32 + fr) * 4;
    asm("" : "+v"(pm)); asm("" : "+v"(pc)); asm("" : "+v"(pm4)); asm("" : "+v"(pcv));
#pragma unroll
    for (int m = 0; m < 4; ++m) nmean[m] = lds_ld<float>(pm + m * 64);
#pragma unroll
    for (int n = 0; n < 4; ++n) cs[n ^ 2] = lds_ld<f32x4>(pc + (((n ^ 2) >> 1) * 64 + (n & 1) * 16) * 4);
#pragma unroll
    for (int m = 0; m < 4; ++m) nm4[m] = lds_ld<f32x4>(pm4 + m * 64);
#pragma unroll
    for (int n = 0; n < 2; ++n) csv[n] = lds_ld<float>(pcv + n * 64);
    rds(1, 1, 4); mms(0, 2);
    step();
    fold(2); fold(3);
    SGB(0x002, 8);
#pragma unroll
    for (int i = 0; i < 8; ++i) { SGB(0x008, 1); SGB(0x100, 2); }
#pragma unroll
    for (int i = 0; i < 11; ++i) { SGB(0x008, 1); SGB(0x100, 1); SGB(0x002, 2); }
#pragma unroll
    for (int i = 0; i < 5; ++i) { SGB(0x008, 1); SGB(0x002, 3); }
    SCHED;
    __builtin_amdgcn_s_waitcnt(0xC07F);
    BAR;
    SCHED;
    rds(0, 0, 2);
    mms(1, 4); fold(4); fold(5);
#pragma unroll
    for (int i = 0; i < 8; ++i) { SGB(0x008, 1); SGB(0x100, 1); SGB(0x002, 1); }
#pragma unroll
    for (int i = 0; i < 8; ++i) { SGB(0x008, 1); SGB(0x100, 1); SGB(0x002, 2); }
#pragma unroll
    for (int i = 0; i < 8; ++i) { SGB(0x008, 1); SGB(0x002, 2); }
    SCHED;
    pre(0);
    SCHED;
    rds(1, 1, 2);
    mms(0, 2, 4); fold(0); fold(1);
    pre(1);
#pragma unroll
    for (int i = 0; i < 16; ++i) { SGB(0x008, 1); SGB(0x100, 1); SGB(0x002, 2); SGB(0x004, 1); }
  }
  SCHED;
  auto mm6 = [&](int m, int n0 = 0, int n1 = 8) {
#pragma unroll
    for (int nn = n0; nn < n1; ++nn) {
      const int n = nn < 4 ? nn + 2 : nn & 1, p = (nn >> 1) != 2;
      acc[m][n] = n < 4 ? MFMA(bf[p][n], af[p][m], acc[m][n]) : MFMA(af[p][m], bf[p][n], acc[m][n]);
    }
  };
  if (!isb) {
    fin(std::false_type{}, acc, mm6);
#pragma unroll
    for (int i = 0; i < 24; ++i) asm volatile("" : "=v"(acc[i / 6][i % 6]));
#pragma unroll
    for (int i = 0; i < 6; ++i) asm volatile("" : "=v"(bf[1][i]));
#pragma unroll
    for (int i = 0; i < 8; ++i) asm volatile("" : "=v"(af[i >> 2][i & 3]));
    asm volatile("" : "=v"(bf[0][0])); asm volatile("" : "=v"(bf[0][1]));
  }
  OPAQUE_S(isb);
  if (isb) fin(std::true_type{}, acc, mm6);
}
__device__ __forceinline__ u32x4 pack(const f32x4& a, const f32x4& b) { return u32x4{pk2(a[0], a[1]), pk2(a[2], a[3]), pk2(b[0], b[1]), pk2(b[2], b[3])}; }

__device__ __forceinline__ u32x2 nosent2(const f32x4& a) { return u32x2{min(pk2(a[0], a[1]), SENT - 1), min(pk2(a[2], a[3]), SENT - 1)}; }
template <int N> __device__ __forceinline__ void hedgehog(const f32x4 (&r)[4][2], const float (&c)[4], float (&ep)[4][8], float (&en)[4][8]) {
  float mx[4], nm[4];
#pragma unroll
  for (int m = 0; m < N; ++m) {
    const f32x4 a = r[m][0], b = r[m][1];
    mx[m] = vmax2(fmaxf(fmaxf(a[3], b[0]), b[1]), fmaxf(fmaxf(fmaxf(fmaxf(a[0], a[1]), a[2]), b[2]), b[3]));
    nm[m] = vmax2(fmaxf(fmaxf(-a[3], -b[0]), -b[1]), fmaxf(fmaxf(fmaxf(fmaxf(-a[0], -a[1]), -a[2]), -b[2]), -b[3]));
  }
  SCHED;
  red4<true>(mx[0], mx[1], nm[0], nm[1], [](float x) { return x; });
  if (N == 4) red4<true>(mx[2], mx[3], nm[2], nm[3], [](float x) { return x; });
#pragma unroll
  for (int m = 0; m < N; ++m) { mx[m] *= -c[m]; nm[m] *= -c[m]; }
  SCHED;
#pragma unroll
  for (int m = 0; m < N; ++m) {
#pragma unroll
    for (int i = 0; i < 8; ++i) { ep[m][i] = __builtin_fmaf(r[m][i >> 2][i & 3], c[m], mx[m]); en[m][i] = __builtin_fmaf(r[m][i >> 2][i & 3], -c[m], nm[m]); }
    SCHED;
#pragma unroll
    for (int i = 0; i < 8; ++i) { ep[m][i] = __builtin_amdgcn_exp2f(ep[m][i]); en[m][i] = __builtin_amdgcn_exp2f(en[m][i]); }
    SCHED;
  }
}
__device__ __forceinline__ u32x4 pack8(const float (&e)[8], float f) {
  return u32x4{pk2(e[0] * f, e[1] * f), pk2(e[2] * f, e[3] * f), pk2(e[4] * f, e[5] * f), pk2(e[6] * f, e[7] * f)};
}
__device__ __forceinline__ u32x4 pack8(const float (&e)[8]) { return u32x4{pk2(e[0], e[1]), pk2(e[2], e[3]), pk2(e[4], e[5]), pk2(e[6], e[7])}; }

constexpr u64 window_mask(int t, int e) {
  u64 mk = 0;
  for (int ln = 0; ln < 64; ++ln) {
    const int delta = (ln & 15) + 16 - t * 16 - (ln >> 4) * 4 - e;
    if (delta >= 0 && delta <= 14 && !(delta & 1)) mk |= 1ull << ln;
  }
  return mk;
}
__device__ __forceinline__ void phik2(const f32x4 (&r)[4][2], const float (&c)[4], unsigned pw) {
  const f32x4 zero = {0.f, 0.f, 0.f, 0.f};
  const bf16x8 ones = {0x3f80, 0x3f80, 0x3f80, 0x3f80, 0x3f80, 0x3f80, 0x3f80, 0x3f80};
  float ep[4][8], en[4][8];
  f32x4 sp[2], sn[2];
  hedgehog<2>(r, c, ep, en);
#pragma unroll
  for (int m = 0; m < 2; ++m) { sp[m] = MFMA(ones, as_frag(pack8(ep[m])), zero); sn[m] = MFMA(ones, as_frag(pack8(en[m])), zero); }
#pragma unroll
  for (int m = 0; m < 2; ++m) { asm("" : "+v"(sp[m])); asm("" : "+v"(sn[m])); }
  SCHED;
#pragma unroll
  for (int m = 0; m < 2; ++m) {
    const float fp = __builtin_amdgcn_rcpf(sp[m][0]), fn = __builtin_amdgcn_rcpf(sn[m][0]);
#pragma unroll
    for (int i = 0; i < 8; ++i) { ep[m][i] *= fp; en[m][i] *= fn; }
    SCHED;
    const u32x4 pp = pack8(ep[m]), pn = pack8(en[m]);
    const unsigned d = pw + m * (16 * 160);
    lds_st<u32x2>(d, u32x2{pp[0], pp[1]});
    lds_st<u32x2>(d + 32, u32x2{pp[2], pp[3]});
    lds_st<u32x2>(d + 64, u32x2{pn[0], pn[1]});
    lds_st<u32x2>(d + 96, u32x2{pn[2], pn[3]});
    SCHED;
  }
}
__device__ __forceinline__ bf16x8 rowsum_sel(int lane) {
  const int fr = lane & 15;
  const unsigned one = (fr < 8) ? (0x3f80u << (16 * (fr & 1))) : 0u;
  const int e = (fr >> 1) & 3;
  return as_frag(u32x4{e == 0 ? one : 0u, e == 1 ? one : 0u, e == 2 ? one : 0u, e == 3 ? one : 0u});
}
__device__ __forceinline__ f32x4 rowsum_fin(f32x4 c) {
#pragma unroll
  for (int k = 0; k < 4; ++k) {
    const float a = c[k];
    c[k] = a + __int_as_float(__builtin_amdgcn_update_dpp(0, __float_as_int(a), 0x128  , 0xf, 0xf, false));
  }
  return c;
}
__device__ __forceinline__ void job_colsum(rsrc_t rs, const u32x4 (&w)[8], int mat, int g, int jc, int hw, int lane) {
  const bf16x8 sel = rowsum_sel(lane);
  f32x4 c = {0.f, 0.f, 0.f, 0.f};
#pragma unroll
  for (int i = 0; i < 8; ++i) c = MFMA(sel, as_frag(w[i]), c);
  const u32x4 w4 = __builtin_bit_cast(u32x4, rowsum_fin(c));
  unsigned so = WS_CS + (unsigned)(((mat * 16 + jc) * 16 + g) * 768);
  OPAQUE_S(so);
  if ((lane & 40) == 0) st16_sc1(rs, (unsigned)(hw * 64 + (lane & 7) * 8 + (lane >> 4) * 4) * 4, so, w4);
}
__device__ __forceinline__ unsigned img_off(int i, int lane) { return (lane >> 3) * 128 + (((lane & 7) ^ ((((lane >> 4) & 1) | ((i & 1) << 1)) << 1)) * 16); }
__device__ __forceinline__ void job_pack(rsrc_t rs, u32x4 w, int i, int mat, int g, int jc, int hw, int lane) {
  st16_sc1(rs, img_off(i, lane),
           WS_WP + (unsigned)(((mat * 16 + jc) * 16 + g) * 24576 + hw * 8192 + i * 1024), w);
}

__global__ __launch_bounds__(512, 2) void mega_kernel(Params p) {
  extern __shared__ __attribute__((aligned(16))) char smem[];
  const int tid = threadIdx.x;
  const int wave0 = __builtin_amdgcn_readfirstlane(tid >> 6);
  const int xx = blockIdx.x & 7, rr = blockIdx.x >> 3;
  const int g0 = (rr / NJ) * NX + (xx & (NX - 1)), j0 = (xx / NX) * NJ + (rr & (NJ - 1));
  constexpr unsigned lds0 = 0, lds00 = 0;
  if ((unsigned)(size_t)(LAS char*)smem != 0u) return;
#define LOCALS0 int g = g0, j = j0, l = l0, wave = wave0;                                              \
  OPAQUE_S(g); OPAQUE_S(j); OPAQUE_S(l); OPAQUE_S(wave);                                               \
  const int hw = wave & 3, wm = wave & 1, h = (wave >> 1) & 1;                                         \
  const unsigned myrep = (unsigned)(JROT(j) / 2 + (g & (NX - 1))) * REP_STRIDE;                              \
  (void)hw; (void)wm; (void)h; (void)myrep;                                                            \
  int lane = tid; OPAQUE_V(lane); lane &= 63
#define LOCALS LOCALS0; const bool isb = l & 1, lead = (wave == 4); const int half = wave >> 2; (void)isb; (void)lead; (void)half
  const rsrc_t rs = mk_rsrc(p.ws, WS_SPAN);
  const bool cw = wave0 < 4;
  if (wave0 == 3) {
    const char* pc;
    unsigned len;
    asm volatile("s_getpc_b64 %0\n.Lpf_base:\n\ts_mov_b32 %1, .Lpf_end-.Lpf_base" : "=s"(pc), "=s"(len));
    const unsigned off = ((unsigned)rr * 16 + (tid & 15)) * 128;
    touch(mk_rsrc(pc, len), ((int)len > 0 && off + 4 <= len) ? off : 0u, lds00 + L_SINK);
  }
  auto slowbit = [](const void* q) { return 1u << ((3 - ((unsigned)(size_t)q >> 7)) & 7); };
  unsigned slowm = slowbit(p.wqkvA) | (slowbit(p.wqkvB) << 8) | (slowbit(p.woA) << 16) | (slowbit(p.woB) << 24);
  OPAQUE_S(slowm);
  u32x2 epoch = {0u, 0u};
  bool dead = false;
      if (cw) {
        const int l0 = 0;
        LOCALS;
        u32x4 v[2][8];
        if (wave < 3) {
          const int jc = JOBC(j, wave), kt = (KB0(g, j) + (unsigned)g / NX) & 15;
          const rsrc_t ra = mk_rsrc(p.wqkvA, 1024 * 6144);
#pragma unroll
          for (int i = 0; i < 8; ++i)
            v[0][i] = __builtin_amdgcn_raw_buffer_load_b128(ra, (lane >> 3) * 6144 + (lane & 7) * 16, (kt * 64 + i * 8) * 6144 + wave * 2048 + jc * 128, 0);
          SCHED;
          BAR; SCHED;
          job_colsum(rs, v[0], 0, g, jc, wave, lane);
          if (SLOWC(0, jc)) {
#pragma unroll
            for (int i = 0; i < 8; ++i) job_pack(rs, v[0][i], i, 0, kt, jc, wave, lane);
          }
        } else {
          const int fr = lane & 15, fq = lane >> 4;
          const rsrc_t rx = mk_rsrc(p.x, 2048 * 2048);
#pragma unroll
          for (int i = 0; i < 16; ++i)
            v[i & 1][i >> 1] = __builtin_amdgcn_raw_buffer_load_b128(rx, fr * 2048 + fq * 16, (g * 128 + (j & (NJ - 1)) * (128 / NJ) + ((i >> 1) % (8 / NJ)) * 16) * 2048 + ((KB0(g, j) + (i >> 1) / (8 / NJ)) & 15) * 128 + (i & 1) * 64, 0);
          SCHED;
          BAR; SCHED;
          const f32x4 zero = {0.f, 0.f, 0.f, 0.f};
          const bf16x8 ones = {0x3f80, 0x3f80, 0x3f80, 0x3f80, 0x3f80, 0x3f80, 0x3f80, 0x3f80};
          float sm[8], q[8];
#pragma unroll
          for (int m = 0; m < 8; ++m) {
            const f32x4 a = MFMA(ones, as_frag(v[1][m]), MFMA(ones, as_frag(v[0][m]), zero));
            const f32x4 b = MFMA(as_frag(v[1][m]), as_frag(v[1][m]), MFMA(as_frag(v[0][m]), as_frag(v[0][m]), zero));
            sm[m] = a[0];
            q[m] = (fr & 2) ? ((fr & 1) ? b[3] : b[2]) : ((fr & 1) ? b[1] : b[0]);
          }
          if (fq == (fr >> 2)) {
#pragma unroll
            for (int m = 0; m < 8; ++m) st8_sc1(rs, ((m % (8 / NJ)) * 16 + fr) * 8 + (m / (8 / NJ)) * 1024, st_off(g, 0) + JROT(j) * 1024 + (j & (NJ - 1)) * (1024 / NJ), u32x2{__float_as_uint(sm[m]), __float_as_uint(q[m])});
          }
        }
      }
  const char *pA = (const char*)p.x, *pW = (const char*)p.wqkvA;
  unsigned zA = 2048 * 2048, bA = g0 * (128 * 2048), kA = 128, pcA = 32 * 2048, vA;
  unsigned zW = 1024 * 6144, bW = j0 * 128, kW = 64 * 6144, pcW = 2048, hW = 32 * 6144, vW;
  int kb = KB0(g0, j0), dj = -1;
  {
    const int lane = tid & 63, hw = wave0 & 3;
    vA = (hw * 8 + (lane >> 3)) * 2048 + (((lane & 7) ^ (((hw & 1) * 4 + (lane >> 4)) & 7)) * 16);
    vW = (hw * 8 + (lane >> 3)) * 6144 + (((lane & 7) ^ ((((lane >> 4) & 1) | ((hw & 1) << 1)) << 1)) * 16);
  }

#pragma unroll 1
  for (int l0 = 0; l0 < 4; ++l0) {
    f32x4 acc[2][4];
    auto kvpart = [&](unsigned va, unsigned pa, unsigned pb, unsigned kvb, int lane, int wave) {
      const f32x4 zero = {0.f, 0.f, 0.f, 0.f};
      const bf16x8 ones = {0x3f80, 0x3f80, 0x3f80, 0x3f80, 0x3f80, 0x3f80, 0x3f80, 0x3f80};
      f32x4 c0 = zero, c1 = zero, z0 = zero;
#pragma unroll
      for (int ks = 0; ks < 4; ++ks) {
        const bf16x8 v = lds_ld<bf16x8>(va + ks * 2048);
        const bf16x8 a0 = tr2(pa + ks * 32 * 160, pa + (ks * 32 + 16) * 160);
        const bf16x8 a1 = tr2(pb + ks * 32 * 160, pb + (ks * 32 + 16) * 160);
        c0 = MFMA(a0, v, c0);
        c1 = MFMA(a1, v, c1);
        z0 = MFMA(a0, ones, z0);
      }
      const u32x4 c = nosent(pack(c0, c1));
      const bool dt = wave & 2;
      st16_sc1(rs, lane * 16, kvb + (unsigned)wave * 1024, u32x4{dt ? c[2] : c[0], dt ? c[3] : c[1], dt ? c[0] : c[2], dt ? c[1] : c[3]});
      if ((lane & 15) == 0)
        st8_sc1(rs, lane, kvb + KVP_Z + (wave >> 2) * 128 + (wave & 1) * 64 + (wave & 2) * 4, nosent2(z0));
    };
#define KV_ADDR                                                                                                            \
    unsigned va = lds0 + SC_VF + (wave >> 2) * 8192 + ((wave >> 1) & 1) * 1024 + lane * 16;                                \
    unsigned pa = lds0 + SC_PHIK + ((wave >> 2) * 128 + fq * 4 + (fr >> 2)) * 160 + (wave & 1) * 64 + (lane & 3) * 8;      \
    unsigned kvj = WS_KVP + (unsigned)j * KVP_J + (unsigned)(l >> 1) * KVP_L;                                              \
    unsigned pb = pa + 32 - (wave & 2) * 16;                                                                               \
    pa += (wave & 2) * 16;                                                                                                 \
    OPAQUE_V(va); OPAQUE_V(pa); OPAQUE_V(pb); OPAQUE_S(kvj)
    if (cw) {
      LOCALS;
      unsigned otile, kvo, l16, pr1, pr4;
      int g2, j2, l2, ln2;
      qkv_loop(l & 1, wm, h, lane, l == 0, [&](int st) {
        if (st == 0) {
          g2 = g0; j2 = j0; l2 = l0; ln2 = tid;
          OPAQUE_S(g2); OPAQUE_S(j2); OPAQUE_S(l2); OPAQUE_V(ln2);
          return;
        }
        otile = ob_off(g2, j2, l2) + (unsigned)(wm * 8 + h) * 1024;
        kvo = WS_KVP + (unsigned)j2 * KVP_J + (unsigned)g2 * KVP_G + (unsigned)(l2 >> 1) * KVP_L;
        l16 = (ln2 & 63) * 16;
        pr1 = lds0 + L_RSTD + (16 + wm * 64 + (ln2 & 15)) * 4;
        pr4 = lds0 + L_RSTD + wm * 256 + (ln2 & 48);
        asm("" : "+s"(otile)); asm("" : "+s"(kvo)); asm("" : "+v"(l16)); asm("" : "+v"(pr1)); asm("" : "+v"(pr4));
      }, [&](auto isb_c, f32x4 (&a)[4][6], auto& mm6) {
        const f32x4 zero = {0.f, 0.f, 0.f, 0.f};
        constexpr float L2E = 1.4426950408889634f;
        float rq[4];
#define ilv8(nv) do { _Pragma("unroll") for (int i_ = 0; i_ < 8; ++i_) { SGB(0x008, 1); SGB(0x100, 2); SGB(0x002, nv); } SCHED; } while (0)
        if constexpr (decltype(isb_c)::value) {
          constexpr float QS = 0.17677669529663687f * L2E, NEG = -3.0e38f;
          const bf16x8 ones = {0x3f80, 0x3f80, 0x3f80, 0x3f80, 0x3f80, 0x3f80, 0x3f80, 0x3f80};
          u32x4 kf[5], vf[5], qf[4], pf[4];
          f32x4 rs4[4], bias[2], s0[4], s1[4], dn[4];
          float x[4][4], mx[4];
          unsigned psel0, psel1;
          auto pkkv = [&](int m) { kf[m + 1] = pack(a[m][2] * rq[m], a[m][3] * rq[m]); vf[m + 1] = pack(a[m][4] * rs4[m], a[m][5] * rs4[m]); };
          auto pkq = [&](int m) { qf[m] = pack(a[m][0], a[m][1]); };
          auto st1 = [&](int m) {
            x[m][0] = vmax2(s0[m][0], s0[m][1]); x[m][1] = vmax2(s0[m][2], s0[m][3]); x[m][2] = vmax2(s1[m][0], s1[m][1]); x[m][3] = vmax2(s1[m][2], s1[m][3]);
            mx[m] = vmax2(vmax2(x[m][0], x[m][1]), vmax2(x[m][2], x[m][3]));
          };
          auto st2 = [&](int m) {
            const float mxl = mx[m] * -rq[m];
#pragma unroll
            for (int e = 0; e < 4; ++e) x[m][e] = __builtin_fmaf(x[m][e], rq[m], mxl);
          };
          auto st3 = [&](int m) {
#pragma unroll
            for (int e = 0; e < 4; ++e) x[m][e] = __builtin_amdgcn_exp2f(x[m][e]);
          };
          auto st5 = [&](int m) {
            const unsigned p0 = pk2(x[m][0], x[m][1]), p1 = pk2(x[m][2], x[m][3]);
            pf[m] = u32x4{__builtin_amdgcn_perm(0u, p0, psel0), __builtin_amdgcn_perm(0u, p0, psel1), __builtin_amdgcn_perm(0u, p1, psel0), __builtin_amdgcn_perm(0u, p1, psel1)};
          };
          auto st6 = [&](int m) {
            dn[m] = MFMA(ones, as_frag(pf[m]), zero);
            s0[m] = MFMA(as_frag(u32x4{vf[m][0], vf[m][1], vf[m + 1][0], vf[m + 1][1]}), as_frag(pf[m]), zero);
            s1[m] = MFMA(as_frag(u32x4{vf[m][2], vf[m][3], vf[m + 1][2], vf[m + 1][3]}), as_frag(pf[m]), zero);
            asm("" : "+v"(dn[m]));
          };
          auto st7 = [&](int m) { const float inv = __builtin_amdgcn_rcpf(dn[m][0]); pf[m] = nosent(pack(s0[m] * inv, s1[m] * inv)); };
          mm6(3);
#pragma unroll
          for (int m = 0; m < 4; ++m) rq[m] = lds_ld<float>(pr1 + m * 64);
#pragma unroll
          for (int m = 3; m >= 0; --m) rs4[m] = lds_ld<f32x4>(pr4 + 64 + m * 64);
          unsigned pv_ = l16 + lds0 + L_PREV + h * 2048;
          asm("" : "+v"(pv_));
          static_for<0, 8>([&](auto i_c) __attribute__((always_inline)) {
            constexpr int i = decltype(i_c)::value;
            constexpr u64 mk = window_mask(i >> 2, i & 3);
            bias[i >> 2][i & 3] = __builtin_amdgcn_inverse_ballot_w64(mk) ? 0.f : NEG;
          });
          asm("" : "+v"(bias[0])); asm("" : "+v"(bias[1]));
          const unsigned hso = kvo + KVP_HALO + h * 2048, hsi = hso - (g > 0 ? KVP_G : 0u);
          ilv8(2);
          mm6(2);
          pkkv(3);
          ilv8(2);
          OPAQUE_V(kf[4]); OPAQUE_V(vf[4]);
          if (wm) {
            st16_sc1(rs, l16, hso, nosent(kf[4]));
            st16_sc1(rs, l16, hso + 1024, nosent(vf[4]));
          } else {
            lds_st<u32x4>(pv_, kf[4]);
            lds_st<u32x4>(pv_ + 1024, vf[4]);
          }
          SCHED;
          mm6(1);
          pkq(3); pkkv(2);
          ilv8(3);
          WAITL0;
          BAR;
          SCHED;
          mm6(0);
          pkq(2); pkkv(1);
          ilv8(3);
          s0[3] = MFMA(as_frag(kf[3]), as_frag(qf[3]), bias[0]); s1[3] = MFMA(as_frag(kf[4]), as_frag(qf[3]), bias[1]);
          s0[2] = MFMA(as_frag(kf[2]), as_frag(qf[2]), bias[0]); s1[2] = MFMA(as_frag(kf[3]), as_frag(qf[2]), bias[1]);
          pkq(1); pkkv(0);
#pragma unroll
          for (int i = 0; i < 4; ++i) { SGB(0x008, 1); SGB(0x002, 5); }
          SCHED;
          const bool halo = wm == 0 && g > 0;
          const u32x4 hk = ld16_sc1(rs, l16, hsi), hv = ld16_sc1(rs, l16, hsi + 1024);
          SCHED;
          s0[1] = MFMA(as_frag(kf[1]), as_frag(qf[1]), bias[0]); s1[1] = MFMA(as_frag(kf[2]), as_frag(qf[1]), bias[1]);
          st1(3); st1(2);
#pragma unroll
          for (int m = 0; m < 4; ++m) rq[m] *= QS;
          SCHED;
          st1(1);
          pkq(0);
          psel0 = __builtin_amdgcn_inverse_ballot_w64(0xaaaaaaaaaaaaaaaaull) ? 0x01000c0cu : 0x0c0c0100u;
          psel1 = __builtin_amdgcn_inverse_ballot_w64(0xaaaaaaaaaaaaaaaaull) ? 0x03020c0cu : 0x0c0c0302u;
          { float d = mx[1]; red4<true>(mx[3], mx[2], mx[1], d, [](float v) { return v; }); }
          s1[0] = MFMA(as_frag(kf[1]), as_frag(qf[0]), bias[1]);
          SCHED;
          st2(3); st2(2); st2(1);
          SCHED;
          st3(3); st3(2); st3(1);
          SCHED;
          st5(3); st6(3); st5(2); st6(2); st5(1); st6(1);
          SCHED;
          u32x4 pk = lds_ld<u32x4>(pv_), pv = lds_ld<u32x4>(pv_ + 1024);
          st7(3); st7(2); st7(1);
          SCHED;
#pragma unroll
          for (int m = 3; m > 0; --m) st16_sc1(rs, l16, otile + m * 2048, pf[m]);
          if (!wm && g == 0) bias[0] = f32x4{NEG, NEG, NEG, NEG};
          SCHED;
          if (halo) {
            pk = hk; pv = hv;
            for (unsigned spins = 0; !dead && __any(umax4v(umax4v(0u, pk), pv) == SENT); ++spins) {
              if (spins > SPIN_CAP) { dead = true; break; }
              CLOBBER;
              pk = ld16_sc1(rs, l16, hsi); pv = ld16_sc1(rs, l16, hsi + 1024);
              OPAQUE_V(pk); OPAQUE_V(pv);
            }
          }
          vf[0] = pv;
          s0[0] = MFMA(as_frag(pk), as_frag(qf[0]), bias[0]);
          st1(0);
          mx[0] = red_max4(mx[0]);
          st2(0); st3(0); st5(0); st6(0); st7(0);
          st16_sc1(rs, l16, otile, pf[0]);
        } else {
          LOCALS;
          const int fr = lane & 15, fq = lane >> 4;
          f32x4 rs4[4], r[4][2];
          const bf16x8 ones = {0x3f80, 0x3f80, 0x3f80, 0x3f80, 0x3f80, 0x3f80, 0x3f80, 0x3f80};
          auto proj = [&](int m, int n, const bf16x8& w0, const bf16x8& w1) {
            const u32x4 f = pack(a[m][n], a[m][n + 1]);
            r[m][0] = MFMA(w0, as_frag(f), zero); r[m][1] = MFMA(w1, as_frag(f), zero);
          };
          mm6(3, 0, 2); mm6(2, 0, 2); mm6(1, 0, 2); mm6(0, 0, 2);
          unsigned wa = lds0 + L_WPHI + h * 2048 + (fq * 4 + (fr >> 2)) * 64 + (lane & 3) * 8, kw = lds0 + L_PREV + hw * 2048 + l16;
          asm("" : "+v"(wa)); asm("" : "+v"(kw));
#pragma unroll
          for (int m = 0; m < 4; ++m) rq[m] = lds_ld<float>(pr1 + m * 64);
#pragma unroll
          for (int m = 0; m < 4; ++m) rs4[m] = lds_ld<f32x4>(pr4 + 64 + m * 64);
          const bf16x8 w0 = tr2(wa, wa + 1024);
          const bf16x8 w1 = tr2(wa + 32, wa + 32 + 1024);
#pragma unroll
          for (int i = 0; i < 8; ++i) { SGB(0x008, 1); SGB(0x100, 2); SGB(0x002, 2); }
          SCHED;
          mm6(3, 2, 4); mm6(2, 2, 4);
          lds_st<u32x4>(kw + 1024, pack(a[3][2], a[3][3]));
          lds_st<u32x4>(kw, pack(a[2][2], a[2][3]));
#pragma unroll
          for (int i = 0; i < 2; ++i) { SGB(0x008, 1); SGB(0x002, 2); SGB(0x008, 1); SGB(0x002, 2); SGB(0x200, 1); }
          SCHED;
          mm6(1, 2, 4); mm6(0, 2, 4);
          SCHED;
          WAITL0;
          BAR;
          SCHED;
          proj(0, 2, w0, w1); proj(1, 2, w0, w1);
          unsigned pw = lds0 + SC_PHIK + (h * 128 + wm * 64 + fr) * 160 + fq * 8, vw = lds0 + SC_VF + h * 8192 + wm * 4096 + l16;
          asm("" : "+v"(pw)); asm("" : "+v"(vw));
#pragma unroll
          for (int m = 0; m < 4; ++m) rq[m] *= L2E;
#pragma unroll
          for (int i = 0; i < 4; ++i)
            lds_st<u32x4>(vw + i * 1024, pack(a[i & 2][4 + (i & 1)] * rs4[i & 2], a[(i & 2) + 1][4 + (i & 1)] * rs4[(i & 2) + 1]));
          SCHED;
          phik2(r, rq, pw);
          KV_ADDR;
          WAITL0;
          BAR;
          kvpart(va, pa, pb, kvo, lane, wave);
          SCHED;
#pragma unroll
          for (int m = 0; m < 4; ++m) mm6(m, 4, 8);
          SCHED;
          CLOBBER;
          BAR;
          u32x4 qp[4], qn[4];
          float sqp[4], sqn[4];
#pragma unroll
          for (int m = 0; m < 4; ++m) proj(m, 0, w0, w1);
          SCHED;
          float ep[4][8], en[4][8];
          hedgehog<4>(r, rq, ep, en);
          const unsigned zo = (lane >> 3) * KVP_G + (lane & 7) * 16, zs = kvj + KVP_Z + h * 128;
          u32x4 zv[2] = {ld16_sc1(rs, zo, zs), ld16_sc1(rs, zo, zs + 8 * KVP_G)};
          SCHED;
#pragma unroll
          for (int m = 0; m < 4; ++m) {
            qp[m] = pack8(ep[m]); qn[m] = pack8(en[m]);
            sqp[m] = MFMA(ones, as_frag(qp[m]), zero)[0];
            sqn[m] = MFMA(ones, as_frag(qn[m]), zero)[0];
          }
          SCHED;
#pragma unroll
          for (int m = 0; m < 4; ++m) { sqp[m] = __builtin_amdgcn_rcpf(sqp[m]); sqn[m] = __builtin_amdgcn_rcpf(sqn[m]); }
#pragma unroll
          for (int m = 0; m < 4; ++m) { OPAQUE_V(qp[m]); OPAQUE_V(qn[m]); OPAQUE_V(sqp[m]); OPAQUE_V(sqn[m]); }
          unsigned ko = (lane >> 5) * KVP_G + (lane & 31) * 16, ks = kvj + (unsigned)((j ^ RX) - j) * KVP_J + KVP_SUM + h * (8 * KVP_G);
          bf16x8 sel = rowsum_sel(lane);
          OPAQUE_V(ko); OPAQUE_S(ks); OPAQUE_V(sel);
          for (unsigned spins = 0; !dead && __any(umax4v(umax4v(0u, zv[0]), zv[1]) == SENT); ++spins) {
            if (spins > SPIN_CAP) { dead = true; break; }
            CLOBBER;
            zv[0] = ld16_sc1(rs, zo, zs); zv[1] = ld16_sc1(rs, zo, zs + 8 * KVP_G);
          }
          {
            const f32x4 z = rowsum_fin(MFMA(sel, as_frag(zv[1]), MFMA(sel, as_frag(zv[0]), zero)));
            if ((lane & 40) == 0) lds_st<u32x2>(lds0 + L_ZF + hw * 128 + fr * 16 + fq * 8, u32x2{pk2(z[0], z[1]), pk2(z[2], z[3])});
            WAITL0;
            const bf16x8 zf0 = lds_ld<bf16x8>(lds0 + L_ZF + hw * 128 + fq * 16), zf1 = lds_ld<bf16x8>(lds0 + L_ZF + hw * 128 + 64 + fq * 16);
            float dp[4], dn[4];
#pragma unroll
            for (int m = 0; m < 4; ++m) { dp[m] = MFMA(zf0, as_frag(qp[m]), zero)[0]; dn[m] = MFMA(zf1, as_frag(qn[m]), zero)[0]; }
            SCHED;
#pragma unroll
            for (int m = 0; m < 4; ++m) {
              const float inv = __builtin_amdgcn_rcpf(__builtin_fmaf(dp[m], sqp[m], __builtin_fmaf(dn[m], sqn[m], 1e-6f)));
              sqp[m] *= inv; sqn[m] *= inv;
              OPAQUE_V(sqp[m]); OPAQUE_V(sqn[m]);
            }
          }
          BAR;
          __builtin_amdgcn_s_sleep(H2D);
          u32x4 kv[4];
#pragma unroll
          for (int i = 0; i < 4; ++i) kv[i] = ld16_sc1(rs, ko, ks + i * (2 * KVP_G));
          SCHED;
#pragma unroll
          for (int m = 0; m < 4; ++m) { qp[m] = pack8(ep[m], sqp[m]); qn[m] = pack8(en[m], sqn[m]); }
#pragma unroll
          for (int m = 0; m < 4; ++m) { OPAQUE_V(qp[m]); OPAQUE_V(qn[m]); }
          SCHED;
          for (unsigned spins = 0; !dead && __any(max(umax4v(umax4v(0u, kv[0]), kv[1]), umax4v(umax4v(0u, kv[2]), kv[3])) == SENT); ++spins) {
            if (spins > SPIN_CAP) { dead = true; break; }
            CLOBBER;
#pragma unroll
            for (int i = 0; i < 4; ++i) kv[i] = ld16_sc1(rs, ko, ks + i * (2 * KVP_G));
          }
          f32x4 o[4][2];
#pragma unroll
          for (int m = 0; m < 4; ++m)
#pragma unroll
            for (int i = 0; i < 2; ++i) o[m][i] = MFMA(as_frag(kv[2 * i + 1]), as_frag(qn[m]), MFMA(as_frag(kv[2 * i]), as_frag(qp[m]), zero));
#pragma unroll
          for (int m = 0; m < 4; ++m) st16_sc1(rs, l16, otile + m * 2048, nosent(pack(o[m][0], o[m][1])));
        }
      });
#pragma unroll
      for (int i = 0; i < 8; ++i) asm volatile("" : "=v"(acc[i >> 2][i & 3]));
    } else {
      __builtin_amdgcn_s_setprio(3);
      if (l0) {
        WAITV(3);
        WAITL0;
        BAR;
      }
      LOCALS;
      u32x4 xr[2][2];
      const unsigned lin = lane * 16, vlin = lin + hw * 1024;
      const int jr = j & ~3;
      u32x4 sent4 = {SENT, SENT, SENT, SENT};
      OPAQUE_V(sent4);
      const unsigned cb0 = ob_off(g, j, l), cb2 = xb_off(g, l) + j * TS;
      const unsigned cb1 = WS_KVP + (unsigned)j * KVP_J + (unsigned)g * KVP_G + (unsigned)(l >> 1) * KVP_L;
      unsigned ra0;
      int tr = 0;
      auto ldres = [&](int t) {
        if (tr == t) {
#pragma unroll
          for (int i = 0; i < 4; ++i) xr[i >> 1][i & 1] = lds_ld<u32x4>((ra0 ^ ((i & 1) * 64)) + (t % 3) * XS + ((i >> 1) ^ 1) * 2048);
        }
      };
      auto aslot = [&](int t) { return lds0 + L_R2 + (t % 3) * XS + hw * 1024; };
      auto wslot = [&](int t) { return lds0 + L_R1 + (t & 3) * 24576 + hw * 1024; };
      const bool l0f = l == 0;
      const rsrc_t rA = mk_rsrc(pA, zA);
      auto kseq = [&](int t) { return (kb + t - (t <= dj ? 1 : 0)) & 15; };
      auto offA = [&](int t) { return bA + (unsigned)kseq(t) * kA; };
      auto offW = [&](int t) { return bW + (unsigned)kseq(t) * kW; };
      auto ldA = [&](int t) { dma4<4096>(rA, vA, offA(t), pcA, aslot(t)); };
      auto ldWh = [&](int t, unsigned o, int hf) { dma3<8192>(mk_rsrc(pW, zW), vW, o + (hf ? hW : 0u), pcW, wslot(t) + hf * 4096); };
      const bool slowj = l0f && SLOWC(0, j);
      auto packedW = [&]() {
        pW = p.ws; zW = WS_SPAN; bW = WS_WP + (unsigned)j * (16 * 24576); kW = 24576; pcW = 8192; hW = 4096; vW = vlin;
      };
      auto ldW = [&](int t) { const unsigned o = offW(t); ldWh(t, o, 0); ldWh(t, o, 1); };
      constexpr auto spread = [](int n) { unsigned m = 0; for (int t = 0; t < 26; ++t) if ((t + 1) * n / 13 > t * n / 13) m |= 1u << t; return m; };
      constexpr unsigned sp4 = spread(4), sp8 = spread(8), sp12 = spread(12);
      const unsigned smask = (isb ? (l == 3 ? sp4 : sp8) : sp12) >> ((((g + j * 5) & 15) * 13) >> 4);
      const unsigned cbm = isb ? cb2 : cb1;
      auto clean = [&](int t) __attribute__((always_inline)) {
        if (t < 13 && ((smask >> t) & 1)) {
          const unsigned cc = __builtin_popcount(smask & ((1u << t) - 1));
          const unsigned cb = cb0 + (cc >= 4 ? cbm - cb0 : 0u) + (cc >= 8 ? cb2 - cbm : 0u), pc = (cc + g + j) & 3;
          st16_sc1(rs, (lane >> 4) * 4096 + (lane & 15) * 16, cb + hw * 1024 + pc * 256, sent4);
        }
      };
      unsigned flo;
      unsigned ready = ~0u;
      u32x4 cv[4];
#pragma unroll
      for (int ii = 0; ii < 4; ++ii) asm volatile("" : "=v"(cv[ii]));
      auto chkrd = [&](int t) {
#pragma unroll
        for (int ii = 0; ii < 4; ++ii) cv[ii] = lds_ld<u32x4>(aslot(t) + lin + ii * 4096);
      };
      auto chkbad = [&]() { return __any(max(umax4v(umax4v(0u, cv[0]), cv[1]), umax4v(umax4v(0u, cv[2]), cv[3])) == SENT) && !l0f; };
      auto refetch = [&](int t) __attribute__((always_inline)) {
        for (unsigned spins = 0; !dead; ++spins) {
          if (spins > SPIN_CAP) { dead = true; break; }
          WAITV(0);
          ldA(t);
          ldA(t + 1);
          WAITV(0);
          chkrd(t);
          if (!chkbad()) break;
          __builtin_amdgcn_s_sleep(1);
        }
      };
      f32x4 jc[4];
#pragma unroll
      for (int i = 0; i < 4; ++i) asm volatile("" : "=v"(jc[i]));
      auto csld = [&]() {
#pragma unroll
        for (int i = 0; i < 4; ++i)
          ld16_asm(jc[i], rs, (lane >> 4) * 768 + hw * 192 + ((lane & 15) < 12 ? (lane & 15) : 0) * 16, WS_CS + (unsigned)((l * 16 + j) * 16 + i * 4) * 768);
      };
      auto csred = [&]() {
#pragma unroll
        for (int i = 0; i < 4; ++i) OPAQUE_V(jc[i]);
        const f32x4 s = (jc[0] + jc[1]) + (jc[2] + jc[3]);
        auto r = SWAP16(s[0], s[1]), q = SWAP16(s[2], s[3]);
        r = SWAP32(__uint_as_float(r[0]) + __uint_as_float(r[1]), __uint_as_float(q[0]) + __uint_as_float(q[1]));
        if ((lane & 15) < 12) lds_st<float>(lds0 + L_CS + l * 768 + hw * 192 + (lane & 15) * 16 + (lane >> 4) * 4, __uint_as_float(r[0]) + __uint_as_float(r[1]));
      };
      if (l0f) {
        u32x2 gw2;
        ld8_asm(gw2, rs, WS_EPOCH + 0u * lin);
        BAR;
        ldW(0); ldA(0);
        dma1(mk_rsrc(p.wphi, 32 * 2048), vlin, j * 4096, lds0 + L_WPHI + hw * 1024);
        ldW(1);
        WAITV(17);
        OPAQUE_V(gw2);
        const u64 gword = ((u64)(unsigned)UNI(gw2[1]) << 32) | (unsigned)UNI(gw2[0]);
        u64 z = gword + 0x9e3779b97f4a7c15ull;
        z = (z ^ (z >> 30)) * 0xbf58476d1ce4e5b9ull;
        z = (z ^ (z >> 27)) * 0x94d049bb133111ebull;
        z = z ^ (z >> 31);
        z = (z == gword) ? ~z : z;
        epoch = u32x2{(unsigned)z, (unsigned)(z >> 32)};
        tr = (j - kb) & 15;
        WAITV(7);
        BAR;
        ldA(1);
      } else {
        ldW(1);
        if (l == 1) csld();
        else __builtin_amdgcn_s_sleep(XD);
        ldA(1);
      }
      ldA(2);
      ldW(2);
#pragma unroll
      for (int i = 0; i < 4; ++i) asm volatile("" : "=v"(xr[i >> 1][i & 1]));
      unsigned oA = offA(3), oW = 0;
      OPAQUE_S(oA);
      ra0 = lds0 + L_R2 + (hw * 32 + (lane & 15)) * 128 + (((((lane >> 4) & 1) * 2 + (lane >> 5)) ^ ((lane >> 1) & 7)) * 16);
      OPAQUE_V(ra0);
      ldres(0);
      clean(0);
      if (l == 1) { WAITV(14); csred(); }
      WAITV(10);
      chkrd(1);
      if (__builtin_expect(chkbad(), 0)) refetch(1);
      const int fr = lane & 15, fq = lane >> 4;
      u32x2 fl;
      f32x4 js[4];
      float rb[4];
      const bool slowo = SLOWC(2 + (l & 1), j);
      const char* pO;
      unsigned zO, bO, pcO, vO, clo;
      u32x4 wo2[2];
      asm volatile("" : "=v"(wo2[0])); asm volatile("" : "=v"(wo2[1]));
      const unsigned fwo = myrep + fl_line(FL_WO + (isb ? 1 : 0), jr + (lane >> 4)) + (lane & 15) * 8;
      static_for<1, 14>([&](auto t_c) __attribute__((always_inline)) {
        constexpr int t = decltype(t_c)::value;
        constexpr auto rest = [](int s) { return 6 + (s == NVAL - 2 || s == 5 || s == 11) + (s == 8 ? 4 : 0); };
        constexpr int NW = t == 1 ? 63 : 4 + rest(t - 1);
        if (t >= NVAL && t <= 6 && __builtin_expect((ready & (1u << kseq(t + 2))) == 0, 0)) {
          WAITV(NW); WAITL0; BAR;
          ready = poll_need(rs, flo, epoch, ready, 1u << kseq(t + 2), dead);
          dma4<4096>(rA, vA, oA, pcA, aslot(t + 2));
        } else if (t == 7 && __builtin_expect(ready != ~0u, 0)) {
          WAITV(NW); WAITL0; BAR;
          poll_all(rs, flo, epoch, dead);
          dma4<4096>(rA, vA, oA, pcA, aslot(t + 2));
        } else {
          bar_dma4<4096, NW>(rA, vA, oA, pcA, aslot(t + 2));
        }
        if (t == 1 && lead) set_flags(rs, fl_line(FL_X + l, g), j, l0f ? fl_line(FL_CS, j) : fl_line(FL_X + l, g), l0f ? g : j, epoch, lane);
        if (t == 7 && slowj) packedW();
        oW = offW(t + 2);
        if (t < 13) oA = offA(t + 3);
        OPAQUE_S(oA); OPAQUE_S(oW);
        clean(t);
        if (t <= NVAL && !l0f) { WAITV(rest(t - 1) + 4); chkrd(t + 1); }
        if (t == WT + 3 && l0f && SLOWC(2, jr + hw)) {
          OPAQUE_V(wo2[0]); OPAQUE_V(wo2[1]);
#pragma unroll
          for (int i = 0; i < 2; ++i) st16_sc1(rs, lin, WS_WO + (unsigned)((jr + hw) * 131072 + g * 8192 + ((j & 3) * 2 + i) * 1024), wo2[i]);
        }
        if (t == WT + 6 && l0f && lead) set_flags(rs, fl_line(FL_WO, j), g, fl_line(FL_WO, j), g, epoch, lane);
        ldres(t);
        if (t == 8) {
#pragma unroll
          for (int i = 0; i < 4; ++i) ld16_asm(js[i], rs, fq * 1024 + hw * 256 + fr * 16, st_off(g, l) + i * 4096);
        }
        SCHED;
        ldWh(t + 2, oW, 0);
        SCHED;
        bool bad = false;
        if (t <= NVAL) bad = chkbad();
        if (t == 1) {
          const unsigned fx = fl_line(FL_X + l, g), fc = fl_line(FL_CS, jr) + ((j + fq) & 3) * 128;
          flo = myrep + ((l0f && lane >= 16) ? fc : fx) + fr * 8;
          OPAQUE_V(flo);
        }
        if (t == 9) {
          pO = slowo ? p.ws : (const char*)(isb ? p.woB : p.woA);
          zO = slowo ? WS_SPAN : 1024 * 2048; bO = slowo ? WS_WO + (unsigned)((isb ? 16 : 0) + j) * 131072 : j * 128; pcO = slowo ? 4096 : 32 * 2048;
          vO = slowo ? vlin : (hw * 8 + (lane >> 3)) * 2048 + wo_swz(lane);
          clo = myrep + ((lane & 16) ? fl_line(FL_CLC + l, (lane & 32) ? j ^ RX : j) : fl_line(FL_CLR + l, g)) + fr * 8;
          OPAQUE_S(pO); OPAQUE_S(zO); OPAQUE_S(bO); OPAQUE_S(pcO); OPAQUE_V(vO); OPAQUE_V(clo);
        }
        if (t == 10 && l0f) csred();
        if (t == 11) {
#pragma unroll
          for (int i = 0; i < 4; ++i) OPAQUE_V(js[i]);
          const f32x4 s = (js[0] + js[1]) + (js[2] + js[3]);
          const auto r = SWAP16(s[0], s[2]), q = SWAP16(s[1], s[3]);
          rb[0] = __uint_as_float(r[0]) + __uint_as_float(r[1]); rb[1] = __uint_as_float(q[0]) + __uint_as_float(q[1]);
          OPAQUE_V(rb[0]); OPAQUE_V(rb[1]);
        }
        if (t == 12) {
          const auto r = SWAP32(rb[0], rb[0]), q = SWAP32(rb[1], rb[1]);
          const float m = (__uint_as_float(r[0]) + __uint_as_float(r[1])) * (-1.0f / 1024), q2 = __uint_as_float(q[0]) + __uint_as_float(q[1]);
          const float rstd = __builtin_amdgcn_rsqf(fmaxf(__builtin_fmaf(q2, 1.0f / 1024, -m * m), 0.f) + 1e-5f);
          lds_st<float>(lds0 + L_MEAN + 64 + hw * 128 + fr * 8 + (fq & 1) * 4 + (fq >> 1) * (L_RSTD - L_MEAN), (lane & 32) ? rstd : m);
        }
        SCHED;
        ldWh(t + 2, oW, 1);
        if (t == NVAL - 1 || t == 6) {
          WAITV(10);
          OPAQUE_V(fl);
          if (t == 6) ready = __all(is_set(fl, epoch)) ? ~0u : 0u;
          else {
            ready = l0f ? ~0u : (unsigned)__ballot(is_set(fl, epoch));
            if (slowj && __all(is_set(fl, epoch))) packedW();
          }
        }
        if (t == 12) { WAITV(10); OPAQUE_V(fl); }
        if (t == 13 && slowo && __builtin_expect(!__all(is_set(fl, epoch)), 0)) poll_all(rs, fwo, epoch, dead);
        if (t == NVAL - 2 || t == 5) ld8_asm(fl, rs, flo);
        if (t == 11) ld8_asm(fl, rs, fwo);
        if (t == 7 && l0f) csld();
        if (t == WT && l0f) {
#pragma unroll
          for (int i = 0; i < 2; ++i) ld16in_asm(wo2[i], mk_rsrc(p.woA, 1024 * 2048), (lane >> 3) * 2048 + wo_swz(lane), (g * 64 + (j & 3) * 16 + i * 8) * 2048 + (jr + hw) * 128);
        }
        if constexpr (t <= NVAL) { if (__builtin_expect(bad, 0)) refetch(t + 1); }
      });
      auto stO = [&](int i) { dma4<4096>(mk_rsrc(pO, zO), vO, bO + wo_src(i, j) * (4 * pcO), pcO, lds0 + wo_slot(i) + hw * 1024); };
      auto clcheck = [&]() {
        OPAQUE_V(fl);
        if (__builtin_expect(!__all(is_set(fl, epoch)), 0)) poll_all(rs, clo, epoch, dead);
      };
      auto accinit = [&]() {
#pragma unroll
        for (int m = 0; m < 2; ++m)
#pragma unroll
          for (int pp = 0; pp < 2; ++pp) {
            auto r0_ = __builtin_amdgcn_permlane16_swap(xr[m][pp][0], xr[m][pp][2], false, false);
            auto r1_ = __builtin_amdgcn_permlane16_swap(xr[m][pp][1], xr[m][pp][3], false, false);
            acc[m][2 * pp] = f32x4{lo16(r0_[0]), hi16(r0_[0]), lo16(r1_[0]), hi16(r1_[0])};
            acc[m][2 * pp + 1] = f32x4{lo16(r0_[1]), hi16(r0_[1]), lo16(r1_[1]), hi16(r1_[1])};
          }
#pragma unroll
        for (int i = 0; i < 8; ++i) OPAQUE_V(acc[i >> 2][i & 3]);
      };
      WAITV(10);
      WAITL0;
      BAR;
      if (lead) set_flags(rs, fl_line(FL_CLR + l, g), j, fl_line(FL_CLC + l, j), g, epoch, lane);
      if (isb) {
        stO(0); stO(2); stO(3);
        WAITV(12);
        BAR;
        stO(1); stO(4); stO(5);
        __builtin_amdgcn_s_setprio(0);
        BAR;
        __builtin_amdgcn_s_sleep(BD);
        stO(7); stO(6);
        ld8_asm(fl, rs, clo);
        accinit();
        WAITV(0);
        clcheck();
      } else {
        u32x4 vb[8];
#pragma unroll
        for (int i = 0; i < 8; ++i) asm volatile("" : "=v"(vb[i]));
        stO(0); stO(2); stO(3);
        ldres(14);
        WAITV(12);
        WAITL0;
        BAR;
        stO(1);
        ldres(15);
        if (l0f) {
          const bool wj = hw != 0;
          const char* bp = (const char*)(wj ? p.wqkvB : p.woB);
          unsigned rs_ = wj ? 6144 : 2048, sz = rs_ * 1024, ss = rs_ * 8, s2 = wj ? 16 * 6144 : 128;
          unsigned sb = g * 64 * rs_ + (wj ? (hw - 1) * 2048 + JOBC(j, hw - 1) * 128 : (j & 3) * (16 * 2048) + jr * 128);
          OPAQUE_S(bp); OPAQUE_S(rs_); OPAQUE_S(sz); OPAQUE_S(sb); OPAQUE_S(ss); OPAQUE_S(s2);
          const rsrc_t rb = mk_rsrc(bp, sz);
#pragma unroll
          for (int i = 0; i < 8; ++i) ld16in_asm(vb[i], rb, (lane >> 3) * rs_ + (lane & 7) * 16, sb + (i & 1) * ss + (i >> 1) * s2);
        }
        __builtin_amdgcn_s_setprio(0);
        KV_ADDR;
        bf16x8 sel = rowsum_sel(lane);
        unsigned h1 = (lane >> 3) * KVP_G + (lane & 7) * 16;
        unsigned pw = lds0 + SC_PHIK + (h * 128 + wm * 64 + 32 + fr) * 160 + fq * 8, kw = lds0 + L_PREV + hw * 2048 + lin;
        const unsigned wa = lds0 + L_WPHI + h * 2048 + (fq * 4 + (fr >> 2)) * 64 + (lane & 3) * 8;
        bf16x8 w0 = tr2(wa, wa + 1024), w1 = tr2(wa + 32, wa + 32 + 1024);
        float cq[4];
#pragma unroll
        for (int m = 0; m < 2; ++m) { cq[m] = lds_ld<float>(lds0 + L_RSTD + (16 + wm * 64 + 32 + m * 16 + fr) * 4) * 1.4426950408889634f; OPAQUE_V(cq[m]); }
        OPAQUE_V(sel); OPAQUE_V(h1); OPAQUE_V(pw); OPAQUE_V(kw); OPAQUE_V(w0); OPAQUE_V(w1);
        BAR;
        CLOBBER;
        {
          const f32x4 zero = {0.f, 0.f, 0.f, 0.f};
          f32x4 r[4][2];
#pragma unroll
          for (int m = 0; m < 2; ++m) {
            const bf16x8 f = lds_ld<bf16x8>(kw + m * 1024);
            r[m][0] = MFMA(w0, f, zero); r[m][1] = MFMA(w1, f, zero);
          }
          ld8_asm(fl, rs, clo);
          phik2(r, cq, pw);
        }
        WAITL0;
        BAR;
        kvpart(va, pa, pb, kvj + (unsigned)g * KVP_G, lane, wave);
        WAITV(2);
#pragma unroll
        for (int i = 0; i < 8; ++i) OPAQUE_V(vb[i]);
        clcheck();
        BAR;
        __builtin_amdgcn_s_setprio(3);
        stO(4); stO(5);
        const f32x4 zero = {0.f, 0.f, 0.f, 0.f};
        const unsigned s1 = kvj + (unsigned)((j ^ RX) - j) * KVP_J + g * 512 + hw * 128;
        __builtin_amdgcn_s_sleep(H1S);
        u32x4 v[2] = {ld16_sc1(rs, h1, s1), ld16_sc1(rs, h1, s1 + 8 * KVP_G)};
        for (unsigned spins = 0; !dead && __any(umax4v(umax4v(0u, v[0]), v[1]) == SENT); ++spins) {
          if (spins > SPIN_CAP) { dead = true; break; }
          CLOBBER;
          v[0] = ld16_sc1(rs, h1, s1); v[1] = ld16_sc1(rs, h1, s1 + 8 * KVP_G);
        }
        const f32x4 c = rowsum_fin(MFMA(sel, as_frag(v[1]), MFMA(sel, as_frag(v[0]), zero)));
        if ((lane & 40) == 0)
          st8_sc1(rs, fr * 16 + fq * 8, kvj + (unsigned)g * KVP_G + KVP_SUM + hw * 128, nosent2(c));
        CLOBBER;
        __builtin_amdgcn_s_setprio(0);
        BAR;
        SCHED;
        accinit();
        stO(6); stO(7);
        if (l0f) {
          if (hw) {
            job_colsum(rs, vb, 1, g, JOBC(j, hw - 1), hw - 1, lane);
            if (SLOWC(1, JOBC(j, hw - 1))) {
#pragma unroll
              for (int i = 0; i < 8; ++i) job_pack(rs, vb[i], i, 1, g, JOBC(j, hw - 1), hw - 1, lane);
            }
          } else {
#pragma unroll
            for (int i = 0; i < 8; ++i)
              if (SLOWC(3, jr + (i >> 1))) st16_sc1(rs, (lane >> 3) * 128 + wo_swz(lane), WS_WO + (unsigned)((16 + jr + (i >> 1)) * 131072 + g * 8192 + ((j & 3) * 2 + (i & 1)) * 1024), vb[i]);
          }
        }
        WAITV(0);
      }
    }
    {
      LOCALS;
      const int fr = lane & 15, fq = lane >> 4;
      const int kh = half;
      unsigned v0 = lane * 16 + hw * 4096 + kh * 2048, v1 = lane * 16 + hw * 4096 + (kh ^ 1) * 2048;
      const int r0 = (JROT(j) >> 1) + NJ / 2;
      const unsigned ob0 = ob_off(g, kh, l);
      const unsigned pko = myrep + fl_line(FL_PK, JOBC(j, fq - 1)) + fr * 8;
      u32x4 R[8][4];
      u32x2 pkf;
      auto issue_ = [&](int i) {
        const unsigned so = ob0 + (unsigned)((r0 + i) & 7) * (2 * TS);
        R[i][0] = ld16_sc1(rs, v0, so); R[i][1] = ld16_sc1(rs, v1, so);
        R[i][2] = ld16_sc1(rs, v0 + 1024, so); R[i][3] = ld16_sc1(rs, v1 + 1024, so);
      };
      auto issue = [&](int i) { issue_(i); SCHED; };
      constexpr int LA = LOOKAHEAD;
      __builtin_amdgcn_s_setprio(3);
      unsigned so0 = ob0 + (unsigned)(r0 & 7) * (2 * TS);
      OPAQUE_V(v0); OPAQUE_V(v1); OPAQUE_S(so0);
      BAR;
      SCHED;
      R[0][0] = ld16_sc1(rs, v0, so0); R[0][1] = ld16_sc1(rs, v1, so0);
      R[0][2] = ld16_sc1(rs, v0 + 1024, so0); R[0][3] = ld16_sc1(rs, v1 + 1024, so0);
      SCHED;
      __builtin_amdgcn_s_setprio(2);
      if (l == 0 && lead) set_flags(rs, fl_line(FL_PK, j), g, fl_line(FL_WO + 1, j), g, epoch, lane);

      auto vmax = [&](int i) { return umax4v(umax4v(umax4v(umax4v(0u, R[i][0]), R[i][1]), R[i][2]), R[i][3]); };
      auto bad = [&](int i) { return __any(umax4v(umax4v(umax4v(umax4v(0u, R[i][0]), R[i][1]), R[i][2]), R[i][3]) == SENT); };
      auto poll = [&](int i, int nfl) {
        for (unsigned spins = 0; !dead; ++spins) {
          if (spins > SPIN_CAP) { dead = true; break; }
          __builtin_amdgcn_s_sleep(2);
          CLOBBER;
          issue(i);
#pragma unroll
          for (int k = i + 1; k < i + nfl && k < 8; ++k)
            if (bad(k)) issue(k);
          WAITV(0);
#pragma unroll
          for (int k = i + 1; k < i + nfl && k < 8; ++k)
#pragma unroll
            for (int f = 0; f < 4; ++f) OPAQUE_V(R[k][f]);
          if (!bad(i)) break;
        }
      };
      unsigned bb[4];
      {
        const int kr = fq * 4 + (fr >> 2);
        const int gs = (kr >> 1) & 3;
#pragma unroll
        for (int c = 0; c < 4; ++c) bb[c] = lds0 + kh * 8192 + kr * 128 + ((c ^ gs) * 32) + (lane & 3) * 8;
      }
      bf16x8 bf[2][4];
      auto rdB = [&](int i, int hh) {
        if (hh == 0) {
#pragma unroll
          for (int c = 0; c < 4; ++c) { bb[c] += wo_slot(i) - (i ? wo_slot(i - 1) : 0u); asm("" : "+v"(bb[c])); }
        }
#pragma unroll
        for (int c = 0; c < 4; ++c) bf[hh][c] = tr2(bb[c] + hh * 4096, bb[c] + hh * 4096 + 2048);
      };
      auto mms = [&](int i, int hh) {
#pragma unroll
        for (int n = 0; n < 4; ++n)
#pragma unroll
          for (int m = 0; m < 2; ++m) acc[m][n] = MFMA(bf[hh][n], as_frag(R[i][hh * 2 + m]), acc[m][n]);
      };
      if (!half) {
#pragma unroll
        for (int i = 0; i < 8; ++i) acc[i >> 2][i & 3] = f32x4{0.f, 0.f, 0.f, 0.f};
      }
      const char* obase = (l == 3) ? (const char*)p.out : (const char*)p.ws;
      unsigned osize = (l == 3) ? 2048 * 2048 : WS_SPAN;
      const bool slown = SLOWC((l & 1) ^ 1, j);
      pW = slown ? p.ws : (const char*)(isb ? p.wqkvA : p.wqkvB);
      bW = slown ? WS_WP + (unsigned)((isb ? 0 : 16) + j) * (16 * 24576) : j * 128;
      kW = slown ? 24576 : 64 * 6144; zW = slown ? WS_SPAN : 1024 * 6144; pcW = slown ? 8192 : 2048; hW = slown ? 4096 : 32 * 6144;
      vA = hw * 1024 + lane * 16;
      vW = slown ? vA : (hw * 8 + (lane >> 3)) * 6144 + (((lane & 7) ^ ((((lane >> 4) & 1) | ((hw & 1) << 1)) << 1)) * 16);
      pA = p.ws; zA = WS_SPAN; bA = xb_off(g, l); kA = TS; pcA = 4096; kb = JROT(j) + KX; dj = (j - kb) & 15;
      OPAQUE_S(pW); OPAQUE_S(bW); OPAQUE_S(kW); OPAQUE_S(zW); OPAQUE_S(pcW); OPAQUE_S(hW); OPAQUE_V(vA); OPAQUE_V(vW); OPAQUE_S(bA);
      unsigned sq = bW + j * kW + kh * hW, dst = lds0 + L_R1 + kh * 4096 + hw * 1024;
      OPAQUE_S(obase); OPAQUE_S(osize); OPAQUE_S(sq); OPAQUE_S(dst);
      const rsrc_t ro = mk_rsrc(obase, osize);
      unsigned so[2];
      unsigned xw = lds0 + SC_XCH + wave * 4096 + lane * 16;
      unsigned xa = lds0 + SC_XCH + (wave ^ 4) * 4096 + lane * 16;
      unsigned sb0 = (l == 3) ? g * (128 * 2048) + j * 128 : xb_off(g, l) + j * TS;
      {
        const int row = (hw * 2 + kh) * 16 + fr, ch = (fq & 1) * 2 + (fq >> 1);
        const unsigned sw = (l == 3) ? 0u : (unsigned)((row >> 1) & 7), rb = (unsigned)row << ((l == 3) ? 11 : 7);
#pragma unroll
        for (int pp = 0; pp < 2; ++pp) { so[pp] = rb + (((ch + 4 * pp) ^ sw) * 16); OPAQUE_V(so[pp]); }
        OPAQUE_V(xa); OPAQUE_V(xw); OPAQUE_S(sb0);
      }
      SCHED;
#pragma unroll
      for (int i = 1; i < LA; ++i) issue(i);
      rdB(0, 0);
      unsigned mx = vmax(0);
#pragma unroll
      for (int i = 0; i < 8; ++i) {
        SCHED;
        if (__builtin_expect(__any(mx == SENT), 0)) poll(i, LA);
        if (i == W1S - 2) { pkf = ld8_sc1(rs, pko, 0); SCHED; }
        if (i == W1S) {
          OPAQUE_V(pkf);
          if (l == 0 && lead && !__all(is_set(pkf, epoch))) poll_all(rs, pko, epoch, dead);
          BAR;
        }
        if (i == 7 && l < 3) dma3<8192>(mk_rsrc(pW, zW), vW, sq, pcW, dst);
        if (i < 4 && !isb) __builtin_amdgcn_s_sleep(PACE);
        if (i == 2) __builtin_amdgcn_s_setprio(1);
        if (i == 5) __builtin_amdgcn_s_setprio(0);
        SCHED;
        rdB(i, 1);
        if (i == 7) break;
        if (i + LA < 8) issue_(i + LA);
        mms(i, 0);
#pragma unroll
        for (int k = 0; k < 4; ++k) { SGB(0x008, 1); SGB(0x100, 1); SGB(0x020, 1); SGB(0x008, 1); SGB(0x100, 1); }
        SCHED;
        rdB(i + 1, 0); mx = vmax(i + 1);
        mms(i, 1);
#pragma unroll
        for (int k = 0; k < 4; ++k) { SGB(0x008, 1); SGB(0x100, 2); }
#pragma unroll
        for (int k = 4; k < 8; ++k) { SGB(0x008, 1); SGB(0x002, 8 / (8 - 4)); }
      }
#pragma unroll
      for (int n = 0; n < 4; ++n) acc[1][n] = MFMA(bf[0][n], as_frag(R[7][1]), acc[1][n]);
#pragma unroll
      for (int n = 0; n < 4; ++n) acc[0][n] = MFMA(bf[0][n], as_frag(R[7][0]), acc[0][n]);
#pragma unroll
      for (int k = 0; k < 8; ++k) { SGB(0x008, 1); SGB(0x100, 1); }
      SCHED;
#pragma unroll
      for (int n = 0; n < 4; ++n) acc[1][n] = MFMA(bf[1][n], as_frag(R[7][3]), acc[1][n]);
#pragma unroll
      for (int n = 0; n < 4; ++n) acc[0][n] = MFMA(bf[1][n], as_frag(R[7][2]), acc[0][n]);
#pragma unroll
      for (int n = 0; n < 4; ++n) lds_st<f32x4>(xw + n * 1024, acc[1][n]);
      SGB(0x008, 4);
#pragma unroll
      for (int k = 0; k < 4; ++k) { SGB(0x008, 1); SGB(0x200, 1); }
      SCHED;
      WAITL0;
      BAR;
      SCHED;
      f32x4 pa[4];
#pragma unroll
      for (int n = 0; n < 4; ++n) pa[n] = lds_ld<f32x4>(xa + n * 1024);
      SCHED;
      u32x4 o[2];
#pragma unroll
      for (int pp = 0; pp < 2; ++pp) {
        const f32x4 a = acc[0][2 * pp] + pa[2 * pp], b = acc[0][2 * pp + 1] + pa[2 * pp + 1];
        auto r0_ = __builtin_amdgcn_permlane16_swap(pk2(a[0], a[1]), pk2(b[0], b[1]), false, false);
        auto r1_ = __builtin_amdgcn_permlane16_swap(pk2(a[2], a[3]), pk2(b[2], b[3]), false, false);
        o[pp] = nosent(u32x4{r0_[0], r1_[0], r0_[1], r1_[1]});
        st16_sc1(ro, so[pp], sb0, o[pp]);
      }
      if (l < 3) {
        lds_st<u32x4>(lds0 + L_R2 + so[0], o[0]);
        lds_st<u32x4>(lds0 + L_R2 + so[1], o[1]);
        const f32x4 zero = {0.f, 0.f, 0.f, 0.f};
        const bf16x8 ones = {0x3f80, 0x3f80, 0x3f80, 0x3f80, 0x3f80, 0x3f80, 0x3f80, 0x3f80};
        const f32x4 a = MFMA(ones, as_frag(o[1]), MFMA(ones, as_frag(o[0]), zero));
        const f32x4 b = MFMA(as_frag(o[1]), as_frag(o[1]), MFMA(as_frag(o[0]), as_frag(o[0]), zero));
        const float q = (fr & 2) ? ((fr & 1) ? b[3] : b[2]) : ((fr & 1) ? b[1] : b[0]);
        if (fq == (fr >> 2)) st8_sc1(rs, ((hw * 2 + kh) * 16 + fr) * 8, st_off(g, l + 1) + j * 1024, u32x2{__float_as_uint(a[0]), __float_as_uint(q)});
      } else if (lead && blockIdx.x == 0 && lane == 0) {
        st8_sc1(rs, WS_EPOCH, 0, epoch);
      }
    }
  }
  asm volatile(".Lpf_end:");
}

extern "C" void kernel_launch(void* const* d_in, const int* in_sizes, int n_in, void* d_out, int out_size,
                              void* d_ws, size_t ws_size, hipStream_t stream) {
  Params p;
  p.x = (const u16*)d_in[0];
  p.wqkvA = (const u16*)d_in[1];
  p.wphi = (const u16*)d_in[2];
  p.woA = (const u16*)d_in[3];
  p.wqkvB = (const u16*)d_in[5];
  p.woB = (const u16*)d_in[6];
  p.out = (u16*)d_out;
  p.ws = (char*)d_ws;
  (void)hipFuncSetAttribute((const void*)mega_kernel, hipFuncAttributeMaxDynamicSharedMemorySize, L_TOTAL);
  mega_kernel<<<256, 512, L_TOTAL, stream>>>(p);
}
```

```cpp
#include <hip/hip_runtime.h>
#include <stdint.h>
#include <type_traits>
constexpr int LOOKAHEAD = 2;
constexpr int NVAL = 4;
constexpr int WT = 1;
constexpr int W1S = 6;
constexpr int NJ = 8;
constexpr int KX = 8;
constexpr int RX = 8;
constexpr unsigned TS = 17664;
#define XD 3
#define H1S 6
#define H2D 5
#define BD 8
#define PACE 4

typedef unsigned short u16;
typedef unsigned long long u64;
typedef __attribute__((ext_vector_type(8))) short bf16x8;
typedef __attribute__((ext_vector_type(4))) short s16x4;
typedef __attribute__((ext_vector_type(4))) float f32x4;
typedef __attribute__((ext_vector_type(2))) float f32x2;
typedef __attribute__((ext_vector_type(4))) unsigned u32x4;
typedef __attribute__((ext_vector_type(2))) unsigned u32x2;
typedef __attribute__((ext_vector_type(2))) __bf16 bf16x2_t;
#define LAS __attribute__((address_space(3)))
typedef LAS s16x4* lds_s16x4_ptr;

constexpr unsigned NREP = 8, REP_STRIDE = 17 * 2048;
enum { FL_X = 0, FL_CS = 4, FL_CLR = 5, FL_CLC = 9, FL_PK = 13, FL_WO = 14 };
__device__ __forceinline__ unsigned fl_line(int set, int a) { return (unsigned)(set * 16 + a) * 128; }
constexpr unsigned WS_EPOCH = 512u << 10;
constexpr unsigned WS_CS = 1u << 20;
constexpr unsigned WS_KVP = 4u << 20;
constexpr unsigned KVP_G = 32768, KVP_J = 16 * KVP_G, KVP_L = 16384, KVP_Z = 8192, KVP_SUM = 8704, KVP_HALO = 12u << 10;
constexpr unsigned SENT = 0xffffffffu;
constexpr unsigned WS_X = 14u << 20, WS_O = 29u << 20, WS_ST = 2u << 20;
__device__ __forceinline__ unsigned xb_off(int g, int l) { return WS_X + (unsigned)(l * 16 + g) * (16 * TS); }
__device__ __forceinline__ unsigned ob_off(int g, int j, int l) { return WS_O + (unsigned)((l * 16 + g) * 16 + j) * TS; }
__device__ __forceinline__ unsigned st_off(int g, int l) { return WS_ST + (unsigned)(l * 16 + g) * (16u << 10); }
constexpr unsigned WS_WP = 48u << 20;
constexpr unsigned WS_WO = 60u << 20;
constexpr unsigned WS_SPAN = 64u << 20;

constexpr unsigned L_R1 = 0;
constexpr unsigned L_R2 = 98304, XS = 16384;
constexpr unsigned L_MISC = 147456;
constexpr unsigned L_MEAN = L_MISC;
constexpr unsigned L_RSTD = L_MISC + 576;
constexpr unsigned L_CS = L_MISC + 1152;
constexpr unsigned L_WPHI = L_MISC + 2688;
constexpr unsigned L_SINK = L_MISC + 6912;
constexpr unsigned L_ZF = L_MISC + 7168;
constexpr unsigned L_PREV = L_MISC + 8192;
constexpr unsigned L_TOTAL = 163840;

#define JROT(j) ((j) & ~(NJ - 1))
constexpr int NX = NJ / 2;
#define KB0(g, j) (JROT(j) + (16 / NX) * ((g) & (NX - 1)))
#define JOBC(j, w) (((j) & ~3) + (((j) + (w) + 1) & 3))

#define SLOWC(m, jc) ((slowm >> ((m) * 8 + ((jc) & 7))) & 1)

struct Params {
  const u16* x;
  const u16* wqkvA;
  const u16* wphi;
  const u16* woA;
  const u16* wqkvB;
  const u16* woB;
  u16* out;
  char* ws;
};

#define SCHED __builtin_amdgcn_sched_barrier(0)
#define BAR __builtin_amdgcn_s_barrier()
#define SGB(m, n) __builtin_amdgcn_sched_group_barrier(m, n, 0)
#define WAITV(n) asm volatile("s_waitcnt vmcnt(%0)" ::"n"(n) : "memory")
#define WAITL0 asm volatile("s_waitcnt lgkmcnt(0)" ::: "memory")

#define TRV(slot, val) do {} while (0)
#define TRQ(slot) do {} while (0)
#define TRC(slot) do {} while (0)
#define TRR(slot) do {} while (0)
#define STAMP_INIT do {} while (0)
#define STAMP_DUMP do {} while (0)

__device__ __forceinline__ unsigned pk2(float a, float b) {
  f32x2 v = {a, b};
  return __builtin_bit_cast(unsigned, __builtin_convertvector(v, bf16x2_t));
}
__device__ __forceinline__ float lo16(unsigned u) { return __uint_as_float(u << 16); }
__device__ __forceinline__ float hi16(unsigned u) { return __uint_as_float(u & 0xffff0000u); }

template <int A, int B, typename F> __device__ __forceinline__ void static_for(F f) {
  if constexpr (A < B) { f(std::integral_constant<int, A>{}); static_for<A + 1, B>(f); }
}
typedef __amdgpu_buffer_rsrc_t rsrc_t;
__device__ __forceinline__ rsrc_t mk_rsrc(const void* p, unsigned bytes) { return __builtin_amdgcn_make_buffer_rsrc((void*)p, 0, bytes, 0x00020000); }
#define UNI(x) __builtin_amdgcn_readfirstlane(x)
#define DMA_HEAD "s_mov_b32 m0, %4\n\ts_mov_b32 %0, %3\n\ts_nop 2\n\tbuffer_load_dwordx4 %1, %2, %0 offen sc1 lds\n\t"
#define DMA_NEXT "s_add_u32 m0, m0, %6\n\ts_add_u32 %0, %0, %5\n\tbuffer_load_dwordx4 %1, %2, %0 offen sc1 lds\n\t"
#define DMA_OPS : "=&s"(t) : "v"(voff), "s"(r), "s"(UNI(soff)), "s"(UNI(lds)), "s"(UNI(sstep)), "i"(LSTEP) : "memory", "scc", "m0"
template <int LSTEP> __device__ __forceinline__ void dma4(rsrc_t r, unsigned voff, unsigned soff, unsigned sstep, unsigned lds) {
  unsigned t;
  asm volatile(DMA_HEAD DMA_NEXT DMA_NEXT DMA_NEXT DMA_OPS);
}
template <int LSTEP> __device__ __forceinline__ void dma3(rsrc_t r, unsigned voff, unsigned soff, unsigned sstep, unsigned lds) {
  unsigned t;
  asm volatile(DMA_HEAD DMA_NEXT DMA_NEXT DMA_OPS);
}
template <int LSTEP, int N> __device__ __forceinline__ void bar_dma4(rsrc_t r, unsigned voff, unsigned soff, unsigned sstep, unsigned lds) {
  unsigned t;
  asm volatile("s_mov_b32 m0, %4\n\ts_mov_b32 %0, %3\n\ts_nop 0\n\ts_waitcnt vmcnt(%7) lgkmcnt(0)\n\ts_barrier\n\t"
               "buffer_load_dwordx4 %1, %2, %0 offen sc1 lds\n\t" DMA_NEXT DMA_NEXT DMA_NEXT
               : "=&s"(t) : "v"(voff), "s"(r), "s"(UNI(soff)), "s"(UNI(lds)), "s"(UNI(sstep)), "i"(LSTEP), "n"(N) : "memory", "scc", "m0");
}
__device__ __forceinline__ void dma1(rsrc_t r, unsigned voff, unsigned soff, unsigned lds) {
  asm volatile("s_mov_b32 m0, %3\n\ts_nop 3\n\tbuffer_load_dwordx4 %0, %1, %2 offen sc1 lds" ::"v"(voff), "s"(r), "s"(UNI(soff)), "s"(UNI(lds)) : "memory", "m0");
}
__device__ __forceinline__ void touch(rsrc_t r, unsigned voff, unsigned lds) {
  asm volatile("s_mov_b32 m0, %2\n\ts_nop 3\n\tbuffer_load_dword %0, %1, 0 offen lds" ::"v"(voff), "s"(r), "s"(UNI(lds)) : "memory", "m0");
}
__device__ __forceinline__ void ld8_asm(u32x2& d, rsrc_t r, unsigned voff) {
  asm volatile("s_nop 4\n\tbuffer_load_dwordx2 %0, %1, %2, 0 offen sc1" : "=v"(d) : "v"(voff), "s"(r) : "memory");
}

__device__ __forceinline__ void ld16in_asm(u32x4& d, rsrc_t r, unsigned voff, unsigned soff) {
  asm volatile("s_nop 4\n\tbuffer_load_dwordx4 %0, %1, %2, %3 offen" : "=v"(d) : "v"(voff), "s"(r), "s"(UNI(soff)) : "memory");
}
__device__ __forceinline__ void ld16_asm(f32x4& d, rsrc_t r, unsigned voff, unsigned soff) {
  asm volatile("s_nop 4\n\tbuffer_load_dwordx4 %0, %1, %2, %3 offen sc1" : "=v"(d) : "v"(voff), "s"(r), "s"(UNI(soff)) : "memory");
}

template <typename T> __device__ __forceinline__ T lds_ld(unsigned a) { return *(LAS T*)(a); }
template <typename T> __device__ __forceinline__ void lds_st(unsigned a, T v) { *(LAS T*)(a) = v; }
__device__ __forceinline__ bf16x8 tr2(unsigned a0, unsigned a1) {
  s16x4 v0 = __builtin_amdgcn_ds_read_tr16_b64_v4i16((lds_s16x4_ptr)(a0));
  s16x4 v1 = __builtin_amdgcn_ds_read_tr16_b64_v4i16((lds_s16x4_ptr)(a1));
  return __builtin_shufflevector(v0, v1, 0, 1, 2, 3, 4, 5, 6, 7);
}
__device__ __forceinline__ bf16x8 as_frag(u32x4 v) { return __builtin_bit_cast(bf16x8, v); }
#define MFMA(a, b, c) __builtin_amdgcn_mfma_f32_16x16x32_bf16(a, b, c, 0, 0, 0)

__device__ __forceinline__ float vmax2(float a, float b) { float inf = __builtin_inff(); asm("" : "+s"(inf)); return __builtin_amdgcn_fmed3f(a, b, inf); }
#define SWAP16(a, b) __builtin_amdgcn_permlane16_swap(__float_as_uint(a), __float_as_uint(b), false, false)
#define SWAP32(a, b) __builtin_amdgcn_permlane32_swap(__float_as_uint(a), __float_as_uint(b), false, false)
__device__ __forceinline__ void red2_max(float& a, float& b) {
  auto r = SWAP16(a, b);
  float x = vmax2(__uint_as_float(r[0]), __uint_as_float(r[1]));
  r = SWAP32(x, x);
  x = vmax2(__uint_as_float(r[0]), __uint_as_float(r[1]));
  r = SWAP16(x, x);
  a = __uint_as_float(r[0]); b = __uint_as_float(r[1]);
}
__device__ __forceinline__ void red2_add(float& a, float& b) {
  auto r = SWAP16(a, b);
  float x = __uint_as_float(r[0]) + __uint_as_float(r[1]);
  r = SWAP32(x, x);
  x = __uint_as_float(r[0]) + __uint_as_float(r[1]);
  r = SWAP16(x, x);
  a = __uint_as_float(r[0]); b = __uint_as_float(r[1]);
}
template <bool MAX, typename M> __device__ __forceinline__ void red4(float& a, float& b, float& c, float& d, M mid) {
  auto op = [](unsigned x, unsigned y) { return MAX ? vmax2(__uint_as_float(x), __uint_as_float(y)) : __uint_as_float(x) + __uint_as_float(y); };
  auto r = SWAP16(a, b), q = SWAP16(c, d);
  const float x = op(r[0], r[1]), y = op(q[0], q[1]);
  r = SWAP32(x, y);
  const float w = mid(op(r[0], r[1]));
  r = SWAP32(w, w);
  q = __builtin_amdgcn_permlane16_swap(r[1], r[1], false, false);
  r = __builtin_amdgcn_permlane16_swap(r[0], r[0], false, false);
  a = __uint_as_float(r[0]); b = __uint_as_float(r[1]); c = __uint_as_float(q[0]); d = __uint_as_float(q[1]);
}
__device__ __forceinline__ float red_max4(float x) {
  auto r = __builtin_amdgcn_permlane16_swap(__float_as_uint(x), __float_as_uint(x), false, false);
  x = vmax2(__uint_as_float(r[0]), __uint_as_float(r[1]));
  r = __builtin_amdgcn_permlane32_swap(__float_as_uint(x), __float_as_uint(x), false, false);
  return vmax2(__uint_as_float(r[0]), __uint_as_float(r[1]));
}
__device__ __forceinline__ float red_add4(float x) {
  auto r = __builtin_amdgcn_permlane16_swap(__float_as_uint(x), __float_as_uint(x), false, false);
  x = __uint_as_float(r[0]) + __uint_as_float(r[1]);
  r = __builtin_amdgcn_permlane32_swap(__float_as_uint(x), __float_as_uint(x), false, false);
  return __uint_as_float(r[0]) + __uint_as_float(r[1]);
}

__device__ __forceinline__ void st16_sc1(rsrc_t r, unsigned voff, unsigned soff, u32x4 v) {
  __builtin_amdgcn_raw_buffer_store_b128(v, r, voff, soff, 16);
  asm volatile("s_nop 1" ::"v"(v) : "memory");
}
__device__ __forceinline__ void st8_sc1(rsrc_t r, unsigned voff, unsigned soff, u32x2 v) { __builtin_amdgcn_raw_buffer_store_b64(v, r, voff, soff, 16); }
__device__ __forceinline__ u32x4 ld16_sc1(rsrc_t r, unsigned voff, unsigned soff) { return __builtin_amdgcn_raw_buffer_load_b128(r, voff, soff, 16); }
__device__ __forceinline__ u32x2 ld8_sc1(rsrc_t r, unsigned voff, unsigned soff) { return __builtin_amdgcn_raw_buffer_load_b64(r, voff, soff, 16); }
__device__ __forceinline__ f32x4 ld4f_sc1(rsrc_t r, unsigned voff, unsigned soff) { return __builtin_bit_cast(f32x4, __builtin_amdgcn_raw_buffer_load_b128(r, voff, soff, 16)); }
#define OPAQUE_V(x) asm volatile("" : "+v"(x))
#define OPAQUE_S(x) asm volatile("" : "+s"(x))

#define CLOBBER asm volatile("" ::: "memory")
constexpr unsigned SPIN_CAP = 1u << 17;
__device__ __forceinline__ bool is_set(u32x2 v, u32x2 epoch) { return v[0] == epoch[0] && v[1] == epoch[1]; }
__device__ __forceinline__ void set_flags(rsrc_t rs, unsigned lineA, int colA, unsigned lineB, int colB, u32x2 epoch, int lane) {
  const unsigned off = (lane & 7) * REP_STRIDE + ((lane & 8) ? lineB + colB * 8 : lineA + colA * 8);
  if (lane < 16) st8_sc1(rs, off, 0, epoch);
}
__device__ __forceinline__ unsigned poll_need(rsrc_t rs, unsigned voff, u32x2 epoch, unsigned ready, unsigned need, bool& dead) {
  for (unsigned spins = 0; !dead; ++spins) {
    CLOBBER;
    ready |= (unsigned)__ballot(is_set(ld8_sc1(rs, voff, 0), epoch));
    if ((ready & need) == need) break;
    if (spins > SPIN_CAP) dead = true;
    __builtin_amdgcn_s_sleep(1);
  }
  CLOBBER;
  return dead ? ~0u : ready;
}

__device__ __forceinline__ void poll_all(rsrc_t rs, unsigned voff, u32x2 epoch, bool& dead) {
  for (unsigned spins = 0; !dead; ++spins) {
    CLOBBER;
    if (__all(is_set(ld8_sc1(rs, voff, 0), epoch))) break;
    if (spins > SPIN_CAP) dead = true;
    __builtin_amdgcn_s_sleep(1);
  }
  CLOBBER;
}

constexpr unsigned SC_VF = L_R1 + 32768;
constexpr unsigned SC_PHIK = L_R1 + 49152;
constexpr unsigned SC_XCH = L_R2 + XS;
constexpr unsigned wo_slot(int s) {
  return s < 2 ? L_R2 + (unsigned)(s + 1) * XS : (s < 6 ? L_R1 + (unsigned)(s - 2) * 16384 : (s == 6 ? L_R1 + 81920 : L_R2));
}
__device__ __forceinline__ unsigned wo_src(int s, int j) { return (unsigned)(((JROT(j) >> 1) + NJ / 2 + s) & 7); }
__device__ __forceinline__ unsigned wo_swz(int lane) { return (unsigned)(((lane & 7) ^ ((lane >> 3) & 6)) * 16); }
__device__ __forceinline__ unsigned umax3(unsigned a, unsigned b, unsigned c) { return max(max(a, b), c); }
__device__ __forceinline__ unsigned umax4v(unsigned m, const u32x4& v) { return umax3(umax3(m, v[0], v[1]), v[2], v[3]); }
__device__ __forceinline__ u32x4 nosent(u32x4 v) { return u32x4{min(v[0], SENT - 1), min(v[1], SENT - 1), min(v[2], SENT - 1), min(v[3], SENT - 1)}; }

template <typename P, typename F>
__device__ __forceinline__ void qkv_loop(int isb, int wm, int h, int lane, bool first, int& trv, P pre, F fin) {
  constexpr unsigned lds0 = 0;
  f32x4 acc[4][6];
  const int fr = lane & 15, fq = lane >> 4;
  unsigned ab[2], bb[2];
  {
    const int r = wm * 64 + fr;
#pragma unroll
    for (int ks = 0; ks < 2; ++ks) ab[ks] = lds0 + L_R2 + r * 128 + (((ks * 4 + fq) ^ ((fr >> 1) & 7)) * 16);
    const int kr = fq * 8 + (fr >> 2);
    const int gs = ((kr >> 1) & 1) | (((kr >> 3) & 1) << 1);
#pragma unroll
    for (int c = 0; c < 2; ++c) bb[c] = lds0 + L_R1 + kr * 128 + (((h * 2 + c) ^ gs) * 32) + (lane & 3) * 8;
  }
#pragma unroll
  for (int m = 0; m < 4; ++m)
#pragma unroll
    for (int n = 0; n < 6; ++n) acc[m][n] = f32x4{0.f, 0.f, 0.f, 0.f};
  bf16x8 af[2][4], bf[2][6];
  auto rds = [&](int ks, int p, int rot = 0) {
#pragma unroll
    for (int m = 0; m < 4; ++m) af[p][m] = lds_ld<bf16x8>(ab[ks] + m * 2048);
#pragma unroll
    for (int nn = 0; nn < 6; ++nn) {
      const int n = (nn + rot) % 6;
      const unsigned a = bb[n & 1] + (n >> 1) * 8192 + ks * 4096;
      bf[p][n] = tr2(a, a + 512);
    }
  };
  auto mms = [&](int p, int rot = 0, int cnt = 6) {
#pragma unroll
    for (int nn = 0; nn < cnt; ++nn)
#pragma unroll
      for (int mm = 0; mm < 4; ++mm) { const int n = (nn + rot) % 6, m = (n & 1) ? 3 - mm : mm; acc[m][n] = n < 4 ? MFMA(bf[p][n], af[p][m], acc[m][n]) : MFMA(af[p][m], bf[p][n], acc[m][n]); }
  };
  auto ilv2 = [&]() {
#pragma unroll
    for (int i = 0; i < 16; ++i) { SGB(0x008, 1); SGB(0x100, 1); }
#pragma unroll
    for (int i = 0; i < 4; ++i) { SGB(0x008, 1); SGB(0x002, 1); }
    SGB(0x008, 4);
  };
  auto ilv3 = [&]() {
#pragma unroll
    for (int i = 0; i < 16; ++i) { SGB(0x008, 1); SGB(0x100, 1); }
#pragma unroll
    for (int i = 0; i < 7; ++i) { SGB(0x008, 1); SGB(0x004, 2); }
    SGB(0x008, 1);
  };
#pragma unroll
  for (int i = 0; i < 24; ++i) OPAQUE_V(acc[i / 6][i % 6]);
  OPAQUE_V(ab[0]); OPAQUE_V(ab[1]); OPAQUE_V(bb[0]); OPAQUE_V(bb[1]);
  if (!first) { WAITV(3); WAITL0; }
  TRQ(1); BAR;
  int tslot = 2;
  rds(0, 0);
  int sa = 0, sb = 0;
  int da = XS, db = 24576;
  auto step = [&]() {
    ab[0] += da; ab[1] += da;
    bb[0] += db; bb[1] += db;
  };
  auto ring = [&]() {
    sa = (sa == 2) ? 0 : sa + 1;
    sb = (sb == 3) ? 0 : sb + 1;
    da = (sa == 2) ? -2 * (int)XS : (int)XS;
    db = (sb == 3) ? -3 * 24576 : 24576;
  };
#pragma unroll 1
  for (int t = 14; t > 0; --t) {
    SCHED;
    rds(1, 1); mms(0);
    step();
    ilv2();
    SCHED;
    __builtin_amdgcn_s_waitcnt(0x0070);
    TRQ(tslot); tslot++; BAR;
    SCHED;
    rds(0, 0);
    ring();
    mms(1); ilv3();
  }
  SCHED;
  f32x4 cs[4], nm4[4];
  float nmean[4], csv[2];
  auto fold = [&](int n) {
#pragma unroll
    for (int mm = 0; mm < 4; ++mm) {
      const int m = (n & 1) ? 3 - mm : mm;
#pragma unroll
      for (int e = 0; e < 4; ++e) {
        float t = n < 4 ? __builtin_fmaf(nmean[m], cs[n & 3][e], acc[m][n][e]) : __builtin_fmaf(nm4[m][e], csv[n & 1], acc[m][n][e]);
        asm("" : "+v"(t));
        acc[m][n][e] = t;
      }
    }
  };
  {
    unsigned pm = lds0 + L_MEAN + (16 + wm * 64 + fr) * 4, pc = lds0 + L_CS + isb * 768 + (h * 32 + fq * 4) * 4;
    unsigned pm4 = lds0 + L_MEAN + (16 + wm * 64 + fq * 4) * 4, pcv = lds0 + L_CS + isb * 768 + (128 + h * 32 + fr) * 4;
    asm("" : "+v"(pm)); asm("" : "+v"(pc)); asm("" : "+v"(pm4)); asm("" : "+v"(pcv));
#pragma unroll
    for (int m = 0; m < 4; ++m) nmean[m] = lds_ld<float>(pm + m * 64);
#pragma unroll
    for (int n = 0; n < 4; ++n) cs[n ^ 2] = lds_ld<f32x4>(pc + (((n ^ 2) >> 1) * 64 + (n & 1) * 16) * 4);
#pragma unroll
    for (int m = 0; m < 4; ++m) nm4[m] = lds_ld<f32x4>(pm4 + m * 64);
#pragma unroll
    for (int n = 0; n < 2; ++n) csv[n] = lds_ld<float>(pcv + n * 64);
    rds(1, 1, 4); mms(0, 2);
    step();
    fold(2); fold(3);
    SGB(0x002, 8);
#pragma unroll
    for (int i = 0; i < 8; ++i) { SGB(0x008, 1); SGB(0x100, 2); }
#pragma unroll
    for (int i = 0; i < 11; ++i) { SGB(0x008, 1); SGB(0x100, 1); SGB(0x002, 2); }
#pragma unroll
    for (int i = 0; i < 5; ++i) { SGB(0x008, 1); SGB(0x002, 3); }
    SCHED;
    __builtin_amdgcn_s_waitcnt(0xC07F);
    TRQ(16); BAR;
    SCHED;
    rds(0, 0, 2);
    mms(1, 4); fold(4); fold(5);
#pragma unroll
    for (int i = 0; i < 8; ++i) { SGB(0x008, 1); SGB(0x100, 1); SGB(0x002, 1); }
#pragma unroll
    for (int i = 0; i < 8; ++i) { SGB(0x008, 1); SGB(0x100, 1); SGB(0x002, 2); }
#pragma unroll
    for (int i = 0; i < 8; ++i) { SGB(0x008, 1); SGB(0x002, 2); }
    SCHED;
    pre(0);
    SCHED;
    rds(1, 1, 2);
    mms(0, 2, 4); fold(0); fold(1);
    pre(1);
#pragma unroll
    for (int i = 0; i < 16; ++i) { SGB(0x008, 1); SGB(0x100, 1); SGB(0x002, 2); SGB(0x004, 1); }
  }
  SCHED;
  auto mm6 = [&](int m, int n0 = 0, int n1 = 8) {
#pragma unroll
    for (int nn = n0; nn < n1; ++nn) {
      const int n = nn < 4 ? nn + 2 : nn & 1, p = (nn >> 1) != 2;
      acc[m][n] = n < 4 ? MFMA(bf[p][n], af[p][m], acc[m][n]) : MFMA(af[p][m], bf[p][n], acc[m][n]);
    }
  };
  if (!isb) {
    fin(std::false_type{}, acc, mm6);
#pragma unroll
    for (int i = 0; i < 24; ++i) asm volatile("" : "=v"(acc[i / 6][i % 6]));
#pragma unroll
    for (int i = 0; i < 6; ++i) asm volatile("" : "=v"(bf[1][i]));
#pragma unroll
    for (int i = 0; i < 8; ++i) asm volatile("" : "=v"(af[i >> 2][i & 3]));
    asm volatile("" : "=v"(bf[0][0])); asm volatile("" : "=v"(bf[0][1]));
  }
  OPAQUE_S(isb);
  if (isb) fin(std::true_type{}, acc, mm6);
}
__device__ __forceinline__ u32x4 pack(const f32x4& a, const f32x4& b) { return u32x4{pk2(a[0], a[1]), pk2(a[2], a[3]), pk2(b[0], b[1]), pk2(b[2], b[3])}; }

__device__ __forceinline__ u32x2 nosent2(const f32x4& a) { return u32x2{min(pk2(a[0], a[1]), SENT - 1), min(pk2(a[2], a[3]), SENT - 1)}; }
template <int N> __device__ __forceinline__ void hedgehog(const f32x4 (&r)[4][2], const float (&c)[4], float (&ep)[4][8], float (&en)[4][8]) {
  float mx[4], nm[4];
#pragma unroll
  for (int m = 0; m < N; ++m) {
    const f32x4 a = r[m][0], b = r[m][1];
    mx[m] = vmax2(fmaxf(fmaxf(a[3], b[0]), b[1]), fmaxf(fmaxf(fmaxf(fmaxf(a[0], a[1]), a[2]), b[2]), b[3]));
    nm[m] = vmax2(fmaxf(fmaxf(-a[3], -b[0]), -b[1]), fmaxf(fmaxf(fmaxf(fmaxf(-a[0], -a[1]), -a[2]), -b[2]), -b[3]));
  }
  SCHED;
  red4<true>(mx[0], mx[1], nm[0], nm[1], [](float x) { return x; });
  if (N == 4) red4<true>(mx[2], mx[3], nm[2], nm[3], [](float x) { return x; });
#pragma unroll
  for (int m = 0; m < N; ++m) { mx[m] *= -c[m]; nm[m] *= -c[m]; }
  SCHED;
#pragma unroll
  for (int m = 0; m < N; ++m) {
#pragma unroll
    for (int i = 0; i < 8; ++i) { ep[m][i] = __builtin_fmaf(r[m][i >> 2][i & 3], c[m], mx[m]); en[m][i] = __builtin_fmaf(r[m][i >> 2][i & 3], -c[m], nm[m]); }
    SCHED;
#pragma unroll
    for (int i = 0; i < 8; ++i) { ep[m][i] = __builtin_amdgcn_exp2f(ep[m][i]); en[m][i] = __builtin_amdgcn_exp2f(en[m][i]); }
    SCHED;
  }
}
__device__ __forceinline__ u32x4 pack8(const float (&e)[8], float f) {
  return u32x4{pk2(e[0] * f, e[1] * f), pk2(e[2] * f, e[3] * f), pk2(e[4] * f, e[5] * f), pk2(e[6] * f, e[7] * f)};
}
__device__ __forceinline__ u32x4 pack8(const float (&e)[8]) { return u32x4{pk2(e[0], e[1]), pk2(e[2], e[3]), pk2(e[4], e[5]), pk2(e[6], e[7])}; }

constexpr u64 window_mask(int t, int e) {
  u64 mk = 0;
  for (int ln = 0; ln < 64; ++ln) {
    const int delta = (ln & 15) + 16 - t * 16 - (ln >> 4) * 4 - e;
    if (delta >= 0 && delta <= 14 && !(delta & 1)) mk |= 1ull << ln;
  }
  return mk;
}
__device__ __forceinline__ void phik2(const f32x4 (&r)[4][2], const float (&c)[4], unsigned pw) {
  const f32x4 zero = {0.f, 0.f, 0.f, 0.f};
  const bf16x8 ones = {0x3f80, 0x3f80, 0x3f80, 0x3f80, 0x3f80, 0x3f80, 0x3f80, 0x3f80};
  float ep[4][8], en[4][8];
  f32x4 sp[2], sn[2];
  hedgehog<2>(r, c, ep, en);
#pragma unroll
  for (int m = 0; m < 2; ++m) { sp[m] = MFMA(ones, as_frag(pack8(ep[m])), zero); sn[m] = MFMA(ones, as_frag(pack8(en[m])), zero); }
#pragma unroll
  for (int m = 0; m < 2; ++m) { asm("" : "+v"(sp[m])); asm("" : "+v"(sn[m])); }
  SCHED;
#pragma unroll
  for (int m = 0; m < 2; ++m) {
    const float fp = __builtin_amdgcn_rcpf(sp[m][0]), fn = __builtin_amdgcn_rcpf(sn[m][0]);
#pragma unroll
    for (int i = 0; i < 8; ++i) { ep[m][i] *= fp; en[m][i] *= fn; }
    SCHED;
    const u32x4 pp = pack8(ep[m]), pn = pack8(en[m]);
    const unsigned d = pw + m * (16 * 160);
    lds_st<u32x2>(d, u32x2{pp[0], pp[1]});
    lds_st<u32x2>(d + 32, u32x2{pp[2], pp[3]});
    lds_st<u32x2>(d + 64, u32x2{pn[0], pn[1]});
    lds_st<u32x2>(d + 96, u32x2{pn[2], pn[3]});
    SCHED;
  }
}
__device__ __forceinline__ bf16x8 rowsum_sel(int lane) {
  const int fr = lane & 15;
  const unsigned one = (fr < 8) ? (0x3f80u << (16 * (fr & 1))) : 0u;
  const int e = (fr >> 1) & 3;
  return as_frag(u32x4{e == 0 ? one : 0u, e == 1 ? one : 0u, e == 2 ? one : 0u, e == 3 ? one : 0u});
}
__device__ __forceinline__ f32x4 rowsum_fin(f32x4 c) {
#pragma unroll
  for (int k = 0; k < 4; ++k) {
    const float a = c[k];
    c[k] = a + __int_as_float(__builtin_amdgcn_update_dpp(0, __float_as_int(a), 0x128  , 0xf, 0xf, false));
  }
  return c;
}
__device__ __forceinline__ void job_colsum(rsrc_t rs, const u32x4 (&w)[8], int mat, int g, int jc, int hw, int lane) {
  const bf16x8 sel = rowsum_sel(lane);
  f32x4 c = {0.f, 0.f, 0.f, 0.f};
#pragma unroll
  for (int i = 0; i < 8; ++i) c = MFMA(sel, as_frag(w[i]), c);
  const u32x4 w4 = __builtin_bit_cast(u32x4, rowsum_fin(c));
  unsigned so = WS_CS + (unsigned)(((mat * 16 + jc) * 16 + g) * 768);
  OPAQUE_S(so);
  if ((lane & 40) == 0) st16_sc1(rs, (unsigned)(hw * 64 + (lane & 7) * 8 + (lane >> 4) * 4) * 4, so, w4);
}
__device__ __forceinline__ unsigned img_off(int i, int lane) { return (lane >> 3) * 128 + (((lane & 7) ^ ((((lane >> 4) & 1) | ((i & 1) << 1)) << 1)) * 16); }
__device__ __forceinline__ void job_pack(rsrc_t rs, u32x4 w, int i, int mat, int g, int jc, int hw, int lane) {
  st16_sc1(rs, img_off(i, lane),
           WS_WP + (unsigned)(((mat * 16 + jc) * 16 + g) * 24576 + hw * 8192 + i * 1024), w);
}

__global__ __launch_bounds__(512, 2) void mega_kernel(Params p) {
  extern __shared__ __attribute__((aligned(16))) char smem[];
  const int tid = threadIdx.x;
  const int wave0 = __builtin_amdgcn_readfirstlane(tid >> 6);
  const int xx = blockIdx.x & 7, rr = blockIdx.x >> 3;
  const int g0 = (rr / NJ) * NX + (xx & (NX - 1)), j0 = (xx / NX) * NJ + (rr & (NJ - 1));
  constexpr unsigned lds0 = 0, lds00 = 0;
  if ((unsigned)(size_t)(LAS char*)smem != 0u) return;
#define LOCALS0 int g = g0, j = j0, l = l0, wave = wave0;                                              \
  OPAQUE_S(g); OPAQUE_S(j); OPAQUE_S(l); OPAQUE_S(wave);                                               \
  const int hw = wave & 3, wm = wave & 1, h = (wave >> 1) & 1;                                         \
  const unsigned myrep = (unsigned)(JROT(j) / 2 + (g & (NX - 1))) * REP_STRIDE;                              \
  (void)hw; (void)wm; (void)h; (void)myrep;                                                            \
  int lane = tid; OPAQUE_V(lane); lane &= 63
#define LOCALS LOCALS0; const bool isb = l & 1, lead = (wave == 4); const int half = wave >> 2; (void)isb; (void)lead; (void)half
  const rsrc_t rs = mk_rsrc(p.ws, WS_SPAN);
  const bool cw = wave0 < 4;
  int trv = 0, trA = 0, trB = 0, trC = 0;
  STAMP_INIT;
  TRC(0); TRR(62);
  if (wave0 == 3) {
    const char* pc;
    unsigned len;
    asm volatile("s_getpc_b64 %0\n.Lpf_base:\n\ts_mov_b32 %1, .Lpf_end-.Lpf_base" : "=s"(pc), "=s"(len));
    const unsigned off = ((unsigned)rr * 16 + (tid & 15)) * 128;
    touch(mk_rsrc(pc, len), ((int)len > 0 && off + 4 <= len) ? off : 0u, lds00 + L_SINK);
  }
  auto slowbit = [](const void* q) { return 1u << ((3 - ((unsigned)(size_t)q >> 7)) & 7); };
  unsigned slowm = slowbit(p.wqkvA) | (slowbit(p.wqkvB) << 8) | (slowbit(p.woA) << 16) | (slowbit(p.woB) << 24);
  OPAQUE_S(slowm);
  u32x2 epoch = {0u, 0u};
  bool dead = false;
      if (cw) {
        const int l0 = 0;
        LOCALS;
        u32x4 v[2][8];
        if (wave < 3) {
          const int jc = JOBC(j, wave), kt = (KB0(g, j) + (unsigned)g / NX) & 15;
          const rsrc_t ra = mk_rsrc(p.wqkvA, 1024 * 6144);
#pragma unroll
          for (int i = 0; i < 8; ++i)
            v[0][i] = __builtin_amdgcn_raw_buffer_load_b128(ra, (lane >> 3) * 6144 + (lane & 7) * 16, (kt * 64 + i * 8) * 6144 + wave * 2048 + jc * 128, 0);
          SCHED;
          TRC(44); BAR; SCHED;
          job_colsum(rs, v[0], 0, g, jc, wave, lane);
          if (SLOWC(0, jc)) {
#pragma unroll
            for (int i = 0; i < 8; ++i) job_pack(rs, v[0][i], i, 0, kt, jc, wave, lane);
          }
        } else {
          const int fr = lane & 15, fq = lane >> 4;
          const rsrc_t rx = mk_rsrc(p.x, 2048 * 2048);
#pragma unroll
          for (int i = 0; i < 16; ++i)
            v[i & 1][i >> 1] = __builtin_amdgcn_raw_buffer_load_b128(rx, fr * 2048 + fq * 16, (g * 128 + (j & (NJ - 1)) * (128 / NJ) + ((i >> 1) % (8 / NJ)) * 16) * 2048 + ((KB0(g, j) + (i >> 1) / (8 / NJ)) & 15) * 128 + (i & 1) * 64, 0);
          SCHED;
          TRC(44); BAR; SCHED;
          const f32x4 zero = {0.f, 0.f, 0.f, 0.f};
          const bf16x8 ones = {0x3f80, 0x3f80, 0x3f80, 0x3f80, 0x3f80, 0x3f80, 0x3f80, 0x3f80};
          float sm[8], q[8];
#pragma unroll
          for (int m = 0; m < 8; ++m) {
            const f32x4 a = MFMA(ones, as_frag(v[1][m]), MFMA(ones, as_frag(v[0][m]), zero));
            const f32x4 b = MFMA(as_frag(v[1][m]), as_frag(v[1][m]), MFMA(as_frag(v[0][m]), as_frag(v[0][m]), zero));
            sm[m] = a[0];
            q[m] = (fr & 2) ? ((fr & 1) ? b[3] : b[2]) : ((fr & 1) ? b[1] : b[0]);
          }
          if (fq == (fr >> 2)) {
#pragma unroll
            for (int m = 0; m < 8; ++m) st8_sc1(rs, ((m % (8 / NJ)) * 16 + fr) * 8 + (m / (8 / NJ)) * 1024, st_off(g, 0) + JROT(j) * 1024 + (j & (NJ - 1)) * (1024 / NJ), u32x2{__float_as_uint(sm[m]), __float_as_uint(q[m])});
          }
        }
      }
  const char *pA = (const char*)p.x, *pW = (const char*)p.wqkvA;
  unsigned zA = 2048 * 2048, bA = g0 * (128 * 2048), kA = 128, pcA = 32 * 2048, vA;
  unsigned zW = 1024 * 6144, bW = j0 * 128, kW = 64 * 6144, pcW = 2048, hW = 32 * 6144, vW;
  int kb = KB0(g0, j0), dj = -1;
  {
    const int lane = tid & 63, hw = wave0 & 3;
    vA = (hw * 8 + (lane >> 3)) * 2048 + (((lane & 7) ^ (((hw & 1) * 4 + (lane >> 4)) & 7)) * 16);
    vW = (hw * 8 + (lane >> 3)) * 6144 + (((lane & 7) ^ ((((lane >> 4) & 1) | ((hw & 1) << 1)) << 1)) * 16);
  }

#pragma unroll 1
  for (int l0 = 0; l0 < 4; ++l0) {
    if (l0) { TRC(0); TRR(62); }

    f32x4 acc[2][4];
    auto kvpart = [&](unsigned va, unsigned pa, unsigned pb, unsigned kvb, int lane, int wave) {
      const f32x4 zero = {0.f, 0.f, 0.f, 0.f};
      const bf16x8 ones = {0x3f80, 0x3f80, 0x3f80, 0x3f80, 0x3f80, 0x3f80, 0x3f80, 0x3f80};
      f32x4 c0 = zero, c1 = zero, z0 = zero;
#pragma unroll
      for (int ks = 0; ks < 4; ++ks) {
        const bf16x8 v = lds_ld<bf16x8>(va + ks * 2048);
        const bf16x8 a0 = tr2(pa + ks * 32 * 160, pa + (ks * 32 + 16) * 160);
        const bf16x8 a1 = tr2(pb + ks * 32 * 160, pb + (ks * 32 + 16) * 160);
        c0 = MFMA(a0, v, c0);
        c1 = MFMA(a1, v, c1);
        z0 = MFMA(a0, ones, z0);
      }
      const u32x4 c = nosent(pack(c0, c1));
      const bool dt = wave & 2;
      st16_sc1(rs, lane * 16, kvb + (unsigned)wave * 1024, u32x4{dt ? c[2] : c[0], dt ? c[3] : c[1], dt ? c[0] : c[2], dt ? c[1] : c[3]});
      if ((lane & 15) == 0)
        st8_sc1(rs, lane, kvb + KVP_Z + (wave >> 2) * 128 + (wave & 1) * 64 + (wave & 2) * 4, nosent2(z0));
    };
#define KV_ADDR                                                                                                            \
    unsigned va = lds0 + SC_VF + (wave >> 2) * 8192 + ((wave >> 1) & 1) * 1024 + lane * 16;                                \
    unsigned pa = lds0 + SC_PHIK + ((wave >> 2) * 128 + fq * 4 + (fr >> 2)) * 160 + (wave & 1) * 64 + (lane & 3) * 8;      \
    unsigned kvj = WS_KVP + (unsigned)j * KVP_J + (unsigned)(l >> 1) * KVP_L;                                              \
    unsigned pb = pa + 32 - (wave & 2) * 16;                                                                               \
    pa += (wave & 2) * 16;                                                                                                 \
    OPAQUE_V(va); OPAQUE_V(pa); OPAQUE_V(pb); OPAQUE_S(kvj)
    if (cw) {
      LOCALS;
      unsigned otile, kvo, l16, pr1, pr4;
      int g2, j2, l2, ln2;
      qkv_loop(l & 1, wm, h, lane, l == 0, trv, [&](int st) {
        if (st == 0) {
          g2 = g0; j2 = j0; l2 = l0; ln2 = tid;
          OPAQUE_S(g2); OPAQUE_S(j2); OPAQUE_S(l2); OPAQUE_V(ln2);
          return;
        }
        otile = ob_off(g2, j2, l2) + (unsigned)(wm * 8 + h) * 1024;
        kvo = WS_KVP + (unsigned)j2 * KVP_J + (unsigned)g2 * KVP_G + (unsigned)(l2 >> 1) * KVP_L;
        l16 = (ln2 & 63) * 16;
        pr1 = lds0 + L_RSTD + (16 + wm * 64 + (ln2 & 15)) * 4;
        pr4 = lds0 + L_RSTD + wm * 256 + (ln2 & 48);
        asm("" : "+s"(otile)); asm("" : "+s"(kvo)); asm("" : "+v"(l16)); asm("" : "+v"(pr1)); asm("" : "+v"(pr4));
      }, [&](auto isb_c, f32x4 (&a)[4][6], auto& mm6) {
        const f32x4 zero = {0.f, 0.f, 0.f, 0.f};
        constexpr float L2E = 1.4426950408889634f;
        float rq[4];
#define ilv8(nv) do { _Pragma("unroll") for (int i_ = 0; i_ < 8; ++i_) { SGB(0x008, 1); SGB(0x100, 2); SGB(0x002, nv); } SCHED; } while (0)
        if constexpr (decltype(isb_c)::value) {
          constexpr float QS = 0.17677669529663687f * L2E, NEG = -3.0e38f;
          const bf16x8 ones = {0x3f80, 0x3f80, 0x3f80, 0x3f80, 0x3f80, 0x3f80, 0x3f80, 0x3f80};
          u32x4 kf[5], vf[5], qf[4], pf[4];
          f32x4 rs4[4], bias[2], s0[4], s1[4], dn[4];
          float x[4][4], mx[4];
          unsigned psel0, psel1;
          auto pkkv = [&](int m) { kf[m + 1] = pack(a[m][2] * rq[m], a[m][3] * rq[m]); vf[m + 1] = pack(a[m][4] * rs4[m], a[m][5] * rs4[m]); };
          auto pkq = [&](int m) { qf[m] = pack(a[m][0], a[m][1]); };
          auto st1 = [&](int m) {
            x[m][0] = vmax2(s0[m][0], s0[m][1]); x[m][1] = vmax2(s0[m][2], s0[m][3]); x[m][2] = vmax2(s1[m][0], s1[m][1]); x[m][3] = vmax2(s1[m][2], s1[m][3]);
            mx[m] = vmax2(vmax2(x[m][0], x[m][1]), vmax2(x[m][2], x[m][3]));
          };
          auto st2 = [&](int m) {
            const float mxl = mx[m] * -rq[m];
#pragma unroll
            for (int e = 0; e < 4; ++e) x[m][e] = __builtin_fmaf(x[m][e], rq[m], mxl);
          };
          auto st3 = [&](int m) {
#pragma unroll
            for (int e = 0; e < 4; ++e) x[m][e] = __builtin_amdgcn_exp2f(x[m][e]);
          };
          auto st5 = [&](int m) {
            const unsigned p0 = pk2(x[m][0], x[m][1]), p1 = pk2(x[m][2], x[m][3]);
            pf[m] = u32x4{__builtin_amdgcn_perm(0u, p0, psel0), __builtin_amdgcn_perm(0u, p0, psel1), __builtin_amdgcn_perm(0u, p1, psel0), __builtin_amdgcn_perm(0u, p1, psel1)};
          };
          auto st6 = [&](int m) {
            dn[m] = MFMA(ones, as_frag(pf[m]), zero);
            s0[m] = MFMA(as_frag(u32x4{vf[m][0], vf[m][1], vf[m + 1][0], vf[m + 1][1]}), as_frag(pf[m]), zero);
            s1[m] = MFMA(as_frag(u32x4{vf[m][2], vf[m][3], vf[m + 1][2], vf[m + 1][3]}), as_frag(pf[m]), zero);
            asm("" : "+v"(dn[m]));
          };
          auto st7 = [&](int m) { const float inv = __builtin_amdgcn_rcpf(dn[m][0]); pf[m] = nosent(pack(s0[m] * inv, s1[m] * inv)); };
          mm6(3);
#pragma unroll
          for (int m = 0; m < 4; ++m) rq[m] = lds_ld<float>(pr1 + m * 64);
#pragma unroll
          for (int m = 3; m >= 0; --m) rs4[m] = lds_ld<f32x4>(pr4 + 64 + m * 64);
          unsigned pv_ = l16 + lds0 + L_PREV + h * 2048;
          asm("" : "+v"(pv_));
          static_for<0, 8>([&](auto i_c) __attribute__((always_inline)) {
            constexpr int i = decltype(i_c)::value;
            constexpr u64 mk = window_mask(i >> 2, i & 3);
            bias[i >> 2][i & 3] = __builtin_amdgcn_inverse_ballot_w64(mk) ? 0.f : NEG;
          });
          asm("" : "+v"(bias[0])); asm("" : "+v"(bias[1]));
          const unsigned hso = kvo + KVP_HALO + h * 2048, hsi = hso - (g > 0 ? KVP_G : 0u);
          ilv8(2);
          mm6(2);
          pkkv(3);
          ilv8(2);
          OPAQUE_V(kf[4]); OPAQUE_V(vf[4]);
          if (wm) {
            st16_sc1(rs, l16, hso, nosent(kf[4]));
            st16_sc1(rs, l16, hso + 1024, nosent(vf[4]));
          } else {
            lds_st<u32x4>(pv_, kf[4]);
            lds_st<u32x4>(pv_ + 1024, vf[4]);
          }
          SCHED;
          mm6(1);
          pkq(3); pkkv(2);
          ilv8(3);
          WAITL0;
          TRQ(17); BAR;
          SCHED;
          mm6(0);
          pkq(2); pkkv(1);
          ilv8(3);
          s0[3] = MFMA(as_frag(kf[3]), as_frag(qf[3]), bias[0]); s1[3] = MFMA(as_frag(kf[4]), as_frag(qf[3]), bias[1]);
          s0[2] = MFMA(as_frag(kf[2]), as_frag(qf[2]), bias[0]); s1[2] = MFMA(as_frag(kf[3]), as_frag(qf[2]), bias[1]);
          pkq(1); pkkv(0);
#pragma unroll
          for (int i = 0; i < 4; ++i) { SGB(0x008, 1); SGB(0x002, 5); }
          SCHED;
          const bool halo = wm == 0 && g > 0;
          const u32x4 hk = ld16_sc1(rs, l16, hsi), hv = ld16_sc1(rs, l16, hsi + 1024);
          SCHED;
          s0[1] = MFMA(as_frag(kf[1]), as_frag(qf[1]), bias[0]); s1[1] = MFMA(as_frag(kf[2]), as_frag(qf[1]), bias[1]);
          st1(3); st1(2);
#pragma unroll
          for (int m = 0; m < 4; ++m) rq[m] *= QS;
          SCHED;
          st1(1);
          pkq(0);
          psel0 = __builtin_amdgcn_inverse_ballot_w64(0xaaaaaaaaaaaaaaaaull) ? 0x01000c0cu : 0x0c0c0100u;
          psel1 = __builtin_amdgcn_inverse_ballot_w64(0xaaaaaaaaaaaaaaaaull) ? 0x03020c0cu : 0x0c0c0302u;
          { float d = mx[1]; red4<true>(mx[3], mx[2], mx[1], d, [](float v) { return v; }); }
          s1[0] = MFMA(as_frag(kf[1]), as_frag(qf[0]), bias[1]);
          SCHED;
          st2(3); st2(2); st2(1);
          SCHED;
          st3(3); st3(2); st3(1);
          SCHED;
          st5(3); st6(3); st5(2); st6(2); st5(1); st6(1);
          SCHED;
          TRR(60);
          TRC(18);
          u32x4 pk = lds_ld<u32x4>(pv_), pv = lds_ld<u32x4>(pv_ + 1024);
          st7(3); st7(2); st7(1);
          SCHED;
#pragma unroll
          for (int m = 3; m > 0; --m) st16_sc1(rs, l16, otile + m * 2048, pf[m]);
          if (!wm && g == 0) bias[0] = f32x4{NEG, NEG, NEG, NEG};
          SCHED;
          TRV(55, 0); TRC(21);
          if (halo) {
            pk = hk; pv = hv;
            for (unsigned spins = 0; !dead && __any(umax4v(umax4v(0u, pk), pv) == SENT); ++spins) {
              if (spins > SPIN_CAP) { dead = true; break; }
              CLOBBER;
              TRV(55, spins + 1);
              pk = ld16_sc1(rs, l16, hsi); pv = ld16_sc1(rs, l16, hsi + 1024);
              OPAQUE_V(pk); OPAQUE_V(pv);
            }
          }
          vf[0] = pv;
          s0[0] = MFMA(as_frag(pk), as_frag(qf[0]), bias[0]);
          st1(0);
          mx[0] = red_max4(mx[0]);
          st2(0); st3(0); st5(0); st6(0); st7(0);
          st16_sc1(rs, l16, otile, pf[0]);
        } else {
          LOCALS;
          const int fr = lane & 15, fq = lane >> 4;
          f32x4 rs4[4], r[4][2];
          const bf16x8 ones = {0x3f80, 0x3f80, 0x3f80, 0x3f80, 0x3f80, 0x3f80, 0x3f80, 0x3f80};
          auto proj = [&](int m, int n, const bf16x8& w0, const bf16x8& w1) {
            const u32x4 f = pack(a[m][n], a[m][n + 1]);
            r[m][0] = MFMA(w0, as_frag(f), zero); r[m][1] = MFMA(w1, as_frag(f), zero);
          };
          mm6(3, 0, 2); mm6(2, 0, 2); mm6(1, 0, 2); mm6(0, 0, 2);
          unsigned wa = lds0 + L_WPHI + h * 2048 + (fq * 4 + (fr >> 2)) * 64 + (lane & 3) * 8, kw = lds0 + L_PREV + hw * 2048 + l16;
          asm("" : "+v"(wa)); asm("" : "+v"(kw));
#pragma unroll
          for (int m = 0; m < 4; ++m) rq[m] = lds_ld<float>(pr1 + m * 64);
#pragma unroll
          for (int m = 0; m < 4; ++m) rs4[m] = lds_ld<f32x4>(pr4 + 64 + m * 64);
          const bf16x8 w0 = tr2(wa, wa + 1024);
          const bf16x8 w1 = tr2(wa + 32, wa + 32 + 1024);
#pragma unroll
          for (int i = 0; i < 8; ++i) { SGB(0x008, 1); SGB(0x100, 2); SGB(0x002, 2); }
          SCHED;
          mm6(3, 2, 4); mm6(2, 2, 4);
          lds_st<u32x4>(kw + 1024, pack(a[3][2], a[3][3]));
          lds_st<u32x4>(kw, pack(a[2][2], a[2][3]));
#pragma unroll
          for (int i = 0; i < 2; ++i) { SGB(0x008, 1); SGB(0x002, 2); SGB(0x008, 1); SGB(0x002, 2); SGB(0x200, 1); }
          SCHED;
          mm6(1, 2, 4); mm6(0, 2, 4);
          SCHED;
          WAITL0;
          TRQ(17); BAR;
          SCHED;
          proj(0, 2, w0, w1); proj(1, 2, w0, w1);
          unsigned pw = lds0 + SC_PHIK + (h * 128 + wm * 64 + fr) * 160 + fq * 8, vw = lds0 + SC_VF + h * 8192 + wm * 4096 + l16;
          asm("" : "+v"(pw)); asm("" : "+v"(vw));
#pragma unroll
          for (int m = 0; m < 4; ++m) rq[m] *= L2E;
#pragma unroll
          for (int i = 0; i < 4; ++i)
            lds_st<u32x4>(vw + i * 1024, pack(a[i & 2][4 + (i & 1)] * rs4[i & 2], a[(i & 2) + 1][4 + (i & 1)] * rs4[(i & 2) + 1]));
          SCHED;
          phik2(r, rq, pw);
          KV_ADDR;
          WAITL0;
          TRR(60);
          TRC(19); BAR;
          kvpart(va, pa, pb, kvo, lane, wave);
          SCHED;
#pragma unroll
          for (int m = 0; m < 4; ++m) mm6(m, 4, 8);
          SCHED;
          CLOBBER;
          TRC(20); BAR;
          u32x4 qp[4], qn[4];
          float sqp[4], sqn[4];
#pragma unroll
          for (int m = 0; m < 4; ++m) proj(m, 0, w0, w1);
          SCHED;
          float ep[4][8], en[4][8];
          hedgehog<4>(r, rq, ep, en);
          const unsigned zo = (lane >> 3) * KVP_G + (lane & 7) * 16, zs = kvj + KVP_Z + h * 128;
          u32x4 zv[2] = {ld16_sc1(rs, zo, zs), ld16_sc1(rs, zo, zs + 8 * KVP_G)};
          SCHED;
#pragma unroll
          for (int m = 0; m < 4; ++m) {
            qp[m] = pack8(ep[m]); qn[m] = pack8(en[m]);
            sqp[m] = MFMA(ones, as_frag(qp[m]), zero)[0];
            sqn[m] = MFMA(ones, as_frag(qn[m]), zero)[0];
          }
          SCHED;
#pragma unroll
          for (int m = 0; m < 4; ++m) { sqp[m] = __builtin_amdgcn_rcpf(sqp[m]); sqn[m] = __builtin_amdgcn_rcpf(sqn[m]); }
#pragma unroll
          for (int m = 0; m < 4; ++m) { OPAQUE_V(qp[m]); OPAQUE_V(qn[m]); OPAQUE_V(sqp[m]); OPAQUE_V(sqn[m]); }
          unsigned ko = (lane >> 5) * KVP_G + (lane & 31) * 16, ks = kvj + (unsigned)((j ^ RX) - j) * KVP_J + KVP_SUM + h * (8 * KVP_G);
          bf16x8 sel = rowsum_sel(lane);
          OPAQUE_V(ko); OPAQUE_S(ks); OPAQUE_V(sel);
          TRC(21); TRV(50, 0);
          for (unsigned spins = 0; !dead && __any(umax4v(umax4v(0u, zv[0]), zv[1]) == SENT); ++spins) {
            if (spins > SPIN_CAP) { dead = true; break; }
            CLOBBER;
            TRV(50, spins + 1);
            zv[0] = ld16_sc1(rs, zo, zs); zv[1] = ld16_sc1(rs, zo, zs + 8 * KVP_G);
          }
          TRC(22);
          {
            const f32x4 z = rowsum_fin(MFMA(sel, as_frag(zv[1]), MFMA(sel, as_frag(zv[0]), zero)));
            if ((lane & 40) == 0) lds_st<u32x2>(lds0 + L_ZF + hw * 128 + fr * 16 + fq * 8, u32x2{pk2(z[0], z[1]), pk2(z[2], z[3])});
            WAITL0;
            const bf16x8 zf0 = lds_ld<bf16x8>(lds0 + L_ZF + hw * 128 + fq * 16), zf1 = lds_ld<bf16x8>(lds0 + L_ZF + hw * 128 + 64 + fq * 16);
            float dp[4], dn[4];
#pragma unroll
            for (int m = 0; m < 4; ++m) { dp[m] = MFMA(zf0, as_frag(qp[m]), zero)[0]; dn[m] = MFMA(zf1, as_frag(qn[m]), zero)[0]; }
            SCHED;
#pragma unroll
            for (int m = 0; m < 4; ++m) {
              const float inv = __builtin_amdgcn_rcpf(__builtin_fmaf(dp[m], sqp[m], __builtin_fmaf(dn[m], sqn[m], 1e-6f)));
              sqp[m] *= inv; sqn[m] *= inv;
              OPAQUE_V(sqp[m]); OPAQUE_V(sqn[m]);
            }
          }
          TRV(51, 0);
          TRC(43); BAR;
          __builtin_amdgcn_s_sleep(H2D);
          u32x4 kv[4];
#pragma unroll
          for (int i = 0; i < 4; ++i) kv[i] = ld16_sc1(rs, ko, ks + i * (2 * KVP_G));
          SCHED;
#pragma unroll
          for (int m = 0; m < 4; ++m) { qp[m] = pack8(ep[m], sqp[m]); qn[m] = pack8(en[m], sqn[m]); }
#pragma unroll
          for (int m = 0; m < 4; ++m) { OPAQUE_V(qp[m]); OPAQUE_V(qn[m]); }
          SCHED;
          for (unsigned spins = 0; !dead && __any(max(umax4v(umax4v(0u, kv[0]), kv[1]), umax4v(umax4v(0u, kv[2]), kv[3])) == SENT); ++spins) {
            if (spins > SPIN_CAP) { dead = true; break; }
            CLOBBER;
            TRV(51, spins + 1);
#pragma unroll
            for (int i = 0; i < 4; ++i) kv[i] = ld16_sc1(rs, ko, ks + i * (2 * KVP_G));
          }
          TRC(23);
          f32x4 o[4][2];
#pragma unroll
          for (int m = 0; m < 4; ++m)
#pragma unroll
            for (int i = 0; i < 2; ++i) o[m][i] = MFMA(as_frag(kv[2 * i + 1]), as_frag(qn[m]), MFMA(as_frag(kv[2 * i]), as_frag(qp[m]), zero));
#pragma unroll
          for (int m = 0; m < 4; ++m) st16_sc1(rs, l16, otile + m * 2048, nosent(pack(o[m][0], o[m][1])));
        }
      });
#pragma unroll
      for (int i = 0; i < 8; ++i) asm volatile("" : "=v"(acc[i >> 2][i & 3]));
    } else {
      __builtin_amdgcn_s_setprio(3);
      if (l0) {
        WAITV(3);
        WAITL0;
        TRC(1); BAR;
      }
      LOCALS;
      u32x4 xr[2][2];
      const unsigned lin = lane * 16, vlin = lin + hw * 1024;
      const int jr = j & ~3;
      u32x4 sent4 = {SENT, SENT, SENT, SENT};
      OPAQUE_V(sent4);
      const unsigned cb0 = ob_off(g, j, l), cb2 = xb_off(g, l) + j * TS;
      const unsigned cb1 = WS_KVP + (unsigned)j * KVP_J + (unsigned)g * KVP_G + (unsigned)(l >> 1) * KVP_L;
      unsigned ra0;
      int tr = 0;
      auto ldres = [&](int t) {
        if (tr == t) {
#pragma unroll
          for (int i = 0; i < 4; ++i) xr[i >> 1][i & 1] = lds_ld<u32x4>((ra0 ^ ((i & 1) * 64)) + (t % 3) * XS + ((i >> 1) ^ 1) * 2048);
        }
      };
      auto aslot = [&](int t) { return lds0 + L_R2 + (t % 3) * XS + hw * 1024; };
      auto wslot = [&](int t) { return lds0 + L_R1 + (t & 3) * 24576 + hw * 1024; };
      const bool l0f = l == 0;
      const rsrc_t rA = mk_rsrc(pA, zA);
      auto kseq = [&](int t) { return (kb + t - (t <= dj ? 1 : 0)) & 15; };
      auto offA = [&](int t) { return bA + (unsigned)kseq(t) * kA; };
      auto offW = [&](int t) { return bW + (unsigned)kseq(t) * kW; };
      auto ldA = [&](int t) { dma4<4096>(rA, vA, offA(t), pcA, aslot(t)); };
      auto ldWh = [&](int t, unsigned o, int hf) { dma3<8192>(mk_rsrc(pW, zW), vW, o + (hf ? hW : 0u), pcW, wslot(t) + hf * 4096); };
      const bool slowj = l0f && SLOWC(0, j);
      auto packedW = [&]() {
        pW = p.ws; zW = WS_SPAN; bW = WS_WP + (unsigned)j * (16 * 24576); kW = 24576; pcW = 8192; hW = 4096; vW = vlin;
      };
      auto ldW = [&](int t) { const unsigned o = offW(t); ldWh(t, o, 0); ldWh(t, o, 1); };
      constexpr auto spread = [](int n) { unsigned m = 0; for (int t = 0; t < 26; ++t) if ((t + 1) * n / 13 > t * n / 13) m |= 1u << t; return m; };
      constexpr unsigned sp4 = spread(4), sp8 = spread(8), sp12 = spread(12);
      const unsigned smask = (isb ? (l == 3 ? sp4 : sp8) : sp12) >> ((((g + j * 5) & 15) * 13) >> 4);
      const unsigned cbm = isb ? cb2 : cb1;
      auto clean = [&](int t) __attribute__((always_inline)) {
        if (t < 13 && ((smask >> t) & 1)) {
          const unsigned cc = __builtin_popcount(smask & ((1u << t) - 1));
          const unsigned cb = cb0 + (cc >= 4 ? cbm - cb0 : 0u) + (cc >= 8 ? cb2 - cbm : 0u), pc = (cc + g + j) & 3;
          st16_sc1(rs, (lane >> 4) * 4096 + (lane & 15) * 16, cb + hw * 1024 + pc * 256, sent4);
        }
      };
      unsigned flo;
      unsigned ready = ~0u;
      u32x4 cv[4];
#pragma unroll
      for (int ii = 0; ii < 4; ++ii) asm volatile("" : "=v"(cv[ii]));
      auto chkrd = [&](int t) {
#pragma unroll
        for (int ii = 0; ii < 4; ++ii) cv[ii] = lds_ld<u32x4>(aslot(t) + lin + ii * 4096);
      };
      auto chkbad = [&]() { return __any(max(umax4v(umax4v(0u, cv[0]), cv[1]), umax4v(umax4v(0u, cv[2]), cv[3])) == SENT) && !l0f; };
      auto refetch = [&](int t) __attribute__((always_inline)) {
        for (unsigned spins = 0; !dead; ++spins) {
          if (spins > SPIN_CAP) { dead = true; break; }
          WAITV(0);
          TRV(t == 1 ? 56 : 57, spins + 1);
          ldA(t);
          ldA(t + 1);
          WAITV(0);
          chkrd(t);
          if (!chkbad()) break;
          __builtin_amdgcn_s_sleep(1);
        }
      };
      f32x4 jc[4];
#pragma unroll
      for (int i = 0; i < 4; ++i) asm volatile("" : "=v"(jc[i]));
      auto csld = [&]() {
#pragma unroll
        for (int i = 0; i < 4; ++i)
          ld16_asm(jc[i], rs, (lane >> 4) * 768 + hw * 192 + ((lane & 15) < 12 ? (lane & 15) : 0) * 16, WS_CS + (unsigned)((l * 16 + j) * 16 + i * 4) * 768);
      };
      auto csred = [&]() {
#pragma unroll
        for (int i = 0; i < 4; ++i) OPAQUE_V(jc[i]);
        const f32x4 s = (jc[0] + jc[1]) + (jc[2] + jc[3]);
        auto r = SWAP16(s[0], s[1]), q = SWAP16(s[2], s[3]);
        r = SWAP32(__uint_as_float(r[0]) + __uint_as_float(r[1]), __uint_as_float(q[0]) + __uint_as_float(q[1]));
        if ((lane & 15) < 12) lds_st<float>(lds0 + L_CS + l * 768 + hw * 192 + (lane & 15) * 16 + (lane >> 4) * 4, __uint_as_float(r[0]) + __uint_as_float(r[1]));
      };
      if (l0f) {
        u32x2 gw2;
        ld8_asm(gw2, rs, WS_EPOCH + 0u * lin);
        TRC(44); BAR;
        ldW(0); ldA(0);
        dma1(mk_rsrc(p.wphi, 32 * 2048), vlin, j * 4096, lds0 + L_WPHI + hw * 1024);
        ldW(1);
        WAITV(17);
        OPAQUE_V(gw2);
        const u64 gword = ((u64)(unsigned)UNI(gw2[1]) << 32) | (unsigned)UNI(gw2[0]);
        u64 z = gword + 0x9e3779b97f4a7c15ull;
        z = (z ^ (z >> 30)) * 0xbf58476d1ce4e5b9ull;
        z = (z ^ (z >> 27)) * 0x94d049bb133111ebull;
        z = z ^ (z >> 31);
        z = (z == gword) ? ~z : z;
        epoch = u32x2{(unsigned)z, (unsigned)(z >> 32)};
        tr = (j - kb) & 15;
        WAITV(7);
        TRC(1); BAR;
        ldA(1);
      } else {
        ldW(1);
        if (l == 1) csld();
        else __builtin_amdgcn_s_sleep(XD);
        ldA(1);
      }
      ldA(2);
      TRC(40);
      ldW(2);
#pragma unroll
      for (int i = 0; i < 4; ++i) asm volatile("" : "=v"(xr[i >> 1][i & 1]));
      unsigned oA = offA(3), oW = 0;
      OPAQUE_S(oA);
      TRV(56, 0); TRV(57, 0);
      ra0 = lds0 + L_R2 + (hw * 32 + (lane & 15)) * 128 + (((((lane >> 4) & 1) * 2 + (lane >> 5)) ^ ((lane >> 1) & 7)) * 16);
      OPAQUE_V(ra0);
      ldres(0);
      clean(0);
      if (l == 1) { WAITV(14); csred(); }
      WAITV(10);
      chkrd(1);
      if (__builtin_expect(chkbad(), 0)) refetch(1);
      const int fr = lane & 15, fq = lane >> 4;
      u32x2 fl;
      f32x4 js[4];
      float rb[4];
      const bool slowo = SLOWC(2 + (l & 1), j);
      const char* pO;
      unsigned zO, bO, pcO, vO, clo;
      u32x4 wo2[2];
      asm volatile("" : "=v"(wo2[0])); asm volatile("" : "=v"(wo2[1]));
      const unsigned fwo = myrep + fl_line(FL_WO + (isb ? 1 : 0), jr + (lane >> 4)) + (lane & 15) * 8;
      static_for<1, 14>([&](auto t_c) __attribute__((always_inline)) {
        constexpr int t = decltype(t_c)::value;
        constexpr auto rest = [](int s) { return 6 + (s == NVAL - 2 || s == 5 || s == 11) + (s == 8 ? 4 : 0); };
        constexpr int NW = t == 1 ? 63 : 4 + rest(t - 1);
        TRC(1 + t);
        if (t >= NVAL && t <= 6 && __builtin_expect((ready & (1u << kseq(t + 2))) == 0, 0)) {
          WAITV(NW); WAITL0; BAR;
          ready = poll_need(rs, flo, epoch, ready, 1u << kseq(t + 2), dead);
          dma4<4096>(rA, vA, oA, pcA, aslot(t + 2));
        } else if (t == 7 && __builtin_expect(ready != ~0u, 0)) {
          WAITV(NW); WAITL0; BAR;
          poll_all(rs, flo, epoch, dead);
          dma4<4096>(rA, vA, oA, pcA, aslot(t + 2));
        } else {
          bar_dma4<4096, NW>(rA, vA, oA, pcA, aslot(t + 2));
        }
        if (t == 1 && lead) set_flags(rs, fl_line(FL_X + l, g), j, l0f ? fl_line(FL_CS, j) : fl_line(FL_X + l, g), l0f ? g : j, epoch, lane);
        if (t == 7 && slowj) packedW();
        oW = offW(t + 2);
        if (t < 13) oA = offA(t + 3);
        OPAQUE_S(oA); OPAQUE_S(oW);
        clean(t);
        if (t <= NVAL && !l0f) { WAITV(rest(t - 1) + 4); chkrd(t + 1); }
        if (t == WT + 3 && l0f && SLOWC(2, jr + hw)) {
          OPAQUE_V(wo2[0]); OPAQUE_V(wo2[1]);
#pragma unroll
          for (int i = 0; i < 2; ++i) st16_sc1(rs, lin, WS_WO + (unsigned)((jr + hw) * 131072 + g * 8192 + ((j & 3) * 2 + i) * 1024), wo2[i]);
        }
        if (t == WT + 6 && l0f && lead) set_flags(rs, fl_line(FL_WO, j), g, fl_line(FL_WO, j), g, epoch, lane);
        ldres(t);
        if (t == 8) {
#pragma unroll
          for (int i = 0; i < 4; ++i) ld16_asm(js[i], rs, fq * 1024 + hw * 256 + fr * 16, st_off(g, l) + i * 4096);
        }
        SCHED;
        ldWh(t + 2, oW, 0);
        SCHED;
        bool bad = false;
        if (t <= NVAL) bad = chkbad();
        if (t == 1) {
          const unsigned fx = fl_line(FL_X + l, g), fc = fl_line(FL_CS, jr) + ((j + fq) & 3) * 128;
          flo = myrep + ((l0f && lane >= 16) ? fc : fx) + fr * 8;
          OPAQUE_V(flo);
        }
        if (t == 9) {
          pO = slowo ? p.ws : (const char*)(isb ? p.woB : p.woA);
          zO = slowo ? WS_SPAN : 1024 * 2048; bO = slowo ? WS_WO + (unsigned)((isb ? 16 : 0) + j) * 131072 : j * 128; pcO = slowo ? 4096 : 32 * 2048;
          vO = slowo ? vlin : (hw * 8 + (lane >> 3)) * 2048 + wo_swz(lane);
          clo = myrep + ((lane & 16) ? fl_line(FL_CLC + l, (lane & 32) ? j ^ RX : j) : fl_line(FL_CLR + l, g)) + fr * 8;
          OPAQUE_S(pO); OPAQUE_S(zO); OPAQUE_S(bO); OPAQUE_S(pcO); OPAQUE_V(vO); OPAQUE_V(clo);
        }
        if (t == 10 && l0f) csred();
        if (t == 11) {
#pragma unroll
          for (int i = 0; i < 4; ++i) OPAQUE_V(js[i]);
          const f32x4 s = (js[0] + js[1]) + (js[2] + js[3]);
          const auto r = SWAP16(s[0], s[2]), q = SWAP16(s[1], s[3]);
          rb[0] = __uint_as_float(r[0]) + __uint_as_float(r[1]); rb[1] = __uint_as_float(q[0]) + __uint_as_float(q[1]);
          OPAQUE_V(rb[0]); OPAQUE_V(rb[1]);
        }
        if (t == 12) {
          const auto r = SWAP32(rb[0], rb[0]), q = SWAP32(rb[1], rb[1]);
          const float m = (__uint_as_float(r[0]) + __uint_as_float(r[1])) * (-1.0f / 1024), q2 = __uint_as_float(q[0]) + __uint_as_float(q[1]);
          const float rstd = __builtin_amdgcn_rsqf(fmaxf(__builtin_fmaf(q2, 1.0f / 1024, -m * m), 0.f) + 1e-5f);
          lds_st<float>(lds0 + L_MEAN + 64 + hw * 128 + fr * 8 + (fq & 1) * 4 + (fq >> 1) * (L_RSTD - L_MEAN), (lane & 32) ? rstd : m);
        }
        SCHED;
        ldWh(t + 2, oW, 1);
        if (t == NVAL - 1 || t == 6) {
          WAITV(10);
          OPAQUE_V(fl);
          if (t == 6) ready = __all(is_set(fl, epoch)) ? ~0u : 0u;
          else {
            ready = l0f ? ~0u : (unsigned)__ballot(is_set(fl, epoch));
            if (slowj && __all(is_set(fl, epoch))) packedW();
          }
        }
        if (t == 12) { WAITV(10); OPAQUE_V(fl); }
        if (t == 13 && slowo && __builtin_expect(!__all(is_set(fl, epoch)), 0)) poll_all(rs, fwo, epoch, dead);
        if (t == NVAL - 2 || t == 5) ld8_asm(fl, rs, flo);
        if (t == 11) ld8_asm(fl, rs, fwo);
        if (t == 7 && l0f) csld();
        if (t == WT && l0f) {
#pragma unroll
          for (int i = 0; i < 2; ++i) ld16in_asm(wo2[i], mk_rsrc(p.woA, 1024 * 2048), (lane >> 3) * 2048 + wo_swz(lane), (g * 64 + (j & 3) * 16 + i * 8) * 2048 + (jr + hw) * 128);
        }
        if constexpr (t <= NVAL) { if (__builtin_expect(bad, 0)) refetch(t + 1); }
      });
      auto stO = [&](int i) { dma4<4096>(mk_rsrc(pO, zO), vO, bO + wo_src(i, j) * (4 * pcO), pcO, lds0 + wo_slot(i) + hw * 1024); };
      auto clcheck = [&]() {
        OPAQUE_V(fl);
        TRV(58, 0);
        if (__builtin_expect(!__all(is_set(fl, epoch)), 0)) { TRV(58, 1); poll_all(rs, clo, epoch, dead); }
      };
      auto accinit = [&]() {
#pragma unroll
        for (int m = 0; m < 2; ++m)
#pragma unroll
          for (int pp = 0; pp < 2; ++pp) {
            auto r0_ = __builtin_amdgcn_permlane16_swap(xr[m][pp][0], xr[m][pp][2], false, false);
            auto r1_ = __builtin_amdgcn_permlane16_swap(xr[m][pp][1], xr[m][pp][3], false, false);
            acc[m][2 * pp] = f32x4{lo16(r0_[0]), hi16(r0_[0]), lo16(r1_[0]), hi16(r1_[0])};
            acc[m][2 * pp + 1] = f32x4{lo16(r0_[1]), hi16(r0_[1]), lo16(r1_[1]), hi16(r1_[1])};
          }
#pragma unroll
        for (int i = 0; i < 8; ++i) OPAQUE_V(acc[i >> 2][i & 3]);
      };
      WAITV(10);
      WAITL0;
      TRC(15); BAR;
      if (lead) set_flags(rs, fl_line(FL_CLR + l, g), j, fl_line(FL_CLC + l, j), g, epoch, lane);
      if (isb) {
        stO(0); stO(2); stO(3);
        WAITV(12);
        TRC(16); BAR;
        stO(1); stO(4); stO(5);
        __builtin_amdgcn_s_setprio(0);
        TRC(17); BAR;
        __builtin_amdgcn_s_sleep(BD);
        stO(7); stO(6);
        ld8_asm(fl, rs, clo);
        accinit();
        WAITV(0);
        clcheck();
      } else {
        u32x4 vb[8];
#pragma unroll
        for (int i = 0; i < 8; ++i) asm volatile("" : "=v"(vb[i]));
        stO(0); stO(2); stO(3);
        ldres(14);
        WAITV(12);
        WAITL0;
        TRC(16); BAR;
        stO(1);
        ldres(15);
        if (l0f) {
          const bool wj = hw != 0;
          const char* bp = (const char*)(wj ? p.wqkvB : p.woB);
          unsigned rs_ = wj ? 6144 : 2048, sz = rs_ * 1024, ss = rs_ * 8, s2 = wj ? 16 * 6144 : 128;
          unsigned sb = g * 64 * rs_ + (wj ? (hw - 1) * 2048 + JOBC(j, hw - 1) * 128 : (j & 3) * (16 * 2048) + jr * 128);
          OPAQUE_S(bp); OPAQUE_S(rs_); OPAQUE_S(sz); OPAQUE_S(sb); OPAQUE_S(ss); OPAQUE_S(s2);
          const rsrc_t rb = mk_rsrc(bp, sz);
#pragma unroll
          for (int i = 0; i < 8; ++i) ld16in_asm(vb[i], rb, (lane >> 3) * rs_ + (lane & 7) * 16, sb + (i & 1) * ss + (i >> 1) * s2);
        }
        __builtin_amdgcn_s_setprio(0);
        KV_ADDR;
        bf16x8 sel = rowsum_sel(lane);
        unsigned h1 = (lane >> 3) * KVP_G + (lane & 7) * 16;
        unsigned pw = lds0 + SC_PHIK + (h * 128 + wm * 64 + 32 + fr) * 160 + fq * 8, kw = lds0 + L_PREV + hw * 2048 + lin;
        const unsigned wa = lds0 + L_WPHI + h * 2048 + (fq * 4 + (fr >> 2)) * 64 + (lane & 3) * 8;
        bf16x8 w0 = tr2(wa, wa + 1024), w1 = tr2(wa + 32, wa + 32 + 1024);
        float cq[4];
#pragma unroll
        for (int m = 0; m < 2; ++m) { cq[m] = lds_ld<float>(lds0 + L_RSTD + (16 + wm * 64 + 32 + m * 16 + fr) * 4) * 1.4426950408889634f; OPAQUE_V(cq[m]); }
        OPAQUE_V(sel); OPAQUE_V(h1); OPAQUE_V(pw); OPAQUE_V(kw); OPAQUE_V(w0); OPAQUE_V(w1);
        TRC(17); BAR;
        CLOBBER;
        {
          const f32x4 zero = {0.f, 0.f, 0.f, 0.f};
          f32x4 r[4][2];
#pragma unroll
          for (int m = 0; m < 2; ++m) {
            const bf16x8 f = lds_ld<bf16x8>(kw + m * 1024);
            r[m][0] = MFMA(w0, f, zero); r[m][1] = MFMA(w1, f, zero);
          }
          ld8_asm(fl, rs, clo);
          phik2(r, cq, pw);
        }
        WAITL0;
        TRC(19); BAR;
        kvpart(va, pa, pb, kvj + (unsigned)g * KVP_G, lane, wave);
        WAITV(2);
#pragma unroll
        for (int i = 0; i < 8; ++i) OPAQUE_V(vb[i]);
        clcheck();
        TRC(20); BAR;
        __builtin_amdgcn_s_setprio(3);
        stO(4); stO(5);
        const f32x4 zero = {0.f, 0.f, 0.f, 0.f};
        const unsigned s1 = kvj + (unsigned)((j ^ RX) - j) * KVP_J + g * 512 + hw * 128;
        __builtin_amdgcn_s_sleep(H1S);
        u32x4 v[2] = {ld16_sc1(rs, h1, s1), ld16_sc1(rs, h1, s1 + 8 * KVP_G)};
        TRC(21); TRV(50, 0);
        for (unsigned spins = 0; !dead && __any(umax4v(umax4v(0u, v[0]), v[1]) == SENT); ++spins) {
          if (spins > SPIN_CAP) { dead = true; break; }
          CLOBBER;
          TRV(50, spins + 1);
          v[0] = ld16_sc1(rs, h1, s1); v[1] = ld16_sc1(rs, h1, s1 + 8 * KVP_G);
        }
        TRC(22);
        const f32x4 c = rowsum_fin(MFMA(sel, as_frag(v[1]), MFMA(sel, as_frag(v[0]), zero)));
        if ((lane & 40) == 0)
          st8_sc1(rs, fr * 16 + fq * 8, kvj + (unsigned)g * KVP_G + KVP_SUM + hw * 128, nosent2(c));
        CLOBBER;
        __builtin_amdgcn_s_setprio(0);
        TRC(43); BAR;
        SCHED;
        accinit();
        stO(6); stO(7);
        if (l0f) {
          if (hw) {
            job_colsum(rs, vb, 1, g, JOBC(j, hw - 1), hw - 1, lane);
            if (SLOWC(1, JOBC(j, hw - 1))) {
#pragma unroll
              for (int i = 0; i < 8; ++i) job_pack(rs, vb[i], i, 1, g, JOBC(j, hw - 1), hw - 1, lane);
            }
          } else {
#pragma unroll
            for (int i = 0; i < 8; ++i)
              if (SLOWC(3, jr + (i >> 1))) st16_sc1(rs, (lane >> 3) * 128 + wo_swz(lane), WS_WO + (unsigned)((16 + jr + (i >> 1)) * 131072 + g * 8192 + ((j & 3) * 2 + (i & 1)) * 1024), vb[i]);
          }
        }
        WAITV(0);
      }
    }
    {
      LOCALS;
      const int fr = lane & 15, fq = lane >> 4;
      const int kh = half;
      unsigned v0 = lane * 16 + hw * 4096 + kh * 2048, v1 = lane * 16 + hw * 4096 + (kh ^ 1) * 2048;
      const int r0 = (JROT(j) >> 1) + NJ / 2;
      const unsigned ob0 = ob_off(g, kh, l);
      const unsigned pko = myrep + fl_line(FL_PK, JOBC(j, fq - 1)) + fr * 8;
      u32x4 R[8][4];
      u32x2 pkf;
      auto issue_ = [&](int i) {
        const unsigned so = ob0 + (unsigned)((r0 + i) & 7) * (2 * TS);
        R[i][0] = ld16_sc1(rs, v0, so); R[i][1] = ld16_sc1(rs, v1, so);
        R[i][2] = ld16_sc1(rs, v0 + 1024, so); R[i][3] = ld16_sc1(rs, v1 + 1024, so);
      };
      auto issue = [&](int i) { issue_(i); SCHED; };
      constexpr int LA = LOOKAHEAD;
      TRV(52, 0); TRV(53, 0); TRV(54, 0);
      TRR(61);
      __builtin_amdgcn_s_setprio(3);
      unsigned so0 = ob0 + (unsigned)(r0 & 7) * (2 * TS);
      OPAQUE_V(v0); OPAQUE_V(v1); OPAQUE_S(so0);
      TRC(25); BAR;
      SCHED;
      R[0][0] = ld16_sc1(rs, v0, so0); R[0][1] = ld16_sc1(rs, v1, so0);
      R[0][2] = ld16_sc1(rs, v0 + 1024, so0); R[0][3] = ld16_sc1(rs, v1 + 1024, so0);
      SCHED;
      __builtin_amdgcn_s_setprio(2);
      if (l == 0 && lead) set_flags(rs, fl_line(FL_PK, j), g, fl_line(FL_WO + 1, j), g, epoch, lane);

      auto vmax = [&](int i) { return umax4v(umax4v(umax4v(umax4v(0u, R[i][0]), R[i][1]), R[i][2]), R[i][3]); };
      auto bad = [&](int i) { return __any(umax4v(umax4v(umax4v(umax4v(0u, R[i][0]), R[i][1]), R[i][2]), R[i][3]) == SENT); };
      auto poll = [&](int i, int nfl) {
        for (unsigned spins = 0; !dead; ++spins) {
          if (spins > SPIN_CAP) { dead = true; break; }
          __builtin_amdgcn_s_sleep(2);
          CLOBBER;
          TRV(52 + (i > 2 ? 2 : i), spins + 1);
          issue(i);
#pragma unroll
          for (int k = i + 1; k < i + nfl && k < 8; ++k)
            if (bad(k)) issue(k);
          WAITV(0);
#pragma unroll
          for (int k = i + 1; k < i + nfl && k < 8; ++k)
#pragma unroll
            for (int f = 0; f < 4; ++f) OPAQUE_V(R[k][f]);
          if (!bad(i)) break;
        }
      };
      unsigned bb[4];
      {
        const int kr = fq * 4 + (fr >> 2);
        const int gs = (kr >> 1) & 3;
#pragma unroll
        for (int c = 0; c < 4; ++c) bb[c] = lds0 + kh * 8192 + kr * 128 + ((c ^ gs) * 32) + (lane & 3) * 8;
      }
      bf16x8 bf[2][4];
      auto rdB = [&](int i, int hh) {
        if (hh == 0) {
#pragma unroll
          for (int c = 0; c < 4; ++c) { bb[c] += wo_slot(i) - (i ? wo_slot(i - 1) : 0u); asm("" : "+v"(bb[c])); }
        }
#pragma unroll
        for (int c = 0; c < 4; ++c) bf[hh][c] = tr2(bb[c] + hh * 4096, bb[c] + hh * 4096 + 2048);
      };
      auto mms = [&](int i, int hh) {
#pragma unroll
        for (int n = 0; n < 4; ++n)
#pragma unroll
          for (int m = 0; m < 2; ++m) acc[m][n] = MFMA(bf[hh][n], as_frag(R[i][hh * 2 + m]), acc[m][n]);
      };
      if (!half) {
#pragma unroll
        for (int i = 0; i < 8; ++i) acc[i >> 2][i & 3] = f32x4{0.f, 0.f, 0.f, 0.f};
      }
      const char* obase = (l == 3) ? (const char*)p.out : (const char*)p.ws;
      unsigned osize = (l == 3) ? 2048 * 2048 : WS_SPAN;
      const bool slown = SLOWC((l & 1) ^ 1, j);
      pW = slown ? p.ws : (const char*)(isb ? p.wqkvA : p.wqkvB);
      bW = slown ? WS_WP + (unsigned)((isb ? 0 : 16) + j) * (16 * 24576) : j * 128;
      kW = slown ? 24576 : 64 * 6144; zW = slown ? WS_SPAN : 1024 * 6144; pcW = slown ? 8192 : 2048; hW = slown ? 4096 : 32 * 6144;
      vA = hw * 1024 + lane * 16;
      vW = slown ? vA : (hw * 8 + (lane >> 3)) * 6144 + (((lane & 7) ^ ((((lane >> 4) & 1) | ((hw & 1) << 1)) << 1)) * 16);
      pA = p.ws; zA = WS_SPAN; bA = xb_off(g, l); kA = TS; pcA = 4096; kb = JROT(j) + KX; dj = (j - kb) & 15;
      OPAQUE_S(pW); OPAQUE_S(bW); OPAQUE_S(kW); OPAQUE_S(zW); OPAQUE_S(pcW); OPAQUE_S(hW); OPAQUE_V(vA); OPAQUE_V(vW); OPAQUE_S(bA);
      unsigned sq = bW + j * kW + kh * hW, dst = lds0 + L_R1 + kh * 4096 + hw * 1024;
      OPAQUE_S(obase); OPAQUE_S(osize); OPAQUE_S(sq); OPAQUE_S(dst);
      const rsrc_t ro = mk_rsrc(obase, osize);
      unsigned so[2];
      unsigned xw = lds0 + SC_XCH + wave * 4096 + lane * 16;
      unsigned xa = lds0 + SC_XCH + (wave ^ 4) * 4096 + lane * 16;
      unsigned sb0 = (l == 3) ? g * (128 * 2048) + j * 128 : xb_off(g, l) + j * TS;
      {
        const int row = (hw * 2 + kh) * 16 + fr, ch = (fq & 1) * 2 + (fq >> 1);
        const unsigned sw = (l == 3) ? 0u : (unsigned)((row >> 1) & 7), rb = (unsigned)row << ((l == 3) ? 11 : 7);
#pragma unroll
        for (int pp = 0; pp < 2; ++pp) { so[pp] = rb + (((ch + 4 * pp) ^ sw) * 16); OPAQUE_V(so[pp]); }
        OPAQUE_V(xa); OPAQUE_V(xw); OPAQUE_S(sb0);
      }
      SCHED;
#pragma unroll
      for (int i = 1; i < LA; ++i) issue(i);
      rdB(0, 0);
      unsigned mx = vmax(0);
#pragma unroll
      for (int i = 0; i < 8; ++i) {
        SCHED;
        if (__builtin_expect(__any(mx == SENT), 0)) poll(i, LA);
        if (i == 0) TRC(27); else TRQ(27 + i);
        if (i == W1S - 2) { pkf = ld8_sc1(rs, pko, 0); SCHED; }
        if (i == W1S) {
          OPAQUE_V(pkf);
          if (l == 0 && lead && !__all(is_set(pkf, epoch))) poll_all(rs, pko, epoch, dead);
          TRC(35); BAR;
        }
        if (i == 7 && l < 3) dma3<8192>(mk_rsrc(pW, zW), vW, sq, pcW, dst);
        if (i < 4 && !isb) __builtin_amdgcn_s_sleep(PACE);
        if (i == 2) __builtin_amdgcn_s_setprio(1);
        if (i == 5) __builtin_amdgcn_s_setprio(0);
        SCHED;
        rdB(i, 1);
        if (i == 7) break;
        if (i + LA < 8) issue_(i + LA);
        mms(i, 0);
#pragma unroll
        for (int k = 0; k < 4; ++k) { SGB(0x008, 1); SGB(0x100, 1); SGB(0x020, 1); SGB(0x008, 1); SGB(0x100, 1); }
        SCHED;
        rdB(i + 1, 0); mx = vmax(i + 1);
        mms(i, 1);
#pragma unroll
        for (int k = 0; k < 4; ++k) { SGB(0x008, 1); SGB(0x100, 2); }
#pragma unroll
        for (int k = 4; k < 8; ++k) { SGB(0x008, 1); SGB(0x002, 8 / (8 - 4)); }
      }
#pragma unroll
      for (int n = 0; n < 4; ++n) acc[1][n] = MFMA(bf[0][n], as_frag(R[7][1]), acc[1][n]);
#pragma unroll
      for (int n = 0; n < 4; ++n) acc[0][n] = MFMA(bf[0][n], as_frag(R[7][0]), acc[0][n]);
#pragma unroll
      for (int k = 0; k < 8; ++k) { SGB(0x008, 1); SGB(0x100, 1); }
      SCHED;
#pragma unroll
      for (int n = 0; n < 4; ++n) acc[1][n] = MFMA(bf[1][n], as_frag(R[7][3]), acc[1][n]);
#pragma unroll
      for (int n = 0; n < 4; ++n) acc[0][n] = MFMA(bf[1][n], as_frag(R[7][2]), acc[0][n]);
#pragma unroll
      for (int n = 0; n < 4; ++n) lds_st<f32x4>(xw + n * 1024, acc[1][n]);
      SGB(0x008, 4);
#pragma unroll
      for (int k = 0; k < 4; ++k) { SGB(0x008, 1); SGB(0x200, 1); }
      SCHED;
      TRC(36);
      WAITL0;
      TRC(37); BAR;
      SCHED;
      f32x4 pa[4];
#pragma unroll
      for (int n = 0; n < 4; ++n) pa[n] = lds_ld<f32x4>(xa + n * 1024);
      SCHED;
      u32x4 o[2];
#pragma unroll
      for (int pp = 0; pp < 2; ++pp) {
        const f32x4 a = acc[0][2 * pp] + pa[2 * pp], b = acc[0][2 * pp + 1] + pa[2 * pp + 1];
        auto r0_ = __builtin_amdgcn_permlane16_swap(pk2(a[0], a[1]), pk2(b[0], b[1]), false, false);
        auto r1_ = __builtin_amdgcn_permlane16_swap(pk2(a[2], a[3]), pk2(b[2], b[3]), false, false);
        o[pp] = nosent(u32x4{r0_[0], r1_[0], r0_[1], r1_[1]});
        st16_sc1(ro, so[pp], sb0, o[pp]);
      }
      if (l < 3) {
        lds_st<u32x4>(lds0 + L_R2 + so[0], o[0]);
        lds_st<u32x4>(lds0 + L_R2 + so[1], o[1]);
        const f32x4 zero = {0.f, 0.f, 0.f, 0.f};
        const bf16x8 ones = {0x3f80, 0x3f80, 0x3f80, 0x3f80, 0x3f80, 0x3f80, 0x3f80, 0x3f80};
        const f32x4 a = MFMA(ones, as_frag(o[1]), MFMA(ones, as_frag(o[0]), zero));
        const f32x4 b = MFMA(as_frag(o[1]), as_frag(o[1]), MFMA(as_frag(o[0]), as_frag(o[0]), zero));
        const float q = (fr & 2) ? ((fr & 1) ? b[3] : b[2]) : ((fr & 1) ? b[1] : b[0]);
        if (fq == (fr >> 2)) st8_sc1(rs, ((hw * 2 + kh) * 16 + fr) * 8, st_off(g, l + 1) + j * 1024, u32x2{__float_as_uint(a[0]), __float_as_uint(q)});
      } else if (lead && blockIdx.x == 0 && lane == 0) {
        st8_sc1(rs, WS_EPOCH, 0, epoch);
      }
    }
    TRC(38); TRR(63);
  }
  STAMP_DUMP;
  asm volatile(".Lpf_end:");
}

extern "C" void kernel_launch(void* const* d_in, const int* in_sizes, int n_in, void* d_out, int out_size,
                              void* d_ws, size_t ws_size, hipStream_t stream) {
  Params p;
  p.x = (const u16*)d_in[0];
  p.wqkvA = (const u16*)d_in[1];
  p.wphi = (const u16*)d_in[2];
  p.woA = (const u16*)d_in[3];
  p.wqkvB = (const u16*)d_in[5];
  p.woB = (const u16*)d_in[6];
  p.out = (u16*)d_out;
  p.ws = (char*)d_ws;
  (void)hipFuncSetAttribute((const void*)mega_kernel, hipFuncAttributeMaxDynamicSharedMemorySize, L_TOTAL);
  mega_kernel<<<256, 512, L_TOTAL, stream>>>(p);
}
```

```cpp
#include <hip/hip_runtime.h>
#include <stdint.h>
#include <type_traits>
constexpr int LOOKAHEAD = 2;
constexpr int NVAL = 4;
constexpr int WT = 1;
constexpr int W1S = 6;
constexpr int NJ = 8;
constexpr int KX = 8;
constexpr int RX = 8;
constexpr unsigned TS = 17664;
#define XD 3
#define H1S 6
#define H2D 5
#define BD 8
#define PACE 4

typedef unsigned short u16;
typedef unsigned long long u64;
typedef __attribute__((ext_vector_type(8))) short bf16x8;
typedef __attribute__((ext_vector_type(4))) short s16x4;
typedef __attribute__((ext_vector_type(4))) float f32x4;
typedef __attribute__((ext_vector_type(2))) float f32x2;
typedef __attribute__((ext_vector_type(4))) unsigned u32x4;
typedef __attribute__((ext_vector_type(2))) unsigned u32x2;
typedef __attribute__((ext_vector_type(2))) __bf16 bf16x2_t;
#define LAS __attribute__((address_space(3)))
typedef LAS s16x4* lds_s16x4_ptr;

constexpr unsigned NREP = 8, REP_STRIDE = 17 * 2048;
enum { FL_X = 0, FL_CS = 4, FL_CLR = 5, FL_CLC = 9, FL_PK = 13, FL_WO = 14 };
__device__ __forceinline__ unsigned fl_line(int set, int a) { return (unsigned)(set * 16 + a) * 128; }
constexpr unsigned WS_EPOCH = 512u << 10;
constexpr unsigned WS_CS = 1u << 20;
constexpr unsigned WS_KVP = 4u << 20;
constexpr unsigned KVP_G = 32768, KVP_J = 16 * KVP_G, KVP_L = 16384, KVP_Z = 8192, KVP_SUM = 8704, KVP_HALO = 12u << 10;
constexpr unsigned SENT = 0xffffffffu;
constexpr unsigned WS_X = 14u << 20, WS_O = 29u << 20, WS_ST = 2u << 20;
__device__ __forceinline__ unsigned xb_off(int g, int l) { return WS_X + (unsigned)(l * 16 + g) * (16 * TS); }
__device__ __forceinline__ unsigned ob_off(int g, int j, int l) { return WS_O + (unsigned)((l * 16 + g) * 16 + j) * TS; }
__device__ __forceinline__ unsigned st_off(int g, int l) { return WS_ST + (unsigned)(l * 16 + g) * (16u << 10); }
constexpr unsigned WS_WP = 48u << 20;
constexpr unsigned WS_WO = 60u << 20;
constexpr unsigned WS_SPAN = 64u << 20;

constexpr unsigned L_R1 = 0;
constexpr unsigned L_R2 = 98304, XS = 16384;
constexpr unsigned L_MISC = 147456;
constexpr unsigned L_MEAN = L_MISC;
constexpr unsigned L_RSTD = L_MISC + 576;
constexpr unsigned L_CS = L_MISC + 1152;
constexpr unsigned L_WPHI = L_MISC + 2688;
constexpr unsigned L_SINK = L_MISC + 6912;
constexpr unsigned L_ZF = L_MISC + 7168;
constexpr unsigned L_PREV = L_MISC + 8192;
constexpr unsigned L_TOTAL = 163840;

#define JROT(j) ((j) & ~(NJ - 1))
constexpr int NX = NJ / 2;
#define KB0(g, j) (JROT(j) + (16 / NX) * ((g) & (NX - 1)))
#define JOBC(j, w) (((j) & ~3) + (((j) + (w) + 1) & 3))

#define SLOWC(m, jc) ((slowm >> ((m) * 8 + ((jc) & 7))) & 1)

struct Params {
  const u16* x;
  const u16* wqkvA;
  const u16* wphi;
  const u16* woA;
  const u16* wqkvB;
  const u16* woB;
  u16* out;
  char* ws;
};

#define SCHED __builtin_amdgcn_sched_barrier(0)
#define BAR __builtin_amdgcn_s_barrier()
#define SGB(m, n) __builtin_amdgcn_sched_group_barrier(m, n, 0)
#define WAITV(n) asm volatile("s_waitcnt vmcnt(%0)" ::"n"(n) : "memory")
#define WAITL0 asm volatile("s_waitcnt lgkmcnt(0)" ::: "memory")

__device__ __forceinline__ unsigned pk2(float a, float b) {
  f32x2 v = {a, b};
  return __builtin_bit_cast(unsigned, __builtin_convertvector(v, bf16x2_t));
}
__device__ __forceinline__ float lo16(unsigned u) { return __uint_as_float(u << 16); }
__device__ __forceinline__ float hi16(unsigned u) { return __uint_as_float(u & 0xffff0000u); }

template <int A, int B, typename F> __device__ __forceinline__ void static_for(F f) {
  if constexpr (A < B) { f(std::integral_constant<int, A>{}); static_for<A + 1, B>(f); }
}
typedef __amdgpu_buffer_rsrc_t rsrc_t;
__device__ __forceinline__ rsrc_t mk_rsrc(const void* p, unsigned bytes) { return __builtin_amdgcn_make_buffer_rsrc((void*)p, 0, bytes, 0x00020000); }
#define UNI(x) __builtin_amdgcn_readfirstlane(x)
#define DMA_HEAD "s_mov_b32 m0, %4\n\ts_mov_b32 %0, %3\n\ts_nop 2\n\tbuffer_load_dwordx4 %1, %2, %0 offen sc1 lds\n\t"
#define DMA_NEXT "s_add_u32 m0, m0, %6\n\ts_add_u32 %0, %0, %5\n\tbuffer_load_dwordx4 %1, %2, %0 offen sc1 lds\n\t"
#define DMA_OPS : "=&s"(t) : "v"(voff), "s"(r), "s"(UNI(soff)), "s"(UNI(lds)), "s"(UNI(sstep)), "i"(LSTEP) : "memory", "scc", "m0"
template <int LSTEP> __device__ __forceinline__ void dma4(rsrc_t r, unsigned voff, unsigned soff, unsigned sstep, unsigned lds) {
  unsigned t;
  asm volatile(DMA_HEAD DMA_NEXT DMA_NEXT DMA_NEXT DMA_OPS);
}
template <int LSTEP> __device__ __forceinline__ void dma3(rsrc_t r, unsigned voff, unsigned soff, unsigned sstep, unsigned lds) {
  unsigned t;
  asm volatile(DMA_HEAD DMA_NEXT DMA_NEXT DMA_OPS);
}
template <int LSTEP, int N> __device__ __forceinline__ void bar_dma4(rsrc_t r, unsigned voff, unsigned soff, unsigned sstep, unsigned lds) {
  unsigned t;
  asm volatile("s_mov_b32 m0, %4\n\ts_mov_b32 %0, %3\n\ts_nop 0\n\ts_waitcnt vmcnt(%7) lgkmcnt(0)\n\ts_barrier\n\t"
               "buffer_load_dwordx4 %1, %2, %0 offen sc1 lds\n\t" DMA_NEXT DMA_NEXT DMA_NEXT
               : "=&s"(t) : "v"(voff), "s"(r), "s"(UNI(soff)), "s"(UNI(lds)), "s"(UNI(sstep)), "i"(LSTEP), "n"(N) : "memory", "scc", "m0");
}
__device__ __forceinline__ void dma1(rsrc_t r, unsigned voff, unsigned soff, unsigned lds) {
  asm volatile("s_mov_b32 m0, %3\n\ts_nop 3\n\tbuffer_load_dwordx4 %0, %1, %2 offen sc1 lds" ::"v"(voff), "s"(r), "s"(UNI(soff)), "s"(UNI(lds)) : "memory", "m0");
}
__device__ __forceinline__ void touch(rsrc_t r, unsigned voff, unsigned lds) {
  asm volatile("s_mov_b32 m0, %2\n\ts_nop 3\n\tbuffer_load_dword %0, %1, 0 offen lds" ::"v"(voff), "s"(r), "s"(UNI(lds)) : "memory", "m0");
}
__device__ __forceinline__ void ld8_asm(u32x2& d, rsrc_t r, unsigned voff) {
  asm volatile("s_nop 4\n\tbuffer_load_dwordx2 %0, %1, %2, 0 offen sc1" : "=v"(d) : "v"(voff), "s"(r) : "memory");
}

__device__ __forceinline__ void ld16in_asm(u32x4& d, rsrc_t r, unsigned voff, unsigned soff) {
  asm volatile("s_nop 4\n\tbuffer_load_dwordx4 %0, %1, %2, %3 offen" : "=v"(d) : "v"(voff), "s"(r), "s"(UNI(soff)) : "memory");
}
__device__ __forceinline__ void ld16_asm(f32x4& d, rsrc_t r, unsigned voff, unsigned soff) {
  asm volatile("s_nop 4\n\tbuffer_load_dwordx4 %0, %1, %2, %3 offen sc1" : "=v"(d) : "v"(voff), "s"(r), "s"(UNI(soff)) : "memory");
}

template <typename T> __device__ __forceinline__ T lds_ld(unsigned a) { return *(LAS T*)(a); }
template <typename T> __device__ __forceinline__ void lds_st(unsigned a, T v) { *(LAS T*)(a) = v; }
__device__ __forceinline__ bf16x8 tr2(unsigned a0, unsigned a1) {
  s16x4 v0 = __builtin_amdgcn_ds_read_tr16_b64_v4i16((lds_s16x4_ptr)(a0));
  s16x4 v1 = __builtin_amdgcn_ds_read_tr16_b64_v4i16((lds_s16x4_ptr)(a1));
  return __builtin_shufflevector(v0, v1, 0, 1, 2, 3, 4, 5, 6, 7);
}
__device__ __forceinline__ bf16x8 as_frag(u32x4 v) { return __builtin_bit_cast(bf16x8, v); }
#define MFMA(a, b, c) __builtin_amdgcn_mfma_f32_16x16x32_bf16(a, b, c, 0, 0, 0)

__device__ __forceinline__ float vmax2(float a, float b) { float inf = __builtin_inff(); asm("" : "+s"(inf)); return __builtin_amdgcn_fmed3f(a, b, inf); }
#define SWAP16(a, b) __builtin_amdgcn_permlane16_swap(__float_as_uint(a), __float_as_uint(b), false, false)
#define SWAP32(a, b) __builtin_amdgcn_permlane32_swap(__float_as_uint(a), __float_as_uint(b), false, false)
__device__ __forceinline__ void red2_max(float& a, float& b) {
  auto r = SWAP16(a, b);
  float x = vmax2(__uint_as_float(r[0]), __uint_as_float(r[1]));
  r = SWAP32(x, x);
  x = vmax2(__uint_as_float(r[0]), __uint_as_float(r[1]));
  r = SWAP16(x, x);
  a = __uint_as_float(r[0]); b = __uint_as_float(r[1]);
}
__device__ __forceinline__ void red2_add(float& a, float& b) {
  auto r = SWAP16(a, b);
  float x = __uint_as_float(r[0]) + __uint_as_float(r[1]);
  r = SWAP32(x, x);
  x = __uint_as_float(r[0]) + __uint_as_float(r[1]);
  r = SWAP16(x, x);
  a = __uint_as_float(r[0]); b = __uint_as_float(r[1]);
}
template <bool MAX, typename M> __device__ __forceinline__ void red4(float& a, float& b, float& c, float& d, M mid) {
  auto op = [](unsigned x, unsigned y) { return MAX ? vmax2(__uint_as_float(x), __uint_as_float(y)) : __uint_as_float(x) + __uint_as_float(y); };
  auto r = SWAP16(a, b), q = SWAP16(c, d);
  const float x = op(r[0], r[1]), y = op(q[0], q[1]);
  r = SWAP32(x, y);
  const float w = mid(op(r[0], r[1]));
  r = SWAP32(w, w);
  q = __builtin_amdgcn_permlane16_swap(r[1], r[1], false, false);
  r = __builtin_amdgcn_permlane16_swap(r[0], r[0], false, false);
  a = __uint_as_float(r[0]); b = __uint_as_float(r[1]); c = __uint_as_float(q[0]); d = __uint_as_float(q[1]);
}
__device__ __forceinline__ float red_max4(float x) {
  auto r = __builtin_amdgcn_permlane16_swap(__float_as_uint(x), __float_as_uint(x), false, false);
  x = vmax2(__uint_as_float(r[0]), __uint_as_float(r[1]));
  r = __builtin_amdgcn_permlane32_swap(__float_as_uint(x), __float_as_uint(x), false, false);
  return vmax2(__uint_as_float(r[0]), __uint_as_float(r[1]));
}
__device__ __forceinline__ float red_add4(float x) {
  auto r = __builtin_amdgcn_permlane16_swap(__float_as_uint(x), __float_as_uint(x), false, false);
  x = __uint_as_float(r[0]) + __uint_as_float(r[1]);
  r = __builtin_amdgcn_permlane32_swap(__float_as_uint(x), __float_as_uint(x), false, false);
  return __uint_as_float(r[0]) + __uint_as_float(r[1]);
}

__device__ __forceinline__ void st16_sc1(rsrc_t r, unsigned voff, unsigned soff, u32x4 v) {
  __builtin_amdgcn_raw_buffer_store_b128(v, r, voff, soff, 16);
  asm volatile("s_nop 1" ::"v"(v) : "memory");
}
__device__ __forceinline__ void st8_sc1(rsrc_t r, unsigned voff, unsigned soff, u32x2 v) { __builtin_amdgcn_raw_buffer_store_b64(v, r, voff, soff, 16); }
__device__ __forceinline__ u32x4 ld16_sc1(rsrc_t r, unsigned voff, unsigned soff) { return __builtin_amdgcn_raw_buffer_load_b128(r, voff, soff, 16); }
__device__ __forceinline__ u32x2 ld8_sc1(rsrc_t r, unsigned voff, unsigned soff) { return __builtin_amdgcn_raw_buffer_load_b64(r, voff, soff, 16); }
__device__ __forceinline__ f32x4 ld4f_sc1(rsrc_t r, unsigned voff, unsigned soff) { return __builtin_bit_cast(f32x4, __builtin_amdgcn_raw_buffer_load_b128(r, voff, soff, 16)); }
#define OPAQUE_V(x) asm volatile("" : "+v"(x))
#define OPAQUE_S(x) asm volatile("" : "+s"(x))

#define CLOBBER asm volatile("" ::: "memory")
constexpr unsigned SPIN_CAP = 1u << 17;
__device__ __forceinline__ bool is_set(u32x2 v, u32x2 epoch) { return v[0] == epoch[0] && v[1] == epoch[1]; }
__device__ __forceinline__ void set_flags(rsrc_t rs, unsigned lineA, int colA, unsigned lineB, int colB, u32x2 epoch, int lane) {
  const unsigned off = (lane & 7) * REP_STRIDE + ((lane & 8) ? lineB + colB * 8 : lineA + colA * 8);
  if (lane < 16) st8_sc1(rs, off, 0, epoch);
}
__device__ __forceinline__ unsigned poll_need(rsrc_t rs, unsigned voff, u32x2 epoch, unsigned ready, unsigned need, bool& dead) {
  for (unsigned spins = 0; !dead; ++spins) {
    CLOBBER;
    ready |= (unsigned)__ballot(is_set(ld8_sc1(rs, voff, 0), epoch));
    if ((ready & need) == need) break;
    if (spins > SPIN_CAP) dead = true;
    __builtin_amdgcn_s_sleep(1);
  }
  CLOBBER;
  return dead ? ~0u : ready;
}

__device__ __forceinline__ void poll_all(rsrc_t rs, unsigned voff, u32x2 epoch, bool& dead) {
  for (unsigned spins = 0; !dead; ++spins) {
    CLOBBER;
    if (__all(is_set(ld8_sc1(rs, voff, 0), epoch))) break;
    if (spins > SPIN_CAP) dead = true;
    __builtin_amdgcn_s_sleep(1);
  }
  CLOBBER;
}

constexpr unsigned SC_VF = L_R1 + 32768;
constexpr unsigned SC_PHIK = L_R1 + 49152;
constexpr unsigned SC_XCH = L_R2 + XS;
constexpr unsigned wo_slot(int s) {
  return s < 2 ? L_R2 + (unsigned)(s + 1) * XS : (s < 6 ? L_R1 + (unsigned)(s - 2) * 16384 : (s == 6 ? L_R1 + 81920 : L_R2));
}
__device__ __forceinline__ unsigned wo_src(int s, int j) { return (unsigned)(((JROT(j) >> 1) + NJ / 2 + s) & 7); }
__device__ __forceinline__ unsigned wo_swz(int lane) { return (unsigned)(((lane & 7) ^ ((lane >> 3) & 6)) * 16); }
__device__ __forceinline__ unsigned umax3(unsigned a, unsigned b, unsigned c) { return max(max(a, b), c); }
__device__ __forceinline__ unsigned umax4v(unsigned m, const u32x4& v) { return umax3(umax3(m, v[0], v[1]), v[2], v[3]); }
__device__ __forceinline__ u32x4 nosent(u32x4 v) { return u32x4{min(v[0], SENT - 1), min(v[1], SENT - 1), min(v[2], SENT - 1), min(v[3], SENT - 1)}; }

template <typename P, typename F>
__device__ __forceinline__ void qkv_loop(int isb, int wm, int h, int lane, bool first, P pre, F fin) {
  constexpr unsigned lds0 = 0;
  f32x4 acc[4][6];
  const int fr = lane & 15, fq = lane >> 4;
  unsigned ab[2], bb[2];
  {
    const int r = wm * 64 + fr;
#pragma unroll
    for (int ks = 0; ks < 2; ++ks) ab[ks] = lds0 + L_R2 + r * 128 + (((ks * 4 + fq) ^ ((fr >> 1) & 7)) * 16);
    const int kr = fq * 8 + (fr >> 2);
    const int gs = ((kr >> 1) & 1) | (((kr >> 3) & 1) << 1);
#pragma unroll
    for (int c = 0; c < 2; ++c) bb[c] = lds0 + L_R1 + kr * 128 + (((h * 2 + c) ^ gs) * 32) + (lane & 3) * 8;
  }
#pragma unroll
  for (int m = 0; m < 4; ++m)
#pragma unroll
    for (int n = 0; n < 6; ++n) acc[m][n] = f32x4{0.f, 0.f, 0.f, 0.f};
  bf16x8 af[2][4], bf[2][6];
  auto rds = [&](int ks, int p, int rot = 0) {
#pragma unroll
    for (int m = 0; m < 4; ++m) af[p][m] = lds_ld<bf16x8>(ab[ks] + m * 2048);
#pragma unroll
    for (int nn = 0; nn < 6; ++nn) {
      const int n = (nn + rot) % 6;
      const unsigned a = bb[n & 1] + (n >> 1) * 8192 + ks * 4096;
      bf[p][n] = tr2(a, a + 512);
    }
  };
  auto mms = [&](int p, int rot = 0, int cnt = 6) {
#pragma unroll
    for (int nn = 0; nn < cnt; ++nn)
#pragma unroll
      for (int mm = 0; mm < 4; ++mm) { const int n = (nn + rot) % 6, m = (n & 1) ? 3 - mm : mm; acc[m][n] = n < 4 ? MFMA(bf[p][n], af[p][m], acc[m][n]) : MFMA(af[p][m], bf[p][n], acc[m][n]); }
  };
  auto ilv2 = [&]() {
#pragma unroll
    for (int i = 0; i < 16; ++i) { SGB(0x008, 1); SGB(0x100, 1); }
#pragma unroll
    for (int i = 0; i < 4; ++i) { SGB(0x008, 1); SGB(0x002, 1); }
    SGB(0x008, 4);
  };
  auto ilv3 = [&]() {
#pragma unroll
    for (int i = 0; i < 16; ++i) { SGB(0x008, 1); SGB(0x100, 1); }
#pragma unroll
    for (int i = 0; i < 7; ++i) { SGB(0x008, 1); SGB(0x004, 2); }
    SGB(0x008, 1);
  };
#pragma unroll
  for (int i = 0; i < 24; ++i) OPAQUE_V(acc[i / 6][i % 6]);
  OPAQUE_V(ab[0]); OPAQUE_V(ab[1]); OPAQUE_V(bb[0]); OPAQUE_V(bb[1]);
  if (!first) { WAITV(3); WAITL0; }
  BAR;
  rds(0, 0);
  int sa = 0, sb = 0;
  int da = XS, db = 24576;
  auto step = [&]() {
    ab[0] += da; ab[1] += da;
    bb[0] += db; bb[1] += db;
  };
  auto ring = [&]() {
    sa = (sa == 2) ? 0 : sa + 1;
    sb = (sb == 3) ? 0 : sb + 1;
    da = (sa == 2) ? -2 * (int)XS : (int)XS;
    db = (sb == 3) ? -3 * 24576 : 24576;
  };
#pragma unroll 1
  for (int t = 14; t > 0; --t) {
    SCHED;
    rds(1, 1); mms(0);
    step();
    ilv2();
    SCHED;
    __builtin_amdgcn_s_waitcnt(0x0070);
    BAR;
    SCHED;
    rds(0, 0);
    ring();
    mms(1); ilv3();
  }
  SCHED;
  f32x4 cs[4], nm4[4];
  float nmean[4], csv[2];
  auto fold = [&](int n) {
#pragma unroll
    for (int mm = 0; mm < 4; ++mm) {
      const int m = (n & 1) ? 3 - mm : mm;
#pragma unroll
      for (int e = 0; e < 4; ++e) {
        float t = n < 4 ? __builtin_fmaf(nmean[m], cs[n & 3][e], acc[m][n][e]) : __builtin_fmaf(nm4[m][e], csv[n & 1], acc[m][n][e]);
        asm("" : "+v"(t));
        acc[m][n][e] = t;
      }
    }
  };
  {
    unsigned pm = lds0 + L_MEAN + (16 + wm * 64 + fr) * 4, pc = lds0 + L_CS + isb * 768 + (h * 32 + fq * 4) * 4;
    unsigned pm4 = lds0 + L_MEAN + (16 + wm * 64 + fq * 4) * 4, pcv = lds0 + L_CS + isb * 768 + (128 + h * 32 + fr) * 4;
    asm("" : "+v"(pm)); asm("" : "+v"(pc)); asm("" : "+v"(pm4)); asm("" : "+v"(pcv));
#pragma unroll
    for (int m = 0; m < 4; ++m) nmean[m] = lds_ld<float>(pm + m * 64);
#pragma unroll
    for (int n = 0; n < 4; ++n) cs[n ^ 2] = lds_ld<f32x4>(pc + (((n ^ 2) >> 1) * 64 + (n & 1) * 16) * 4);
#pragma unroll
    for (int m = 0; m < 4; ++m) nm4[m] = lds_ld<f32x4>(pm4 + m * 64);
#pragma unroll
    for (int n = 0; n < 2; ++n) csv[n] = lds_ld<float>(pcv + n * 64);
    rds(1, 1, 4); mms(0, 2);
    step();
    fold(2); fold(3);
    SGB(0x002, 8);
#pragma unroll
    for (int i = 0; i < 8; ++i) { SGB(0x008, 1); SGB(0x100, 2); }
#pragma unroll
    for (int i = 0; i < 11; ++i) { SGB(0x008, 1); SGB(0x100, 1); SGB(0x002, 2); }
#pragma unroll
    for (int i = 0; i < 5; ++i) { SGB(0x008, 1); SGB(0x002, 3); }
    SCHED;
    __builtin_amdgcn_s_waitcnt(0xC07F);
    BAR;
    SCHED;
    rds(0, 0, 2);
    mms(1, 4); fold(4); fold(5);
#pragma unroll
    for (int i = 0; i < 8; ++i) { SGB(0x008, 1); SGB(0x100, 1); SGB(0x002, 1); }
#pragma unroll
    for (int i = 0; i < 8; ++i) { SGB(0x008, 1); SGB(0x100, 1); SGB(0x002, 2); }
#pragma unroll
    for (int i = 0; i < 8; ++i) { SGB(0x008, 1); SGB(0x002, 2); }
    SCHED;
    pre(0);
    SCHED;
    rds(1, 1, 2);
    mms(0, 2, 4); fold(0); fold(1);
    pre(1);
#pragma unroll
    for (int i = 0; i < 16; ++i) { SGB(0x008, 1); SGB(0x100, 1); SGB(0x002, 2); SGB(0x004, 1); }
  }
  SCHED;
  auto mm6 = [&](int m, int n0 = 0, int n1 = 8) {
#pragma unroll
    for (int nn = n0; nn < n1; ++nn) {
      const int n = nn < 4 ? nn + 2 : nn & 1, p = (nn >> 1) != 2;
      acc[m][n] = n < 4 ? MFMA(bf[p][n], af[p][m], acc[m][n]) : MFMA(af[p][m], bf[p][n], acc[m][n]);
    }
  };
  if (!isb) {
    fin(std::false_type{}, acc, mm6);
#pragma unroll
    for (int i = 0; i < 24; ++i) asm volatile("" : "=v"(acc[i / 6][i % 6]));
#pragma unroll
    for (int i = 0; i < 6; ++i) asm volatile("" : "=v"(bf[1][i]));
#pragma unroll
    for (int i = 0; i < 8; ++i) asm volatile("" : "=v"(af[i >> 2][i & 3]));
    asm volatile("" : "=v"(bf[0][0])); asm volatile("" : "=v"(bf[0][1]));
  }
  OPAQUE_S(isb);
  if (isb) fin(std::true_type{}, acc, mm6);
}
__device__ __forceinline__ u32x4 pack(const f32x4& a, const f32x4& b) { return u32x4{pk2(a[0], a[1]), pk2(a[2], a[3]), pk2(b[0], b[1]), pk2(b[2], b[3])}; }

__device__ __forceinline__ u32x2 nosent2(const f32x4& a) { return u32x2{min(pk2(a[0], a[1]), SENT - 1), min(pk2(a[2], a[3]), SENT - 1)}; }
template <int N> __device__ __forceinline__ void hedgehog(const f32x4 (&r)[4][2], const float (&c)[4], float (&ep)[4][8], float (&en)[4][8]) {
  float mx[4], nm[4];
#pragma unroll
  for (int m = 0; m < N; ++m) {
    const f32x4 a = r[m][0], b = r[m][1];
    mx[m] = vmax2(fmaxf(fmaxf(a[3], b[0]), b[1]), fmaxf(fmaxf(fmaxf(fmaxf(a[0], a[1]), a[2]), b[2]), b[3]));
    nm[m] = vmax2(fmaxf(fmaxf(-a[3], -b[0]), -b[1]), fmaxf(fmaxf(fmaxf(fmaxf(-a[0], -a[1]), -a[2]), -b[2]), -b[3]));
  }
  SCHED;
  red4<true>(mx[0], mx[1], nm[0], nm[1], [](float x) { return x; });
  if (N == 4) red4<true>(mx[2], mx[3], nm[2], nm[3], [](float x) { return x; });
#pragma unroll
  for (int m = 0; m < N; ++m) { mx[m] *= -c[m]; nm[m] *= -c[m]; }
  SCHED;
#pragma unroll
  for (int m = 0; m < N; ++m) {
#pragma unroll
    for (int i = 0; i < 8; ++i) { ep[m][i] = __builtin_fmaf(r[m][i >> 2][i & 3], c[m], mx[m]); en[m][i] = __builtin_fmaf(r[m][i >> 2][i & 3], -c[m], nm[m]); }
    SCHED;
#pragma unroll
    for (int i = 0; i < 8; ++i) { ep[m][i] = __builtin_amdgcn_exp2f(ep[m][i]); en[m][i] = __builtin_amdgcn_exp2f(en[m][i]); }
    SCHED;
  }
}
__device__ __forceinline__ u32x4 pack8(const float (&e)[8], float f) {
  return u32x4{pk2(e[0] * f, e[1] * f), pk2(e[2] * f, e[3] * f), pk2(e[4] * f, e[5] * f), pk2(e[6] * f, e[7] * f)};
}
__device__ __forceinline__ u32x4 pack8(const float (&e)[8]) { return u32x4{pk2(e[0], e[1]), pk2(e[2], e[3]), pk2(e[4], e[5]), pk2(e[6], e[7])}; }

constexpr u64 window_mask(int t, int e) {
  u64 mk = 0;
  for (int ln = 0; ln < 64; ++ln) {
    const int delta = (ln & 15) + 16 - t * 16 - (ln >> 4) * 4 - e;
    if (delta >= 0 && delta <= 14 && !(delta & 1)) mk |= 1ull << ln;
  }
  return mk;
}
__device__ __forceinline__ void phik2(const f32x4 (&r)[4][2], const float (&c)[4], unsigned pw) {
  const f32x4 zero = {0.f, 0.f, 0.f, 0.f};
  const bf16x8 ones = {0x3f80, 0x3f80, 0x3f80, 0x3f80, 0x3f80, 0x3f80, 0x3f80, 0x3f80};
  float ep[4][8], en[4][8];
  f32x4 sp[2], sn[2];
  hedgehog<2>(r, c, ep, en);
#pragma unroll
  for (int m = 0; m < 2; ++m) { sp[m] = MFMA(ones, as_frag(pack8(ep[m])), zero); sn[m] = MFMA(ones, as_frag(pack8(en[m])), zero); }
#pragma unroll
  for (int m = 0; m < 2; ++m) { asm("" : "+v"(sp[m])); asm("" : "+v"(sn[m])); }
  SCHED;
#pragma unroll
  for (int m = 0; m < 2; ++m) {
    const float fp = __builtin_amdgcn_rcpf(sp[m][0]), fn = __builtin_amdgcn_rcpf(sn[m][0]);
#pragma unroll
    for (int i = 0; i < 8; ++i) { ep[m][i] *= fp; en[m][i] *= fn; }
    SCHED;
    const u32x4 pp = pack8(ep[m]), pn = pack8(en[m]);
    const unsigned d = pw + m * (16 * 160);
    lds_st<u32x2>(d, u32x2{pp[0], pp[1]});
    lds_st<u32x2>(d + 32, u32x2{pp[2], pp[3]});
    lds_st<u32x2>(d + 64, u32x2{pn[0], pn[1]});
    lds_st<u32x2>(d + 96, u32x2{pn[2], pn[3]});
    SCHED;
  }
}
__device__ __forceinline__ bf16x8 rowsum_sel(int lane) {
  const int fr = lane & 15;
  const unsigned one = (fr < 8) ? (0x3f80u << (16 * (fr & 1))) : 0u;
  const int e = (fr >> 1) & 3;
  return as_frag(u32x4{e == 0 ? one : 0u, e == 1 ? one : 0u, e == 2 ? one : 0u, e == 3 ? one : 0u});
}
__device__ __forceinline__ f32x4 rowsum_fin(f32x4 c) {
#pragma unroll
  for (int k = 0; k < 4; ++k) {
    const float a = c[k];
    c[k] = a + __int_as_float(__builtin_amdgcn_update_dpp(0, __float_as_int(a), 0x128  , 0xf, 0xf, false));
  }
  return c;
}
__device__ __forceinline__ void job_colsum(rsrc_t rs, const u32x4 (&w)[8], int mat, int g, int jc, int hw, int lane) {
  const bf16x8 sel = rowsum_sel(lane);
  f32x4 c = {0.f, 0.f, 0.f, 0.f};
#pragma unroll
  for (int i = 0; i < 8; ++i) c = MFMA(sel, as_frag(w[i]), c);
  const u32x4 w4 = __builtin_bit_cast(u32x4, rowsum_fin(c));
  unsigned so = WS_CS + (unsigned)(((mat * 16 + jc) * 16 + g) * 768);
  OPAQUE_S(so);
  if ((lane & 40) == 0) st16_sc1(rs, (unsigned)(hw * 64 + (lane & 7) * 8 + (lane >> 4) * 4) * 4, so, w4);
}
__device__ __forceinline__ unsigned img_off(int i, int lane) { return (lane >> 3) * 128 + (((lane & 7) ^ ((((lane >> 4) & 1) | ((i & 1) << 1)) << 1)) * 16); }
__device__ __forceinline__ void job_pack(rsrc_t rs, u32x4 w, int i, int mat, int g, int jc, int hw, int lane) {
  st16_sc1(rs, img_off(i, lane),
           WS_WP + (unsigned)(((mat * 16 + jc) * 16 + g) * 24576 + hw * 8192 + i * 1024), w);
}

__global__ __launch_bounds__(512, 2) void mega_kernel(Params p) {
  extern __shared__ __attribute__((aligned(16))) char smem[];
  const int tid = threadIdx.x;
  const int wave0 = __builtin_amdgcn_readfirstlane(tid >> 6);
  const int xx = blockIdx.x & 7, rr = blockIdx.x >> 3;
  const int g0 = (rr / NJ) * NX + (xx & (NX - 1)), j0 = (xx / NX) * NJ + (rr & (NJ - 1));
  constexpr unsigned lds0 = 0, lds00 = 0;
  if ((unsigned)(size_t)(LAS char*)smem != 0u) return;
#define LOCALS0 int g = g0, j = j0, l = l0, wave = wave0;                                              \
  OPAQUE_S(g); OPAQUE_S(j); OPAQUE_S(l); OPAQUE_S(wave);                                               \
  const int hw = wave & 3, wm = wave & 1, h = (wave >> 1) & 1;                                         \
  const unsigned myrep = (unsigned)(JROT(j) / 2 + (g & (NX - 1))) * REP_STRIDE;                              \
  (void)hw; (void)wm; (void)h; (void)myrep;                                                            \
  int lane = tid; OPAQUE_V(lane); lane &= 63
#define LOCALS LOCALS0; const bool isb = l & 1, lead = (wave == 4); const int half = wave >> 2; (void)isb; (void)lead; (void)half
  const rsrc_t rs = mk_rsrc(p.ws, WS_SPAN);
  const bool cw = wave0 < 4;
  if (wave0 == 3) {
    const char* pc;
    unsigned len;
    asm volatile("s_getpc_b64 %0\n.Lpf_base:\n\ts_mov_b32 %1, .Lpf_end-.Lpf_base" : "=s"(pc), "=s"(len));
    const unsigned off = ((unsigned)rr * 16 + (tid & 15)) * 128;
    touch(mk_rsrc(pc, len), ((int)len > 0 && off + 4 <= len) ? off : 0u, lds00 + L_SINK);
  }
  if (wave0 == 2) touch(rs, WS_EPOCH, lds00 + L_SINK);
  if (wave0 == 4) touch(mk_rsrc(p.wphi, 32 * 2048), j0 * 4096, lds00 + L_SINK);
  if (wave0 == 5) touch(mk_rsrc(p.woA, 1024 * 2048), (tid & 1) << 20, lds00 + L_SINK);
  if (wave0 == 6) touch(mk_rsrc(p.wqkvB, 1024 * 6144), min(tid & 7, 5) << 20, lds00 + L_SINK);
  if (wave0 == 7) touch(mk_rsrc(p.woB, 1024 * 2048), (tid & 1) << 20, lds00 + L_SINK);
  auto slowbit = [](const void* q) { return 1u << ((3 - ((unsigned)(size_t)q >> 7)) & 7); };
  unsigned slowm = slowbit(p.wqkvA) | (slowbit(p.wqkvB) << 8) | (slowbit(p.woA) << 16) | (slowbit(p.woB) << 24);
  OPAQUE_S(slowm);
  u32x2 epoch = {0u, 0u};
  bool dead = false;
      if (cw) {
        const int l0 = 0;
        LOCALS;
        u32x4 v[2][8];
        if (wave < 3) {
          const int jc = JOBC(j, wave), kt = (KB0(g, j) + (unsigned)g / NX) & 15;
          const rsrc_t ra = mk_rsrc(p.wqkvA, 1024 * 6144);
#pragma unroll
          for (int i = 0; i < 8; ++i)
            v[0][i] = __builtin_amdgcn_raw_buffer_load_b128(ra, (lane >> 3) * 6144 + (lane & 7) * 16, (kt * 64 + i * 8) * 6144 + wave * 2048 + jc * 128, 0);
          SCHED;
          BAR; SCHED;
          job_colsum(rs, v[0], 0, g, jc, wave, lane);
          if (SLOWC(0, jc)) {
#pragma unroll
            for (int i = 0; i < 8; ++i) job_pack(rs, v[0][i], i, 0, kt, jc, wave, lane);
          }
        } else {
          const int fr = lane & 15, fq = lane >> 4;
          const rsrc_t rx = mk_rsrc(p.x, 2048 * 2048);
#pragma unroll
          for (int i = 0; i < 16; ++i)
            v[i & 1][i >> 1] = __builtin_amdgcn_raw_buffer_load_b128(rx, fr * 2048 + fq * 16, (g * 128 + (j & (NJ - 1)) * (128 / NJ) + ((i >> 1) % (8 / NJ)) * 16) * 2048 + ((KB0(g, j) + (i >> 1) / (8 / NJ)) & 15) * 128 + (i & 1) * 64, 0);
          SCHED;
          BAR; SCHED;
          const f32x4 zero = {0.f, 0.f, 0.f, 0.f};
          const bf16x8 ones = {0x3f80, 0x3f80, 0x3f80, 0x3f80, 0x3f80, 0x3f80, 0x3f80, 0x3f80};
          float sm[8], q[8];
#pragma unroll
          for (int m = 0; m < 8; ++m) {
            const f32x4 a = MFMA(ones, as_frag(v[1][m]), MFMA(ones, as_frag(v[0][m]), zero));
            const f32x4 b = MFMA(as_frag(v[1][m]), as_frag(v[1][m]), MFMA(as_frag(v[0][m]), as_frag(v[0][m]), zero));
            sm[m] = a[0];
            q[m] = (fr & 2) ? ((fr & 1) ? b[3] : b[2]) : ((fr & 1) ? b[1] : b[0]);
          }
          if (fq == (fr >> 2)) {
#pragma unroll
            for (int m = 0; m < 8; ++m) st8_sc1(rs, ((m % (8 / NJ)) * 16 + fr) * 8 + (m / (8 / NJ)) * 1024, st_off(g, 0) + JROT(j) * 1024 + (j & (NJ - 1)) * (1024 / NJ), u32x2{__float_as_uint(sm[m]), __float_as_uint(q[m])});
          }
        }
      }
  const char *pA = (const char*)p.x, *pW = (const char*)p.wqkvA;
  unsigned zA = 2048 * 2048, bA = g0 * (128 * 2048), kA = 128, pcA = 32 * 2048, vA;
  unsigned zW = 1024 * 6144, bW = j0 * 128, kW = 64 * 6144, pcW = 2048, hW = 32 * 6144, vW;
  int kb = KB0(g0, j0), dj = -1;
  {
    const int lane = tid & 63, hw = wave0 & 3;
    vA = (hw * 8 + (lane >> 3)) * 2048 + (((lane & 7) ^ (((hw & 1) * 4 + (lane >> 4)) & 7)) * 16);
    vW = (hw * 8 + (lane >> 3)) * 6144 + (((lane & 7) ^ ((((lane >> 4) & 1) | ((hw & 1) << 1)) << 1)) * 16);
  }

#pragma unroll 1
  for (int l0 = 0; l0 < 4; ++l0) {
    f32x4 acc[2][4];
    auto kvpart = [&](unsigned va, unsigned pa, unsigned pb, unsigned kvb, int lane, int wave) {
      const f32x4 zero = {0.f, 0.f, 0.f, 0.f};
      const bf16x8 ones = {0x3f80, 0x3f80, 0x3f80, 0x3f80, 0x3f80, 0x3f80, 0x3f80, 0x3f80};
      f32x4 c0 = zero, c1 = zero, z0 = zero;
#pragma unroll
      for (int ks = 0; ks < 4; ++ks) {
        const bf16x8 v = lds_ld<bf16x8>(va + ks * 2048);
        const bf16x8 a0 = tr2(pa + ks * 32 * 160, pa + (ks * 32 + 16) * 160);
        const bf16x8 a1 = tr2(pb + ks * 32 * 160, pb + (ks * 32 + 16) * 160);
        c0 = MFMA(a0, v, c0);
        c1 = MFMA(a1, v, c1);
        z0 = MFMA(a0, ones, z0);
      }
      const u32x4 c = nosent(pack(c0, c1));
      const bool dt = wave & 2;
      st16_sc1(rs, lane * 16, kvb + (unsigned)wave * 1024, u32x4{dt ? c[2] : c[0], dt ? c[3] : c[1], dt ? c[0] : c[2], dt ? c[1] : c[3]});
      if ((lane & 15) == 0)
        st8_sc1(rs, lane, kvb + KVP_Z + (wave >> 2) * 128 + (wave & 1) * 64 + (wave & 2) * 4, nosent2(z0));
    };
#define KV_ADDR                                                                                                            \
    unsigned va = lds0 + SC_VF + (wave >> 2) * 8192 + ((wave >> 1) & 1) * 1024 + lane * 16;                                \
    unsigned pa = lds0 + SC_PHIK + ((wave >> 2) * 128 + fq * 4 + (fr >> 2)) * 160 + (wave & 1) * 64 + (lane & 3) * 8;      \
    unsigned kvj = WS_KVP + (unsigned)j * KVP_J + (unsigned)(l >> 1) * KVP_L;                                              \
    unsigned pb = pa + 32 - (wave & 2) * 16;                                                                               \
    pa += (wave & 2) * 16;                                                                                                 \
    OPAQUE_V(va); OPAQUE_V(pa); OPAQUE_V(pb); OPAQUE_S(kvj)
    if (cw) {
      LOCALS;
      unsigned otile, kvo, l16, pr1, pr4;
      int g2, j2, l2, ln2;
      qkv_loop(l & 1, wm, h, lane, l == 0, [&](int st) {
        if (st == 0) {
          g2 = g0; j2 = j0; l2 = l0; ln2 = tid;
          OPAQUE_S(g2); OPAQUE_S(j2); OPAQUE_S(l2); OPAQUE_V(ln2);
          return;
        }
        otile = ob_off(g2, j2, l2) + (unsigned)(wm * 8 + h) * 1024;
        kvo = WS_KVP + (unsigned)j2 * KVP_J + (unsigned)g2 * KVP_G + (unsigned)(l2 >> 1) * KVP_L;
        l16 = (ln2 & 63) * 16;
        pr1 = lds0 + L_RSTD + (16 + wm * 64 + (ln2 & 15)) * 4;
        pr4 = lds0 + L_RSTD + wm * 256 + (ln2 & 48);
        asm("" : "+s"(otile)); asm("" : "+s"(kvo)); asm("" : "+v"(l16)); asm("" : "+v"(pr1)); asm("" : "+v"(pr4));
      }, [&](auto isb_c, f32x4 (&a)[4][6], auto& mm6) {
        const f32x4 zero = {0.f, 0.f, 0.f, 0.f};
        constexpr float L2E = 1.4426950408889634f;
        float rq[4];
#define ilv8(nv) do { _Pragma("unroll") for (int i_ = 0; i_ < 8; ++i_) { SGB(0x008, 1); SGB(0x100, 2); SGB(0x002, nv); } SCHED; } while (0)
        if constexpr (decltype(isb_c)::value) {
          constexpr float QS = 0.17677669529663687f * L2E, NEG = -3.0e38f;
          const bf16x8 ones = {0x3f80, 0x3f80, 0x3f80, 0x3f80, 0x3f80, 0x3f80, 0x3f80, 0x3f80};
          u32x4 kf[5], vf[5], qf[4], pf[4];
          f32x4 rs4[4], bias[2], s0[4], s1[4], dn[4];
          float x[4][4], mx[4];
          unsigned psel0, psel1;
          auto pkkv = [&](int m) { kf[m + 1] = pack(a[m][2] * rq[m], a[m][3] * rq[m]); vf[m + 1] = pack(a[m][4] * rs4[m], a[m][5] * rs4[m]); };
          auto pkq = [&](int m) { qf[m] = pack(a[m][0], a[m][1]); };
          auto st1 = [&](int m) {
            x[m][0] = vmax2(s0[m][0], s0[m][1]); x[m][1] = vmax2(s0[m][2], s0[m][3]); x[m][2] = vmax2(s1[m][0], s1[m][1]); x[m][3] = vmax2(s1[m][2], s1[m][3]);
            mx[m] = vmax2(vmax2(x[m][0], x[m][1]), vmax2(x[m][2], x[m][3]));
          };
          auto st2 = [&](int m) {
            const float mxl = mx[m] * -rq[m];
#pragma unroll
            for (int e = 0; e < 4; ++e) x[m][e] = __builtin_fmaf(x[m][e], rq[m], mxl);
          };
          auto st3 = [&](int m) {
#pragma unroll
            for (int e = 0; e < 4; ++e) x[m][e] = __builtin_amdgcn_exp2f(x[m][e]);
          };
          auto st5 = [&](int m) {
            const unsigned p0 = pk2(x[m][0], x[m][1]), p1 = pk2(x[m][2], x[m][3]);
            pf[m] = u32x4{__builtin_amdgcn_perm(0u, p0, psel0), __builtin_amdgcn_perm(0u, p0, psel1), __builtin_amdgcn_perm(0u, p1, psel0), __builtin_amdgcn_perm(0u, p1, psel1)};
          };
          auto st6 = [&](int m) {
            dn[m] = MFMA(ones, as_frag(pf[m]), zero);
            s0[m] = MFMA(as_frag(u32x4{vf[m][0], vf[m][1], vf[m + 1][0], vf[m + 1][1]}), as_frag(pf[m]), zero);
            s1[m] = MFMA(as_frag(u32x4{vf[m][2], vf[m][3], vf[m + 1][2], vf[m + 1][3]}), as_frag(pf[m]), zero);
            asm("" : "+v"(dn[m]));
          };
          auto st7 = [&](int m) { const float inv = __builtin_amdgcn_rcpf(dn[m][0]); pf[m] = nosent(pack(s0[m] * inv, s1[m] * inv)); };
          mm6(3);
#pragma unroll
          for (int m = 0; m < 4; ++m) rq[m] = lds_ld<float>(pr1 + m * 64);
#pragma unroll
          for (int m = 3; m >= 0; --m) rs4[m] = lds_ld<f32x4>(pr4 + 64 + m * 64);
          unsigned pv_ = l16 + lds0 + L_PREV + h * 2048;
          asm("" : "+v"(pv_));
          static_for<0, 8>([&](auto i_c) __attribute__((always_inline)) {
            constexpr int i = decltype(i_c)::value;
            constexpr u64 mk = window_mask(i >> 2, i & 3);
            bias[i >> 2][i & 3] = __builtin_amdgcn_inverse_ballot_w64(mk) ? 0.f : NEG;
          });
          asm("" : "+v"(bias[0])); asm("" : "+v"(bias[1]));
          const unsigned hso = kvo + KVP_HALO + h * 2048, hsi = hso - (g > 0 ? KVP_G : 0u);
          ilv8(2);
          mm6(2);
          pkkv(3);
          ilv8(2);
          OPAQUE_V(kf[4]); OPAQUE_V(vf[4]);
          if (wm) {
            st16_sc1(rs, l16, hso, nosent(kf[4]));
            st16_sc1(rs, l16, hso + 1024, nosent(vf[4]));
          } else {
            lds_st<u32x4>(pv_, kf[4]);
            lds_st<u32x4>(pv_ + 1024, vf[4]);
          }
          SCHED;
          mm6(1);
          pkq(3); pkkv(2);
          ilv8(3);
          WAITL0;
          BAR;
          SCHED;
          mm6(0);
          pkq(2); pkkv(1);
          ilv8(3);
          s0[3] = MFMA(as_frag(kf[3]), as_frag(qf[3]), bias[0]); s1[3] = MFMA(as_frag(kf[4]), as_frag(qf[3]), bias[1]);
          s0[2] = MFMA(as_frag(kf[2]), as_frag(qf[2]), bias[0]); s1[2] = MFMA(as_frag(kf[3]), as_frag(qf[2]), bias[1]);
          pkq(1); pkkv(0);
#pragma unroll
          for (int i = 0; i < 4; ++i) { SGB(0x008, 1); SGB(0x002, 5); }
          SCHED;
          const bool halo = wm == 0 && g > 0;
          const u32x4 hk = ld16_sc1(rs, l16, hsi), hv = ld16_sc1(rs, l16, hsi + 1024);
          SCHED;
          s0[1] = MFMA(as_frag(kf[1]), as_frag(qf[1]), bias[0]); s1[1] = MFMA(as_frag(kf[2]), as_frag(qf[1]), bias[1]);
          st1(3); st1(2);
#pragma unroll
          for (int m = 0; m < 4; ++m) rq[m] *= QS;
          SCHED;
          st1(1);
          pkq(0);
          psel0 = __builtin_amdgcn_inverse_ballot_w64(0xaaaaaaaaaaaaaaaaull) ? 0x01000c0cu : 0x0c0c0100u;
          psel1 = __builtin_amdgcn_inverse_ballot_w64(0xaaaaaaaaaaaaaaaaull) ? 0x03020c0cu : 0x0c0c0302u;
          { float d = mx[1]; red4<true>(mx[3], mx[2], mx[1], d, [](float v) { return v; }); }
          s1[0] = MFMA(as_frag(kf[1]), as_frag(qf[0]), bias[1]);
          SCHED;
          st2(3); st2(2); st2(1);
          SCHED;
          st3(3); st3(2); st3(1);
          SCHED;
          st5(3); st6(3); st5(2); st6(2); st5(1); st6(1);
          SCHED;
          u32x4 pk = lds_ld<u32x4>(pv_), pv = lds_ld<u32x4>(pv_ + 1024);
          st7(3); st7(2); st7(1);
          SCHED;
#pragma unroll
          for (int m = 3; m > 0; --m) st16_sc1(rs, l16, otile + m * 2048, pf[m]);
          if (!wm && g == 0) bias[0] = f32x4{NEG, NEG, NEG, NEG};
          SCHED;
          if (halo) {
            pk = hk; pv = hv;
            for (unsigned spins = 0; !dead && __any(umax4v(umax4v(0u, pk), pv) == SENT); ++spins) {
              if (spins > SPIN_CAP) { dead = true; break; }
              CLOBBER;
              pk = ld16_sc1(rs, l16, hsi); pv = ld16_sc1(rs, l16, hsi + 1024);
              OPAQUE_V(pk); OPAQUE_V(pv);
            }
          }
          vf[0] = pv;
          s0[0] = MFMA(as_frag(pk), as_frag(qf[0]), bias[0]);
          st1(0);
          mx[0] = red_max4(mx[0]);
          st2(0); st3(0); st5(0); st6(0); st7(0);
          st16_sc1(rs, l16, otile, pf[0]);
        } else {
          LOCALS;
          const int fr = lane & 15, fq = lane >> 4;
          f32x4 rs4[4], r[4][2];
          const bf16x8 ones = {0x3f80, 0x3f80, 0x3f80, 0x3f80, 0x3f80, 0x3f80, 0x3f80, 0x3f80};
          auto proj = [&](int m, int n, const bf16x8& w0, const bf16x8& w1) {
            const u32x4 f = pack(a[m][n], a[m][n + 1]);
            r[m][0] = MFMA(w0, as_frag(f), zero); r[m][1] = MFMA(w1, as_frag(f), zero);
          };
          mm6(3, 0, 2); mm6(2, 0, 2); mm6(1, 0, 2); mm6(0, 0, 2);
          unsigned wa = lds0 + L_WPHI + h * 2048 + (fq * 4 + (fr >> 2)) * 64 + (lane & 3) * 8, kw = lds0 + L_PREV + hw * 2048 + l16;
          asm("" : "+v"(wa)); asm("" : "+v"(kw));
#pragma unroll
          for (int m = 0; m < 4; ++m) rq[m] = lds_ld<float>(pr1 + m * 64);
#pragma unroll
          for (int m = 0; m < 4; ++m) rs4[m] = lds_ld<f32x4>(pr4 + 64 + m * 64);
          const bf16x8 w0 = tr2(wa, wa + 1024);
          const bf16x8 w1 = tr2(wa + 32, wa + 32 + 1024);
#pragma unroll
          for (int i = 0; i < 8; ++i) { SGB(0x008, 1); SGB(0x100, 2); SGB(0x002, 2); }
          SCHED;
          mm6(3, 2, 4); mm6(2, 2, 4);
          lds_st<u32x4>(kw + 1024, pack(a[3][2], a[3][3]));
          lds_st<u32x4>(kw, pack(a[2][2], a[2][3]));
#pragma unroll
          for (int i = 0; i < 2; ++i) { SGB(0x008, 1); SGB(0x002, 2); SGB(0x008, 1); SGB(0x002, 2); SGB(0x200, 1); }
          SCHED;
          mm6(1, 2, 4); mm6(0, 2, 4);
          SCHED;
          WAITL0;
          BAR;
          SCHED;
          proj(0, 2, w0, w1); proj(1, 2, w0, w1);
          unsigned pw = lds0 + SC_PHIK + (h * 128 + wm * 64 + fr) * 160 + fq * 8, vw = lds0 + SC_VF + h * 8192 + wm * 4096 + l16;
          asm("" : "+v"(pw)); asm("" : "+v"(vw));
#pragma unroll
          for (int m = 0; m < 4; ++m) rq[m] *= L2E;
#pragma unroll
          for (int i = 0; i < 4; ++i)
            lds_st<u32x4>(vw + i * 1024, pack(a[i & 2][4 + (i & 1)] * rs4[i & 2], a[(i & 2) + 1][4 + (i & 1)] * rs4[(i & 2) + 1]));
          SCHED;
          phik2(r, rq, pw);
          KV_ADDR;
          WAITL0;
          BAR;
          kvpart(va, pa, pb, kvo, lane, wave);
          SCHED;
#pragma unroll
          for (int m = 0; m < 4; ++m) mm6(m, 4, 8);
          SCHED;
          CLOBBER;
          BAR;
          u32x4 qp[4], qn[4];
          float sqp[4], sqn[4];
#pragma unroll
          for (int m = 0; m < 4; ++m) proj(m, 0, w0, w1);
          SCHED;
          float ep[4][8], en[4][8];
          hedgehog<4>(r, rq, ep, en);
          const unsigned zo = (lane >> 3) * KVP_G + (lane & 7) * 16, zs = kvj + KVP_Z + h * 128;
          u32x4 zv[2] = {ld16_sc1(rs, zo, zs), ld16_sc1(rs, zo, zs + 8 * KVP_G)};
          SCHED;
#pragma unroll
          for (int m = 0; m < 4; ++m) {
            qp[m] = pack8(ep[m]); qn[m] = pack8(en[m]);
            sqp[m] = MFMA(ones, as_frag(qp[m]), zero)[0];
            sqn[m] = MFMA(ones, as_frag(qn[m]), zero)[0];
          }
          SCHED;
#pragma unroll
          for (int m = 0; m < 4; ++m) { sqp[m] = __builtin_amdgcn_rcpf(sqp[m]); sqn[m] = __builtin_amdgcn_rcpf(sqn[m]); }
#pragma unroll
          for (int m = 0; m < 4; ++m) { OPAQUE_V(qp[m]); OPAQUE_V(qn[m]); OPAQUE_V(sqp[m]); OPAQUE_V(sqn[m]); }
          unsigned ko = (lane >> 5) * KVP_G + (lane & 31) * 16, ks = kvj + (unsigned)((j ^ RX) - j) * KVP_J + KVP_SUM + h * (8 * KVP_G);
          bf16x8 sel = rowsum_sel(lane);
          OPAQUE_V(ko); OPAQUE_S(ks); OPAQUE_V(sel);
          for (unsigned spins = 0; !dead && __any(umax4v(umax4v(0u, zv[0]), zv[1]) == SENT); ++spins) {
            if (spins > SPIN_CAP) { dead = true; break; }
            CLOBBER;
            zv[0] = ld16_sc1(rs, zo, zs); zv[1] = ld16_sc1(rs, zo, zs + 8 * KVP_G);
          }
          {
            const f32x4 z = rowsum_fin(MFMA(sel, as_frag(zv[1]), MFMA(sel, as_frag(zv[0]), zero)));
            if ((lane & 40) == 0) lds_st<u32x2>(lds0 + L_ZF + hw * 128 + fr * 16 + fq * 8, u32x2{pk2(z[0], z[1]), pk2(z[2], z[3])});
            WAITL0;
            const bf16x8 zf0 = lds_ld<bf16x8>(lds0 + L_ZF + hw * 128 + fq * 16), zf1 = lds_ld<bf16x8>(lds0 + L_ZF + hw * 128 + 64 + fq * 16);
            float dp[4], dn[4];
#pragma unroll
            for (int m = 0; m < 4; ++m) { dp[m] = MFMA(zf0, as_frag(qp[m]), zero)[0]; dn[m] = MFMA(zf1, as_frag(qn[m]), zero)[0]; }
            SCHED;
#pragma unroll
            for (int m = 0; m < 4; ++m) {
              const float inv = __builtin_amdgcn_rcpf(__builtin_fmaf(dp[m], sqp[m], __builtin_fmaf(dn[m], sqn[m], 1e-6f)));
              sqp[m] *= inv; sqn[m] *= inv;
              OPAQUE_V(sqp[m]); OPAQUE_V(sqn[m]);
            }
          }
          BAR;
          __builtin_amdgcn_s_sleep(H2D);
          u32x4 kv[4];
#pragma unroll
          for (int i = 0; i < 4; ++i) kv[i] = ld16_sc1(rs, ko, ks + i * (2 * KVP_G));
          SCHED;
#pragma unroll
          for (int m = 0; m < 4; ++m) { qp[m] = pack8(ep[m], sqp[m]); qn[m] = pack8(en[m], sqn[m]); }
#pragma unroll
          for (int m = 0; m < 4; ++m) { OPAQUE_V(qp[m]); OPAQUE_V(qn[m]); }
          SCHED;
          for (unsigned spins = 0; !dead && __any(max(umax4v(umax4v(0u, kv[0]), kv[1]), umax4v(umax4v(0u, kv[2]), kv[3])) == SENT); ++spins) {
            if (spins > SPIN_CAP) { dead = true; break; }
            CLOBBER;
#pragma unroll
            for (int i = 0; i < 4; ++i) kv[i] = ld16_sc1(rs, ko, ks + i * (2 * KVP_G));
          }
          f32x4 o[4][2];
#pragma unroll
          for (int m = 0; m < 4; ++m)
#pragma unroll
            for (int i = 0; i < 2; ++i) o[m][i] = MFMA(as_frag(kv[2 * i + 1]), as_frag(qn[m]), MFMA(as_frag(kv[2 * i]), as_frag(qp[m]), zero));
#pragma unroll
          for (int m = 0; m < 4; ++m) st16_sc1(rs, l16, otile + m * 2048, nosent(pack(o[m][0], o[m][1])));
        }
      });
#pragma unroll
      for (int i = 0; i < 8; ++i) asm volatile("" : "=v"(acc[i >> 2][i & 3]));
    } else {
      __builtin_amdgcn_s_setprio(3);
      if (l0) {
        WAITV(3);
        WAITL0;
        BAR;
      }
      LOCALS;
      u32x4 xr[2][2];
      const unsigned lin = lane * 16, vlin = lin + hw * 1024;
      const int jr = j & ~3;
      u32x4 sent4 = {SENT, SENT, SENT, SENT};
      OPAQUE_V(sent4);
      const unsigned cb0 = ob_off(g, j, l), cb2 = xb_off(g, l) + j * TS;
      const unsigned cb1 = WS_KVP + (unsigned)j * KVP_J + (unsigned)g * KVP_G + (unsigned)(l >> 1) * KVP_L;
      unsigned ra0;
      int tr = 0;
      auto ldres = [&](int t) {
        if (tr == t) {
#pragma unroll
          for (int i = 0; i < 4; ++i) xr[i >> 1][i & 1] = lds_ld<u32x4>((ra0 ^ ((i & 1) * 64)) + (t % 3) * XS + ((i >> 1) ^ 1) * 2048);
        }
      };
      auto aslot = [&](int t) { return lds0 + L_R2 + (t % 3) * XS + hw * 1024; };
      auto wslot = [&](int t) { return lds0 + L_R1 + (t & 3) * 24576 + hw * 1024; };
      const bool l0f = l == 0;
      const rsrc_t rA = mk_rsrc(pA, zA);
      auto kseq = [&](int t) { return (kb + t - (t <= dj ? 1 : 0)) & 15; };
      auto offA = [&](int t) { return bA + (unsigned)kseq(t) * kA; };
      auto offW = [&](int t) { return bW + (unsigned)kseq(t) * kW; };
      auto ldA = [&](int t) { dma4<4096>(rA, vA, offA(t), pcA, aslot(t)); };
      auto ldWh = [&](int t, unsigned o, int hf) { dma3<8192>(mk_rsrc(pW, zW), vW, o + (hf ? hW : 0u), pcW, wslot(t) + hf * 4096); };
      const bool slowj = l0f && SLOWC(0, j);
      auto packedW = [&]() {
        pW = p.ws; zW = WS_SPAN; bW = WS_WP + (unsigned)j * (16 * 24576); kW = 24576; pcW = 8192; hW = 4096; vW = vlin;
      };
      auto ldW = [&](int t) { const unsigned o = offW(t); ldWh(t, o, 0); ldWh(t, o, 1); };
      constexpr auto spread = [](int n) { unsigned m = 0; for (int t = 0; t < 26; ++t) if ((t + 1) * n / 13 > t * n / 13) m |= 1u << t; return m; };
      constexpr unsigned sp4 = spread(4), sp8 = spread(8), sp12 = spread(12);
      const unsigned smask = (isb ? (l == 3 ? sp4 : sp8) : sp12) >> ((((g + j * 5) & 15) * 13) >> 4);
      const unsigned cbm = isb ? cb2 : cb1;
      auto clean = [&](int t) __attribute__((always_inline)) {
        if (t < 13 && ((smask >> t) & 1)) {
          const unsigned cc = __builtin_popcount(smask & ((1u << t) - 1));
          const unsigned cb = cb0 + (cc >= 4 ? cbm - cb0 : 0u) + (cc >= 8 ? cb2 - cbm : 0u), pc = (cc + g + j) & 3;
          st16_sc1(rs, (lane >> 4) * 4096 + (lane & 15) * 16, cb + hw * 1024 + pc * 256, sent4);
        }
      };
      unsigned flo;
      unsigned ready = ~0u;
      u32x4 cv[4];
#pragma unroll
      for (int ii = 0; ii < 4; ++ii) asm volatile("" : "=v"(cv[ii]));
      auto chkrd = [&](int t) {
#pragma unroll
        for (int ii = 0; ii < 4; ++ii) cv[ii] = lds_ld<u32x4>(aslot(t) + lin + ii * 4096);
      };
      auto chkbad = [&]() { return __any(max(umax4v(umax4v(0u, cv[0]), cv[1]), umax4v(umax4v(0u, cv[2]), cv[3])) == SENT) && !l0f; };
      auto refetch = [&](int t) __attribute__((always_inline)) {
        for (unsigned spins = 0; !dead; ++spins) {
          if (spins > SPIN_CAP) { dead = true; break; }
          WAITV(0);
          ldA(t);
          ldA(t + 1);
          WAITV(0);
          chkrd(t);
          if (!chkbad()) break;
          __builtin_amdgcn_s_sleep(1);
        }
      };
      f32x4 jc[4];
#pragma unroll
      for (int i = 0; i < 4; ++i) asm volatile("" : "=v"(jc[i]));
      auto csld = [&]() {
#pragma unroll
        for (int i = 0; i < 4; ++i)
          ld16_asm(jc[i], rs, (lane >> 4) * 768 + hw * 192 + ((lane & 15) < 12 ? (lane & 15) : 0) * 16, WS_CS + (unsigned)((l * 16 + j) * 16 + i * 4) * 768);
      };
      auto csred = [&]() {
#pragma unroll
        for (int i = 0; i < 4; ++i) OPAQUE_V(jc[i]);
        const f32x4 s = (jc[0] + jc[1]) + (jc[2] + jc[3]);
        auto r = SWAP16(s[0], s[1]), q = SWAP16(s[2], s[3]);
        r = SWAP32(__uint_as_float(r[0]) + __uint_as_float(r[1]), __uint_as_float(q[0]) + __uint_as_float(q[1]));
        if ((lane & 15) < 12) lds_st<float>(lds0 + L_CS + l * 768 + hw * 192 + (lane & 15) * 16 + (lane >> 4) * 4, __uint_as_float(r[0]) + __uint_as_float(r[1]));
      };
      if (l0f) {
        u32x2 gw2;
        ld8_asm(gw2, rs, WS_EPOCH + 0u * lin);
        BAR;
        ldW(0); ldA(0);
        dma1(mk_rsrc(p.wphi, 32 * 2048), vlin, j * 4096, lds0 + L_WPHI + hw * 1024);
        ldW(1);
        WAITV(17);
        OPAQUE_V(gw2);
        const u64 gword = ((u64)(unsigned)UNI(gw2[1]) << 32) | (unsigned)UNI(gw2[0]);
        u64 z = gword + 0x9e3779b97f4a7c15ull;
        z = (z ^ (z >> 30)) * 0xbf58476d1ce4e5b9ull;
        z = (z ^ (z >> 27)) * 0x94d049bb133111ebull;
        z = z ^ (z >> 31);
        z = (z == gword) ? ~z : z;
        epoch = u32x2{(unsigned)z, (unsigned)(z >> 32)};
        tr = (j - kb) & 15;
        WAITV(7);
        BAR;
        ldA(1);
      } else {
        ldW(1);
        if (l == 1) csld();
        else __builtin_amdgcn_s_sleep(XD);
        ldA(1);
      }
      ldA(2);
      ldW(2);
#pragma unroll
      for (int i = 0; i < 4; ++i) asm volatile("" : "=v"(xr[i >> 1][i & 1]));
      unsigned oA = offA(3), oW = 0;
      OPAQUE_S(oA);
      ra0 = lds0 + L_R2 + (hw * 32 + (lane & 15)) * 128 + (((((lane >> 4) & 1) * 2 + (lane >> 5)) ^ ((lane >> 1) & 7)) * 16);
      OPAQUE_V(ra0);
      ldres(0);
      clean(0);
      if (l == 1) { WAITV(14); csred(); }
      WAITV(10);
      chkrd(1);
      if (__builtin_expect(chkbad(), 0)) refetch(1);
      const int fr = lane & 15, fq = lane >> 4;
      u32x2 fl;
      f32x4 js[4];
      float rb[4];
      const bool slowo = SLOWC(2 + (l & 1), j);
      const char* pO;
      unsigned zO, bO, pcO, vO, clo;
      u32x4 wo2[2];
      asm volatile("" : "=v"(wo2[0])); asm volatile("" : "=v"(wo2[1]));
      const unsigned fwo = myrep + fl_line(FL_WO + (isb ? 1 : 0), jr + (lane >> 4)) + (lane & 15) * 8;
      static_for<1, 14>([&](auto t_c) __attribute__((always_inline)) {
        constexpr int t = decltype(t_c)::value;
        constexpr auto rest = [](int s) { return 6 + (s == NVAL - 2 || s == 5 || s == 11) + (s == 8 ? 4 : 0); };
        constexpr int NW = t == 1 ? 63 : 4 + rest(t - 1);
        if (t >= NVAL && t <= 6 && __builtin_expect((ready & (1u << kseq(t + 2))) == 0, 0)) {
          WAITV(NW); WAITL0; BAR;
          ready = poll_need(rs, flo, epoch, ready, 1u << kseq(t + 2), dead);
          dma4<4096>(rA, vA, oA, pcA, aslot(t + 2));
        } else if (t == 7 && __builtin_expect(ready != ~0u, 0)) {
          WAITV(NW); WAITL0; BAR;
          poll_all(rs, flo, epoch, dead);
          dma4<4096>(rA, vA, oA, pcA, aslot(t + 2));
        } else {
          bar_dma4<4096, NW>(rA, vA, oA, pcA, aslot(t + 2));
        }
        if (t == 1 && lead) set_flags(rs, fl_line(FL_X + l, g), j, l0f ? fl_line(FL_CS, j) : fl_line(FL_X + l, g), l0f ? g : j, epoch, lane);
        if (t == 7 && slowj) packedW();
        oW = offW(t + 2);
        if (t < 13) oA = offA(t + 3);
        OPAQUE_S(oA); OPAQUE_S(oW);
        clean(t);
        if (t <= NVAL && !l0f) { WAITV(rest(t - 1) + 4); chkrd(t + 1); }
        if (t == WT + 3 && l0f && SLOWC(2, jr + hw)) {
          OPAQUE_V(wo2[0]); OPAQUE_V(wo2[1]);
#pragma unroll
          for (int i = 0; i < 2; ++i) st16_sc1(rs, lin, WS_WO + (unsigned)((jr + hw) * 131072 + g * 8192 + ((j & 3) * 2 + i) * 1024), wo2[i]);
        }
        if (t == WT + 6 && l0f && lead) set_flags(rs, fl_line(FL_WO, j), g, fl_line(FL_WO, j), g, epoch, lane);
        ldres(t);
        if (t == 8) {
#pragma unroll
          for (int i = 0; i < 4; ++i) ld16_asm(js[i], rs, fq * 1024 + hw * 256 + fr * 16, st_off(g, l) + i * 4096);
        }
        SCHED;
        ldWh(t + 2, oW, 0);
        SCHED;
        bool bad = false;
        if (t <= NVAL) bad = chkbad();
        if (t == 1) {
          const unsigned fx = fl_line(FL_X + l, g), fc = fl_line(FL_CS, jr) + ((j + fq) & 3) * 128;
          flo = myrep + ((l0f && lane >= 16) ? fc : fx) + fr * 8;
          OPAQUE_V(flo);
        }
        if (t == 9) {
          pO = slowo ? p.ws : (const char*)(isb ? p.woB : p.woA);
          zO = slowo ? WS_SPAN : 1024 * 2048; bO = slowo ? WS_WO + (unsigned)((isb ? 16 : 0) + j) * 131072 : j * 128; pcO = slowo ? 4096 : 32 * 2048;
          vO = slowo ? vlin : (hw * 8 + (lane >> 3)) * 2048 + wo_swz(lane);
          clo = myrep + ((lane & 16) ? fl_line(FL_CLC + l, (lane & 32) ? j ^ RX : j) : fl_line(FL_CLR + l, g)) + fr * 8;
          OPAQUE_S(pO); OPAQUE_S(zO); OPAQUE_S(bO); OPAQUE_S(pcO); OPAQUE_V(vO); OPAQUE_V(clo);
        }
        if (t == 10 && l0f) csred();
        if (t == 11) {
#pragma unroll
          for (int i = 0; i < 4; ++i) OPAQUE_V(js[i]);
          const f32x4 s = (js[0] + js[1]) + (js[2] + js[3]);
          const auto r = SWAP16(s[0], s[2]), q = SWAP16(s[1], s[3]);
          rb[0] = __uint_as_float(r[0]) + __uint_as_float(r[1]); rb[1] = __uint_as_float(q[0]) + __uint_as_float(q[1]);
          OPAQUE_V(rb[0]); OPAQUE_V(rb[1]);
        }
        if (t == 12) {
          const auto r = SWAP32(rb[0], rb[0]), q = SWAP32(rb[1], rb[1]);
          const float m = (__uint_as_float(r[0]) + __uint_as_float(r[1])) * (-1.0f / 1024), q2 = __uint_as_float(q[0]) + __uint_as_float(q[1]);
          const float rstd = __builtin_amdgcn_rsqf(fmaxf(__builtin_fmaf(q2, 1.0f / 1024, -m * m), 0.f) + 1e-5f);
          lds_st<float>(lds0 + L_MEAN + 64 + hw * 128 + fr * 8 + (fq & 1) * 4 + (fq >> 1) * (L_RSTD - L_MEAN), (lane & 32) ? rstd : m);
        }
        SCHED;
        ldWh(t + 2, oW, 1);
        if (t == NVAL - 1 || t == 6) {
          WAITV(10);
          OPAQUE_V(fl);
          if (t == 6) ready = __all(is_set(fl, epoch)) ? ~0u : 0u;
          else {
            ready = l0f ? ~0u : (unsigned)__ballot(is_set(fl, epoch));
            if (slowj && __all(is_set(fl, epoch))) packedW();
          }
        }
        if (t == 12) { WAITV(10); OPAQUE_V(fl); }
        if (t == 13 && slowo && __builtin_expect(!__all(is_set(fl, epoch)), 0)) poll_all(rs, fwo, epoch, dead);
        if (t == NVAL - 2 || t == 5) ld8_asm(fl, rs, flo);
        if (t == 11) ld8_asm(fl, rs, fwo);
        if (t == 7 && l0f) csld();
        if (t == WT && l0f) {
#pragma unroll
          for (int i = 0; i < 2; ++i) ld16in_asm(wo2[i], mk_rsrc(p.woA, 1024 * 2048), (lane >> 3) * 2048 + wo_swz(lane), (g * 64 + (j & 3) * 16 + i * 8) * 2048 + (jr + hw) * 128);
        }
        if constexpr (t <= NVAL) { if (__builtin_expect(bad, 0)) refetch(t + 1); }
      });
      auto stO = [&](int i) { dma4<4096>(mk_rsrc(pO, zO), vO, bO + wo_src(i, j) * (4 * pcO), pcO, lds0 + wo_slot(i) + hw * 1024); };
      auto clcheck = [&]() {
        OPAQUE_V(fl);
        if (__builtin_expect(!__all(is_set(fl, epoch)), 0)) poll_all(rs, clo, epoch, dead);
      };
      auto accinit = [&]() {
#pragma unroll
        for (int m = 0; m < 2; ++m)
#pragma unroll
          for (int pp = 0; pp < 2; ++pp) {
            auto r0_ = __builtin_amdgcn_permlane16_swap(xr[m][pp][0], xr[m][pp][2], false, false);
            auto r1_ = __builtin_amdgcn_permlane16_swap(xr[m][pp][1], xr[m][pp][3], false, false);
            acc[m][2 * pp] = f32x4{lo16(r0_[0]), hi16(r0_[0]), lo16(r1_[0]), hi16(r1_[0])};
            acc[m][2 * pp + 1] = f32x4{lo16(r0_[1]), hi16(r0_[1]), lo16(r1_[1]), hi16(r1_[1])};
          }
#pragma unroll
        for (int i = 0; i < 8; ++i) OPAQUE_V(acc[i >> 2][i & 3]);
      };
      WAITV(10);
      WAITL0;
      BAR;
      if (lead) set_flags(rs, fl_line(FL_CLR + l, g), j, fl_line(FL_CLC + l, j), g, epoch, lane);
      if (isb) {
        stO(0); stO(2); stO(3);
        WAITV(12);
        BAR;
        stO(1); stO(4); stO(5);
        __builtin_amdgcn_s_setprio(0);
        BAR;
        __builtin_amdgcn_s_sleep(BD);
        stO(7); stO(6);
        ld8_asm(fl, rs, clo);
        accinit();
        WAITV(0);
        clcheck();
      } else {
        u32x4 vb[8];
#pragma unroll
        for (int i = 0; i < 8; ++i) asm volatile("" : "=v"(vb[i]));
        stO(0); stO(2); stO(3);
        ldres(14);
        WAITV(12);
        WAITL0;
        BAR;
        stO(1);
        ldres(15);
        if (l0f) {
          const bool wj = hw != 0;
          const char* bp = (const char*)(wj ? p.wqkvB : p.woB);
          unsigned rs_ = wj ? 6144 : 2048, sz = rs_ * 1024, ss = rs_ * 8, s2 = wj ? 16 * 6144 : 128;
          unsigned sb = g * 64 * rs_ + (wj ? (hw - 1) * 2048 + JOBC(j, hw - 1) * 128 : (j & 3) * (16 * 2048) + jr * 128);
          OPAQUE_S(bp); OPAQUE_S(rs_); OPAQUE_S(sz); OPAQUE_S(sb); OPAQUE_S(ss); OPAQUE_S(s2);
          const rsrc_t rb = mk_rsrc(bp, sz);
#pragma unroll
          for (int i = 0; i < 8; ++i) ld16in_asm(vb[i], rb, (lane >> 3) * rs_ + (lane & 7) * 16, sb + (i & 1) * ss + (i >> 1) * s2);
        }
        __builtin_amdgcn_s_setprio(0);
        KV_ADDR;
        bf16x8 sel = rowsum_sel(lane);
        unsigned h1 = (lane >> 3) * KVP_G + (lane & 7) * 16;
        unsigned pw = lds0 + SC_PHIK + (h * 128 + wm * 64 + 32 + fr) * 160 + fq * 8, kw = lds0 + L_PREV + hw * 2048 + lin;
        const unsigned wa = lds0 + L_WPHI + h * 2048 + (fq * 4 + (fr >> 2)) * 64 + (lane & 3) * 8;
        bf16x8 w0 = tr2(wa, wa + 1024), w1 = tr2(wa + 32, wa + 32 + 1024);
        float cq[4];
#pragma unroll
        for (int m = 0; m < 2; ++m) { cq[m] = lds_ld<float>(lds0 + L_RSTD + (16 + wm * 64 + 32 + m * 16 + fr) * 4) * 1.4426950408889634f; OPAQUE_V(cq[m]); }
        OPAQUE_V(sel); OPAQUE_V(h1); OPAQUE_V(pw); OPAQUE_V(kw); OPAQUE_V(w0); OPAQUE_V(w1);
        BAR;
        CLOBBER;
        {
          const f32x4 zero = {0.f, 0.f, 0.f, 0.f};
          f32x4 r[4][2];
#pragma unroll
          for (int m = 0; m < 2; ++m) {
            const bf16x8 f = lds_ld<bf16x8>(kw + m * 1024);
            r[m][0] = MFMA(w0, f, zero); r[m][1] = MFMA(w1, f, zero);
          }
          ld8_asm(fl, rs, clo);
          phik2(r, cq, pw);
        }
        WAITL0;
        BAR;
        kvpart(va, pa, pb, kvj + (unsigned)g * KVP_G, lane, wave);
        WAITV(2);
#pragma unroll
        for (int i = 0; i < 8; ++i) OPAQUE_V(vb[i]);
        clcheck();
        BAR;
        __builtin_amdgcn_s_setprio(3);
        stO(4); stO(5);
        const f32x4 zero = {0.f, 0.f, 0.f, 0.f};
        const unsigned s1 = kvj + (unsigned)((j ^ RX) - j) * KVP_J + g * 512 + hw * 128;
        __builtin_amdgcn_s_sleep(H1S);
        u32x4 v[2] = {ld16_sc1(rs, h1, s1), ld16_sc1(rs, h1, s1 + 8 * KVP_G)};
        for (unsigned spins = 0; !dead && __any(umax4v(umax4v(0u, v[0]), v[1]) == SENT); ++spins) {
          if (spins > SPIN_CAP) { dead = true; break; }
          CLOBBER;
          v[0] = ld16_sc1(rs, h1, s1); v[1] = ld16_sc1(rs, h1, s1 + 8 * KVP_G);
        }
        const f32x4 c = rowsum_fin(MFMA(sel, as_frag(v[1]), MFMA(sel, as_frag(v[0]), zero)));
        if ((lane & 40) == 0)
          st8_sc1(rs, fr * 16 + fq * 8, kvj + (unsigned)g * KVP_G + KVP_SUM + hw * 128, nosent2(c));
        CLOBBER;
        __builtin_amdgcn_s_setprio(0);
        BAR;
        SCHED;
        accinit();
        stO(6); stO(7);
        if (l0f) {
          if (hw) {
            job_colsum(rs, vb, 1, g, JOBC(j, hw - 1), hw - 1, lane);
            if (SLOWC(1, JOBC(j, hw - 1))) {
#pragma unroll
              for (int i = 0; i < 8; ++i) job_pack(rs, vb[i], i, 1, g, JOBC(j, hw - 1), hw - 1, lane);
            }
          } else {
#pragma unroll
            for (int i = 0; i < 8; ++i)
              if (SLOWC(3, jr + (i >> 1))) st16_sc1(rs, (lane >> 3) * 128 + wo_swz(lane), WS_WO + (unsigned)((16 + jr + (i >> 1)) * 131072 + g * 8192 + ((j & 3) * 2 + (i & 1)) * 1024), vb[i]);
          }
        }
        WAITV(0);
      }
    }
    {
      LOCALS;
      const int fr = lane & 15, fq = lane >> 4;
      const int kh = half;
      unsigned v0 = lane * 16 + hw * 4096 + kh * 2048, v1 = lane * 16 + hw * 4096 + (kh ^ 1) * 2048;
      const int r0 = (JROT(j) >> 1) + NJ / 2;
      const unsigned ob0 = ob_off(g, kh, l);
      const unsigned pko = myrep + fl_line(FL_PK, JOBC(j, fq - 1)) + fr * 8;
      u32x4 R[8][4];
      u32x2 pkf;
      auto issue_ = [&](int i) {
        const unsigned so = ob0 + (unsigned)((r0 + i) & 7) * (2 * TS);
        R[i][0] = ld16_sc1(rs, v0, so); R[i][1] = ld16_sc1(rs, v1, so);
        R[i][2] = ld16_sc1(rs, v0 + 1024, so); R[i][3] = ld16_sc1(rs, v1 + 1024, so);
      };
      auto issue = [&](int i) { issue_(i); SCHED; };
      constexpr int LA = LOOKAHEAD;
      __builtin_amdgcn_s_setprio(3);
      unsigned so0 = ob0 + (unsigned)(r0 & 7) * (2 * TS);
      OPAQUE_V(v0); OPAQUE_V(v1); OPAQUE_S(so0);
      BAR;
      SCHED;
      R[0][0] = ld16_sc1(rs, v0, so0); R[0][1] = ld16_sc1(rs, v1, so0);
      R[0][2] = ld16_sc1(rs, v0 + 1024, so0); R[0][3] = ld16_sc1(rs, v1 + 1024, so0);
      SCHED;
      __builtin_amdgcn_s_setprio(2);
      if (l == 0 && lead) set_flags(rs, fl_line(FL_PK, j), g, fl_line(FL_WO + 1, j), g, epoch, lane);

      auto vmax = [&](int i) { return umax4v(umax4v(umax4v(umax4v(0u, R[i][0]), R[i][1]), R[i][2]), R[i][3]); };
      auto bad = [&](int i) { return __any(umax4v(umax4v(umax4v(umax4v(0u, R[i][0]), R[i][1]), R[i][2]), R[i][3]) == SENT); };
      auto poll = [&](int i, int nfl) {
        for (unsigned spins = 0; !dead; ++spins) {
          if (spins > SPIN_CAP) { dead = true; break; }
          __builtin_amdgcn_s_sleep(2);
          CLOBBER;
          issue(i);
#pragma unroll
          for (int k = i + 1; k < i + nfl && k < 8; ++k)
            if (bad(k)) issue(k);
          WAITV(0);
#pragma unroll
          for (int k = i + 1; k < i + nfl && k < 8; ++k)
#pragma unroll
            for (int f = 0; f < 4; ++f) OPAQUE_V(R[k][f]);
          if (!bad(i)) break;
        }
      };
      unsigned bb[4];
      {
        const int kr = fq * 4 + (fr >> 2);
        const int gs = (kr >> 1) & 3;
#pragma unroll
        for (int c = 0; c < 4; ++c) bb[c] = lds0 + kh * 8192 + kr * 128 + ((c ^ gs) * 32) + (lane & 3) * 8;
      }
      bf16x8 bf[2][4];
      auto rdB = [&](int i, int hh) {
        if (hh == 0) {
#pragma unroll
          for (int c = 0; c < 4; ++c) { bb[c] += wo_slot(i) - (i ? wo_slot(i - 1) : 0u); asm("" : "+v"(bb[c])); }
        }
#pragma unroll
        for (int c = 0; c < 4; ++c) bf[hh][c] = tr2(bb[c] + hh * 4096, bb[c] + hh * 4096 + 2048);
      };
      auto mms = [&](int i, int hh) {
#pragma unroll
        for (int n = 0; n < 4; ++n)
#pragma unroll
          for (int m = 0; m < 2; ++m) acc[m][n] = MFMA(bf[hh][n], as_frag(R[i][hh * 2 + m]), acc[m][n]);
      };
      if (!half) {
#pragma unroll
        for (int i = 0; i < 8; ++i) acc[i >> 2][i & 3] = f32x4{0.f, 0.f, 0.f, 0.f};
      }
      const char* obase = (l == 3) ? (const char*)p.out : (const char*)p.ws;
      unsigned osize = (l == 3) ? 2048 * 2048 : WS_SPAN;
      const bool slown = SLOWC((l & 1) ^ 1, j);
      pW = slown ? p.ws : (const char*)(isb ? p.wqkvA : p.wqkvB);
      bW = slown ? WS_WP + (unsigned)((isb ? 0 : 16) + j) * (16 * 24576) : j * 128;
      kW = slown ? 24576 : 64 * 6144; zW = slown ? WS_SPAN : 1024 * 6144; pcW = slown ? 8192 : 2048; hW = slown ? 4096 : 32 * 6144;
      vA = hw * 1024 + lane * 16;
      vW = slown ? vA : (hw * 8 + (lane >> 3)) * 6144 + (((lane & 7) ^ ((((lane >> 4) & 1) | ((hw & 1) << 1)) << 1)) * 16);
      pA = p.ws; zA = WS_SPAN; bA = xb_off(g, l); kA = TS; pcA = 4096; kb = JROT(j) + KX; dj = (j - kb) & 15;
      OPAQUE_S(pW); OPAQUE_S(bW); OPAQUE_S(kW); OPAQUE_S(zW); OPAQUE_S(pcW); OPAQUE_S(hW); OPAQUE_V(vA); OPAQUE_V(vW); OPAQUE_S(bA);
      unsigned sq = bW + j * kW + kh * hW, dst = lds0 + L_R1 + kh * 4096 + hw * 1024;
      OPAQUE_S(obase); OPAQUE_S(osize); OPAQUE_S(sq); OPAQUE_S(dst);
      const rsrc_t ro = mk_rsrc(obase, osize);
      unsigned so[2];
      unsigned xw = lds0 + SC_XCH + wave * 4096 + lane * 16;
      unsigned xa = lds0 + SC_XCH + (wave ^ 4) * 4096 + lane * 16;
      unsigned sb0 = (l == 3) ? g * (128 * 2048) + j * 128 : xb_off(g, l) + j * TS;
      {
        const int row = (hw * 2 + kh) * 16 + fr, ch = (fq & 1) * 2 + (fq >> 1);
        const unsigned sw = (l == 3) ? 0u : (unsigned)((row >> 1) & 7), rb = (unsigned)row << ((l == 3) ? 11 : 7);
#pragma unroll
        for (int pp = 0; pp < 2; ++pp) { so[pp] = rb + (((ch + 4 * pp) ^ sw) * 16); OPAQUE_V(so[pp]); }
        OPAQUE_V(xa); OPAQUE_V(xw); OPAQUE_S(sb0);
      }
      SCHED;
#pragma unroll
      for (int i = 1; i < LA; ++i) issue(i);
      rdB(0, 0);
      unsigned mx = vmax(0);
#pragma unroll
      for (int i = 0; i < 8; ++i) {
        SCHED;
        if (__builtin_expect(__any(mx == SENT), 0)) poll(i, LA);
        if (i == W1S - 2) { pkf = ld8_sc1(rs, pko, 0); SCHED; }
        if (i == W1S) {
          OPAQUE_V(pkf);
          if (l == 0 && lead && !__all(is_set(pkf, epoch))) poll_all(rs, pko, epoch, dead);
          BAR;
        }
        if (i == 7 && l < 3) dma3<8192>(mk_rsrc(pW, zW), vW, sq, pcW, dst);
        if (i < 4 && !isb) __builtin_amdgcn_s_sleep(PACE);
        if (i == 2) __builtin_amdgcn_s_setprio(1);
        if (i == 5) __builtin_amdgcn_s_setprio(0);
        SCHED;
        rdB(i, 1);
        if (i == 7) break;
        if (i + LA < 8) issue_(i + LA);
        mms(i, 0);
#pragma unroll
        for (int k = 0; k < 4; ++k) { SGB(0x008, 1); SGB(0x100, 1); SGB(0x020, 1); SGB(0x008, 1); SGB(0x100, 1); }
        SCHED;
        rdB(i + 1, 0); mx = vmax(i + 1);
        mms(i, 1);
#pragma unroll
        for (int k = 0; k < 4; ++k) { SGB(0x008, 1); SGB(0x100, 2); }
#pragma unroll
        for (int k = 4; k < 8; ++k) { SGB(0x008, 1); SGB(0x002, 8 / (8 - 4)); }
      }
#pragma unroll
      for (int n = 0; n < 4; ++n) acc[1][n] = MFMA(bf[0][n], as_frag(R[7][1]), acc[1][n]);
#pragma unroll
      for (int n = 0; n < 4; ++n) acc[0][n] = MFMA(bf[0][n], as_frag(R[7][0]), acc[0][n]);
#pragma unroll
      for (int k = 0; k < 8; ++k) { SGB(0x008, 1); SGB(0x100, 1); }
      SCHED;
#pragma unroll
      for (int n = 0; n < 4; ++n) acc[1][n] = MFMA(bf[1][n], as_frag(R[7][3]), acc[1][n]);
#pragma unroll
      for (int n = 0; n < 4; ++n) acc[0][n] = MFMA(bf[1][n], as_frag(R[7][2]), acc[0][n]);
#pragma unroll
      for (int n = 0; n < 4; ++n) lds_st<f32x4>(xw + n * 1024, acc[1][n]);
      SGB(0x008, 4);
#pragma unroll
      for (int k = 0; k < 4; ++k) { SGB(0x008, 1); SGB(0x200, 1); }
      SCHED;
      WAITL0;
      BAR;
      SCHED;
      f32x4 pa[4];
#pragma unroll
      for (int n = 0; n < 4; ++n) pa[n] = lds_ld<f32x4>(xa + n * 1024);
      SCHED;
      u32x4 o[2];
#pragma unroll
      for (int pp = 0; pp < 2; ++pp) {
        const f32x4 a = acc[0][2 * pp] + pa[2 * pp], b = acc[0][2 * pp + 1] + pa[2 * pp + 1];
        auto r0_ = __builtin_amdgcn_permlane16_swap(pk2(a[0], a[1]), pk2(b[0], b[1]), false, false);
        auto r1_ = __builtin_amdgcn_permlane16_swap(pk2(a[2], a[3]), pk2(b[2], b[3]), false, false);
        o[pp] = nosent(u32x4{r0_[0], r1_[0], r0_[1], r1_[1]});
        st16_sc1(ro, so[pp], sb0, o[pp]);
      }
      if (l < 3) {
        lds_st<u32x4>(lds0 + L_R2 + so[0], o[0]);
        lds_st<u32x4>(lds0 + L_R2 + so[1], o[1]);
        const f32x4 zero = {0.f, 0.f, 0.f, 0.f};
        const bf16x8 ones = {0x3f80, 0x3f80, 0x3f80, 0x3f80, 0x3f80, 0x3f80, 0x3f80, 0x3f80};
        const f32x4 a = MFMA(ones, as_frag(o[1]), MFMA(ones, as_frag(o[0]), zero));
        const f32x4 b = MFMA(as_frag(o[1]), as_frag(o[1]), MFMA(as_frag(o[0]), as_frag(o[0]), zero));
        const float q = (fr & 2) ? ((fr & 1) ? b[3] : b[2]) : ((fr & 1) ? b[1] : b[0]);
        if (fq == (fr >> 2)) st8_sc1(rs, ((hw * 2 + kh) * 16 + fr) * 8, st_off(g, l + 1) + j * 1024, u32x2{__float_as_uint(a[0]), __float_as_uint(q)});
      } else if (lead && blockIdx.x == 0 && lane == 0) {
        st8_sc1(rs, WS_EPOCH, 0, epoch);
      }
    }
  }
  asm volatile(".Lpf_end:");
}

extern "C" void kernel_launch(void* const* d_in, const int* in_sizes, int n_in, void* d_out, int out_size,
                              void* d_ws, size_t ws_size, hipStream_t stream) {
  Params p;
  p.x = (const u16*)d_in[0];
  p.wqkvA = (const u16*)d_in[1];
  p.wphi = (const u16*)d_in[2];
  p.woA = (const u16*)d_in[3];
  p.wqkvB = (const u16*)d_in[5];
  p.woB = (const u16*)d_in[6];
  p.out = (u16*)d_out;
  p.ws = (char*)d_ws;
  (void)hipFuncSetAttribute((const void*)mega_kernel, hipFuncAttributeMaxDynamicSharedMemorySize, L_TOTAL);
  mega_kernel<<<256, 512, L_TOTAL, stream>>>(p);
}
```

```cpp
#include <hip/hip_runtime.h>
#include <stdint.h>
#include <type_traits>
constexpr int LOOKAHEAD = 2;
constexpr int NVAL = 4;
constexpr int WT = 1;
constexpr int W1S = 6;
constexpr int NJ = 8;
constexpr int KX = 8;
constexpr int RX = 8;
constexpr unsigned TS = 17664;
#define FT 1
#define XD 3
#define H1S 6
#define H2D 5
#define BD 8
#define PACE 4

typedef unsigned short u16;
typedef unsigned long long u64;
typedef __attribute__((ext_vector_type(8))) short bf16x8;
typedef __attribute__((ext_vector_type(4))) short s16x4;
typedef __attribute__((ext_vector_type(4))) float f32x4;
typedef __attribute__((ext_vector_type(2))) float f32x2;
typedef __attribute__((ext_vector_type(4))) unsigned u32x4;
typedef __attribute__((ext_vector_type(2))) unsigned u32x2;
typedef __attribute__((ext_vector_type(2))) __bf16 bf16x2_t;
#define LAS __attribute__((address_space(3)))
typedef LAS s16x4* lds_s16x4_ptr;

constexpr unsigned NREP = 8, REP_STRIDE = 17 * 2048;
enum { FL_X = 0, FL_CS = 4, FL_CLR = 5, FL_CLC = 9, FL_PK = 13, FL_WO = 14 };
__device__ __forceinline__ unsigned fl_line(int set, int a) { return (unsigned)(set * 16 + a) * 128; }
constexpr unsigned WS_EPOCH = 512u << 10;
constexpr unsigned WS_CS = 1u << 20;
constexpr unsigned WS_KVP = 4u << 20;
constexpr unsigned KVP_G = 32768, KVP_J = 16 * KVP_G, KVP_L = 16384, KVP_Z = 8192, KVP_SUM = 8704, KVP_HALO = 12u << 10;
constexpr unsigned SENT = 0xffffffffu;
constexpr unsigned WS_X = 14u << 20, WS_O = 29u << 20, WS_ST = 2u << 20;
__device__ __forceinline__ unsigned xb_off(int g, int l) { return WS_X + (unsigned)(l * 16 + g) * (16 * TS); }
__device__ __forceinline__ unsigned ob_off(int g, int j, int l) { return WS_O + (unsigned)((l * 16 + g) * 16 + j) * TS; }
__device__ __forceinline__ unsigned st_off(int g, int l) { return WS_ST + (unsigned)(l * 16 + g) * (16u << 10); }
constexpr unsigned WS_WP = 48u << 20;
constexpr unsigned WS_WO = 60u << 20;
constexpr unsigned WS_SPAN = 64u << 20;

constexpr unsigned L_R1 = 0;
constexpr unsigned L_R2 = 98304, XS = 16384;
constexpr unsigned L_MISC = 147456;
constexpr unsigned L_MEAN = L_MISC;
constexpr unsigned L_RSTD = L_MISC + 576;
constexpr unsigned L_CS = L_MISC + 1152;
constexpr unsigned L_WPHI = L_MISC + 2688;
constexpr unsigned L_SINK = L_MISC + 6912;
constexpr unsigned L_ZF = L_MISC + 7168;
constexpr unsigned L_PREV = L_MISC + 8192;
constexpr unsigned L_TOTAL = 163840;

#define JROT(j) ((j) & ~(NJ - 1))
constexpr int NX = NJ / 2;
#define KB0(g, j) (JROT(j) + (16 / NX) * ((g) & (NX - 1)))
#define JOBC(j, w) (((j) & ~3) + (((j) + (w) + 1) & 3))

#define SLOWC(m, jc) ((slowm >> ((m) * 8 + ((jc) & 7))) & 1)

struct Params {
  const u16* x;
  const u16* wqkvA;
  const u16* wphi;
  const u16* woA;
  const u16* wqkvB;
  const u16* woB;
  u16* out;
  char* ws;
};

#define SCHED __builtin_amdgcn_sched_barrier(0)
#define BAR __builtin_amdgcn_s_barrier()
#define SGB(m, n) __builtin_amdgcn_sched_group_barrier(m, n, 0)
#define WAITV(n) asm volatile("s_waitcnt vmcnt(%0)" ::"n"(n) : "memory")
#define WAITL0 asm volatile("s_waitcnt lgkmcnt(0)" ::: "memory")

#define TRV(slot, val) do {} while (0)
#define TRQ(slot) do {} while (0)
#define TRC(slot) do {} while (0)
#define TRR(slot) do {} while (0)
#define STAMP_INIT do {} while (0)
#define STAMP_DUMP do {} while (0)

__device__ __forceinline__ unsigned pk2(float a, float b) {
  f32x2 v = {a, b};
  return __builtin_bit_cast(unsigned, __builtin_convertvector(v, bf16x2_t));
}
__device__ __forceinline__ float lo16(unsigned u) { return __uint_as_float(u << 16); }
__device__ __forceinline__ float hi16(unsigned u) { return __uint_as_float(u & 0xffff0000u); }

template <int A, int B, typename F> __device__ __forceinline__ void static_for(F f) {
  if constexpr (A < B) { f(std::integral_constant<int, A>{}); static_for<A + 1, B>(f); }
}
typedef __amdgpu_buffer_rsrc_t rsrc_t;
__device__ __forceinline__ rsrc_t mk_rsrc(const void* p, unsigned bytes) { return __builtin_amdgcn_make_buffer_rsrc((void*)p, 0, bytes, 0x00020000); }
#define UNI(x) __builtin_amdgcn_readfirstlane(x)
#define DMA_HEAD "s_mov_b32 m0, %4\n\ts_mov_b32 %0, %3\n\ts_nop 2\n\tbuffer_load_dwordx4 %1, %2, %0 offen sc1 lds\n\t"
#define DMA_NEXT "s_add_u32 m0, m0, %6\n\ts_add_u32 %0, %0, %5\n\tbuffer_load_dwordx4 %1, %2, %0 offen sc1 lds\n\t"
#define DMA_OPS : "=&s"(t) : "v"(voff), "s"(r), "s"(UNI(soff)), "s"(UNI(lds)), "s"(UNI(sstep)), "i"(LSTEP) : "memory", "scc", "m0"
template <int LSTEP> __device__ __forceinline__ void dma4(rsrc_t r, unsigned voff, unsigned soff, unsigned sstep, unsigned lds) {
  unsigned t;
  asm volatile(DMA_HEAD DMA_NEXT DMA_NEXT DMA_NEXT DMA_OPS);
}
template <int LSTEP> __device__ __forceinline__ void dma3(rsrc_t r, unsigned voff, unsigned soff, unsigned sstep, unsigned lds) {
  unsigned t;
  asm volatile(DMA_HEAD DMA_NEXT DMA_NEXT DMA_OPS);
}
template <int LSTEP, int N> __device__ __forceinline__ void bar_dma4(rsrc_t r, unsigned voff, unsigned soff, unsigned sstep, unsigned lds) {
  unsigned t;
  asm volatile("s_mov_b32 m0, %4\n\ts_mov_b32 %0, %3\n\ts_nop 0\n\ts_waitcnt vmcnt(%7) lgkmcnt(0)\n\ts_barrier\n\t"
               "buffer_load_dwordx4 %1, %2, %0 offen sc1 lds\n\t" DMA_NEXT DMA_NEXT DMA_NEXT
               : "=&s"(t) : "v"(voff), "s"(r), "s"(UNI(soff)), "s"(UNI(lds)), "s"(UNI(sstep)), "i"(LSTEP), "n"(N) : "memory", "scc", "m0");
}
__device__ __forceinline__ void dma1(rsrc_t r, unsigned voff, unsigned soff, unsigned lds) {
  asm volatile("s_mov_b32 m0, %3\n\ts_nop 3\n\tbuffer_load_dwordx4 %0, %1, %2 offen sc1 lds" ::"v"(voff), "s"(r), "s"(UNI(soff)), "s"(UNI(lds)) : "memory", "m0");
}
__device__ __forceinline__ void touch(rsrc_t r, unsigned voff, unsigned lds) {
  asm volatile("s_mov_b32 m0, %2\n\ts_nop 3\n\tbuffer_load_dword %0, %1, 0 offen lds" ::"v"(voff), "s"(r), "s"(UNI(lds)) : "memory", "m0");
}
__device__ __forceinline__ void ld8_asm(u32x2& d, rsrc_t r, unsigned voff) {
  asm volatile("s_nop 4\n\tbuffer_load_dwordx2 %0, %1, %2, 0 offen sc1" : "=v"(d) : "v"(voff), "s"(r) : "memory");
}

__device__ __forceinline__ void ld16in_asm(u32x4& d, rsrc_t r, unsigned voff, unsigned soff) {
  asm volatile("s_nop 4\n\tbuffer_load_dwordx4 %0, %1, %2, %3 offen" : "=v"(d) : "v"(voff), "s"(r), "s"(UNI(soff)) : "memory");
}
__device__ __forceinline__ void ld16_asm(f32x4& d, rsrc_t r, unsigned voff, unsigned soff) {
  asm volatile("s_nop 4\n\tbuffer_load_dwordx4 %0, %1, %2, %3 offen sc1" : "=v"(d) : "v"(voff), "s"(r), "s"(UNI(soff)) : "memory");
}

template <typename T> __device__ __forceinline__ T lds_ld(unsigned a) { return *(LAS T*)(a); }
template <typename T> __device__ __forceinline__ void lds_st(unsigned a, T v) { *(LAS T*)(a) = v; }
__device__ __forceinline__ bf16x8 tr2(unsigned a0, unsigned a1) {
  s16x4 v0 = __builtin_amdgcn_ds_read_tr16_b64_v4i16((lds_s16x4_ptr)(a0));
  s16x4 v1 = __builtin_amdgcn_ds_read_tr16_b64_v4i16((lds_s16x4_ptr)(a1));
  return __builtin_shufflevector(v0, v1, 0, 1, 2, 3, 4, 5, 6, 7);
}
__device__ __forceinline__ bf16x8 as_frag(u32x4 v) { return __builtin_bit_cast(bf16x8, v); }
#define MFMA(a, b, c) __builtin_amdgcn_mfma_f32_16x16x32_bf16(a, b, c, 0, 0, 0)

__device__ __forceinline__ float vmax2(float a, float b) { float inf = __builtin_inff(); asm("" : "+s"(inf)); return __builtin_amdgcn_fmed3f(a, b, inf); }
#define SWAP16(a, b) __builtin_amdgcn_permlane16_swap(__float_as_uint(a), __float_as_uint(b), false, false)
#define SWAP32(a, b) __builtin_amdgcn_permlane32_swap(__float_as_uint(a), __float_as_uint(b), false, false)
__device__ __forceinline__ void red2_max(float& a, float& b) {
  auto r = SWAP16(a, b);
  float x = vmax2(__uint_as_float(r[0]), __uint_as_float(r[1]));
  r = SWAP32(x, x);
  x = vmax2(__uint_as_float(r[0]), __uint_as_float(r[1]));
  r = SWAP16(x, x);
  a = __uint_as_float(r[0]); b = __uint_as_float(r[1]);
}
__device__ __forceinline__ void red2_add(float& a, float& b) {
  auto r = SWAP16(a, b);
  float x = __uint_as_float(r[0]) + __uint_as_float(r[1]);
  r = SWAP32(x, x);
  x = __uint_as_float(r[0]) + __uint_as_float(r[1]);
  r = SWAP16(x, x);
  a = __uint_as_float(r[0]); b = __uint_as_float(r[1]);
}
template <bool MAX, typename M> __device__ __forceinline__ void red4(float& a, float& b, float& c, float& d, M mid) {
  auto op = [](unsigned x, unsigned y) { return MAX ? vmax2(__uint_as_float(x), __uint_as_float(y)) : __uint_as_float(x) + __uint_as_float(y); };
  auto r = SWAP16(a, b), q = SWAP16(c, d);
  const float x = op(r[0], r[1]), y = op(q[0], q[1]);
  r = SWAP32(x, y);
  const float w = mid(op(r[0], r[1]));
  r = SWAP32(w, w);
  q = __builtin_amdgcn_permlane16_swap(r[1], r[1], false, false);
  r = __builtin_amdgcn_permlane16_swap(r[0], r[0], false, false);
  a = __uint_as_float(r[0]); b = __uint_as_float(r[1]); c = __uint_as_float(q[0]); d = __uint_as_float(q[1]);
}
__device__ __forceinline__ float red_max4(float x) {
  auto r = __builtin_amdgcn_permlane16_swap(__float_as_uint(x), __float_as_uint(x), false, false);
  x = vmax2(__uint_as_float(r[0]), __uint_as_float(r[1]));
  r = __builtin_amdgcn_permlane32_swap(__float_as_uint(x), __float_as_uint(x), false, false);
  return vmax2(__uint_as_float(r[0]), __uint_as_float(r[1]));
}
__device__ __forceinline__ float red_add4(float x) {
  auto r = __builtin_amdgcn_permlane16_swap(__float_as_uint(x), __float_as_uint(x), false, false);
  x = __uint_as_float(r[0]) + __uint_as_float(r[1]);
  r = __builtin_amdgcn_permlane32_swap(__float_as_uint(x), __float_as_uint(x), false, false);
  return __uint_as_float(r[0]) + __uint_as_float(r[1]);
}

__device__ __forceinline__ void st16_sc1(rsrc_t r, unsigned voff, unsigned soff, u32x4 v) {
  __builtin_amdgcn_raw_buffer_store_b128(v, r, voff, soff, 16);
  asm volatile("s_nop 1" ::"v"(v) : "memory");
}
__device__ __forceinline__ void st8_sc1(rsrc_t r, unsigned voff, unsigned soff, u32x2 v) { __builtin_amdgcn_raw_buffer_store_b64(v, r, voff, soff, 16); }
__device__ __forceinline__ u32x4 ld16_sc1(rsrc_t r, unsigned voff, unsigned soff) { return __builtin_amdgcn_raw_buffer_load_b128(r, voff, soff, 16); }
__device__ __forceinline__ u32x2 ld8_sc1(rsrc_t r, unsigned voff, unsigned soff) { return __builtin_amdgcn_raw_buffer_load_b64(r, voff, soff, 16); }
__device__ __forceinline__ f32x4 ld4f_sc1(rsrc_t r, unsigned voff, unsigned soff) { return __builtin_bit_cast(f32x4, __builtin_amdgcn_raw_buffer_load_b128(r, voff, soff, 16)); }
#define OPAQUE_V(x) asm volatile("" : "+v"(x))
#define OPAQUE_S(x) asm volatile("" : "+s"(x))

#define CLOBBER asm volatile("" ::: "memory")
constexpr unsigned SPIN_CAP = 1u << 17;
__device__ __forceinline__ bool is_set(u32x2 v, u32x2 epoch) { return v[0] == epoch[0] && v[1] == epoch[1]; }
__device__ __forceinline__ void set_flags(rsrc_t rs, unsigned lineA, int colA, unsigned lineB, int colB, u32x2 epoch, int lane) {
  const unsigned off = (lane & 7) * REP_STRIDE + ((lane & 8) ? lineB + colB * 8 : lineA + colA * 8);
  if (lane < 16) st8_sc1(rs, off, 0, epoch);
}
__device__ __forceinline__ unsigned poll_need(rsrc_t rs, unsigned voff, u32x2 epoch, unsigned ready, unsigned need, bool& dead) {
  for (unsigned spins = 0; !dead; ++spins) {
    CLOBBER;
    ready |= (unsigned)__ballot(is_set(ld8_sc1(rs, voff, 0), epoch));
    if ((ready & need) == need) break;
    if (spins > SPIN_CAP) dead = true;
    __builtin_amdgcn_s_sleep(1);
  }
  CLOBBER;
  return dead ? ~0u : ready;
}

__device__ __forceinline__ void poll_all(rsrc_t rs, unsigned voff, u32x2 epoch, bool& dead) {
  for (unsigned spins = 0; !dead; ++spins) {
    CLOBBER;
    if (__all(is_set(ld8_sc1(rs, voff, 0), epoch))) break;
    if (spins > SPIN_CAP) dead = true;
    __builtin_amdgcn_s_sleep(1);
  }
  CLOBBER;
}

constexpr unsigned SC_VF = L_R1 + 32768;
constexpr unsigned SC_PHIK = L_R1 + 49152;
constexpr unsigned SC_XCH = L_R2 + XS;
constexpr unsigned wo_slot(int s) {
  return s < 2 ? L_R2 + (unsigned)(s + 1) * XS : (s < 6 ? L_R1 + (unsigned)(s - 2) * 16384 : (s == 6 ? L_R1 + 81920 : L_R2));
}
__device__ __forceinline__ unsigned wo_src(int s, int j) { return (unsigned)(((JROT(j) >> 1) + NJ / 2 + s) & 7); }
__device__ __forceinline__ unsigned wo_swz(int lane) { return (unsigned)(((lane & 7) ^ ((lane >> 3) & 6)) * 16); }
__device__ __forceinline__ unsigned umax3(unsigned a, unsigned b, unsigned c) { return max(max(a, b), c); }
__device__ __forceinline__ unsigned umax4v(unsigned m, const u32x4& v) { return umax3(umax3(m, v[0], v[1]), v[2], v[3]); }
__device__ __forceinline__ u32x4 nosent(u32x4 v) { return u32x4{min(v[0], SENT - 1), min(v[1], SENT - 1), min(v[2], SENT - 1), min(v[3], SENT - 1)}; }

template <typename P, typename F>
__device__ __forceinline__ void qkv_loop(int isb, int wm, int h, int lane, bool first, int& trv, P pre, F fin) {
  constexpr unsigned lds0 = 0;
  f32x4 acc[4][6];
  const int fr = lane & 15, fq = lane >> 4;
  unsigned ab[2], bb[2];
  {
    const int r = wm * 64 + fr;
#pragma unroll
    for (int ks = 0; ks < 2; ++ks) ab[ks] = lds0 + L_R2 + r * 128 + (((ks * 4 + fq) ^ ((fr >> 1) & 7)) * 16);
    const int kr = fq * 8 + (fr >> 2);
    const int gs = ((kr >> 1) & 1) | (((kr >> 3) & 1) << 1);
#pragma unroll
    for (int c = 0; c < 2; ++c) bb[c] = lds0 + L_R1 + kr * 128 + (((h * 2 + c) ^ gs) * 32) + (lane & 3) * 8;
  }
#pragma unroll
  for (int m = 0; m < 4; ++m)
#pragma unroll
    for (int n = 0; n < 6; ++n) acc[m][n] = f32x4{0.f, 0.f, 0.f, 0.f};
  bf16x8 af[2][4], bf[2][6];
  auto rds = [&](int ks, int p, int rot = 0) {
#pragma unroll
    for (int m = 0; m < 4; ++m) af[p][m] = lds_ld<bf16x8>(ab[ks] + m * 2048);
#pragma unroll
    for (int nn = 0; nn < 6; ++nn) {
      const int n = (nn + rot) % 6;
      const unsigned a = bb[n & 1] + (n >> 1) * 8192 + ks * 4096;
      bf[p][n] = tr2(a, a + 512);
    }
  };
  auto mms = [&](int p, int rot = 0, int cnt = 6) {
#pragma unroll
    for (int nn = 0; nn < cnt; ++nn)
#pragma unroll
      for (int mm = 0; mm < 4; ++mm) { const int n = (nn + rot) % 6, m = (n & 1) ? 3 - mm : mm; acc[m][n] = n < 4 ? MFMA(bf[p][n], af[p][m], acc[m][n]) : MFMA(af[p][m], bf[p][n], acc[m][n]); }
  };
  auto ilv2 = [&]() {
#pragma unroll
    for (int i = 0; i < 16; ++i) { SGB(0x008, 1); SGB(0x100, 1); }
#pragma unroll
    for (int i = 0; i < 4; ++i) { SGB(0x008, 1); SGB(0x002, 1); }
    SGB(0x008, 4);
  };
  auto ilv3 = [&]() {
#pragma unroll
    for (int i = 0; i < 16; ++i) { SGB(0x008, 1); SGB(0x100, 1); }
#pragma unroll
    for (int i = 0; i < 7; ++i) { SGB(0x008, 1); SGB(0x004, 2); }
    SGB(0x008, 1);
  };
#pragma unroll
  for (int i = 0; i < 24; ++i) OPAQUE_V(acc[i / 6][i % 6]);
  OPAQUE_V(ab[0]); OPAQUE_V(ab[1]); OPAQUE_V(bb[0]); OPAQUE_V(bb[1]);
  if (!first) { WAITV(3); WAITL0; }
  TRQ(1); BAR;
  int tslot = 2;
  rds(0, 0);
  int sa = 0, sb = 0;
  int da = XS, db = 24576;
  auto step = [&]() {
    ab[0] += da; ab[1] += da;
    bb[0] += db; bb[1] += db;
  };
  auto ring = [&]() {
    sa = (sa == 2) ? 0 : sa + 1;
    sb = (sb == 3) ? 0 : sb + 1;
    da = (sa == 2) ? -2 * (int)XS : (int)XS;
    db = (sb == 3) ? -3 * 24576 : 24576;
  };
#pragma unroll 1
  for (int t = 14; t > 0; --t) {
    SCHED;
    rds(1, 1); mms(0);
    step();
    ilv2();
    SCHED;
    __builtin_amdgcn_s_waitcnt(0x0070);
    TRQ(tslot); tslot++; BAR;
    SCHED;
    rds(0, 0);
    ring();
    mms(1); ilv3();
  }
  SCHED;
  f32x4 cs[4], nm4[4];
  float nmean[4], csv[2];
  auto fold = [&](int n) {
#pragma unroll
    for (int mm = 0; mm < 4; ++mm) {
      const int m = (n & 1) ? 3 - mm : mm;
#pragma unroll
      for (int e = 0; e < 4; ++e) {
        float t = n < 4 ? __builtin_fmaf(nmean[m], cs[n & 3][e], acc[m][n][e]) : __builtin_fmaf(nm4[m][e], csv[n & 1], acc[m][n][e]);
        asm("" : "+v"(t));
        acc[m][n][e] = t;
      }
    }
  };
  {
    unsigned pm = lds0 + L_MEAN + (16 + wm * 64 + fr) * 4, pc = lds0 + L_CS + isb * 768 + (h * 32 + fq * 4) * 4;
    unsigned pm4 = lds0 + L_MEAN + (16 + wm * 64 + fq * 4) * 4, pcv = lds0 + L_CS + isb * 768 + (128 + h * 32 + fr) * 4;
    asm("" : "+v"(pm)); asm("" : "+v"(pc)); asm("" : "+v"(pm4)); asm("" : "+v"(pcv));
#pragma unroll
    for (int m = 0; m < 4; ++m) nmean[m] = lds_ld<float>(pm + m * 64);
#pragma unroll
    for (int n = 0; n < 4; ++n) cs[n ^ 2] = lds_ld<f32x4>(pc + (((n ^ 2) >> 1) * 64 + (n & 1) * 16) * 4);
#pragma unroll
    for (int m = 0; m < 4; ++m) nm4[m] = lds_ld<f32x4>(pm4 + m * 64);
#pragma unroll
    for (int n = 0; n < 2; ++n) csv[n] = lds_ld<float>(pcv + n * 64);
    rds(1, 1, 4); mms(0, 2);
    step();
    fold(2); fold(3);
    SGB(0x002, 8);
#pragma unroll
    for (int i = 0; i < 8; ++i) { SGB(0x008, 1); SGB(0x100, 2); }
#pragma unroll
    for (int i = 0; i < 11; ++i) { SGB(0x008, 1); SGB(0x100, 1); SGB(0x002, 2); }
#pragma unroll
    for (int i = 0; i < 5; ++i) { SGB(0x008, 1); SGB(0x002, 3); }
    SCHED;
    __builtin_amdgcn_s_waitcnt(0xC07F);
    TRQ(16); BAR;
    SCHED;
    rds(0, 0, 2);
    mms(1, 4); fold(4); fold(5);
#pragma unroll
    for (int i = 0; i < 8; ++i) { SGB(0x008, 1); SGB(0x100, 1); SGB(0x002, 1); }
#pragma unroll
    for (int i = 0; i < 8; ++i) { SGB(0x008, 1); SGB(0x100, 1); SGB(0x002, 2); }
#pragma unroll
    for (int i = 0; i < 8; ++i) { SGB(0x008, 1); SGB(0x002, 2); }
    SCHED;
    pre(0);
    SCHED;
    rds(1, 1, 2);
    mms(0, 2, 4); fold(0); fold(1);
    pre(1);
#pragma unroll
    for (int i = 0; i < 16; ++i) { SGB(0x008, 1); SGB(0x100, 1); SGB(0x002, 2); SGB(0x004, 1); }
  }
  SCHED;
  auto mm6 = [&](int m, int n0 = 0, int n1 = 8) {
#pragma unroll
    for (int nn = n0; nn < n1; ++nn) {
      const int n = nn < 4 ? nn + 2 : nn & 1, p = (nn >> 1) != 2;
      acc[m][n] = n < 4 ? MFMA(bf[p][n], af[p][m], acc[m][n]) : MFMA(af[p][m], bf[p][n], acc[m][n]);
    }
  };
  if (!isb) {
    fin(std::false_type{}, acc, mm6);
#pragma unroll
    for (int i = 0; i < 24; ++i) asm volatile("" : "=v"(acc[i / 6][i % 6]));
#pragma unroll
    for (int i = 0; i < 6; ++i) asm volatile("" : "=v"(bf[1][i]));
#pragma unroll
    for (int i = 0; i < 8; ++i) asm volatile("" : "=v"(af[i >> 2][i & 3]));
    asm volatile("" : "=v"(bf[0][0])); asm volatile("" : "=v"(bf[0][1]));
  }
  OPAQUE_S(isb);
  if (isb) fin(std::true_type{}, acc, mm6);
}
__device__ __forceinline__ u32x4 pack(const f32x4& a, const f32x4& b) { return u32x4{pk2(a[0], a[1]), pk2(a[2], a[3]), pk2(b[0], b[1]), pk2(b[2], b[3])}; }

__device__ __forceinline__ u32x2 nosent2(const f32x4& a) { return u32x2{min(pk2(a[0], a[1]), SENT - 1), min(pk2(a[2], a[3]), SENT - 1)}; }
template <int N> __device__ __forceinline__ void hedgehog(const f32x4 (&r)[4][2], const float (&c)[4], float (&ep)[4][8], float (&en)[4][8]) {
  float mx[4], nm[4];
#pragma unroll
  for (int m = 0; m < N; ++m) {
    const f32x4 a = r[m][0], b = r[m][1];
    mx[m] = vmax2(fmaxf(fmaxf(a[3], b[0]), b[1]), fmaxf(fmaxf(fmaxf(fmaxf(a[0], a[1]), a[2]), b[2]), b[3]));
    nm[m] = vmax2(fmaxf(fmaxf(-a[3], -b[0]), -b[1]), fmaxf(fmaxf(fmaxf(fmaxf(-a[0], -a[1]), -a[2]), -b[2]), -b[3]));
  }
  SCHED;
  red4<true>(mx[0], mx[1], nm[0], nm[1], [](float x) { return x; });
  if (N == 4) red4<true>(mx[2], mx[3], nm[2], nm[3], [](float x) { return x; });
#pragma unroll
  for (int m = 0; m < N; ++m) { mx[m] *= -c[m]; nm[m] *= -c[m]; }
  SCHED;
#pragma unroll
  for (int m = 0; m < N; ++m) {
#pragma unroll
    for (int i = 0; i < 8; ++i) { ep[m][i] = __builtin_fmaf(r[m][i >> 2][i & 3], c[m], mx[m]); en[m][i] = __builtin_fmaf(r[m][i >> 2][i & 3], -c[m], nm[m]); }
    SCHED;
#pragma unroll
    for (int i = 0; i < 8; ++i) { ep[m][i] = __builtin_amdgcn_exp2f(ep[m][i]); en[m][i] = __builtin_amdgcn_exp2f(en[m][i]); }
    SCHED;
  }
}
__device__ __forceinline__ u32x4 pack8(const float (&e)[8], float f) {
  return u32x4{pk2(e[0] * f, e[1] * f), pk2(e[2] * f, e[3] * f), pk2(e[4] * f, e[5] * f), pk2(e[6] * f, e[7] * f)};
}
__device__ __forceinline__ u32x4 pack8(const float (&e)[8]) { return u32x4{pk2(e[0], e[1]), pk2(e[2], e[3]), pk2(e[4], e[5]), pk2(e[6], e[7])}; }

constexpr u64 window_mask(int t, int e) {
  u64 mk = 0;
  for (int ln = 0; ln < 64; ++ln) {
    const int delta = (ln & 15) + 16 - t * 16 - (ln >> 4) * 4 - e;
    if (delta >= 0 && delta <= 14 && !(delta & 1)) mk |= 1ull << ln;
  }
  return mk;
}
__device__ __forceinline__ void phik2(const f32x4 (&r)[4][2], const float (&c)[4], unsigned pw) {
  const f32x4 zero = {0.f, 0.f, 0.f, 0.f};
  const bf16x8 ones = {0x3f80, 0x3f80, 0x3f80, 0x3f80, 0x3f80, 0x3f80, 0x3f80, 0x3f80};
  float ep[4][8], en[4][8];
  f32x4 sp[2], sn[2];
  hedgehog<2>(r, c, ep, en);
#pragma unroll
  for (int m = 0; m < 2; ++m) { sp[m] = MFMA(ones, as_frag(pack8(ep[m])), zero); sn[m] = MFMA(ones, as_frag(pack8(en[m])), zero); }
#pragma unroll
  for (int m = 0; m < 2; ++m) { asm("" : "+v"(sp[m])); asm("" : "+v"(sn[m])); }
  SCHED;
#pragma unroll
  for (int m = 0; m < 2; ++m) {
    const float fp = __builtin_amdgcn_rcpf(sp[m][0]), fn = __builtin_amdgcn_rcpf(sn[m][0]);
#pragma unroll
    for (int i = 0; i < 8; ++i) { ep[m][i] *= fp; en[m][i] *= fn; }
    SCHED;
    const u32x4 pp = pack8(ep[m]), pn = pack8(en[m]);
    const unsigned d = pw + m * (16 * 160);
    lds_st<u32x2>(d, u32x2{pp[0], pp[1]});
    lds_st<u32x2>(d + 32, u32x2{pp[2], pp[3]});
    lds_st<u32x2>(d + 64, u32x2{pn[0], pn[1]});
    lds_st<u32x2>(d + 96, u32x2{pn[2], pn[3]});
    SCHED;
  }
}
__device__ __forceinline__ bf16x8 rowsum_sel(int lane) {
  const int fr = lane & 15;
  const unsigned one = (fr < 8) ? (0x3f80u << (16 * (fr & 1))) : 0u;
  const int e = (fr >> 1) & 3;
  return as_frag(u32x4{e == 0 ? one : 0u, e == 1 ? one : 0u, e == 2 ? one : 0u, e == 3 ? one : 0u});
}
__device__ __forceinline__ f32x4 rowsum_fin(f32x4 c) {
#pragma unroll
  for (int k = 0; k < 4; ++k) {
    const float a = c[k];
    c[k] = a + __int_as_float(__builtin_amdgcn_update_dpp(0, __float_as_int(a), 0x128  , 0xf, 0xf, false));
  }
  return c;
}
__device__ __forceinline__ void job_colsum(rsrc_t rs, const u32x4 (&w)[8], int mat, int g, int jc, int hw, int lane) {
  const bf16x8 sel = rowsum_sel(lane);
  f32x4 c = {0.f, 0.f, 0.f, 0.f};
#pragma unroll
  for (int i = 0; i < 8; ++i) c = MFMA(sel, as_frag(w[i]), c);
  const u32x4 w4 = __builtin_bit_cast(u32x4, rowsum_fin(c));
  unsigned so = WS_CS + (unsigned)(((mat * 16 + jc) * 16 + g) * 768);
  OPAQUE_S(so);
  if ((lane & 40) == 0) st16_sc1(rs, (unsigned)(hw * 64 + (lane & 7) * 8 + (lane >> 4) * 4) * 4, so, w4);
}
__device__ __forceinline__ unsigned img_off(int i, int lane) { return (lane >> 3) * 128 + (((lane & 7) ^ ((((lane >> 4) & 1) | ((i & 1) << 1)) << 1)) * 16); }
__device__ __forceinline__ void job_pack(rsrc_t rs, u32x4 w, int i, int mat, int g, int jc, int hw, int lane) {
  st16_sc1(rs, img_off(i, lane),
           WS_WP + (unsigned)(((mat * 16 + jc) * 16 + g) * 24576 + hw * 8192 + i * 1024), w);
}

__global__ __launch_bounds__(512, 2) void mega_kernel(Params p) {
  extern __shared__ __attribute__((aligned(16))) char smem[];
  const int tid = threadIdx.x;
  const int wave0 = __builtin_amdgcn_readfirstlane(tid >> 6);
  const int xx = blockIdx.x & 7, rr = blockIdx.x >> 3;
  const int g0 = (rr / NJ) * NX + (xx & (NX - 1)), j0 = (xx / NX) * NJ + (rr & (NJ - 1));
  constexpr unsigned lds0 = 0, lds00 = 0;
  if ((unsigned)(size_t)(LAS char*)smem != 0u) return;
#define LOCALS0 int g = g0, j = j0, l = l0, wave = wave0;                                              \
  OPAQUE_S(g); OPAQUE_S(j); OPAQUE_S(l); OPAQUE_S(wave);                                               \
  const int hw = wave & 3, wm = wave & 1, h = (wave >> 1) & 1;                                         \
  const unsigned myrep = (unsigned)(JROT(j) / 2 + (g & (NX - 1))) * REP_STRIDE;                              \
  (void)hw; (void)wm; (void)h; (void)myrep;                                                            \
  int lane = tid; OPAQUE_V(lane); lane &= 63
#define LOCALS LOCALS0; const bool isb = l & 1, lead = (wave == 4); const int half = wave >> 2; (void)isb; (void)lead; (void)half
  const rsrc_t rs = mk_rsrc(p.ws, WS_SPAN);
  const bool cw = wave0 < 4;
  int trv = 0, trA = 0, trB = 0, trC = 0;
  STAMP_INIT;
  TRC(0); TRR(62);
  if (wave0 == 3) {
    const char* pc;
    unsigned len;
    asm volatile("s_getpc_b64 %0\n.Lpf_base:\n\ts_mov_b32 %1, .Lpf_end-.Lpf_base" : "=s"(pc), "=s"(len));
    const unsigned off = ((unsigned)rr * 16 + (tid & 15)) * 128;
    touch(mk_rsrc(pc, len), ((int)len > 0 && off + 4 <= len) ? off : 0u, lds00 + L_SINK);
  }
  if (wave0 == 2) touch(rs, WS_EPOCH, lds00 + L_SINK);
  if (wave0 == 4) touch(mk_rsrc(p.wphi, 32 * 2048), j0 * 4096, lds00 + L_SINK);
  if (wave0 == 5) touch(mk_rsrc(p.woA, 1024 * 2048), (tid & 1) << 20, lds00 + L_SINK);
  if (wave0 == 6) touch(mk_rsrc(p.wqkvB, 1024 * 6144), min(tid & 7, 5) << 20, lds00 + L_SINK);
  if (wave0 == 7) touch(mk_rsrc(p.woB, 1024 * 2048), (tid & 1) << 20, lds00 + L_SINK);
  auto slowbit = [](const void* q) { return 1u << ((3 - ((unsigned)(size_t)q >> 7)) & 7); };
  unsigned slowm = slowbit(p.wqkvA) | (slowbit(p.wqkvB) << 8) | (slowbit(p.woA) << 16) | (slowbit(p.woB) << 24);
  OPAQUE_S(slowm);
  u32x2 epoch = {0u, 0u};
  bool dead = false;
      if (cw) {
        const int l0 = 0;
        LOCALS;
        u32x4 v[2][8];
        if (wave < 3) {
          const int jc = JOBC(j, wave), kt = (KB0(g, j) + (unsigned)g / NX) & 15;
          const rsrc_t ra = mk_rsrc(p.wqkvA, 1024 * 6144);
#pragma unroll
          for (int i = 0; i < 8; ++i)
            v[0][i] = __builtin_amdgcn_raw_buffer_load_b128(ra, (lane >> 3) * 6144 + (lane & 7) * 16, (kt * 64 + i * 8) * 6144 + wave * 2048 + jc * 128, 0);
          SCHED;
          TRC(44); BAR; SCHED;
          job_colsum(rs, v[0], 0, g, jc, wave, lane);
          if (SLOWC(0, jc)) {
#pragma unroll
            for (int i = 0; i < 8; ++i) job_pack(rs, v[0][i], i, 0, kt, jc, wave, lane);
          }
        } else {
          const int fr = lane & 15, fq = lane >> 4;
          const rsrc_t rx = mk_rsrc(p.x, 2048 * 2048);
#pragma unroll
          for (int i = 0; i < 16; ++i)
            v[i & 1][i >> 1] = __builtin_amdgcn_raw_buffer_load_b128(rx, fr * 2048 + fq * 16, (g * 128 + (j & (NJ - 1)) * (128 / NJ) + ((i >> 1) % (8 / NJ)) * 16) * 2048 + ((KB0(g, j) + (i >> 1) / (8 / NJ)) & 15) * 128 + (i & 1) * 64, 0);
          SCHED;
          TRC(44); BAR; SCHED;
          const f32x4 zero = {0.f, 0.f, 0.f, 0.f};
          const bf16x8 ones = {0x3f80, 0x3f80, 0x3f80, 0x3f80, 0x3f80, 0x3f80, 0x3f80, 0x3f80};
          float sm[8], q[8];
#pragma unroll
          for (int m = 0; m < 8; ++m) {
            const f32x4 a = MFMA(ones, as_frag(v[1][m]), MFMA(ones, as_frag(v[0][m]), zero));
            const f32x4 b = MFMA(as_frag(v[1][m]), as_frag(v[1][m]), MFMA(as_frag(v[0][m]), as_frag(v[0][m]), zero));
            sm[m] = a[0];
            q[m] = (fr & 2) ? ((fr & 1) ? b[3] : b[2]) : ((fr & 1) ? b[1] : b[0]);
          }
          if (fq == (fr >> 2)) {
#pragma unroll
            for (int m = 0; m < 8; ++m) st8_sc1(rs, ((m % (8 / NJ)) * 16 + fr) * 8 + (m / (8 / NJ)) * 1024, st_off(g, 0) + JROT(j) * 1024 + (j & (NJ - 1)) * (1024 / NJ), u32x2{__float_as_uint(sm[m]), __float_as_uint(q[m])});
          }
        }
      }
  const char *pA = (const char*)p.x, *pW = (const char*)p.wqkvA;
  unsigned zA = 2048 * 2048, bA = g0 * (128 * 2048), kA = 128, pcA = 32 * 2048, vA;
  unsigned zW = 1024 * 6144, bW = j0 * 128, kW = 64 * 6144, pcW = 2048, hW = 32 * 6144, vW;
  int kb = KB0(g0, j0), dj = -1;
  {
    const int lane = tid & 63, hw = wave0 & 3;
    vA = (hw * 8 + (lane >> 3)) * 2048 + (((lane & 7) ^ (((hw & 1) * 4 + (lane >> 4)) & 7)) * 16);
    vW = (hw * 8 + (lane >> 3)) * 6144 + (((lane & 7) ^ ((((lane >> 4) & 1) | ((hw & 1) << 1)) << 1)) * 16);
  }

#pragma unroll 1
  for (int l0 = 0; l0 < 4; ++l0) {
    if (l0) { TRC(0); TRR(62); }

    f32x4 acc[2][4];
    auto kvpart = [&](unsigned va, unsigned pa, unsigned pb, unsigned kvb, int lane, int wave) {
      const f32x4 zero = {0.f, 0.f, 0.f, 0.f};
      const bf16x8 ones = {0x3f80, 0x3f80, 0x3f80, 0x3f80, 0x3f80, 0x3f80, 0x3f80, 0x3f80};
      f32x4 c0 = zero, c1 = zero, z0 = zero;
#pragma unroll
      for (int ks = 0; ks < 4; ++ks) {
        const bf16x8 v = lds_ld<bf16x8>(va + ks * 2048);
        const bf16x8 a0 = tr2(pa + ks * 32 * 160, pa + (ks * 32 + 16) * 160);
        const bf16x8 a1 = tr2(pb + ks * 32 * 160, pb + (ks * 32 + 16) * 160);
        c0 = MFMA(a0, v, c0);
        c1 = MFMA(a1, v, c1);
        z0 = MFMA(a0, ones, z0);
      }
      const u32x4 c = nosent(pack(c0, c1));
      const bool dt = wave & 2;
      st16_sc1(rs, lane * 16, kvb + (unsigned)wave * 1024, u32x4{dt ? c[2] : c[0], dt ? c[3] : c[1], dt ? c[0] : c[2], dt ? c[1] : c[3]});
      if ((lane & 15) == 0)
        st8_sc1(rs, lane, kvb + KVP_Z + (wave >> 2) * 128 + (wave & 1) * 64 + (wave & 2) * 4, nosent2(z0));
    };
#define KV_ADDR                                                                                                            \
    unsigned va = lds0 + SC_VF + (wave >> 2) * 8192 + ((wave >> 1) & 1) * 1024 + lane * 16;                                \
    unsigned pa = lds0 + SC_PHIK + ((wave >> 2) * 128 + fq * 4 + (fr >> 2)) * 160 + (wave & 1) * 64 + (lane & 3) * 8;      \
    unsigned kvj = WS_KVP + (unsigned)j * KVP_J + (unsigned)(l >> 1) * KVP_L;                                              \
    unsigned pb = pa + 32 - (wave & 2) * 16;                                                                               \
    pa += (wave & 2) * 16;                                                                                                 \
    OPAQUE_V(va); OPAQUE_V(pa); OPAQUE_V(pb); OPAQUE_S(kvj)
    if (cw) {
      LOCALS;
      unsigned otile, kvo, l16, pr1, pr4;
      int g2, j2, l2, ln2;
      qkv_loop(l & 1, wm, h, lane, l == 0, trv, [&](int st) {
        if (st == 0) {
          g2 = g0; j2 = j0; l2 = l0; ln2 = tid;
          OPAQUE_S(g2); OPAQUE_S(j2); OPAQUE_S(l2); OPAQUE_V(ln2);
          return;
        }
        otile = ob_off(g2, j2, l2) + (unsigned)(wm * 8 + h) * 1024;
        kvo = WS_KVP + (unsigned)j2 * KVP_J + (unsigned)g2 * KVP_G + (unsigned)(l2 >> 1) * KVP_L;
        l16 = (ln2 & 63) * 16;
        pr1 = lds0 + L_RSTD + (16 + wm * 64 + (ln2 & 15)) * 4;
        pr4 = lds0 + L_RSTD + wm * 256 + (ln2 & 48);
        asm("" : "+s"(otile)); asm("" : "+s"(kvo)); asm("" : "+v"(l16)); asm("" : "+v"(pr1)); asm("" : "+v"(pr4));
      }, [&](auto isb_c, f32x4 (&a)[4][6], auto& mm6) {
        const f32x4 zero = {0.f, 0.f, 0.f, 0.f};
        constexpr float L2E = 1.4426950408889634f;
        float rq[4];
#define ilv8(nv) do { _Pragma("unroll") for (int i_ = 0; i_ < 8; ++i_) { SGB(0x008, 1); SGB(0x100, 2); SGB(0x002, nv); } SCHED; } while (0)
        if constexpr (decltype(isb_c)::value) {
          constexpr float QS = 0.17677669529663687f * L2E, NEG = -3.0e38f;
          const bf16x8 ones = {0x3f80, 0x3f80, 0x3f80, 0x3f80, 0x3f80, 0x3f80, 0x3f80, 0x3f80};
          u32x4 kf[5], vf[5], qf[4], pf[4];
          f32x4 rs4[4], bias[2], s0[4], s1[4], dn[4];
          float x[4][4], mx[4];
          unsigned psel0, psel1;
          auto pkkv = [&](int m) { kf[m + 1] = pack(a[m][2] * rq[m], a[m][3] * rq[m]); vf[m + 1] = pack(a[m][4] * rs4[m], a[m][5] * rs4[m]); };
          auto pkq = [&](int m) { qf[m] = pack(a[m][0], a[m][1]); };
          auto st1 = [&](int m) {
            x[m][0] = vmax2(s0[m][0], s0[m][1]); x[m][1] = vmax2(s0[m][2], s0[m][3]); x[m][2] = vmax2(s1[m][0], s1[m][1]); x[m][3] = vmax2(s1[m][2], s1[m][3]);
            mx[m] = vmax2(vmax2(x[m][0], x[m][1]), vmax2(x[m][2], x[m][3]));
          };
          auto st2 = [&](int m) {
            const float mxl = mx[m] * -rq[m];
#pragma unroll
            for (int e = 0; e < 4; ++e) x[m][e] = __builtin_fmaf(x[m][e], rq[m], mxl);
          };
          auto st3 = [&](int m) {
#pragma unroll
            for (int e = 0; e < 4; ++e) x[m][e] = __builtin_amdgcn_exp2f(x[m][e]);
          };
          auto st5 = [&](int m) {
            const unsigned p0 = pk2(x[m][0], x[m][1]), p1 = pk2(x[m][2], x[m][3]);
            pf[m] = u32x4{__builtin_amdgcn_perm(0u, p0, psel0), __builtin_amdgcn_perm(0u, p0, psel1), __builtin_amdgcn_perm(0u, p1, psel0), __builtin_amdgcn_perm(0u, p1, psel1)};
          };
          auto st6 = [&](int m) {
            dn[m] = MFMA(ones, as_frag(pf[m]), zero);
            s0[m] = MFMA(as_frag(u32x4{vf[m][0], vf[m][1], vf[m + 1][0], vf[m + 1][1]}), as_frag(pf[m]), zero);
            s1[m] = MFMA(as_frag(u32x4{vf[m][2], vf[m][3], vf[m + 1][2], vf[m + 1][3]}), as_frag(pf[m]), zero);
            asm("" : "+v"(dn[m]));
          };
          auto st7 = [&](int m) { const float inv = __builtin_amdgcn_rcpf(dn[m][0]); pf[m] = nosent(pack(s0[m] * inv, s1[m] * inv)); };
          mm6(3);
#pragma unroll
          for (int m = 0; m < 4; ++m) rq[m] = lds_ld<float>(pr1 + m * 64);
#pragma unroll
          for (int m = 3; m >= 0; --m) rs4[m] = lds_ld<f32x4>(pr4 + 64 + m * 64);
          unsigned pv_ = l16 + lds0 + L_PREV + h * 2048;
          asm("" : "+v"(pv_));
          static_for<0, 8>([&](auto i_c) __attribute__((always_inline)) {
            constexpr int i = decltype(i_c)::value;
            constexpr u64 mk = window_mask(i >> 2, i & 3);
            bias[i >> 2][i & 3] = __builtin_amdgcn_inverse_ballot_w64(mk) ? 0.f : NEG;
          });
          asm("" : "+v"(bias[0])); asm("" : "+v"(bias[1]));
          const unsigned hso = kvo + KVP_HALO + h * 2048, hsi = hso - (g > 0 ? KVP_G : 0u);
          ilv8(2);
          mm6(2);
          pkkv(3);
          ilv8(2);
          OPAQUE_V(kf[4]); OPAQUE_V(vf[4]);
          if (wm) {
            st16_sc1(rs, l16, hso, nosent(kf[4]));
            st16_sc1(rs, l16, hso + 1024, nosent(vf[4]));
          } else {
            lds_st<u32x4>(pv_, kf[4]);
            lds_st<u32x4>(pv_ + 1024, vf[4]);
          }
          SCHED;
          mm6(1);
          pkq(3); pkkv(2);
          ilv8(3);
          WAITL0;
          TRQ(17); BAR;
          SCHED;
          mm6(0);
          pkq(2); pkkv(1);
          ilv8(3);
          s0[3] = MFMA(as_frag(kf[3]), as_frag(qf[3]), bias[0]); s1[3] = MFMA(as_frag(kf[4]), as_frag(qf[3]), bias[1]);
          s0[2] = MFMA(as_frag(kf[2]), as_frag(qf[2]), bias[0]); s1[2] = MFMA(as_frag(kf[3]), as_frag(qf[2]), bias[1]);
          pkq(1); pkkv(0);
#pragma unroll
          for (int i = 0; i < 4; ++i) { SGB(0x008, 1); SGB(0x002, 5); }
          SCHED;
          const bool halo = wm == 0 && g > 0;
          const u32x4 hk = ld16_sc1(rs, l16, hsi), hv = ld16_sc1(rs, l16, hsi + 1024);
          SCHED;
          s0[1] = MFMA(as_frag(kf[1]), as_frag(qf[1]), bias[0]); s1[1] = MFMA(as_frag(kf[2]), as_frag(qf[1]), bias[1]);
          st1(3); st1(2);
#pragma unroll
          for (int m = 0; m < 4; ++m) rq[m] *= QS;
          SCHED;
          st1(1);
          pkq(0);
          psel0 = __builtin_amdgcn_inverse_ballot_w64(0xaaaaaaaaaaaaaaaaull) ? 0x01000c0cu : 0x0c0c0100u;
          psel1 = __builtin_amdgcn_inverse_ballot_w64(0xaaaaaaaaaaaaaaaaull) ? 0x03020c0cu : 0x0c0c0302u;
          { float d = mx[1]; red4<true>(mx[3], mx[2], mx[1], d, [](float v) { return v; }); }
          s1[0] = MFMA(as_frag(kf[1]), as_frag(qf[0]), bias[1]);
          SCHED;
          st2(3); st2(2); st2(1);
          SCHED;
          st3(3); st3(2); st3(1);
          SCHED;
          st5(3); st6(3); st5(2); st6(2); st5(1); st6(1);
          SCHED;
          TRR(60);
          TRC(18);
          u32x4 pk = lds_ld<u32x4>(pv_), pv = lds_ld<u32x4>(pv_ + 1024);
          st7(3); st7(2); st7(1);
          SCHED;
#pragma unroll
          for (int m = 3; m > 0; --m) st16_sc1(rs, l16, otile + m * 2048, pf[m]);
          if (!wm && g == 0) bias[0] = f32x4{NEG, NEG, NEG, NEG};
          SCHED;
          TRV(55, 0); TRC(21);
          if (halo) {
            pk = hk; pv = hv;
            for (unsigned spins = 0; !dead && __any(umax4v(umax4v(0u, pk), pv) == SENT); ++spins) {
              if (spins > SPIN_CAP) { dead = true; break; }
              CLOBBER;
              TRV(55, spins + 1);
              pk = ld16_sc1(rs, l16, hsi); pv = ld16_sc1(rs, l16, hsi + 1024);
              OPAQUE_V(pk); OPAQUE_V(pv);
            }
          }
          vf[0] = pv;
          s0[0] = MFMA(as_frag(pk), as_frag(qf[0]), bias[0]);
          st1(0);
          mx[0] = red_max4(mx[0]);
          st2(0); st3(0); st5(0); st6(0); st7(0);
          st16_sc1(rs, l16, otile, pf[0]);
        } else {
          LOCALS;
          const int fr = lane & 15, fq = lane >> 4;
          f32x4 rs4[4], r[4][2];
          const bf16x8 ones = {0x3f80, 0x3f80, 0x3f80, 0x3f80, 0x3f80, 0x3f80, 0x3f80, 0x3f80};
          auto proj = [&](int m, int n, const bf16x8& w0, const bf16x8& w1) {
            const u32x4 f = pack(a[m][n], a[m][n + 1]);
            r[m][0] = MFMA(w0, as_frag(f), zero); r[m][1] = MFMA(w1, as_frag(f), zero);
          };
          mm6(3, 0, 2); mm6(2, 0, 2); mm6(1, 0, 2); mm6(0, 0, 2);
          unsigned wa = lds0 + L_WPHI + h * 2048 + (fq * 4 + (fr >> 2)) * 64 + (lane & 3) * 8, kw = lds0 + L_PREV + hw * 2048 + l16;
          asm("" : "+v"(wa)); asm("" : "+v"(kw));
#pragma unroll
          for (int m = 0; m < 4; ++m) rq[m] = lds_ld<float>(pr1 + m * 64);
#pragma unroll
          for (int m = 0; m < 4; ++m) rs4[m] = lds_ld<f32x4>(pr4 + 64 + m * 64);
          const bf16x8 w0 = tr2(wa, wa + 1024);
          const bf16x8 w1 = tr2(wa + 32, wa + 32 + 1024);
#pragma unroll
          for (int i = 0; i < 8; ++i) { SGB(0x008, 1); SGB(0x100, 2); SGB(0x002, 2); }
          SCHED;
          mm6(3, 2, 4); mm6(2, 2, 4);
          lds_st<u32x4>(kw + 1024, pack(a[3][2], a[3][3]));
          lds_st<u32x4>(kw, pack(a[2][2], a[2][3]));
#pragma unroll
          for (int i = 0; i < 2; ++i) { SGB(0x008, 1); SGB(0x002, 2); SGB(0x008, 1); SGB(0x002, 2); SGB(0x200, 1); }
          SCHED;
          mm6(1, 2, 4); mm6(0, 2, 4);
          SCHED;
          WAITL0;
          TRQ(17); BAR;
          SCHED;
          proj(0, 2, w0, w1); proj(1, 2, w0, w1);
          unsigned pw = lds0 + SC_PHIK + (h * 128 + wm * 64 + fr) * 160 + fq * 8, vw = lds0 + SC_VF + h * 8192 + wm * 4096 + l16;
          asm("" : "+v"(pw)); asm("" : "+v"(vw));
#pragma unroll
          for (int m = 0; m < 4; ++m) rq[m] *= L2E;
#pragma unroll
          for (int i = 0; i < 4; ++i)
            lds_st<u32x4>(vw + i * 1024, pack(a[i & 2][4 + (i & 1)] * rs4[i & 2], a[(i & 2) + 1][4 + (i & 1)] * rs4[(i & 2) + 1]));
          SCHED;
          phik2(r, rq, pw);
          KV_ADDR;
          WAITL0;
          TRR(60);
          TRC(19); BAR;
          kvpart(va, pa, pb, kvo, lane, wave);
          SCHED;
#pragma unroll
          for (int m = 0; m < 4; ++m) mm6(m, 4, 8);
          SCHED;
          CLOBBER;
          TRC(20); BAR;
          u32x4 qp[4], qn[4];
          float sqp[4], sqn[4];
#pragma unroll
          for (int m = 0; m < 4; ++m) proj(m, 0, w0, w1);
          SCHED;
          float ep[4][8], en[4][8];
          hedgehog<4>(r, rq, ep, en);
          const unsigned zo = (lane >> 3) * KVP_G + (lane & 7) * 16, zs = kvj + KVP_Z + h * 128;
          u32x4 zv[2] = {ld16_sc1(rs, zo, zs), ld16_sc1(rs, zo, zs + 8 * KVP_G)};
          SCHED;
#pragma unroll
          for (int m = 0; m < 4; ++m) {
            qp[m] = pack8(ep[m]); qn[m] = pack8(en[m]);
            sqp[m] = MFMA(ones, as_frag(qp[m]), zero)[0];
            sqn[m] = MFMA(ones, as_frag(qn[m]), zero)[0];
          }
          SCHED;
#pragma unroll
          for (int m = 0; m < 4; ++m) { sqp[m] = __builtin_amdgcn_rcpf(sqp[m]); sqn[m] = __builtin_amdgcn_rcpf(sqn[m]); }
#pragma unroll
          for (int m = 0; m < 4; ++m) { OPAQUE_V(qp[m]); OPAQUE_V(qn[m]); OPAQUE_V(sqp[m]); OPAQUE_V(sqn[m]); }
          unsigned ko = (lane >> 5) * KVP_G + (lane & 31) * 16, ks = kvj + (unsigned)((j ^ RX) - j) * KVP_J + KVP_SUM + h * (8 * KVP_G);
          bf16x8 sel = rowsum_sel(lane);
          OPAQUE_V(ko); OPAQUE_S(ks); OPAQUE_V(sel);
          TRC(21); TRV(50, 0);
          for (unsigned spins = 0; !dead && __any(umax4v(umax4v(0u, zv[0]), zv[1]) == SENT); ++spins) {
            if (spins > SPIN_CAP) { dead = true; break; }
            CLOBBER;
            TRV(50, spins + 1);
            zv[0] = ld16_sc1(rs, zo, zs); zv[1] = ld16_sc1(rs, zo, zs + 8 * KVP_G);
          }
          TRC(22);
          {
            const f32x4 z = rowsum_fin(MFMA(sel, as_frag(zv[1]), MFMA(sel, as_frag(zv[0]), zero)));
            if ((lane & 40) == 0) lds_st<u32x2>(lds0 + L_ZF + hw * 128 + fr * 16 + fq * 8, u32x2{pk2(z[0], z[1]), pk2(z[2], z[3])});
            WAITL0;
            const bf16x8 zf0 = lds_ld<bf16x8>(lds0 + L_ZF + hw * 128 + fq * 16), zf1 = lds_ld<bf16x8>(lds0 + L_ZF + hw * 128 + 64 + fq * 16);
            float dp[4], dn[4];
#pragma unroll
            for (int m = 0; m < 4; ++m) { dp[m] = MFMA(zf0, as_frag(qp[m]), zero)[0]; dn[m] = MFMA(zf1, as_frag(qn[m]), zero)[0]; }
            SCHED;
#pragma unroll
            for (int m = 0; m < 4; ++m) {
              const float inv = __builtin_amdgcn_rcpf(__builtin_fmaf(dp[m], sqp[m], __builtin_fmaf(dn[m], sqn[m], 1e-6f)));
              sqp[m] *= inv; sqn[m] *= inv;
              OPAQUE_V(sqp[m]); OPAQUE_V(sqn[m]);
            }
          }
          TRV(51, 0);
          TRC(43); BAR;
          __builtin_amdgcn_s_sleep(H2D);
          u32x4 kv[4];
#pragma unroll
          for (int i = 0; i < 4; ++i) kv[i] = ld16_sc1(rs, ko, ks + i * (2 * KVP_G));
          SCHED;
#pragma unroll
          for (int m = 0; m < 4; ++m) { qp[m] = pack8(ep[m], sqp[m]); qn[m] = pack8(en[m], sqn[m]); }
#pragma unroll
          for (int m = 0; m < 4; ++m) { OPAQUE_V(qp[m]); OPAQUE_V(qn[m]); }
          SCHED;
          for (unsigned spins = 0; !dead && __any(max(umax4v(umax4v(0u, kv[0]), kv[1]), umax4v(umax4v(0u, kv[2]), kv[3])) == SENT); ++spins) {
            if (spins > SPIN_CAP) { dead = true; break; }
            CLOBBER;
            TRV(51, spins + 1);
#pragma unroll
            for (int i = 0; i < 4; ++i) kv[i] = ld16_sc1(rs, ko, ks + i * (2 * KVP_G));
          }
          TRC(23);
          f32x4 o[4][2];
#pragma unroll
          for (int m = 0; m < 4; ++m)
#pragma unroll
            for (int i = 0; i < 2; ++i) o[m][i] = MFMA(as_frag(kv[2 * i + 1]), as_frag(qn[m]), MFMA(as_frag(kv[2 * i]), as_frag(qp[m]), zero));
#pragma unroll
          for (int m = 0; m < 4; ++m) st16_sc1(rs, l16, otile + m * 2048, nosent(pack(o[m][0], o[m][1])));
        }
      });
#pragma unroll
      for (int i = 0; i < 8; ++i) asm volatile("" : "=v"(acc[i >> 2][i & 3]));
    } else {
      __builtin_amdgcn_s_setprio(3);
      if (l0) {
        WAITV(3);
        WAITL0;
        TRC(1); BAR;
      }
      LOCALS;
      u32x4 xr[2][2];
      const unsigned lin = lane * 16, vlin = lin + hw * 1024;
      const int jr = j & ~3;
      u32x4 sent4 = {SENT, SENT, SENT, SENT};
      OPAQUE_V(sent4);
      const unsigned cb0 = ob_off(g, j, l), cb2 = xb_off(g, l) + j * TS;
      const unsigned cb1 = WS_KVP + (unsigned)j * KVP_J + (unsigned)g * KVP_G + (unsigned)(l >> 1) * KVP_L;
      unsigned ra0;
      int tr = 0;
      auto ldres = [&](int t) {
        if (tr == t) {
#pragma unroll
          for (int i = 0; i < 4; ++i) xr[i >> 1][i & 1] = lds_ld<u32x4>((ra0 ^ ((i & 1) * 64)) + (t % 3) * XS + ((i >> 1) ^ 1) * 2048);
        }
      };
      auto aslot = [&](int t) { return lds0 + L_R2 + (t % 3) * XS + hw * 1024; };
      auto wslot = [&](int t) { return lds0 + L_R1 + (t & 3) * 24576 + hw * 1024; };
      const bool l0f = l == 0;
      const rsrc_t rA = mk_rsrc(pA, zA);
      auto kseq = [&](int t) { return (kb + t - (t <= dj ? 1 : 0)) & 15; };
      auto offA = [&](int t) { return bA + (unsigned)kseq(t) * kA; };
      auto offW = [&](int t) { return bW + (unsigned)kseq(t) * kW; };
      auto ldA = [&](int t) { dma4<4096>(rA, vA, offA(t), pcA, aslot(t)); };
      auto ldWh = [&](int t, unsigned o, int hf) { dma3<8192>(mk_rsrc(pW, zW), vW, o + (hf ? hW : 0u), pcW, wslot(t) + hf * 4096); };
      const bool slowj = l0f && SLOWC(0, j);
      auto packedW = [&]() {
        pW = p.ws; zW = WS_SPAN; bW = WS_WP + (unsigned)j * (16 * 24576); kW = 24576; pcW = 8192; hW = 4096; vW = vlin;
      };
      auto ldW = [&](int t) { const unsigned o = offW(t); ldWh(t, o, 0); ldWh(t, o, 1); };
      constexpr auto spread = [](int n) { unsigned m = 0; for (int t = 0; t < 26; ++t) if ((t + 1) * n / 13 > t * n / 13) m |= 1u << t; return m; };
      constexpr unsigned sp4 = spread(4), sp8 = spread(8), sp12 = spread(12);
      const unsigned smask = (isb ? (l == 3 ? sp4 : sp8) : sp12) >> ((((g + j * 5) & 15) * 13) >> 4);
      const unsigned cbm = isb ? cb2 : cb1;
      auto clean = [&](int t) __attribute__((always_inline)) {
        if (t < 13 && ((smask >> t) & 1)) {
          const unsigned cc = __builtin_popcount(smask & ((1u << t) - 1));
          const unsigned cb = cb0 + (cc >= 4 ? cbm - cb0 : 0u) + (cc >= 8 ? cb2 - cbm : 0u), pc = (cc + g + j) & 3;
          st16_sc1(rs, (lane >> 4) * 4096 + (lane & 15) * 16, cb + hw * 1024 + pc * 256, sent4);
        }
      };
      unsigned flo;
      unsigned ready = ~0u;
      u32x4 cv[4];
#pragma unroll
      for (int ii = 0; ii < 4; ++ii) asm volatile("" : "=v"(cv[ii]));
      auto chkrd = [&](int t) {
#pragma unroll
        for (int ii = 0; ii < 4; ++ii) cv[ii] = lds_ld<u32x4>(aslot(t) + lin + ii * 4096);
      };
      auto chkbad = [&]() { return __any(max(umax4v(umax4v(0u, cv[0]), cv[1]), umax4v(umax4v(0u, cv[2]), cv[3])) == SENT) && !l0f; };
      auto refetch = [&](int t) __attribute__((always_inline)) {
        for (unsigned spins = 0; !dead; ++spins) {
          if (spins > SPIN_CAP) { dead = true; break; }
          WAITV(0);
          TRV(t == 1 ? 56 : 57, spins + 1);
          ldA(t);
          ldA(t + 1);
          WAITV(0);
          chkrd(t);
          if (!chkbad()) break;
          __builtin_amdgcn_s_sleep(1);
        }
      };
      f32x4 jc[4];
#pragma unroll
      for (int i = 0; i < 4; ++i) asm volatile("" : "=v"(jc[i]));
      auto csld = [&]() {
#pragma unroll
        for (int i = 0; i < 4; ++i)
          ld16_asm(jc[i], rs, (lane >> 4) * 768 + hw * 192 + ((lane & 15) < 12 ? (lane & 15) : 0) * 16, WS_CS + (unsigned)((l * 16 + j) * 16 + i * 4) * 768);
      };
      auto csred = [&]() {
#pragma unroll
        for (int i = 0; i < 4; ++i) OPAQUE_V(jc[i]);
        const f32x4 s = (jc[0] + jc[1]) + (jc[2] + jc[3]);
        auto r = SWAP16(s[0], s[1]), q = SWAP16(s[2], s[3]);
        r = SWAP32(__uint_as_float(r[0]) + __uint_as_float(r[1]), __uint_as_float(q[0]) + __uint_as_float(q[1]));
        if ((lane & 15) < 12) lds_st<float>(lds0 + L_CS + l * 768 + hw * 192 + (lane & 15) * 16 + (lane >> 4) * 4, __uint_as_float(r[0]) + __uint_as_float(r[1]));
      };
      if (l0f) {
        u32x2 gw2;
        ld8_asm(gw2, rs, WS_EPOCH + 0u * lin);
        TRC(44); BAR;
        ldW(0); ldA(0);
        dma1(mk_rsrc(p.wphi, 32 * 2048), vlin, j * 4096, lds0 + L_WPHI + hw * 1024);
        ldW(1);
        WAITV(17);
        OPAQUE_V(gw2);
        const u64 gword = ((u64)(unsigned)UNI(gw2[1]) << 32) | (unsigned)UNI(gw2[0]);
        u64 z = gword + 0x9e3779b97f4a7c15ull;
        z = (z ^ (z >> 30)) * 0xbf58476d1ce4e5b9ull;
        z = (z ^ (z >> 27)) * 0x94d049bb133111ebull;
        z = z ^ (z >> 31);
        z = (z == gword) ? ~z : z;
        epoch = u32x2{(unsigned)z, (unsigned)(z >> 32)};
        tr = (j - kb) & 15;
        WAITV(7);
        TRC(1); BAR;
        ldA(1);
      } else {
        ldW(1);
        if (l == 1) csld();
        else __builtin_amdgcn_s_sleep(XD);
        ldA(1);
      }
      ldA(2);
      TRC(40);
      ldW(2);
#pragma unroll
      for (int i = 0; i < 4; ++i) asm volatile("" : "=v"(xr[i >> 1][i & 1]));
      unsigned oA = offA(3), oW = 0;
      OPAQUE_S(oA);
      TRV(56, 0); TRV(57, 0);
      ra0 = lds0 + L_R2 + (hw * 32 + (lane & 15)) * 128 + (((((lane >> 4) & 1) * 2 + (lane >> 5)) ^ ((lane >> 1) & 7)) * 16);
      OPAQUE_V(ra0);
      ldres(0);
      clean(0);
      if (l == 1) { WAITV(14); csred(); }
      WAITV(10);
      chkrd(1);
      if (__builtin_expect(chkbad(), 0)) refetch(1);
      const int fr = lane & 15, fq = lane >> 4;
      u32x2 fl;
      f32x4 js[4];
      float rb[4];
      const bool slowo = SLOWC(2 + (l & 1), j);
      const char* pO;
      unsigned zO, bO, pcO, vO, clo;
      u32x4 wo2[2];
      asm volatile("" : "=v"(wo2[0])); asm volatile("" : "=v"(wo2[1]));
      const unsigned fwo = myrep + fl_line(FL_WO + (isb ? 1 : 0), jr + (lane >> 4)) + (lane & 15) * 8;
      static_for<1, 14>([&](auto t_c) __attribute__((always_inline)) {
        constexpr int t = decltype(t_c)::value;
        constexpr auto rest = [](int s) { return 6 + (s == NVAL - 2 || s == 5 || s == 11) + (s == 8 ? 4 : 0); };
        constexpr int NW = t == 1 ? 63 : 4 + rest(t - 1);
        TRC(1 + t);
        if (t >= NVAL && t <= 6 && __builtin_expect((ready & (1u << kseq(t + 2))) == 0, 0)) {
          WAITV(NW); WAITL0; BAR;
          ready = poll_need(rs, flo, epoch, ready, 1u << kseq(t + 2), dead);
          dma4<4096>(rA, vA, oA, pcA, aslot(t + 2));
        } else if (t == 7 && __builtin_expect(ready != ~0u, 0)) {
          WAITV(NW); WAITL0; BAR;
          poll_all(rs, flo, epoch, dead);
          dma4<4096>(rA, vA, oA, pcA, aslot(t + 2));
        } else {
          bar_dma4<4096, NW>(rA, vA, oA, pcA, aslot(t + 2));
        }
        if (t == 1 && lead) set_flags(rs, fl_line(FL_X + l, g), j, l0f ? fl_line(FL_CS, j) : fl_line(FL_X + l, g), l0f ? g : j, epoch, lane);
        if (t == 7 && slowj) packedW();
        oW = offW(t + 2);
        if (t < 13) oA = offA(t + 3);
        OPAQUE_S(oA); OPAQUE_S(oW);
        clean(t);
        if (t <= NVAL && !l0f) { WAITV(rest(t - 1) + 4); chkrd(t + 1); }
        if (t == WT + 3 && l0f && SLOWC(2, jr + hw)) {
          OPAQUE_V(wo2[0]); OPAQUE_V(wo2[1]);
#pragma unroll
          for (int i = 0; i < 2; ++i) st16_sc1(rs, lin, WS_WO + (unsigned)((jr + hw) * 131072 + g * 8192 + ((j & 3) * 2 + i) * 1024), wo2[i]);
        }
        if (t == WT + 6 && l0f && lead) set_flags(rs, fl_line(FL_WO, j), g, fl_line(FL_WO, j), g, epoch, lane);
        ldres(t);
        if (t == 8) {
#pragma unroll
          for (int i = 0; i < 4; ++i) ld16_asm(js[i], rs, fq * 1024 + hw * 256 + fr * 16, st_off(g, l) + i * 4096);
        }
        SCHED;
        ldWh(t + 2, oW, 0);
        SCHED;
        bool bad = false;
        if (t <= NVAL) bad = chkbad();
        if (t == 1) {
          const unsigned fx = fl_line(FL_X + l, g), fc = fl_line(FL_CS, jr) + ((j + fq) & 3) * 128;
          flo = myrep + ((l0f && lane >= 16) ? fc : fx) + fr * 8;
          OPAQUE_V(flo);
        }
        if (t == 9) {
          pO = slowo ? p.ws : (const char*)(isb ? p.woB : p.woA);
          zO = slowo ? WS_SPAN : 1024 * 2048; bO = slowo ? WS_WO + (unsigned)((isb ? 16 : 0) + j) * 131072 : j * 128; pcO = slowo ? 4096 : 32 * 2048;
          vO = slowo ? vlin : (hw * 8 + (lane >> 3)) * 2048 + wo_swz(lane);
          clo = myrep + ((lane & 16) ? fl_line(FL_CLC + l, (lane & 32) ? j ^ RX : j) : fl_line(FL_CLR + l, g)) + fr * 8;
          OPAQUE_S(pO); OPAQUE_S(zO); OPAQUE_S(bO); OPAQUE_S(pcO); OPAQUE_V(vO); OPAQUE_V(clo);
        }
        if (t == 10 && l0f) csred();
        if (t == 11) {
#pragma unroll
          for (int i = 0; i < 4; ++i) OPAQUE_V(js[i]);
          const f32x4 s = (js[0] + js[1]) + (js[2] + js[3]);
          const auto r = SWAP16(s[0], s[2]), q = SWAP16(s[1], s[3]);
          rb[0] = __uint_as_float(r[0]) + __uint_as_float(r[1]); rb[1] = __uint_as_float(q[0]) + __uint_as_float(q[1]);
          OPAQUE_V(rb[0]); OPAQUE_V(rb[1]);
        }
        if (t == 12) {
          const auto r = SWAP32(rb[0], rb[0]), q = SWAP32(rb[1], rb[1]);
          const float m = (__uint_as_float(r[0]) + __uint_as_float(r[1])) * (-1.0f / 1024), q2 = __uint_as_float(q[0]) + __uint_as_float(q[1]);
          const float rstd = __builtin_amdgcn_rsqf(fmaxf(__builtin_fmaf(q2, 1.0f / 1024, -m * m), 0.f) + 1e-5f);
          lds_st<float>(lds0 + L_MEAN + 64 + hw * 128 + fr * 8 + (fq & 1) * 4 + (fq >> 1) * (L_RSTD - L_MEAN), (lane & 32) ? rstd : m);
        }
        SCHED;
        ldWh(t + 2, oW, 1);
        if (t == NVAL - 1 || t == 6) {
          WAITV(10);
          OPAQUE_V(fl);
          if (t == 6) ready = __all(is_set(fl, epoch)) ? ~0u : 0u;
          else {
            ready = l0f ? ~0u : (unsigned)__ballot(is_set(fl, epoch));
            if (slowj && __all(is_set(fl, epoch))) packedW();
          }
        }
        if (t == 12) { WAITV(10); OPAQUE_V(fl); }
        if (t == 13 && slowo && __builtin_expect(!__all(is_set(fl, epoch)), 0)) poll_all(rs, fwo, epoch, dead);
        if (t == NVAL - 2 || t == 5) ld8_asm(fl, rs, flo);
        if (t == 11) ld8_asm(fl, rs, fwo);
        if (t == 7 && l0f) csld();
        if (t == WT && l0f) {
          if (FT && hw == 0) touch(mk_rsrc(p.ws, NREP * REP_STRIDE), min((g * 16 + j) * 9 + (lane < 9 ? lane : 0), (int)(NREP * REP_STRIDE / 128 - 1)) * 128, lds0 + L_SINK);
#pragma unroll
          for (int i = 0; i < 2; ++i) ld16in_asm(wo2[i], mk_rsrc(p.woA, 1024 * 2048), (lane >> 3) * 2048 + wo_swz(lane), (g * 64 + (j & 3) * 16 + i * 8) * 2048 + (jr + hw) * 128);
        }
        if constexpr (t <= NVAL) { if (__builtin_expect(bad, 0)) refetch(t + 1); }
      });
      auto stO = [&](int i) { dma4<4096>(mk_rsrc(pO, zO), vO, bO + wo_src(i, j) * (4 * pcO), pcO, lds0 + wo_slot(i) + hw * 1024); };
      auto clcheck = [&]() {
        OPAQUE_V(fl);
        TRV(58, 0);
        if (__builtin_expect(!__all(is_set(fl, epoch)), 0)) { TRV(58, 1); poll_all(rs, clo, epoch, dead); }
      };
      auto accinit = [&]() {
#pragma unroll
        for (int m = 0; m < 2; ++m)
#pragma unroll
          for (int pp = 0; pp < 2; ++pp) {
            auto r0_ = __builtin_amdgcn_permlane16_swap(xr[m][pp][0], xr[m][pp][2], false, false);
            auto r1_ = __builtin_amdgcn_permlane16_swap(xr[m][pp][1], xr[m][pp][3], false, false);
            acc[m][2 * pp] = f32x4{lo16(r0_[0]), hi16(r0_[0]), lo16(r1_[0]), hi16(r1_[0])};
            acc[m][2 * pp + 1] = f32x4{lo16(r0_[1]), hi16(r0_[1]), lo16(r1_[1]), hi16(r1_[1])};
          }
#pragma unroll
        for (int i = 0; i < 8; ++i) OPAQUE_V(acc[i >> 2][i & 3]);
      };
      WAITV(10);
      WAITL0;
      TRC(15); BAR;
      stO(0);
      if (lead) set_flags(rs, fl_line(FL_CLR + l, g), j, fl_line(FL_CLC + l, j), g, epoch, lane);
      if (isb) {
        stO(2); stO(3);
        WAITV(12);
        TRC(16); BAR;
        stO(1); stO(4); stO(5);
        __builtin_amdgcn_s_setprio(0);
        TRC(17); BAR;
        __builtin_amdgcn_s_sleep(BD);
        stO(7); stO(6);
        ld8_asm(fl, rs, clo);
        accinit();
        WAITV(0);
        clcheck();
      } else {
        u32x4 vb[8];
#pragma unroll
        for (int i = 0; i < 8; ++i) asm volatile("" : "=v"(vb[i]));
        stO(2); stO(3);
        ldres(14);
        WAITV(12);
        WAITL0;
        TRC(16); BAR;
        stO(1);
        ldres(15);
        if (l0f) {
          const bool wj = hw != 0;
          const char* bp = (const char*)(wj ? p.wqkvB : p.woB);
          unsigned rs_ = wj ? 6144 : 2048, sz = rs_ * 1024, ss = rs_ * 8, s2 = wj ? 16 * 6144 : 128;
          unsigned sb = g * 64 * rs_ + (wj ? (hw - 1) * 2048 + JOBC(j, hw - 1) * 128 : (j & 3) * (16 * 2048) + jr * 128);
          OPAQUE_S(bp); OPAQUE_S(rs_); OPAQUE_S(sz); OPAQUE_S(sb); OPAQUE_S(ss); OPAQUE_S(s2);
          const rsrc_t rb = mk_rsrc(bp, sz);
#pragma unroll
          for (int i = 0; i < 8; ++i) ld16in_asm(vb[i], rb, (lane >> 3) * rs_ + (lane & 7) * 16, sb + (i & 1) * ss + (i >> 1) * s2);
        }
        __builtin_amdgcn_s_setprio(0);
        KV_ADDR;
        bf16x8 sel = rowsum_sel(lane);
        unsigned h1 = (lane >> 3) * KVP_G + (lane & 7) * 16;
        unsigned pw = lds0 + SC_PHIK + (h * 128 + wm * 64 + 32 + fr) * 160 + fq * 8, kw = lds0 + L_PREV + hw * 2048 + lin;
        const unsigned wa = lds0 + L_WPHI + h * 2048 + (fq * 4 + (fr >> 2)) * 64 + (lane & 3) * 8;
        bf16x8 w0 = tr2(wa, wa + 1024), w1 = tr2(wa + 32, wa + 32 + 1024);
        float cq[4];
#pragma unroll
        for (int m = 0; m < 2; ++m) { cq[m] = lds_ld<float>(lds0 + L_RSTD + (16 + wm * 64 + 32 + m * 16 + fr) * 4) * 1.4426950408889634f; OPAQUE_V(cq[m]); }
        OPAQUE_V(sel); OPAQUE_V(h1); OPAQUE_V(pw); OPAQUE_V(kw); OPAQUE_V(w0); OPAQUE_V(w1);
        TRC(17); BAR;
        CLOBBER;
        {
          const f32x4 zero = {0.f, 0.f, 0.f, 0.f};
          f32x4 r[4][2];
#pragma unroll
          for (int m = 0; m < 2; ++m) {
            const bf16x8 f = lds_ld<bf16x8>(kw + m * 1024);
            r[m][0] = MFMA(w0, f, zero); r[m][1] = MFMA(w1, f, zero);
          }
          ld8_asm(fl, rs, clo);
          phik2(r, cq, pw);
        }
        WAITL0;
        TRC(19); BAR;
        kvpart(va, pa, pb, kvj + (unsigned)g * KVP_G, lane, wave);
        WAITV(2);
#pragma unroll
        for (int i = 0; i < 8; ++i) OPAQUE_V(vb[i]);
        clcheck();
        TRC(20); BAR;
        __builtin_amdgcn_s_setprio(3);
        stO(4); stO(5);
        const f32x4 zero = {0.f, 0.f, 0.f, 0.f};
        const unsigned s1 = kvj + (unsigned)((j ^ RX) - j) * KVP_J + g * 512 + hw * 128;
        __builtin_amdgcn_s_sleep(H1S);
        u32x4 v[2] = {ld16_sc1(rs, h1, s1), ld16_sc1(rs, h1, s1 + 8 * KVP_G)};
        TRC(21); TRV(50, 0);
        for (unsigned spins = 0; !dead && __any(umax4v(umax4v(0u, v[0]), v[1]) == SENT); ++spins) {
          if (spins > SPIN_CAP) { dead = true; break; }
          CLOBBER;
          TRV(50, spins + 1);
          v[0] = ld16_sc1(rs, h1, s1); v[1] = ld16_sc1(rs, h1, s1 + 8 * KVP_G);
        }
        TRC(22);
        const f32x4 c = rowsum_fin(MFMA(sel, as_frag(v[1]), MFMA(sel, as_frag(v[0]), zero)));
        if ((lane & 40) == 0)
          st8_sc1(rs, fr * 16 + fq * 8, kvj + (unsigned)g * KVP_G + KVP_SUM + hw * 128, nosent2(c));
        CLOBBER;
        __builtin_amdgcn_s_setprio(0);
        TRC(43); BAR;
        SCHED;
        accinit();
        stO(6); stO(7);
        if (l0f) {
          if (hw) {
            job_colsum(rs, vb, 1, g, JOBC(j, hw - 1), hw - 1, lane);
            if (SLOWC(1, JOBC(j, hw - 1))) {
#pragma unroll
              for (int i = 0; i < 8; ++i) job_pack(rs, vb[i], i, 1, g, JOBC(j, hw - 1), hw - 1, lane);
            }
          } else {
#pragma unroll
            for (int i = 0; i < 8; ++i)
              if (SLOWC(3, jr + (i >> 1))) st16_sc1(rs, (lane >> 3) * 128 + wo_swz(lane), WS_WO + (unsigned)((16 + jr + (i >> 1)) * 131072 + g * 8192 + ((j & 3) * 2 + (i & 1)) * 1024), vb[i]);
          }
        }
        WAITV(0);
      }
    }
    {
      LOCALS;
      const int fr = lane & 15, fq = lane >> 4;
      const int kh = half;
      unsigned v0 = lane * 16 + hw * 4096 + kh * 2048, v1 = lane * 16 + hw * 4096 + (kh ^ 1) * 2048;
      const int r0 = (JROT(j) >> 1) + NJ / 2;
      const unsigned ob0 = ob_off(g, kh, l);
      const unsigned pko = myrep + fl_line(FL_PK, JOBC(j, fq - 1)) + fr * 8;
      u32x4 R[8][4];
      u32x2 pkf;
      auto issue_ = [&](int i) {
        const unsigned so = ob0 + (unsigned)((r0 + i) & 7) * (2 * TS);
        R[i][0] = ld16_sc1(rs, v0, so); R[i][1] = ld16_sc1(rs, v1, so);
        R[i][2] = ld16_sc1(rs, v0 + 1024, so); R[i][3] = ld16_sc1(rs, v1 + 1024, so);
      };
      auto issue = [&](int i) { issue_(i); SCHED; };
      constexpr int LA = LOOKAHEAD;
      TRV(52, 0); TRV(53, 0); TRV(54, 0);
      TRR(61);
      __builtin_amdgcn_s_setprio(3);
      unsigned so0 = ob0 + (unsigned)(r0 & 7) * (2 * TS);
      OPAQUE_V(v0); OPAQUE_V(v1); OPAQUE_S(so0);
      TRC(25); BAR;
      SCHED;
      R[0][0] = ld16_sc1(rs, v0, so0); R[0][1] = ld16_sc1(rs, v1, so0);
      R[0][2] = ld16_sc1(rs, v0 + 1024, so0); R[0][3] = ld16_sc1(rs, v1 + 1024, so0);
      SCHED;
      __builtin_amdgcn_s_setprio(2);
      if (l == 0 && lead) set_flags(rs, fl_line(FL_PK, j), g, fl_line(FL_WO + 1, j), g, epoch, lane);

      auto vmax = [&](int i) { return umax4v(umax4v(umax4v(umax4v(0u, R[i][0]), R[i][1]), R[i][2]), R[i][3]); };
      auto bad = [&](int i) { return __any(umax4v(umax4v(umax4v(umax4v(0u, R[i][0]), R[i][1]), R[i][2]), R[i][3]) == SENT); };
      auto poll = [&](int i, int nfl) {
        for (unsigned spins = 0; !dead; ++spins) {
          if (spins > SPIN_CAP) { dead = true; break; }
          __builtin_amdgcn_s_sleep(2);
          CLOBBER;
          TRV(52 + (i > 2 ? 2 : i), spins + 1);
          issue(i);
#pragma unroll
          for (int k = i + 1; k < i + nfl && k < 8; ++k)
            if (bad(k)) issue(k);
          WAITV(0);
#pragma unroll
          for (int k = i + 1; k < i + nfl && k < 8; ++k)
#pragma unroll
            for (int f = 0; f < 4; ++f) OPAQUE_V(R[k][f]);
          if (!bad(i)) break;
        }
      };
      unsigned bb[4];
      {
        const int kr = fq * 4 + (fr >> 2);
        const int gs = (kr >> 1) & 3;
#pragma unroll
        for (int c = 0; c < 4; ++c) bb[c] = lds0 + kh * 8192 + kr * 128 + ((c ^ gs) * 32) + (lane & 3) * 8;
      }
      bf16x8 bf[2][4];
      auto rdB = [&](int i, int hh) {
        if (hh == 0) {
#pragma unroll
          for (int c = 0; c < 4; ++c) { bb[c] += wo_slot(i) - (i ? wo_slot(i - 1) : 0u); asm("" : "+v"(bb[c])); }
        }
#pragma unroll
        for (int c = 0; c < 4; ++c) bf[hh][c] = tr2(bb[c] + hh * 4096, bb[c] + hh * 4096 + 2048);
      };
      auto mms = [&](int i, int hh) {
#pragma unroll
        for (int n = 0; n < 4; ++n)
#pragma unroll
          for (int m = 0; m < 2; ++m) acc[m][n] = MFMA(bf[hh][n], as_frag(R[i][hh * 2 + m]), acc[m][n]);
      };
      if (!half) {
#pragma unroll
        for (int i = 0; i < 8; ++i) acc[i >> 2][i & 3] = f32x4{0.f, 0.f, 0.f, 0.f};
      }
      const char* obase = (l == 3) ? (const char*)p.out : (const char*)p.ws;
      unsigned osize = (l == 3) ? 2048 * 2048 : WS_SPAN;
      const bool slown = SLOWC((l & 1) ^ 1, j);
      pW = slown ? p.ws : (const char*)(isb ? p.wqkvA : p.wqkvB);
      bW = slown ? WS_WP + (unsigned)((isb ? 0 : 16) + j) * (16 * 24576) : j * 128;
      kW = slown ? 24576 : 64 * 6144; zW = slown ? WS_SPAN : 1024 * 6144; pcW = slown ? 8192 : 2048; hW = slown ? 4096 : 32 * 6144;
      vA = hw * 1024 + lane * 16;
      vW = slown ? vA : (hw * 8 + (lane >> 3)) * 6144 + (((lane & 7) ^ ((((lane >> 4) & 1) | ((hw & 1) << 1)) << 1)) * 16);
      pA = p.ws; zA = WS_SPAN; bA = xb_off(g, l); kA = TS; pcA = 4096; kb = JROT(j) + KX; dj = (j - kb) & 15;
      OPAQUE_S(pW); OPAQUE_S(bW); OPAQUE_S(kW); OPAQUE_S(zW); OPAQUE_S(pcW); OPAQUE_S(hW); OPAQUE_V(vA); OPAQUE_V(vW); OPAQUE_S(bA);
      unsigned sq = bW + j * kW + kh * hW, dst = lds0 + L_R1 + kh * 4096 + hw * 1024;
      OPAQUE_S(obase); OPAQUE_S(osize); OPAQUE_S(sq); OPAQUE_S(dst);
      const rsrc_t ro = mk_rsrc(obase, osize);
      unsigned so[2];
      unsigned xw = lds0 + SC_XCH + wave * 4096 + lane * 16;
      unsigned xa = lds0 + SC_XCH + (wave ^ 4) * 4096 + lane * 16;
      unsigned sb0 = (l == 3) ? g * (128 * 2048) + j * 128 : xb_off(g, l) + j * TS;
      {
        const int row = (hw * 2 + kh) * 16 + fr, ch = (fq & 1) * 2 + (fq >> 1);
        const unsigned sw = (l == 3) ? 0u : (unsigned)((row >> 1) & 7), rb = (unsigned)row << ((l == 3) ? 11 : 7);
#pragma unroll
        for (int pp = 0; pp < 2; ++pp) { so[pp] = rb + (((ch + 4 * pp) ^ sw) * 16); OPAQUE_V(so[pp]); }
        OPAQUE_V(xa); OPAQUE_V(xw); OPAQUE_S(sb0);
      }
      SCHED;
#pragma unroll
      for (int i = 1; i < LA; ++i) issue(i);
      rdB(0, 0);
      unsigned mx = vmax(0);
#pragma unroll
      for (int i = 0; i < 8; ++i) {
        SCHED;
        if (__builtin_expect(__any(mx == SENT), 0)) poll(i, LA);
        if (i == 0) TRC(27); else TRQ(27 + i);
        if (i == W1S - 2) { pkf = ld8_sc1(rs, pko, 0); SCHED; }
        if (i == W1S) {
          OPAQUE_V(pkf);
          if (l == 0 && lead && !__all(is_set(pkf, epoch))) poll_all(rs, pko, epoch, dead);
          TRC(35); BAR;
        }
        if (i == 7 && l < 3) dma3<8192>(mk_rsrc(pW, zW), vW, sq, pcW, dst);
        if (i < 4 && !isb) __builtin_amdgcn_s_sleep(PACE);
        if (i == 2) __builtin_amdgcn_s_setprio(1);
        if (i == 5) __builtin_amdgcn_s_setprio(0);
        SCHED;
        rdB(i, 1);
        if (i == 7) break;
        if (i + LA < 8) issue_(i + LA);
        mms(i, 0);
#pragma unroll
        for (int k = 0; k < 4; ++k) { SGB(0x008, 1); SGB(0x100, 1); SGB(0x020, 1); SGB(0x008, 1); SGB(0x100, 1); }
        SCHED;
        rdB(i + 1, 0); mx = vmax(i + 1);
        mms(i, 1);
#pragma unroll
        for (int k = 0; k < 4; ++k) { SGB(0x008, 1); SGB(0x100, 2); }
#pragma unroll
        for (int k = 4; k < 8; ++k) { SGB(0x008, 1); SGB(0x002, 8 / (8 - 4)); }
      }
#pragma unroll
      for (int n = 0; n < 4; ++n) acc[1][n] = MFMA(bf[0][n], as_frag(R[7][1]), acc[1][n]);
#pragma unroll
      for (int n = 0; n < 4; ++n) acc[0][n] = MFMA(bf[0][n], as_frag(R[7][0]), acc[0][n]);
#pragma unroll
      for (int k = 0; k < 8; ++k) { SGB(0x008, 1); SGB(0x100, 1); }
      SCHED;
#pragma unroll
      for (int n = 0; n < 4; ++n) acc[1][n] = MFMA(bf[1][n], as_frag(R[7][3]), acc[1][n]);
#pragma unroll
      for (int n = 0; n < 4; ++n) acc[0][n] = MFMA(bf[1][n], as_frag(R[7][2]), acc[0][n]);
#pragma unroll
      for (int n = 0; n < 4; ++n) lds_st<f32x4>(xw + n * 1024, acc[1][n]);
      SGB(0x008, 4);
#pragma unroll
      for (int k = 0; k < 4; ++k) { SGB(0x008, 1); SGB(0x200, 1); }
      SCHED;
      TRC(36);
      WAITL0;
      TRC(37); BAR;
      SCHED;
      f32x4 pa[4];
#pragma unroll
      for (int n = 0; n < 4; ++n) pa[n] = lds_ld<f32x4>(xa + n * 1024);
      SCHED;
      u32x4 o[2];
#pragma unroll
      for (int pp = 0; pp < 2; ++pp) {
        const f32x4 a = acc[0][2 * pp] + pa[2 * pp], b = acc[0][2 * pp + 1] + pa[2 * pp + 1];
        auto r0_ = __builtin_amdgcn_permlane16_swap(pk2(a[0], a[1]), pk2(b[0], b[1]), false, false);
        auto r1_ = __builtin_amdgcn_permlane16_swap(pk2(a[2], a[3]), pk2(b[2], b[3]), false, false);
        o[pp] = nosent(u32x4{r0_[0], r1_[0], r0_[1], r1_[1]});
        st16_sc1(ro, so[pp], sb0, o[pp]);
      }
      if (l < 3) {
        lds_st<u32x4>(lds0 + L_R2 + so[0], o[0]);
        lds_st<u32x4>(lds0 + L_R2 + so[1], o[1]);
        const f32x4 zero = {0.f, 0.f, 0.f, 0.f};
        const bf16x8 ones = {0x3f80, 0x3f80, 0x3f80, 0x3f80, 0x3f80, 0x3f80, 0x3f80, 0x3f80};
        const f32x4 a = MFMA(ones, as_frag(o[1]), MFMA(ones, as_frag(o[0]), zero));
        const f32x4 b = MFMA(as_frag(o[1]), as_frag(o[1]), MFMA(as_frag(o[0]), as_frag(o[0]), zero));
        const float q = (fr & 2) ? ((fr & 1) ? b[3] : b[2]) : ((fr & 1) ? b[1] : b[0]);
        if (fq == (fr >> 2)) st8_sc1(rs, ((hw * 2 + kh) * 16 + fr) * 8, st_off(g, l + 1) + j * 1024, u32x2{__float_as_uint(a[0]), __float_as_uint(q)});
      } else if (lead && blockIdx.x == 0 && lane == 0) {
        st8_sc1(rs, WS_EPOCH, 0, epoch);
      }
    }
    TRC(38); TRR(63);
  }
  STAMP_DUMP;
  asm volatile(".Lpf_end:");
}

extern "C" void kernel_launch(void* const* d_in, const int* in_sizes, int n_in, void* d_out, int out_size,
                              void* d_ws, size_t ws_size, hipStream_t stream) {
  Params p;
  p.x = (const u16*)d_in[0];
  p.wqkvA = (const u16*)d_in[1];
  p.wphi = (const u16*)d_in[2];
  p.woA = (const u16*)d_in[3];
  p.wqkvB = (const u16*)d_in[5];
  p.woB = (const u16*)d_in[6];
  p.out = (u16*)d_out;
  p.ws = (char*)d_ws;
  (void)hipFuncSetAttribute((const void*)mega_kernel, hipFuncAttributeMaxDynamicSharedMemorySize, L_TOTAL);
  mega_kernel<<<256, 512, L_TOTAL, stream>>>(p);
}
```

```cpp
#include <hip/hip_runtime.h>
#include <stdint.h>
#include <type_traits>
constexpr int LOOKAHEAD = 2;
constexpr int NVAL = 4;
constexpr int WT = 1;
constexpr int W1S = 6;
constexpr int NJ = 8;
constexpr int KX = 8;
constexpr int RX = 8;
constexpr unsigned TS = 17664;
#define XD 3
#define H1S 6
#define H2D 5
#define BD 8
#define PACE 4

typedef unsigned short u16;
typedef unsigned long long u64;
typedef __attribute__((ext_vector_type(8))) short bf16x8;
typedef __attribute__((ext_vector_type(4))) short s16x4;
typedef __attribute__((ext_vector_type(4))) float f32x4;
typedef __attribute__((ext_vector_type(2))) float f32x2;
typedef __attribute__((ext_vector_type(4))) unsigned u32x4;
typedef __attribute__((ext_vector_type(2))) unsigned u32x2;
typedef __attribute__((ext_vector_type(2))) __bf16 bf16x2_t;
#define LAS __attribute__((address_space(3)))
typedef LAS s16x4* lds_s16x4_ptr;

constexpr unsigned NREP = 8, REP_STRIDE = 17 * 2048;
enum { FL_X = 0, FL_CS = 4, FL_CLR = 5, FL_CLC = 9, FL_PK = 13, FL_WO = 14 };
__device__ __forceinline__ unsigned fl_line(int set, int a) { return (unsigned)(set * 16 + a) * 128; }
constexpr unsigned WS_EPOCH = 512u << 10;
constexpr unsigned WS_CS = 1u << 20;
constexpr unsigned WS_KVP = 4u << 20;
constexpr unsigned KVP_G = 32768, KVP_J = 16 * KVP_G, KVP_L = 16384, KVP_Z = 8192, KVP_SUM = 8704, KVP_HALO = 12u << 10;
constexpr unsigned SENT = 0xffffffffu;
constexpr unsigned WS_X = 14u << 20, WS_O = 29u << 20, WS_ST = 2u << 20;
__device__ __forceinline__ unsigned xb_off(int g, int l) { return WS_X + (unsigned)(l * 16 + g) * (16 * TS); }
__device__ __forceinline__ unsigned ob_off(int g, int j, int l) { return WS_O + (unsigned)((l * 16 + g) * 16 + j) * TS; }
__device__ __forceinline__ unsigned st_off(int g, int l) { return WS_ST + (unsigned)(l * 16 + g) * (16u << 10); }
constexpr unsigned WS_WP = 48u << 20;
constexpr unsigned WS_WO = 60u << 20;
constexpr unsigned WS_SPAN = 64u << 20;

constexpr unsigned L_R1 = 0;
constexpr unsigned L_R2 = 98304, XS = 16384;
constexpr unsigned L_MISC = 147456;
constexpr unsigned L_MEAN = L_MISC;
constexpr unsigned L_RSTD = L_MISC + 576;
constexpr unsigned L_CS = L_MISC + 1152;
constexpr unsigned L_WPHI = L_MISC + 2688;
constexpr unsigned L_SINK = L_MISC + 6912;
constexpr unsigned L_ZF = L_MISC + 7168;
constexpr unsigned L_PREV = L_MISC + 8192;
constexpr unsigned L_TOTAL = 163840;

#define JROT(j) ((j) & ~(NJ - 1))
constexpr int NX = NJ / 2;
#define KB0(g, j) (JROT(j) + (16 / NX) * ((g) & (NX - 1)))
#define JOBC(j, w) (((j) & ~3) + (((j) + (w) + 1) & 3))

#define SLOWC(m, jc) ((slowm >> ((m) * 8 + ((jc) & 7))) & 1)

struct Params {
  const u16* x;
  const u16* wqkvA;
  const u16* wphi;
  const u16* woA;
  const u16* wqkvB;
  const u16* woB;
  u16* out;
  char* ws;
};

#define SCHED __builtin_amdgcn_sched_barrier(0)
#define BAR __builtin_amdgcn_s_barrier()
#define SGB(m, n) __builtin_amdgcn_sched_group_barrier(m, n, 0)
#define WAITV(n) asm volatile("s_waitcnt vmcnt(%0)" ::"n"(n) : "memory")
#define WAITL0 asm volatile("s_waitcnt lgkmcnt(0)" ::: "memory")

__device__ __forceinline__ unsigned pk2(float a, float b) {
  f32x2 v = {a, b};
  return __builtin_bit_cast(unsigned, __builtin_convertvector(v, bf16x2_t));
}
__device__ __forceinline__ float lo16(unsigned u) { return __uint_as_float(u << 16); }
__device__ __forceinline__ float hi16(unsigned u) { return __uint_as_float(u & 0xffff0000u); }

template <int A, int B, typename F> __device__ __forceinline__ void static_for(F f) {
  if constexpr (A < B) { f(std::integral_constant<int, A>{}); static_for<A + 1, B>(f); }
}
typedef __amdgpu_buffer_rsrc_t rsrc_t;
__device__ __forceinline__ rsrc_t mk_rsrc(const void* p, unsigned bytes) { return __builtin_amdgcn_make_buffer_rsrc((void*)p, 0, bytes, 0x00020000); }
#define UNI(x) __builtin_amdgcn_readfirstlane(x)
#define DMA_HEAD "s_mov_b32 m0, %4\n\ts_mov_b32 %0, %3\n\ts_nop 2\n\tbuffer_load_dwordx4 %1, %2, %0 offen sc1 lds\n\t"
#define DMA_NEXT "s_add_u32 m0, m0, %6\n\ts_add_u32 %0, %0, %5\n\tbuffer_load_dwordx4 %1, %2, %0 offen sc1 lds\n\t"
#define DMA_OPS : "=&s"(t) : "v"(voff), "s"(r), "s"(UNI(soff)), "s"(UNI(lds)), "s"(UNI(sstep)), "i"(LSTEP) : "memory", "scc", "m0"
template <int LSTEP> __device__ __forceinline__ void dma4(rsrc_t r, unsigned voff, unsigned soff, unsigned sstep, unsigned lds) {
  unsigned t;
  asm volatile(DMA_HEAD DMA_NEXT DMA_NEXT DMA_NEXT DMA_OPS);
}
template <int LSTEP> __device__ __forceinline__ void dma3(rsrc_t r, unsigned voff, unsigned soff, unsigned sstep, unsigned lds) {
  unsigned t;
  asm volatile(DMA_HEAD DMA_NEXT DMA_NEXT DMA_OPS);
}
template <int LSTEP, int N> __device__ __forceinline__ void bar_dma4(rsrc_t r, unsigned voff, unsigned soff, unsigned sstep, unsigned lds) {
  unsigned t;
  asm volatile("s_mov_b32 m0, %4\n\ts_mov_b32 %0, %3\n\ts_nop 0\n\ts_waitcnt vmcnt(%7) lgkmcnt(0)\n\ts_barrier\n\t"
               "buffer_load_dwordx4 %1, %2, %0 offen sc1 lds\n\t" DMA_NEXT DMA_NEXT DMA_NEXT
               : "=&s"(t) : "v"(voff), "s"(r), "s"(UNI(soff)), "s"(UNI(lds)), "s"(UNI(sstep)), "i"(LSTEP), "n"(N) : "memory", "scc", "m0");
}
__device__ __forceinline__ void dma1(rsrc_t r, unsigned voff, unsigned soff, unsigned lds) {
  asm volatile("s_mov_b32 m0, %3\n\ts_nop 3\n\tbuffer_load_dwordx4 %0, %1, %2 offen sc1 lds" ::"v"(voff), "s"(r), "s"(UNI(soff)), "s"(UNI(lds)) : "memory", "m0");
}
__device__ __forceinline__ void touch(rsrc_t r, unsigned voff, unsigned lds) {
  asm volatile("s_mov_b32 m0, %2\n\ts_nop 3\n\tbuffer_load_dword %0, %1, 0 offen lds" ::"v"(voff), "s"(r), "s"(UNI(lds)) : "memory", "m0");
}
__device__ __forceinline__ void ld8_asm(u32x2& d, rsrc_t r, unsigned voff) {
  asm volatile("s_nop 4\n\tbuffer_load_dwordx2 %0, %1, %2, 0 offen sc1" : "=v"(d) : "v"(voff), "s"(r) : "memory");
}

__device__ __forceinline__ void ld16in_asm(u32x4& d, rsrc_t r, unsigned voff, unsigned soff) {
  asm volatile("s_nop 4\n\tbuffer_load_dwordx4 %0, %1, %2, %3 offen" : "=v"(d) : "v"(voff), "s"(r), "s"(UNI(soff)) : "memory");
}
__device__ __forceinline__ void ld16_asm(f32x4& d, rsrc_t r, unsigned voff, unsigned soff) {
  asm volatile("s_nop 4\n\tbuffer_load_dwordx4 %0, %1, %2, %3 offen sc1" : "=v"(d) : "v"(voff), "s"(r), "s"(UNI(soff)) : "memory");
}

template <typename T> __device__ __forceinline__ T lds_ld(unsigned a) { return *(LAS T*)(a); }
template <typename T> __device__ __forceinline__ void lds_st(unsigned a, T v) { *(LAS T*)(a) = v; }
__device__ __forceinline__ bf16x8 tr2(unsigned a0, unsigned a1) {
  s16x4 v0 = __builtin_amdgcn_ds_read_tr16_b64_v4i16((lds_s16x4_ptr)(a0));
  s16x4 v1 = __builtin_amdgcn_ds_read_tr16_b64_v4i16((lds_s16x4_ptr)(a1));
  return __builtin_shufflevector(v0, v1, 0, 1, 2, 3, 4, 5, 6, 7);
}
__device__ __forceinline__ bf16x8 as_frag(u32x4 v) { return __builtin_bit_cast(bf16x8, v); }
#define MFMA(a, b, c) __builtin_amdgcn_mfma_f32_16x16x32_bf16(a, b, c, 0, 0, 0)

__device__ __forceinline__ float vmax2(float a, float b) { float inf = __builtin_inff(); asm("" : "+s"(inf)); return __builtin_amdgcn_fmed3f(a, b, inf); }
#define SWAP16(a, b) __builtin_amdgcn_permlane16_swap(__float_as_uint(a), __float_as_uint(b), false, false)
#define SWAP32(a, b) __builtin_amdgcn_permlane32_swap(__float_as_uint(a), __float_as_uint(b), false, false)
__device__ __forceinline__ void red2_max(float& a, float& b) {
  auto r = SWAP16(a, b);
  float x = vmax2(__uint_as_float(r[0]), __uint_as_float(r[1]));
  r = SWAP32(x, x);
  x = vmax2(__uint_as_float(r[0]), __uint_as_float(r[1]));
  r = SWAP16(x, x);
  a = __uint_as_float(r[0]); b = __uint_as_float(r[1]);
}
__device__ __forceinline__ void red2_add(float& a, float& b) {
  auto r = SWAP16(a, b);
  float x = __uint_as_float(r[0]) + __uint_as_float(r[1]);
  r = SWAP32(x, x);
  x = __uint_as_float(r[0]) + __uint_as_float(r[1]);
  r = SWAP16(x, x);
  a = __uint_as_float(r[0]); b = __uint_as_float(r[1]);
}
template <bool MAX, typename M> __device__ __forceinline__ void red4(float& a, float& b, float& c, float& d, M mid) {
  auto op = [](unsigned x, unsigned y) { return MAX ? vmax2(__uint_as_float(x), __uint_as_float(y)) : __uint_as_float(x) + __uint_as_float(y); };
  auto r = SWAP16(a, b), q = SWAP16(c, d);
  const float x = op(r[0], r[1]), y = op(q[0], q[1]);
  r = SWAP32(x, y);
  const float w = mid(op(r[0], r[1]));
  r = SWAP32(w, w);
  q = __builtin_amdgcn_permlane16_swap(r[1], r[1], false, false);
  r = __builtin_amdgcn_permlane16_swap(r[0], r[0], false, false);
  a = __uint_as_float(r[0]); b = __uint_as_float(r[1]); c = __uint_as_float(q[0]); d = __uint_as_float(q[1]);
}
__device__ __forceinline__ float red_max4(float x) {
  auto r = __builtin_amdgcn_permlane16_swap(__float_as_uint(x), __float_as_uint(x), false, false);
  x = vmax2(__uint_as_float(r[0]), __uint_as_float(r[1]));
  r = __builtin_amdgcn_permlane32_swap(__float_as_uint(x), __float_as_uint(x), false, false);
  return vmax2(__uint_as_float(r[0]), __uint_as_float(r[1]));
}
__device__ __forceinline__ float red_add4(float x) {
  auto r = __builtin_amdgcn_permlane16_swap(__float_as_uint(x), __float_as_uint(x), false, false);
  x = __uint_as_float(r[0]) + __uint_as_float(r[1]);
  r = __builtin_amdgcn_permlane32_swap(__float_as_uint(x), __float_as_uint(x), false, false);
  return __uint_as_float(r[0]) + __uint_as_float(r[1]);
}

__device__ __forceinline__ void st16_sc1(rsrc_t r, unsigned voff, unsigned soff, u32x4 v) {
  __builtin_amdgcn_raw_buffer_store_b128(v, r, voff, soff, 16);
  asm volatile("s_nop 1" ::"v"(v) : "memory");
}
__device__ __forceinline__ void st8_sc1(rsrc_t r, unsigned voff, unsigned soff, u32x2 v) { __builtin_amdgcn_raw_buffer_store_b64(v, r, voff, soff, 16); }
__device__ __forceinline__ u32x4 ld16_sc1(rsrc_t r, unsigned voff, unsigned soff) { return __builtin_amdgcn_raw_buffer_load_b128(r, voff, soff, 16); }
__device__ __forceinline__ u32x2 ld8_sc1(rsrc_t r, unsigned voff, unsigned soff) { return __builtin_amdgcn_raw_buffer_load_b64(r, voff, soff, 16); }
__device__ __forceinline__ f32x4 ld4f_sc1(rsrc_t r, unsigned voff, unsigned soff) { return __builtin_bit_cast(f32x4, __builtin_amdgcn_raw_buffer_load_b128(r, voff, soff, 16)); }
#define OPAQUE_V(x) asm volatile("" : "+v"(x))
#define OPAQUE_S(x) asm volatile("" : "+s"(x))

#define CLOBBER asm volatile("" ::: "memory")
constexpr unsigned SPIN_CAP = 1u << 17;
__device__ __forceinline__ bool is_set(u32x2 v, u32x2 epoch) { return v[0] == epoch[0] && v[1] == epoch[1]; }
__device__ __forceinline__ void set_flags(rsrc_t rs, unsigned lineA, int colA, unsigned lineB, int colB, u32x2 epoch, int lane) {
  const unsigned off = (lane & 7) * REP_STRIDE + ((lane & 8) ? lineB + colB * 8 : lineA + colA * 8);
  if (lane < 16) st8_sc1(rs, off, 0, epoch);
}
__device__ __forceinline__ unsigned poll_need(rsrc_t rs, unsigned voff, u32x2 epoch, unsigned ready, unsigned need, bool& dead) {
  for (unsigned spins = 0; !dead; ++spins) {
    CLOBBER;
    ready |= (unsigned)__ballot(is_set(ld8_sc1(rs, voff, 0), epoch));
    if ((ready & need) == need) break;
    if (spins > SPIN_CAP) dead = true;
    __builtin_amdgcn_s_sleep(1);
  }
  CLOBBER;
  return dead ? ~0u : ready;
}

__device__ __forceinline__ void poll_all(rsrc_t rs, unsigned voff, u32x2 epoch, bool& dead) {
  for (unsigned spins = 0; !dead; ++spins) {
    CLOBBER;
    if (__all(is_set(ld8_sc1(rs, voff, 0), epoch))) break;
    if (spins > SPIN_CAP) dead = true;
    __builtin_amdgcn_s_sleep(1);
  }
  CLOBBER;
}

constexpr unsigned SC_VF = L_R1 + 32768;
constexpr unsigned SC_PHIK = L_R1 + 49152;
constexpr unsigned SC_XCH = L_R2 + XS;
constexpr unsigned wo_slot(int s) {
  return s < 2 ? L_R2 + (unsigned)(s + 1) * XS : (s < 6 ? L_R1 + (unsigned)(s - 2) * 16384 : (s == 6 ? L_R1 + 81920 : L_R2));
}
__device__ __forceinline__ unsigned wo_src(int s, int j) { return (unsigned)(((JROT(j) >> 1) + NJ / 2 + s) & 7); }
__device__ __forceinline__ unsigned wo_swz(int lane) { return (unsigned)(((lane & 7) ^ ((lane >> 3) & 6)) * 16); }
__device__ __forceinline__ unsigned umax3(unsigned a, unsigned b, unsigned c) { return max(max(a, b), c); }
__device__ __forceinline__ unsigned umax4v(unsigned m, const u32x4& v) { return umax3(umax3(m, v[0], v[1]), v[2], v[3]); }
__device__ __forceinline__ u32x4 nosent(u32x4 v) { return u32x4{min(v[0], SENT - 1), min(v[1], SENT - 1), min(v[2], SENT - 1), min(v[3], SENT - 1)}; }

template <typename P, typename F>
__device__ __forceinline__ void qkv_loop(int isb, int wm, int h, int lane, bool first, P pre, F fin) {
  constexpr unsigned lds0 = 0;
  f32x4 acc[4][6];
  const int fr = lane & 15, fq = lane >> 4;
  unsigned ab[2], bb[2];
  {
    const int r = wm * 64 + fr;
#pragma unroll
    for (int ks = 0; ks < 2; ++ks) ab[ks] = lds0 + L_R2 + r * 128 + (((ks * 4 + fq) ^ ((fr >> 1) & 7)) * 16);
    const int kr = fq * 8 + (fr >> 2);
    const int gs = ((kr >> 1) & 1) | (((kr >> 3) & 1) << 1);
#pragma unroll
    for (int c = 0; c < 2; ++c) bb[c] = lds0 + L_R1 + kr * 128 + (((h * 2 + c) ^ gs) * 32) + (lane & 3) * 8;
  }
#pragma unroll
  for (int m = 0; m < 4; ++m)
#pragma unroll
    for (int n = 0; n < 6; ++n) acc[m][n] = f32x4{0.f, 0.f, 0.f, 0.f};
  bf16x8 af[2][4], bf[2][6];
  auto rds = [&](int ks, int p, int rot = 0) {
#pragma unroll
    for (int m = 0; m < 4; ++m) af[p][m] = lds_ld<bf16x8>(ab[ks] + m * 2048);
#pragma unroll
    for (int nn = 0; nn < 6; ++nn) {
      const int n = (nn + rot) % 6;
      const unsigned a = bb[n & 1] + (n >> 1) * 8192 + ks * 4096;
      bf[p][n] = tr2(a, a + 512);
    }
  };
  auto mms = [&](int p, int rot = 0, int cnt = 6) {
#pragma unroll
    for (int nn = 0; nn < cnt; ++nn)
#pragma unroll
      for (int mm = 0; mm < 4; ++mm) { const int n = (nn + rot) % 6, m = (n & 1) ? 3 - mm : mm; acc[m][n] = n < 4 ? MFMA(bf[p][n], af[p][m], acc[m][n]) : MFMA(af[p][m], bf[p][n], acc[m][n]); }
  };
  auto ilv2 = [&]() {
#pragma unroll
    for (int i = 0; i < 16; ++i) { SGB(0x008, 1); SGB(0x100, 1); }
#pragma unroll
    for (int i = 0; i < 4; ++i) { SGB(0x008, 1); SGB(0x002, 1); }
    SGB(0x008, 4);
  };
  auto ilv3 = [&]() {
#pragma unroll
    for (int i = 0; i < 16; ++i) { SGB(0x008, 1); SGB(0x100, 1); }
#pragma unroll
    for (int i = 0; i < 7; ++i) { SGB(0x008, 1); SGB(0x004, 2); }
    SGB(0x008, 1);
  };
#pragma unroll
  for (int i = 0; i < 24; ++i) OPAQUE_V(acc[i / 6][i % 6]);
  OPAQUE_V(ab[0]); OPAQUE_V(ab[1]); OPAQUE_V(bb[0]); OPAQUE_V(bb[1]);
  if (!first) { WAITV(3); WAITL0; }
  BAR;
  rds(0, 0);
  int sa = 0, sb = 0;
  int da = XS, db = 24576;
  auto step = [&]() {
    ab[0] += da; ab[1] += da;
    bb[0] += db; bb[1] += db;
  };
  auto ring = [&]() {
    sa = (sa == 2) ? 0 : sa + 1;
    sb = (sb == 3) ? 0 : sb + 1;
    da = (sa == 2) ? -2 * (int)XS : (int)XS;
    db = (sb == 3) ? -3 * 24576 : 24576;
  };
#pragma unroll 1
  for (int t = 14; t > 0; --t) {
    SCHED;
    rds(1, 1); mms(0);
    step();
    ilv2();
    SCHED;
    __builtin_amdgcn_s_waitcnt(0x0070);
    BAR;
    SCHED;
    rds(0, 0);
    ring();
    mms(1); ilv3();
  }
  SCHED;
  f32x4 cs[4], nm4[4];
  float nmean[4], csv[2];
  auto fold = [&](int n) {
#pragma unroll
    for (int mm = 0; mm < 4; ++mm) {
      const int m = (n & 1) ? 3 - mm : mm;
#pragma unroll
      for (int e = 0; e < 4; ++e) {
        float t = n < 4 ? __builtin_fmaf(nmean[m], cs[n & 3][e], acc[m][n][e]) : __builtin_fmaf(nm4[m][e], csv[n & 1], acc[m][n][e]);
        asm("" : "+v"(t));
        acc[m][n][e] = t;
      }
    }
  };
  {
    unsigned pm = lds0 + L_MEAN + (16 + wm * 64 + fr) * 4, pc = lds0 + L_CS + isb * 768 + (h * 32 + fq * 4) * 4;
    unsigned pm4 = lds0 + L_MEAN + (16 + wm * 64 + fq * 4) * 4, pcv = lds0 + L_CS + isb * 768 + (128 + h * 32 + fr) * 4;
    asm("" : "+v"(pm)); asm("" : "+v"(pc)); asm("" : "+v"(pm4)); asm("" : "+v"(pcv));
#pragma unroll
    for (int m = 0; m < 4; ++m) nmean[m] = lds_ld<float>(pm + m * 64);
#pragma unroll
    for (int n = 0; n < 4; ++n) cs[n ^ 2] = lds_ld<f32x4>(pc + (((n ^ 2) >> 1) * 64 + (n & 1) * 16) * 4);
#pragma unroll
    for (int m = 0; m < 4; ++m) nm4[m] = lds_ld<f32x4>(pm4 + m * 64);
#pragma unroll
    for (int n = 0; n < 2; ++n) csv[n] = lds_ld<float>(pcv + n * 64);
    rds(1, 1, 4); mms(0, 2);
    step();
    fold(2); fold(3);
    SGB(0x002, 8);
#pragma unroll
    for (int i = 0; i < 8; ++i) { SGB(0x008, 1); SGB(0x100, 2); }
#pragma unroll
    for (int i = 0; i < 11; ++i) { SGB(0x008, 1); SGB(0x100, 1); SGB(0x002, 2); }
#pragma unroll
    for (int i = 0; i < 5; ++i) { SGB(0x008, 1); SGB(0x002, 3); }
    SCHED;
    __builtin_amdgcn_s_waitcnt(0xC07F);
    BAR;
    SCHED;
    rds(0, 0, 2);
    mms(1, 4); fold(4); fold(5);
#pragma unroll
    for (int i = 0; i < 8; ++i) { SGB(0x008, 1); SGB(0x100, 1); SGB(0x002, 1); }
#pragma unroll
    for (int i = 0; i < 8; ++i) { SGB(0x008, 1); SGB(0x100, 1); SGB(0x002, 2); }
#pragma unroll
    for (int i = 0; i < 8; ++i) { SGB(0x008, 1); SGB(0x002, 2); }
    SCHED;
    pre(0);
    SCHED;
    rds(1, 1, 2);
    mms(0, 2, 4); fold(0); fold(1);
    pre(1);
#pragma unroll
    for (int i = 0; i < 16; ++i) { SGB(0x008, 1); SGB(0x100, 1); SGB(0x002, 2); SGB(0x004, 1); }
  }
  SCHED;
  auto mm6 = [&](int m, int n0 = 0, int n1 = 8) {
#pragma unroll
    for (int nn = n0; nn < n1; ++nn) {
      const int n = nn < 4 ? nn + 2 : nn & 1, p = (nn >> 1) != 2;
      acc[m][n] = n < 4 ? MFMA(bf[p][n], af[p][m], acc[m][n]) : MFMA(af[p][m], bf[p][n], acc[m][n]);
    }
  };
  if (!isb) {
    fin(std::false_type{}, acc, mm6);
#pragma unroll
    for (int i = 0; i < 24; ++i) asm volatile("" : "=v"(acc[i / 6][i % 6]));
#pragma unroll
    for (int i = 0; i < 6; ++i) asm volatile("" : "=v"(bf[1][i]));
#pragma unroll
    for (int i = 0; i < 8; ++i) asm volatile("" : "=v"(af[i >> 2][i & 3]));
    asm volatile("" : "=v"(bf[0][0])); asm volatile("" : "=v"(bf[0][1]));
  }
  OPAQUE_S(isb);
  if (isb) fin(std::true_type{}, acc, mm6);
}
__device__ __forceinline__ u32x4 pack(const f32x4& a, const f32x4& b) { return u32x4{pk2(a[0], a[1]), pk2(a[2], a[3]), pk2(b[0], b[1]), pk2(b[2], b[3])}; }

__device__ __forceinline__ u32x2 nosent2(const f32x4& a) { return u32x2{min(pk2(a[0], a[1]), SENT - 1), min(pk2(a[2], a[3]), SENT - 1)}; }
template <int N> __device__ __forceinline__ void hedgehog(const f32x4 (&r)[4][2], const float (&c)[4], float (&ep)[4][8], float (&en)[4][8]) {
  float mx[4], nm[4];
#pragma unroll
  for (int m = 0; m < N; ++m) {
    const f32x4 a = r[m][0], b = r[m][1];
    mx[m] = vmax2(fmaxf(fmaxf(a[3], b[0]), b[1]), fmaxf(fmaxf(fmaxf(fmaxf(a[0], a[1]), a[2]), b[2]), b[3]));
    nm[m] = vmax2(fmaxf(fmaxf(-a[3], -b[0]), -b[1]), fmaxf(fmaxf(fmaxf(fmaxf(-a[0], -a[1]), -a[2]), -b[2]), -b[3]));
  }
  SCHED;
  red4<true>(mx[0], mx[1], nm[0], nm[1], [](float x) { return x; });
  if (N == 4) red4<true>(mx[2], mx[3], nm[2], nm[3], [](float x) { return x; });
#pragma unroll
  for (int m = 0; m < N; ++m) { mx[m] *= -c[m]; nm[m] *= -c[m]; }
  SCHED;
#pragma unroll
  for (int m = 0; m < N; ++m) {
#pragma unroll
    for (int i = 0; i < 8; ++i) { ep[m][i] = __builtin_fmaf(r[m][i >> 2][i & 3], c[m], mx[m]); en[m][i] = __builtin_fmaf(r[m][i >> 2][i & 3], -c[m], nm[m]); }
    SCHED;
#pragma unroll
    for (int i = 0; i < 8; ++i) { ep[m][i] = __builtin_amdgcn_exp2f(ep[m][i]); en[m][i] = __builtin_amdgcn_exp2f(en[m][i]); }
    SCHED;
  }
}
__device__ __forceinline__ u32x4 pack8(const float (&e)[8], float f) {
  return u32x4{pk2(e[0] * f, e[1] * f), pk2(e[2] * f, e[3] * f), pk2(e[4] * f, e[5] * f), pk2(e[6] * f, e[7] * f)};
}
__device__ __forceinline__ u32x4 pack8(const float (&e)[8]) { return u32x4{pk2(e[0], e[1]), pk2(e[2], e[3]), pk2(e[4], e[5]), pk2(e[6], e[7])}; }

constexpr u64 window_mask(int t, int e) {
  u64 mk = 0;
  for (int ln = 0; ln < 64; ++ln) {
    const int delta = (ln & 15) + 16 - t * 16 - (ln >> 4) * 4 - e;
    if (delta >= 0 && delta <= 14 && !(delta & 1)) mk |= 1ull << ln;
  }
  return mk;
}
__device__ __forceinline__ void phik2(const f32x4 (&r)[4][2], const float (&c)[4], unsigned pw) {
  const f32x4 zero = {0.f, 0.f, 0.f, 0.f};
  const bf16x8 ones = {0x3f80, 0x3f80, 0x3f80, 0x3f80, 0x3f80, 0x3f80, 0x3f80, 0x3f80};
  float ep[4][8], en[4][8];
  f32x4 sp[2], sn[2];
  hedgehog<2>(r, c, ep, en);
#pragma unroll
  for (int m = 0; m < 2; ++m) { sp[m] = MFMA(ones, as_frag(pack8(ep[m])), zero); sn[m] = MFMA(ones, as_frag(pack8(en[m])), zero); }
#pragma unroll
  for (int m = 0; m < 2; ++m) { asm("" : "+v"(sp[m])); asm("" : "+v"(sn[m])); }
  SCHED;
#pragma unroll
  for (int m = 0; m < 2; ++m) {
    const float fp = __builtin_amdgcn_rcpf(sp[m][0]), fn = __builtin_amdgcn_rcpf(sn[m][0]);
#pragma unroll
    for (int i = 0; i < 8; ++i) { ep[m][i] *= fp; en[m][i] *= fn; }
    SCHED;
    const u32x4 pp = pack8(ep[m]), pn = pack8(en[m]);
    const unsigned d = pw + m * (16 * 160);
    lds_st<u32x2>(d, u32x2{pp[0], pp[1]});
    lds_st<u32x2>(d + 32, u32x2{pp[2], pp[3]});
    lds_st<u32x2>(d + 64, u32x2{pn[0], pn[1]});
    lds_st<u32x2>(d + 96, u32x2{pn[2], pn[3]});
    SCHED;
  }
}
__device__ __forceinline__ bf16x8 rowsum_sel(int lane) {
  const int fr = lane & 15;
  const unsigned one = (fr < 8) ? (0x3f80u << (16 * (fr & 1))) : 0u;
  const int e = (fr >> 1) & 3;
  return as_frag(u32x4{e == 0 ? one : 0u, e == 1 ? one : 0u, e == 2 ? one : 0u, e == 3 ? one : 0u});
}
__device__ __forceinline__ f32x4 rowsum_fin(f32x4 c) {
#pragma unroll
  for (int k = 0; k < 4; ++k) {
    const float a = c[k];
    c[k] = a + __int_as_float(__builtin_amdgcn_update_dpp(0, __float_as_int(a), 0x128  , 0xf, 0xf, false));
  }
  return c;
}
__device__ __forceinline__ void job_colsum(rsrc_t rs, const u32x4 (&w)[8], int mat, int g, int jc, int hw, int lane) {
  const bf16x8 sel = rowsum_sel(lane);
  f32x4 c = {0.f, 0.f, 0.f, 0.f};
#pragma unroll
  for (int i = 0; i < 8; ++i) c = MFMA(sel, as_frag(w[i]), c);
  const u32x4 w4 = __builtin_bit_cast(u32x4, rowsum_fin(c));
  unsigned so = WS_CS + (unsigned)(((mat * 16 + jc) * 16 + g) * 768);
  OPAQUE_S(so);
  if ((lane & 40) == 0) st16_sc1(rs, (unsigned)(hw * 64 + (lane & 7) * 8 + (lane >> 4) * 4) * 4, so, w4);
}
__device__ __forceinline__ unsigned img_off(int i, int lane) { return (lane >> 3) * 128 + (((lane & 7) ^ ((((lane >> 4) & 1) | ((i & 1) << 1)) << 1)) * 16); }
__device__ __forceinline__ void job_pack(rsrc_t rs, u32x4 w, int i, int mat, int g, int jc, int hw, int lane) {
  st16_sc1(rs, img_off(i, lane),
           WS_WP + (unsigned)(((mat * 16 + jc) * 16 + g) * 24576 + hw * 8192 + i * 1024), w);
}

__global__ __launch_bounds__(512, 2) void mega_kernel(Params p) {
  extern __shared__ __attribute__((aligned(16))) char smem[];
  const int tid = threadIdx.x;
  const int wave0 = __builtin_amdgcn_readfirstlane(tid >> 6);
  const int xx = blockIdx.x & 7, rr = blockIdx.x >> 3;
  const int g0 = (rr / NJ) * NX + (xx & (NX - 1)), j0 = (xx / NX) * NJ + (rr & (NJ - 1));
  constexpr unsigned lds0 = 0, lds00 = 0;
  if ((unsigned)(size_t)(LAS char*)smem != 0u) return;
#define LOCALS0 int g = g0, j = j0, l = l0, wave = wave0;                                              \
  OPAQUE_S(g); OPAQUE_S(j); OPAQUE_S(l); OPAQUE_S(wave);                                               \
  const int hw = wave & 3, wm = wave & 1, h = (wave >> 1) & 1;                                         \
  const unsigned myrep = (unsigned)(JROT(j) / 2 + (g & (NX - 1))) * REP_STRIDE;                              \
  (void)hw; (void)wm; (void)h; (void)myrep;                                                            \
  int lane = tid; OPAQUE_V(lane); lane &= 63
#define LOCALS LOCALS0; const bool isb = l & 1, lead = (wave == 4); const int half = wave >> 2; (void)isb; (void)lead; (void)half
  const rsrc_t rs = mk_rsrc(p.ws, WS_SPAN);
  const bool cw = wave0 < 4;
  if (wave0 == 3) {
    const char* pc;
    unsigned len;
    asm volatile("s_getpc_b64 %0\n.Lpf_base:\n\ts_mov_b32 %1, .Lpf_end-.Lpf_base" : "=s"(pc), "=s"(len));
    const unsigned off = ((unsigned)rr * 16 + (tid & 15)) * 128;
    touch(mk_rsrc(pc, len), ((int)len > 0 && off + 4 <= len) ? off : 0u, lds00 + L_SINK);
  }
  if (wave0 == 2) touch(rs, WS_EPOCH, lds00 + L_SINK);
  if (wave0 == 4) touch(mk_rsrc(p.wphi, 32 * 2048), j0 * 4096, lds00 + L_SINK);
  if (wave0 == 5) touch(mk_rsrc(p.woA, 1024 * 2048), (tid & 1) << 20, lds00 + L_SINK);
  if (wave0 == 6) touch(mk_rsrc(p.wqkvB, 1024 * 6144), min(tid & 7, 5) << 20, lds00 + L_SINK);
  if (wave0 == 7) touch(mk_rsrc(p.woB, 1024 * 2048), (tid & 1) << 20, lds00 + L_SINK);
  auto slowbit = [](const void* q) { return 1u << ((3 - ((unsigned)(size_t)q >> 7)) & 7); };
  unsigned slowm = slowbit(p.wqkvA) | (slowbit(p.wqkvB) << 8) | (slowbit(p.woA) << 16) | (slowbit(p.woB) << 24);
  OPAQUE_S(slowm);
  u32x2 epoch = {0u, 0u};
  bool dead = false;
      if (cw) {
        const int l0 = 0;
        LOCALS;
        u32x4 v[2][8];
        if (wave < 3) {
          const int jc = JOBC(j, wave), kt = (KB0(g, j) + (unsigned)g / NX) & 15;
          const rsrc_t ra = mk_rsrc(p.wqkvA, 1024 * 6144);
#pragma unroll
          for (int i = 0; i < 8; ++i)
            v[0][i] = __builtin_amdgcn_raw_buffer_load_b128(ra, (lane >> 3) * 6144 + (lane & 7) * 16, (kt * 64 + i * 8) * 6144 + wave * 2048 + jc * 128, 0);
          SCHED;
          BAR; SCHED;
          job_colsum(rs, v[0], 0, g, jc, wave, lane);
          if (SLOWC(0, jc)) {
#pragma unroll
            for (int i = 0; i < 8; ++i) job_pack(rs, v[0][i], i, 0, kt, jc, wave, lane);
          }
        } else {
          const int fr = lane & 15, fq = lane >> 4;
          const rsrc_t rx = mk_rsrc(p.x, 2048 * 2048);
#pragma unroll
          for (int i = 0; i < 16; ++i)
            v[i & 1][i >> 1] = __builtin_amdgcn_raw_buffer_load_b128(rx, fr * 2048 + fq * 16, (g * 128 + (j & (NJ - 1)) * (128 / NJ) + ((i >> 1) % (8 / NJ)) * 16) * 2048 + ((KB0(g, j) + (i >> 1) / (8 / NJ)) & 15) * 128 + (i & 1) * 64, 0);
          SCHED;
          BAR; SCHED;
          const f32x4 zero = {0.f, 0.f, 0.f, 0.f};
          const bf16x8 ones = {0x3f80, 0x3f80, 0x3f80, 0x3f80, 0x3f80, 0x3f80, 0x3f80, 0x3f80};
          float sm[8], q[8];
#pragma unroll
          for (int m = 0; m < 8; ++m) {
            const f32x4 a = MFMA(ones, as_frag(v[1][m]), MFMA(ones, as_frag(v[0][m]), zero));
            const f32x4 b = MFMA(as_frag(v[1][m]), as_frag(v[1][m]), MFMA(as_frag(v[0][m]), as_frag(v[0][m]), zero));
            sm[m] = a[0];
            q[m] = (fr & 2) ? ((fr & 1) ? b[3] : b[2]) : ((fr & 1) ? b[1] : b[0]);
          }
          if (fq == (fr >> 2)) {
#pragma unroll
            for (int m = 0; m < 8; ++m) st8_sc1(rs, ((m % (8 / NJ)) * 16 + fr) * 8 + (m / (8 / NJ)) * 1024, st_off(g, 0) + JROT(j) * 1024 + (j & (NJ - 1)) * (1024 / NJ), u32x2{__float_as_uint(sm[m]), __float_as_uint(q[m])});
          }
        }
      }
  const char *pA = (const char*)p.x, *pW = (const char*)p.wqkvA;
  unsigned zA = 2048 * 2048, bA = g0 * (128 * 2048), kA = 128, pcA = 32 * 2048, vA;
  unsigned zW = 1024 * 6144, bW = j0 * 128, kW = 64 * 6144, pcW = 2048, hW = 32 * 6144, vW;
  int kb = KB0(g0, j0), dj = -1;
  {
    const int lane = tid & 63, hw = wave0 & 3;
    vA = (hw * 8 + (lane >> 3)) * 2048 + (((lane & 7) ^ (((hw & 1) * 4 + (lane >> 4)) & 7)) * 16);
    vW = (hw * 8 + (lane >> 3)) * 6144 + (((lane & 7) ^ ((((lane >> 4) & 1) | ((hw & 1) << 1)) << 1)) * 16);
  }

#pragma unroll 1
  for (int l0 = 0; l0 < 4; ++l0) {
    f32x4 acc[2][4];
    auto kvpart = [&](unsigned va, unsigned pa, unsigned pb, unsigned kvb, int lane, int wave) {
      const f32x4 zero = {0.f, 0.f, 0.f, 0.f};
      const bf16x8 ones = {0x3f80, 0x3f80, 0x3f80, 0x3f80, 0x3f80, 0x3f80, 0x3f80, 0x3f80};
      f32x4 c0 = zero, c1 = zero, z0 = zero;
#pragma unroll
      for (int ks = 0; ks < 4; ++ks) {
        const bf16x8 v = lds_ld<bf16x8>(va + ks * 2048);
        const bf16x8 a0 = tr2(pa + ks * 32 * 160, pa + (ks * 32 + 16) * 160);
        const bf16x8 a1 = tr2(pb + ks * 32 * 160, pb + (ks * 32 + 16) * 160);
        c0 = MFMA(a0, v, c0);
        c1 = MFMA(a1, v, c1);
        z0 = MFMA(a0, ones, z0);
      }
      const u32x4 c = nosent(pack(c0, c1));
      const bool dt = wave & 2;
      st16_sc1(rs, lane * 16, kvb + (unsigned)wave * 1024, u32x4{dt ? c[2] : c[0], dt ? c[3] : c[1], dt ? c[0] : c[2], dt ? c[1] : c[3]});
      if ((lane & 15) == 0)
        st8_sc1(rs, lane, kvb + KVP_Z + (wave >> 2) * 128 + (wave & 1) * 64 + (wave & 2) * 4, nosent2(z0));
    };
#define KV_ADDR                                                                                                            \
    unsigned va = lds0 + SC_VF + (wave >> 2) * 8192 + ((wave >> 1) & 1) * 1024 + lane * 16;                                \
    unsigned pa = lds0 + SC_PHIK + ((wave >> 2) * 128 + fq * 4 + (fr >> 2)) * 160 + (wave & 1) * 64 + (lane & 3) * 8;      \
    unsigned kvj = WS_KVP + (unsigned)j * KVP_J + (unsigned)(l >> 1) * KVP_L;                                              \
    unsigned pb = pa + 32 - (wave & 2) * 16;                                                                               \
    pa += (wave & 2) * 16;                                                                                                 \
    OPAQUE_V(va); OPAQUE_V(pa); OPAQUE_V(pb); OPAQUE_S(kvj)
    if (cw) {
      LOCALS;
      unsigned otile, kvo, l16, pr1, pr4;
      int g2, j2, l2, ln2;
      qkv_loop(l & 1, wm, h, lane, l == 0, [&](int st) {
        if (st == 0) {
          g2 = g0; j2 = j0; l2 = l0; ln2 = tid;
          OPAQUE_S(g2); OPAQUE_S(j2); OPAQUE_S(l2); OPAQUE_V(ln2);
          return;
        }
        otile = ob_off(g2, j2, l2) + (unsigned)(wm * 8 + h) * 1024;
        kvo = WS_KVP + (unsigned)j2 * KVP_J + (unsigned)g2 * KVP_G + (unsigned)(l2 >> 1) * KVP_L;
        l16 = (ln2 & 63) * 16;
        pr1 = lds0 + L_RSTD + (16 + wm * 64 + (ln2 & 15)) * 4;
        pr4 = lds0 + L_RSTD + wm * 256 + (ln2 & 48);
        asm("" : "+s"(otile)); asm("" : "+s"(kvo)); asm("" : "+v"(l16)); asm("" : "+v"(pr1)); asm("" : "+v"(pr4));
      }, [&](auto isb_c, f32x4 (&a)[4][6], auto& mm6) {
        const f32x4 zero = {0.f, 0.f, 0.f, 0.f};
        constexpr float L2E = 1.4426950408889634f;
        float rq[4];
#define ilv8(nv) do { _Pragma("unroll") for (int i_ = 0; i_ < 8; ++i_) { SGB(0x008, 1); SGB(0x100, 2); SGB(0x002, nv); } SCHED; } while (0)
        if constexpr (decltype(isb_c)::value) {
          constexpr float QS = 0.17677669529663687f * L2E, NEG = -3.0e38f;
          const bf16x8 ones = {0x3f80, 0x3f80, 0x3f80, 0x3f80, 0x3f80, 0x3f80, 0x3f80, 0x3f80};
          u32x4 kf[5], vf[5], qf[4], pf[4];
          f32x4 rs4[4], bias[2], s0[4], s1[4], dn[4];
          float x[4][4], mx[4];
          unsigned psel0, psel1;
          auto pkkv = [&](int m) { kf[m + 1] = pack(a[m][2] * rq[m], a[m][3] * rq[m]); vf[m + 1] = pack(a[m][4] * rs4[m], a[m][5] * rs4[m]); };
          auto pkq = [&](int m) { qf[m] = pack(a[m][0], a[m][1]); };
          auto st1 = [&](int m) {
            x[m][0] = vmax2(s0[m][0], s0[m][1]); x[m][1] = vmax2(s0[m][2], s0[m][3]); x[m][2] = vmax2(s1[m][0], s1[m][1]); x[m][3] = vmax2(s1[m][2], s1[m][3]);
            mx[m] = vmax2(vmax2(x[m][0], x[m][1]), vmax2(x[m][2], x[m][3]));
          };
          auto st2 = [&](int m) {
            const float mxl = mx[m] * -rq[m];
#pragma unroll
            for (int e = 0; e < 4; ++e) x[m][e] = __builtin_fmaf(x[m][e], rq[m], mxl);
          };
          auto st3 = [&](int m) {
#pragma unroll
            for (int e = 0; e < 4; ++e) x[m][e] = __builtin_amdgcn_exp2f(x[m][e]);
          };
          auto st5 = [&](int m) {
            const unsigned p0 = pk2(x[m][0], x[m][1]), p1 = pk2(x[m][2], x[m][3]);
            pf[m] = u32x4{__builtin_amdgcn_perm(0u, p0, psel0), __builtin_amdgcn_perm(0u, p0, psel1), __builtin_amdgcn_perm(0u, p1, psel0), __builtin_amdgcn_perm(0u, p1, psel1)};
          };
          auto st6 = [&](int m) {
            dn[m] = MFMA(ones, as_frag(pf[m]), zero);
            s0[m] = MFMA(as_frag(u32x4{vf[m][0], vf[m][1], vf[m + 1][0], vf[m + 1][1]}), as_frag(pf[m]), zero);
            s1[m] = MFMA(as_frag(u32x4{vf[m][2], vf[m][3], vf[m + 1][2], vf[m + 1][3]}), as_frag(pf[m]), zero);
            asm("" : "+v"(dn[m]));
          };
          auto st7 = [&](int m) { const float inv = __builtin_amdgcn_rcpf(dn[m][0]); pf[m] = nosent(pack(s0[m] * inv, s1[m] * inv)); };
          mm6(3);
#pragma unroll
          for (int m = 0; m < 4; ++m) rq[m] = lds_ld<float>(pr1 + m * 64);
#pragma unroll
          for (int m = 3; m >= 0; --m) rs4[m] = lds_ld<f32x4>(pr4 + 64 + m * 64);
          unsigned pv_ = l16 + lds0 + L_PREV + h * 2048;
          asm("" : "+v"(pv_));
          static_for<0, 8>([&](auto i_c) __attribute__((always_inline)) {
            constexpr int i = decltype(i_c)::value;
            constexpr u64 mk = window_mask(i >> 2, i & 3);
            bias[i >> 2][i & 3] = __builtin_amdgcn_inverse_ballot_w64(mk) ? 0.f : NEG;
          });
          asm("" : "+v"(bias[0])); asm("" : "+v"(bias[1]));
          const unsigned hso = kvo + KVP_HALO + h * 2048, hsi = hso - (g > 0 ? KVP_G : 0u);
          ilv8(2);
          mm6(2);
          pkkv(3);
          ilv8(2);
          OPAQUE_V(kf[4]); OPAQUE_V(vf[4]);
          if (wm) {
            st16_sc1(rs, l16, hso, nosent(kf[4]));
            st16_sc1(rs, l16, hso + 1024, nosent(vf[4]));
          } else {
            lds_st<u32x4>(pv_, kf[4]);
            lds_st<u32x4>(pv_ + 1024, vf[4]);
          }
          SCHED;
          mm6(1);
          pkq(3); pkkv(2);
          ilv8(3);
          WAITL0;
          BAR;
          SCHED;
          mm6(0);
          pkq(2); pkkv(1);
          ilv8(3);
          s0[3] = MFMA(as_frag(kf[3]), as_frag(qf[3]), bias[0]); s1[3] = MFMA(as_frag(kf[4]), as_frag(qf[3]), bias[1]);
          s0[2] = MFMA(as_frag(kf[2]), as_frag(qf[2]), bias[0]); s1[2] = MFMA(as_frag(kf[3]), as_frag(qf[2]), bias[1]);
          pkq(1); pkkv(0);
#pragma unroll
          for (int i = 0; i < 4; ++i) { SGB(0x008, 1); SGB(0x002, 5); }
          SCHED;
          const bool halo = wm == 0 && g > 0;
          const u32x4 hk = ld16_sc1(rs, l16, hsi), hv = ld16_sc1(rs, l16, hsi + 1024);
          SCHED;
          s0[1] = MFMA(as_frag(kf[1]), as_frag(qf[1]), bias[0]); s1[1] = MFMA(as_frag(kf[2]), as_frag(qf[1]), bias[1]);
          st1(3); st1(2);
#pragma unroll
          for (int m = 0; m < 4; ++m) rq[m] *= QS;
          SCHED;
          st1(1);
          pkq(0);
          psel0 = __builtin_amdgcn_inverse_ballot_w64(0xaaaaaaaaaaaaaaaaull) ? 0x01000c0cu : 0x0c0c0100u;
          psel1 = __builtin_amdgcn_inverse_ballot_w64(0xaaaaaaaaaaaaaaaaull) ? 0x03020c0cu : 0x0c0c0302u;
          { float d = mx[1]; red4<true>(mx[3], mx[2], mx[1], d, [](float v) { return v; }); }
          s1[0] = MFMA(as_frag(kf[1]), as_frag(qf[0]), bias[1]);
          SCHED;
          st2(3); st2(2); st2(1);
          SCHED;
          st3(3); st3(2); st3(1);
          SCHED;
          st5(3); st6(3); st5(2); st6(2); st5(1); st6(1);
          SCHED;
          u32x4 pk = lds_ld<u32x4>(pv_), pv = lds_ld<u32x4>(pv_ + 1024);
          st7(3); st7(2); st7(1);
          SCHED;
#pragma unroll
          for (int m = 3; m > 0; --m) st16_sc1(rs, l16, otile + m * 2048, pf[m]);
          if (!wm && g == 0) bias[0] = f32x4{NEG, NEG, NEG, NEG};
          SCHED;
          if (halo) {
            pk = hk; pv = hv;
            for (unsigned spins = 0; !dead && __any(umax4v(umax4v(0u, pk), pv) == SENT); ++spins) {
              if (spins > SPIN_CAP) { dead = true; break; }
              CLOBBER;
              pk = ld16_sc1(rs, l16, hsi); pv = ld16_sc1(rs, l16, hsi + 1024);
              OPAQUE_V(pk); OPAQUE_V(pv);
            }
          }
          vf[0] = pv;
          s0[0] = MFMA(as_frag(pk), as_frag(qf[0]), bias[0]);
          st1(0);
          mx[0] = red_max4(mx[0]);
          st2(0); st3(0); st5(0); st6(0); st7(0);
          st16_sc1(rs, l16, otile, pf[0]);
        } else {
          LOCALS;
          const int fr = lane & 15, fq = lane >> 4;
          f32x4 rs4[4], r[4][2];
          const bf16x8 ones = {0x3f80, 0x3f80, 0x3f80, 0x3f80, 0x3f80, 0x3f80, 0x3f80, 0x3f80};
          auto proj = [&](int m, int n, const bf16x8& w0, const bf16x8& w1) {
            const u32x4 f = pack(a[m][n], a[m][n + 1]);
            r[m][0] = MFMA(w0, as_frag(f), zero); r[m][1] = MFMA(w1, as_frag(f), zero);
          };
          mm6(3, 0, 2); mm6(2, 0, 2); mm6(1, 0, 2); mm6(0, 0, 2);
          unsigned wa = lds0 + L_WPHI + h * 2048 + (fq * 4 + (fr >> 2)) * 64 + (lane & 3) * 8, kw = lds0 + L_PREV + hw * 2048 + l16;
          asm("" : "+v"(wa)); asm("" : "+v"(kw));
#pragma unroll
          for (int m = 0; m < 4; ++m) rq[m] = lds_ld<float>(pr1 + m * 64);
#pragma unroll
          for (int m = 0; m < 4; ++m) rs4[m] = lds_ld<f32x4>(pr4 + 64 + m * 64);
          const bf16x8 w0 = tr2(wa, wa + 1024);
          const bf16x8 w1 = tr2(wa + 32, wa + 32 + 1024);
#pragma unroll
          for (int i = 0; i < 8; ++i) { SGB(0x008, 1); SGB(0x100, 2); SGB(0x002, 2); }
          SCHED;
          mm6(3, 2, 4); mm6(2, 2, 4);
          lds_st<u32x4>(kw + 1024, pack(a[3][2], a[3][3]));
          lds_st<u32x4>(kw, pack(a[2][2], a[2][3]));
#pragma unroll
          for (int i = 0; i < 2; ++i) { SGB(0x008, 1); SGB(0x002, 2); SGB(0x008, 1); SGB(0x002, 2); SGB(0x200, 1); }
          SCHED;
          mm6(1, 2, 4); mm6(0, 2, 4);
          SCHED;
          WAITL0;
          BAR;
          SCHED;
          proj(0, 2, w0, w1); proj(1, 2, w0, w1);
          unsigned pw = lds0 + SC_PHIK + (h * 128 + wm * 64 + fr) * 160 + fq * 8, vw = lds0 + SC_VF + h * 8192 + wm * 4096 + l16;
          asm("" : "+v"(pw)); asm("" : "+v"(vw));
#pragma unroll
          for (int m = 0; m < 4; ++m) rq[m] *= L2E;
#pragma unroll
          for (int i = 0; i < 4; ++i)
            lds_st<u32x4>(vw + i * 1024, pack(a[i & 2][4 + (i & 1)] * rs4[i & 2], a[(i & 2) + 1][4 + (i & 1)] * rs4[(i & 2) + 1]));
          SCHED;
          phik2(r, rq, pw);
          KV_ADDR;
          WAITL0;
          BAR;
          kvpart(va, pa, pb, kvo, lane, wave);
          SCHED;
#pragma unroll
          for (int m = 0; m < 4; ++m) mm6(m, 4, 8);
          SCHED;
          CLOBBER;
          BAR;
          u32x4 qp[4], qn[4];
          float sqp[4], sqn[4];
#pragma unroll
          for (int m = 0; m < 4; ++m) proj(m, 0, w0, w1);
          SCHED;
          float ep[4][8], en[4][8];
          hedgehog<4>(r, rq, ep, en);
          const unsigned zo = (lane >> 3) * KVP_G + (lane & 7) * 16, zs = kvj + KVP_Z + h * 128;
          u32x4 zv[2] = {ld16_sc1(rs, zo, zs), ld16_sc1(rs, zo, zs + 8 * KVP_G)};
          SCHED;
#pragma unroll
          for (int m = 0; m < 4; ++m) {
            qp[m] = pack8(ep[m]); qn[m] = pack8(en[m]);
            sqp[m] = MFMA(ones, as_frag(qp[m]), zero)[0];
            sqn[m] = MFMA(ones, as_frag(qn[m]), zero)[0];
          }
          SCHED;
#pragma unroll
          for (int m = 0; m < 4; ++m) { sqp[m] = __builtin_amdgcn_rcpf(sqp[m]); sqn[m] = __builtin_amdgcn_rcpf(sqn[m]); }
#pragma unroll
          for (int m = 0; m < 4; ++m) { OPAQUE_V(qp[m]); OPAQUE_V(qn[m]); OPAQUE_V(sqp[m]); OPAQUE_V(sqn[m]); }
          unsigned ko = (lane >> 5) * KVP_G + (lane & 31) * 16, ks = kvj + (unsigned)((j ^ RX) - j) * KVP_J + KVP_SUM + h * (8 * KVP_G);
          bf16x8 sel = rowsum_sel(lane);
          OPAQUE_V(ko); OPAQUE_S(ks); OPAQUE_V(sel);
          for (unsigned spins = 0; !dead && __any(umax4v(umax4v(0u, zv[0]), zv[1]) == SENT); ++spins) {
            if (spins > SPIN_CAP) { dead = true; break; }
            CLOBBER;
            zv[0] = ld16_sc1(rs, zo, zs); zv[1] = ld16_sc1(rs, zo, zs + 8 * KVP_G);
          }
          {
            const f32x4 z = rowsum_fin(MFMA(sel, as_frag(zv[1]), MFMA(sel, as_frag(zv[0]), zero)));
            if ((lane & 40) == 0) lds_st<u32x2>(lds0 + L_ZF + hw * 128 + fr * 16 + fq * 8, u32x2{pk2(z[0], z[1]), pk2(z[2], z[3])});
            WAITL0;
            const bf16x8 zf0 = lds_ld<bf16x8>(lds0 + L_ZF + hw * 128 + fq * 16), zf1 = lds_ld<bf16x8>(lds0 + L_ZF + hw * 128 + 64 + fq * 16);
            float dp[4], dn[4];
#pragma unroll
            for (int m = 0; m < 4; ++m) { dp[m] = MFMA(zf0, as_frag(qp[m]), zero)[0]; dn[m] = MFMA(zf1, as_frag(qn[m]), zero)[0]; }
            SCHED;
#pragma unroll
            for (int m = 0; m < 4; ++m) {
              const float inv = __builtin_amdgcn_rcpf(__builtin_fmaf(dp[m], sqp[m], __builtin_fmaf(dn[m], sqn[m], 1e-6f)));
              sqp[m] *= inv; sqn[m] *= inv;
              OPAQUE_V(sqp[m]); OPAQUE_V(sqn[m]);
            }
          }
          BAR;
          __builtin_amdgcn_s_sleep(H2D);
          u32x4 kv[4];
#pragma unroll
          for (int i = 0; i < 4; ++i) kv[i] = ld16_sc1(rs, ko, ks + i * (2 * KVP_G));
          SCHED;
#pragma unroll
          for (int m = 0; m < 4; ++m) { qp[m] = pack8(ep[m], sqp[m]); qn[m] = pack8(en[m], sqn[m]); }
#pragma unroll
          for (int m = 0; m < 4; ++m) { OPAQUE_V(qp[m]); OPAQUE_V(qn[m]); }
          SCHED;
          for (unsigned spins = 0; !dead && __any(max(umax4v(umax4v(0u, kv[0]), kv[1]), umax4v(umax4v(0u, kv[2]), kv[3])) == SENT); ++spins) {
            if (spins > SPIN_CAP) { dead = true; break; }
            CLOBBER;
#pragma unroll
            for (int i = 0; i < 4; ++i) kv[i] = ld16_sc1(rs, ko, ks + i * (2 * KVP_G));
          }
          f32x4 o[4][2];
#pragma unroll
          for (int m = 0; m < 4; ++m)
#pragma unroll
            for (int i = 0; i < 2; ++i) o[m][i] = MFMA(as_frag(kv[2 * i + 1]), as_frag(qn[m]), MFMA(as_frag(kv[2 * i]), as_frag(qp[m]), zero));
#pragma unroll
          for (int m = 0; m < 4; ++m) st16_sc1(rs, l16, otile + m * 2048, nosent(pack(o[m][0], o[m][1])));
        }
      });
#pragma unroll
      for (int i = 0; i < 8; ++i) asm volatile("" : "=v"(acc[i >> 2][i & 3]));
    } else {
      __builtin_amdgcn_s_setprio(3);
      if (l0) {
        WAITV(3);
        WAITL0;
        BAR;
      }
      LOCALS;
      u32x4 xr[2][2];
      const unsigned lin = lane * 16, vlin = lin + hw * 1024;
      const int jr = j & ~3;
      u32x4 sent4 = {SENT, SENT, SENT, SENT};
      OPAQUE_V(sent4);
      const unsigned cb0 = ob_off(g, j, l), cb2 = xb_off(g, l) + j * TS;
      const unsigned cb1 = WS_KVP + (unsigned)j * KVP_J + (unsigned)g * KVP_G + (unsigned)(l >> 1) * KVP_L;
      unsigned ra0;
      int tr = 0;
      auto ldres = [&](int t) {
        if (tr == t) {
#pragma unroll
          for (int i = 0; i < 4; ++i) xr[i >> 1][i & 1] = lds_ld<u32x4>((ra0 ^ ((i & 1) * 64)) + (t % 3) * XS + ((i >> 1) ^ 1) * 2048);
        }
      };
      auto aslot = [&](int t) { return lds0 + L_R2 + (t % 3) * XS + hw * 1024; };
      auto wslot = [&](int t) { return lds0 + L_R1 + (t & 3) * 24576 + hw * 1024; };
      const bool l0f = l == 0;
      const rsrc_t rA = mk_rsrc(pA, zA);
      auto kseq = [&](int t) { return (kb + t - (t <= dj ? 1 : 0)) & 15; };
      auto offA = [&](int t) { return bA + (unsigned)kseq(t) * kA; };
      auto offW = [&](int t) { return bW + (unsigned)kseq(t) * kW; };
      auto ldA = [&](int t) { dma4<4096>(rA, vA, offA(t), pcA, aslot(t)); };
      auto ldWh = [&](int t, unsigned o, int hf) { dma3<8192>(mk_rsrc(pW, zW), vW, o + (hf ? hW : 0u), pcW, wslot(t) + hf * 4096); };
      const bool slowj = l0f && SLOWC(0, j);
      auto packedW = [&]() {
        pW = p.ws; zW = WS_SPAN; bW = WS_WP + (unsigned)j * (16 * 24576); kW = 24576; pcW = 8192; hW = 4096; vW = vlin;
      };
      auto ldW = [&](int t) { const unsigned o = offW(t); ldWh(t, o, 0); ldWh(t, o, 1); };
      constexpr auto spread = [](int n) { unsigned m = 0; for (int t = 0; t < 26; ++t) if ((t + 1) * n / 13 > t * n / 13) m |= 1u << t; return m; };
      constexpr unsigned sp4 = spread(4), sp8 = spread(8), sp12 = spread(12);
      const unsigned smask = (isb ? (l == 3 ? sp4 : sp8) : sp12) >> ((((g + j * 5) & 15) * 13) >> 4);
      const unsigned cbm = isb ? cb2 : cb1;
      auto clean = [&](int t) __attribute__((always_inline)) {
        if (t < 13 && ((smask >> t) & 1)) {
          const unsigned cc = __builtin_popcount(smask & ((1u << t) - 1));
          const unsigned cb = cb0 + (cc >= 4 ? cbm - cb0 : 0u) + (cc >= 8 ? cb2 - cbm : 0u), pc = (cc + g + j) & 3;
          st16_sc1(rs, (lane >> 4) * 4096 + (lane & 15) * 16, cb + hw * 1024 + pc * 256, sent4);
        }
      };
      unsigned flo;
      unsigned ready = ~0u;
      u32x4 cv[4];
#pragma unroll
      for (int ii = 0; ii < 4; ++ii) asm volatile("" : "=v"(cv[ii]));
      auto chkrd = [&](int t) {
#pragma unroll
        for (int ii = 0; ii < 4; ++ii) cv[ii] = lds_ld<u32x4>(aslot(t) + lin + ii * 4096);
      };
      auto chkbad = [&]() { return __any(max(umax4v(umax4v(0u, cv[0]), cv[1]), umax4v(umax4v(0u, cv[2]), cv[3])) == SENT) && !l0f; };
      auto refetch = [&](int t) __attribute__((always_inline)) {
        for (unsigned spins = 0; !dead; ++spins) {
          if (spins > SPIN_CAP) { dead = true; break; }
          WAITV(0);
          ldA(t);
          ldA(t + 1);
          WAITV(0);
          chkrd(t);
          if (!chkbad()) break;
          __builtin_amdgcn_s_sleep(1);
        }
      };
      f32x4 jc[4];
#pragma unroll
      for (int i = 0; i < 4; ++i) asm volatile("" : "=v"(jc[i]));
      auto csld = [&]() {
#pragma unroll
        for (int i = 0; i < 4; ++i)
          ld16_asm(jc[i], rs, (lane >> 4) * 768 + hw * 192 + ((lane & 15) < 12 ? (lane & 15) : 0) * 16, WS_CS + (unsigned)((l * 16 + j) * 16 + i * 4) * 768);
      };
      auto csred = [&]() {
#pragma unroll
        for (int i = 0; i < 4; ++i) OPAQUE_V(jc[i]);
        const f32x4 s = (jc[0] + jc[1]) + (jc[2] + jc[3]);
        auto r = SWAP16(s[0], s[1]), q = SWAP16(s[2], s[3]);
        r = SWAP32(__uint_as_float(r[0]) + __uint_as_float(r[1]), __uint_as_float(q[0]) + __uint_as_float(q[1]));
        if ((lane & 15) < 12) lds_st<float>(lds0 + L_CS + l * 768 + hw * 192 + (lane & 15) * 16 + (lane >> 4) * 4, __uint_as_float(r[0]) + __uint_as_float(r[1]));
      };
      if (l0f) {
        u32x2 gw2;
        ld8_asm(gw2, rs, WS_EPOCH + 0u * lin);
        BAR;
        ldW(0); ldA(0);
        dma1(mk_rsrc(p.wphi, 32 * 2048), vlin, j * 4096, lds0 + L_WPHI + hw * 1024);
        ldW(1);
        WAITV(17);
        OPAQUE_V(gw2);
        const u64 gword = ((u64)(unsigned)UNI(gw2[1]) << 32) | (unsigned)UNI(gw2[0]);
        u64 z = gword + 0x9e3779b97f4a7c15ull;
        z = (z ^ (z >> 30)) * 0xbf58476d1ce4e5b9ull;
        z = (z ^ (z >> 27)) * 0x94d049bb133111ebull;
        z = z ^ (z >> 31);
        z = (z == gword) ? ~z : z;
        epoch = u32x2{(unsigned)z, (unsigned)(z >> 32)};
        tr = (j - kb) & 15;
        WAITV(7);
        BAR;
        ldA(1);
      } else {
        ldW(1);
        if (l == 1) csld();
        else __builtin_amdgcn_s_sleep(XD);
        ldA(1);
      }
      ldA(2);
      ldW(2);
#pragma unroll
      for (int i = 0; i < 4; ++i) asm volatile("" : "=v"(xr[i >> 1][i & 1]));
      unsigned oA = offA(3), oW = 0;
      OPAQUE_S(oA);
      ra0 = lds0 + L_R2 + (hw * 32 + (lane & 15)) * 128 + (((((lane >> 4) & 1) * 2 + (lane >> 5)) ^ ((lane >> 1) & 7)) * 16);
      OPAQUE_V(ra0);
      ldres(0);
      clean(0);
      if (l == 1) { WAITV(14); csred(); }
      WAITV(10);
      chkrd(1);
      if (__builtin_expect(chkbad(), 0)) refetch(1);
      const int fr = lane & 15, fq = lane >> 4;
      u32x2 fl;
      f32x4 js[4];
      float rb[4];
      const bool slowo = SLOWC(2 + (l & 1), j);
      const char* pO;
      unsigned zO, bO, pcO, vO, clo;
      u32x4 wo2[2];
      asm volatile("" : "=v"(wo2[0])); asm volatile("" : "=v"(wo2[1]));
      const unsigned fwo = myrep + fl_line(FL_WO + (isb ? 1 : 0), jr + (lane >> 4)) + (lane & 15) * 8;
      static_for<1, 14>([&](auto t_c) __attribute__((always_inline)) {
        constexpr int t = decltype(t_c)::value;
        constexpr auto rest = [](int s) { return 6 + (s == NVAL - 2 || s == 5 || s == 11) + (s == 8 ? 4 : 0); };
        constexpr int NW = t == 1 ? 63 : 4 + rest(t - 1);
        if (t >= NVAL && t <= 6 && __builtin_expect((ready & (1u << kseq(t + 2))) == 0, 0)) {
          WAITV(NW); WAITL0; BAR;
          ready = poll_need(rs, flo, epoch, ready, 1u << kseq(t + 2), dead);
          dma4<4096>(rA, vA, oA, pcA, aslot(t + 2));
        } else if (t == 7 && __builtin_expect(ready != ~0u, 0)) {
          WAITV(NW); WAITL0; BAR;
          poll_all(rs, flo, epoch, dead);
          dma4<4096>(rA, vA, oA, pcA, aslot(t + 2));
        } else {
          bar_dma4<4096, NW>(rA, vA, oA, pcA, aslot(t + 2));
        }
        if (t == 1 && lead) set_flags(rs, fl_line(FL_X + l, g), j, l0f ? fl_line(FL_CS, j) : fl_line(FL_X + l, g), l0f ? g : j, epoch, lane);
        if (t == 7 && slowj) packedW();
        oW = offW(t + 2);
        if (t < 13) oA = offA(t + 3);
        OPAQUE_S(oA); OPAQUE_S(oW);
        clean(t);
        if (t <= NVAL && !l0f) { WAITV(rest(t - 1) + 4); chkrd(t + 1); }
        if (t == WT + 3 && l0f && SLOWC(2, jr + hw)) {
          OPAQUE_V(wo2[0]); OPAQUE_V(wo2[1]);
#pragma unroll
          for (int i = 0; i < 2; ++i) st16_sc1(rs, lin, WS_WO + (unsigned)((jr + hw) * 131072 + g * 8192 + ((j & 3) * 2 + i) * 1024), wo2[i]);
        }
        if (t == WT + 6 && l0f && lead) set_flags(rs, fl_line(FL_WO, j), g, fl_line(FL_WO, j), g, epoch, lane);
        ldres(t);
        if (t == 8) {
#pragma unroll
          for (int i = 0; i < 4; ++i) ld16_asm(js[i], rs, fq * 1024 + hw * 256 + fr * 16, st_off(g, l) + i * 4096);
        }
        SCHED;
        ldWh(t + 2, oW, 0);
        SCHED;
        bool bad = false;
        if (t <= NVAL) bad = chkbad();
        if (t == 1) {
          const unsigned fx = fl_line(FL_X + l, g), fc = fl_line(FL_CS, jr) + ((j + fq) & 3) * 128;
          flo = myrep + ((l0f && lane >= 16) ? fc : fx) + fr * 8;
          OPAQUE_V(flo);
        }
        if (t == 9) {
          pO = slowo ? p.ws : (const char*)(isb ? p.woB : p.woA);
          zO = slowo ? WS_SPAN : 1024 * 2048; bO = slowo ? WS_WO + (unsigned)((isb ? 16 : 0) + j) * 131072 : j * 128; pcO = slowo ? 4096 : 32 * 2048;
          vO = slowo ? vlin : (hw * 8 + (lane >> 3)) * 2048 + wo_swz(lane);
          clo = myrep + ((lane & 16) ? fl_line(FL_CLC + l, (lane & 32) ? j ^ RX : j) : fl_line(FL_CLR + l, g)) + fr * 8;
          OPAQUE_S(pO); OPAQUE_S(zO); OPAQUE_S(bO); OPAQUE_S(pcO); OPAQUE_V(vO); OPAQUE_V(clo);
        }
        if (t == 10 && l0f) csred();
        if (t == 11) {
#pragma unroll
          for (int i = 0; i < 4; ++i) OPAQUE_V(js[i]);
          const f32x4 s = (js[0] + js[1]) + (js[2] + js[3]);
          const auto r = SWAP16(s[0], s[2]), q = SWAP16(s[1], s[3]);
          rb[0] = __uint_as_float(r[0]) + __uint_as_float(r[1]); rb[1] = __uint_as_float(q[0]) + __uint_as_float(q[1]);
          OPAQUE_V(rb[0]); OPAQUE_V(rb[1]);
        }
        if (t == 12) {
          const auto r = SWAP32(rb[0], rb[0]), q = SWAP32(rb[1], rb[1]);
          const float m = (__uint_as_float(r[0]) + __uint_as_float(r[1])) * (-1.0f / 1024), q2 = __uint_as_float(q[0]) + __uint_as_float(q[1]);
          const float rstd = __builtin_amdgcn_rsqf(fmaxf(__builtin_fmaf(q2, 1.0f / 1024, -m * m), 0.f) + 1e-5f);
          lds_st<float>(lds0 + L_MEAN + 64 + hw * 128 + fr * 8 + (fq & 1) * 4 + (fq >> 1) * (L_RSTD - L_MEAN), (lane & 32) ? rstd : m);
        }
        SCHED;
        ldWh(t + 2, oW, 1);
        if (t == NVAL - 1 || t == 6) {
          WAITV(10);
          OPAQUE_V(fl);
          if (t == 6) ready = __all(is_set(fl, epoch)) ? ~0u : 0u;
          else {
            ready = l0f ? ~0u : (unsigned)__ballot(is_set(fl, epoch));
            if (slowj && __all(is_set(fl, epoch))) packedW();
          }
        }
        if (t == 12) { WAITV(10); OPAQUE_V(fl); }
        if (t == 13 && slowo && __builtin_expect(!__all(is_set(fl, epoch)), 0)) poll_all(rs, fwo, epoch, dead);
        if (t == NVAL - 2 || t == 5) ld8_asm(fl, rs, flo);
        if (t == 11) ld8_asm(fl, rs, fwo);
        if (t == 7 && l0f) csld();
        if (t == WT && l0f) {
          if (hw == 0) touch(mk_rsrc(p.ws, NREP * REP_STRIDE), min((g * 16 + j) * 9 + (lane < 9 ? lane : 0), (int)(NREP * REP_STRIDE / 128 - 1)) * 128, lds0 + L_SINK);
#pragma unroll
          for (int i = 0; i < 2; ++i) ld16in_asm(wo2[i], mk_rsrc(p.woA, 1024 * 2048), (lane >> 3) * 2048 + wo_swz(lane), (g * 64 + (j & 3) * 16 + i * 8) * 2048 + (jr + hw) * 128);
        }
        if constexpr (t <= NVAL) { if (__builtin_expect(bad, 0)) refetch(t + 1); }
      });
      auto stO = [&](int i) { dma4<4096>(mk_rsrc(pO, zO), vO, bO + wo_src(i, j) * (4 * pcO), pcO, lds0 + wo_slot(i) + hw * 1024); };
      auto clcheck = [&]() {
        OPAQUE_V(fl);
        if (__builtin_expect(!__all(is_set(fl, epoch)), 0)) poll_all(rs, clo, epoch, dead);
      };
      auto accinit = [&]() {
#pragma unroll
        for (int m = 0; m < 2; ++m)
#pragma unroll
          for (int pp = 0; pp < 2; ++pp) {
            auto r0_ = __builtin_amdgcn_permlane16_swap(xr[m][pp][0], xr[m][pp][2], false, false);
            auto r1_ = __builtin_amdgcn_permlane16_swap(xr[m][pp][1], xr[m][pp][3], false, false);
            acc[m][2 * pp] = f32x4{lo16(r0_[0]), hi16(r0_[0]), lo16(r1_[0]), hi16(r1_[0])};
            acc[m][2 * pp + 1] = f32x4{lo16(r0_[1]), hi16(r0_[1]), lo16(r1_[1]), hi16(r1_[1])};
          }
#pragma unroll
        for (int i = 0; i < 8; ++i) OPAQUE_V(acc[i >> 2][i & 3]);
      };
      WAITV(10);
      WAITL0;
      BAR;
      stO(0);
      if (lead) set_flags(rs, fl_line(FL_CLR + l, g), j, fl_line(FL_CLC + l, j), g, epoch, lane);
      if (isb) {
        stO(2); stO(3);
        WAITV(12);
        BAR;
        stO(1); stO(4); stO(5);
        __builtin_amdgcn_s_setprio(0);
        BAR;
        __builtin_amdgcn_s_sleep(BD);
        stO(7); stO(6);
        ld8_asm(fl, rs, clo);
        accinit();
        WAITV(0);
        clcheck();
      } else {
        u32x4 vb[8];
#pragma unroll
        for (int i = 0; i < 8; ++i) asm volatile("" : "=v"(vb[i]));
        stO(2); stO(3);
        ldres(14);
        WAITV(12);
        WAITL0;
        BAR;
        stO(1);
        ldres(15);
        if (l0f) {
          const bool wj = hw != 0;
          const char* bp = (const char*)(wj ? p.wqkvB : p.woB);
          unsigned rs_ = wj ? 6144 : 2048, sz = rs_ * 1024, ss = rs_ * 8, s2 = wj ? 16 * 6144 : 128;
          unsigned sb = g * 64 * rs_ + (wj ? (hw - 1) * 2048 + JOBC(j, hw - 1) * 128 : (j & 3) * (16 * 2048) + jr * 128);
          OPAQUE_S(bp); OPAQUE_S(rs_); OPAQUE_S(sz); OPAQUE_S(sb); OPAQUE_S(ss); OPAQUE_S(s2);
          const rsrc_t rb = mk_rsrc(bp, sz);
#pragma unroll
          for (int i = 0; i < 8; ++i) ld16in_asm(vb[i], rb, (lane >> 3) * rs_ + (lane & 7) * 16, sb + (i & 1) * ss + (i >> 1) * s2);
        }
        __builtin_amdgcn_s_setprio(0);
        KV_ADDR;
        bf16x8 sel = rowsum_sel(lane);
        unsigned h1 = (lane >> 3) * KVP_G + (lane & 7) * 16;
        unsigned pw = lds0 + SC_PHIK + (h * 128 + wm * 64 + 32 + fr) * 160 + fq * 8, kw = lds0 + L_PREV + hw * 2048 + lin;
        const unsigned wa = lds0 + L_WPHI + h * 2048 + (fq * 4 + (fr >> 2)) * 64 + (lane & 3) * 8;
        bf16x8 w0 = tr2(wa, wa + 1024), w1 = tr2(wa + 32, wa + 32 + 1024);
        float cq[4];
#pragma unroll
        for (int m = 0; m < 2; ++m) { cq[m] = lds_ld<float>(lds0 + L_RSTD + (16 + wm * 64 + 32 + m * 16 + fr) * 4) * 1.4426950408889634f; OPAQUE_V(cq[m]); }
        OPAQUE_V(sel); OPAQUE_V(h1); OPAQUE_V(pw); OPAQUE_V(kw); OPAQUE_V(w0); OPAQUE_V(w1);
        BAR;
        CLOBBER;
        {
          const f32x4 zero = {0.f, 0.f, 0.f, 0.f};
          f32x4 r[4][2];
#pragma unroll
          for (int m = 0; m < 2; ++m) {
            const bf16x8 f = lds_ld<bf16x8>(kw + m * 1024);
            r[m][0] = MFMA(w0, f, zero); r[m][1] = MFMA(w1, f, zero);
          }
          ld8_asm(fl, rs, clo);
          phik2(r, cq, pw);
        }
        WAITL0;
        BAR;
        kvpart(va, pa, pb, kvj + (unsigned)g * KVP_G, lane, wave);
        WAITV(2);
#pragma unroll
        for (int i = 0; i < 8; ++i) OPAQUE_V(vb[i]);
        clcheck();
        BAR;
        __builtin_amdgcn_s_setprio(3);
        stO(4); stO(5);
        const f32x4 zero = {0.f, 0.f, 0.f, 0.f};
        const unsigned s1 = kvj + (unsigned)((j ^ RX) - j) * KVP_J + g * 512 + hw * 128;
        __builtin_amdgcn_s_sleep(H1S);
        u32x4 v[2] = {ld16_sc1(rs, h1, s1), ld16_sc1(rs, h1, s1 + 8 * KVP_G)};
        for (unsigned spins = 0; !dead && __any(umax4v(umax4v(0u, v[0]), v[1]) == SENT); ++spins) {
          if (spins > SPIN_CAP) { dead = true; break; }
          CLOBBER;
          v[0] = ld16_sc1(rs, h1, s1); v[1] = ld16_sc1(rs, h1, s1 + 8 * KVP_G);
        }
        const f32x4 c = rowsum_fin(MFMA(sel, as_frag(v[1]), MFMA(sel, as_frag(v[0]), zero)));
        if ((lane & 40) == 0)
          st8_sc1(rs, fr * 16 + fq * 8, kvj + (unsigned)g * KVP_G + KVP_SUM + hw * 128, nosent2(c));
        CLOBBER;
        __builtin_amdgcn_s_setprio(0);
        BAR;
        SCHED;
        accinit();
        stO(6); stO(7);
        if (l0f) {
          if (hw) {
            job_colsum(rs, vb, 1, g, JOBC(j, hw - 1), hw - 1, lane);
            if (SLOWC(1, JOBC(j, hw - 1))) {
#pragma unroll
              for (int i = 0; i < 8; ++i) job_pack(rs, vb[i], i, 1, g, JOBC(j, hw - 1), hw - 1, lane);
            }
          } else {
#pragma unroll
            for (int i = 0; i < 8; ++i)
              if (SLOWC(3, jr + (i >> 1))) st16_sc1(rs, (lane >> 3) * 128 + wo_swz(lane), WS_WO + (unsigned)((16 + jr + (i >> 1)) * 131072 + g * 8192 + ((j & 3) * 2 + (i & 1)) * 1024), vb[i]);
          }
        }
        WAITV(0);
      }
    }
    {
      LOCALS;
      const int fr = lane & 15, fq = lane >> 4;
      const int kh = half;
      unsigned v0 = lane * 16 + hw * 4096 + kh * 2048, v1 = lane * 16 + hw * 4096 + (kh ^ 1) * 2048;
      const int r0 = (JROT(j) >> 1) + NJ / 2;
      const unsigned ob0 = ob_off(g, kh, l);
      const unsigned pko = myrep + fl_line(FL_PK, JOBC(j, fq - 1)) + fr * 8;
      u32x4 R[8][4];
      u32x2 pkf;
      auto issue_ = [&](int i) {
        const unsigned so = ob0 + (unsigned)((r0 + i) & 7) * (2 * TS);
        R[i][0] = ld16_sc1(rs, v0, so); R[i][1] = ld16_sc1(rs, v1, so);
        R[i][2] = ld16_sc1(rs, v0 + 1024, so); R[i][3] = ld16_sc1(rs, v1 + 1024, so);
      };
      auto issue = [&](int i) { issue_(i); SCHED; };
      constexpr int LA = LOOKAHEAD;
      __builtin_amdgcn_s_setprio(3);
      unsigned so0 = ob0 + (unsigned)(r0 & 7) * (2 * TS);
      OPAQUE_V(v0); OPAQUE_V(v1); OPAQUE_S(so0);
      BAR;
      SCHED;
      R[0][0] = ld16_sc1(rs, v0, so0); R[0][1] = ld16_sc1(rs, v1, so0);
      R[0][2] = ld16_sc1(rs, v0 + 1024, so0); R[0][3] = ld16_sc1(rs, v1 + 1024, so0);
      SCHED;
      __builtin_amdgcn_s_setprio(2);
      if (l == 0 && lead) set_flags(rs, fl_line(FL_PK, j), g, fl_line(FL_WO + 1, j), g, epoch, lane);

      auto vmax = [&](int i) { return umax4v(umax4v(umax4v(umax4v(0u, R[i][0]), R[i][1]), R[i][2]), R[i][3]); };
      auto bad = [&](int i) { return __any(umax4v(umax4v(umax4v(umax4v(0u, R[i][0]), R[i][1]), R[i][2]), R[i][3]) == SENT); };
      auto poll = [&](int i, int nfl) {
        for (unsigned spins = 0; !dead; ++spins) {
          if (spins > SPIN_CAP) { dead = true; break; }
          __builtin_amdgcn_s_sleep(2);
          CLOBBER;
          issue(i);
#pragma unroll
          for (int k = i + 1; k < i + nfl && k < 8; ++k)
            if (bad(k)) issue(k);
          WAITV(0);
#pragma unroll
          for (int k = i + 1; k < i + nfl && k < 8; ++k)
#pragma unroll
            for (int f = 0; f < 4; ++f) OPAQUE_V(R[k][f]);
          if (!bad(i)) break;
        }
      };
      unsigned bb[4];
      {
        const int kr = fq * 4 + (fr >> 2);
        const int gs = (kr >> 1) & 3;
#pragma unroll
        for (int c = 0; c < 4; ++c) bb[c] = lds0 + kh * 8192 + kr * 128 + ((c ^ gs) * 32) + (lane & 3) * 8;
      }
      bf16x8 bf[2][4];
      auto rdB = [&](int i, int hh) {
        if (hh == 0) {
#pragma unroll
          for (int c = 0; c < 4; ++c) { bb[c] += wo_slot(i) - (i ? wo_slot(i - 1) : 0u); asm("" : "+v"(bb[c])); }
        }
#pragma unroll
        for (int c = 0; c < 4; ++c) bf[hh][c] = tr2(bb[c] + hh * 4096, bb[c] + hh * 4096 + 2048);
      };
      auto mms = [&](int i, int hh) {
#pragma unroll
        for (int n = 0; n < 4; ++n)
#pragma unroll
          for (int m = 0; m < 2; ++m) acc[m][n] = MFMA(bf[hh][n], as_frag(R[i][hh * 2 + m]), acc[m][n]);
      };
      if (!half) {
#pragma unroll
        for (int i = 0; i < 8; ++i) acc[i >> 2][i & 3] = f32x4{0.f, 0.f, 0.f, 0.f};
      }
      const char* obase = (l == 3) ? (const char*)p.out : (const char*)p.ws;
      unsigned osize = (l == 3) ? 2048 * 2048 : WS_SPAN;
      const bool slown = SLOWC((l & 1) ^ 1, j);
      pW = slown ? p.ws : (const char*)(isb ? p.wqkvA : p.wqkvB);
      bW = slown ? WS_WP + (unsigned)((isb ? 0 : 16) + j) * (16 * 24576) : j * 128;
      kW = slown ? 24576 : 64 * 6144; zW = slown ? WS_SPAN : 1024 * 6144; pcW = slown ? 8192 : 2048; hW = slown ? 4096 : 32 * 6144;
      vA = hw * 1024 + lane * 16;
      vW = slown ? vA : (hw * 8 + (lane >> 3)) * 6144 + (((lane & 7) ^ ((((lane >> 4) & 1) | ((hw & 1) << 1)) << 1)) * 16);
      pA = p.ws; zA = WS_SPAN; bA = xb_off(g, l); kA = TS; pcA = 4096; kb = JROT(j) + KX; dj = (j - kb) & 15;
      OPAQUE_S(pW); OPAQUE_S(bW); OPAQUE_S(kW); OPAQUE_S(zW); OPAQUE_S(pcW); OPAQUE_S(hW); OPAQUE_V(vA); OPAQUE_V(vW); OPAQUE_S(bA);
      unsigned sq = bW + j * kW + kh * hW, dst = lds0 + L_R1 + kh * 4096 + hw * 1024;
      OPAQUE_S(obase); OPAQUE_S(osize); OPAQUE_S(sq); OPAQUE_S(dst);
      const rsrc_t ro = mk_rsrc(obase, osize);
      unsigned so[2];
      unsigned xw = lds0 + SC_XCH + wave * 4096 + lane * 16;
      unsigned xa = lds0 + SC_XCH + (wave ^ 4) * 4096 + lane * 16;
      unsigned sb0 = (l == 3) ? g * (128 * 2048) + j * 128 : xb_off(g, l) + j * TS;
      {
        const int row = (hw * 2 + kh) * 16 + fr, ch = (fq & 1) * 2 + (fq >> 1);
        const unsigned sw = (l == 3) ? 0u : (unsigned)((row >> 1) & 7), rb = (unsigned)row << ((l == 3) ? 11 : 7);
#pragma unroll
        for (int pp = 0; pp < 2; ++pp) { so[pp] = rb + (((ch + 4 * pp) ^ sw) * 16); OPAQUE_V(so[pp]); }
        OPAQUE_V(xa); OPAQUE_V(xw); OPAQUE_S(sb0);
      }
      SCHED;
#pragma unroll
      for (int i = 1; i < LA; ++i) issue(i);
      rdB(0, 0);
      unsigned mx = vmax(0);
#pragma unroll
      for (int i = 0; i < 8; ++i) {
        SCHED;
        if (__builtin_expect(__any(mx == SENT), 0)) poll(i, LA);
        if (i == W1S - 2) { pkf = ld8_sc1(rs, pko, 0); SCHED; }
        if (i == W1S) {
          OPAQUE_V(pkf);
          if (l == 0 && lead && !__all(is_set(pkf, epoch))) poll_all(rs, pko, epoch, dead);
          BAR;
        }
        if (i == 7 && l < 3) dma3<8192>(mk_rsrc(pW, zW), vW, sq, pcW, dst);
        if (i < 4 && !isb) __builtin_amdgcn_s_sleep(PACE);
        if (i == 2) __builtin_amdgcn_s_setprio(1);
        if (i == 5) __builtin_amdgcn_s_setprio(0);
        SCHED;
        rdB(i, 1);
        if (i == 7) break;
        if (i + LA < 8) issue_(i + LA);
        mms(i, 0);
#pragma unroll
        for (int k = 0; k < 4; ++k) { SGB(0x008, 1); SGB(0x100, 1); SGB(0x020, 1); SGB(0x008, 1); SGB(0x100, 1); }
        SCHED;
        rdB(i + 1, 0); mx = vmax(i + 1);
        mms(i, 1);
#pragma unroll
        for (int k = 0; k < 4; ++k) { SGB(0x008, 1); SGB(0x100, 2); }
#pragma unroll
        for (int k = 4; k < 8; ++k) { SGB(0x008, 1); SGB(0x002, 8 / (8 - 4)); }
      }
#pragma unroll
      for (int n = 0; n < 4; ++n) acc[1][n] = MFMA(bf[0][n], as_frag(R[7][1]), acc[1][n]);
#pragma unroll
      for (int n = 0; n < 4; ++n) acc[0][n] = MFMA(bf[0][n], as_frag(R[7][0]), acc[0][n]);
#pragma unroll
      for (int k = 0; k < 8; ++k) { SGB(0x008, 1); SGB(0x100, 1); }
      SCHED;
#pragma unroll
      for (int n = 0; n < 4; ++n) acc[1][n] = MFMA(bf[1][n], as_frag(R[7][3]), acc[1][n]);
#pragma unroll
      for (int n = 0; n < 4; ++n) acc[0][n] = MFMA(bf[1][n], as_frag(R[7][2]), acc[0][n]);
#pragma unroll
      for (int n = 0; n < 4; ++n) lds_st<f32x4>(xw + n * 1024, acc[1][n]);
      SGB(0x008, 4);
#pragma unroll
      for (int k = 0; k < 4; ++k) { SGB(0x008, 1); SGB(0x200, 1); }
      SCHED;
      WAITL0;
      BAR;
      SCHED;
      f32x4 pa[4];
#pragma unroll
      for (int n = 0; n < 4; ++n) pa[n] = lds_ld<f32x4>(xa + n * 1024);
      SCHED;
      u32x4 o[2];
#pragma unroll
      for (int pp = 0; pp < 2; ++pp) {
        const f32x4 a = acc[0][2 * pp] + pa[2 * pp], b = acc[0][2 * pp + 1] + pa[2 * pp + 1];
        auto r0_ = __builtin_amdgcn_permlane16_swap(pk2(a[0], a[1]), pk2(b[0], b[1]), false, false);
        auto r1_ = __builtin_amdgcn_permlane16_swap(pk2(a[2], a[3]), pk2(b[2], b[3]), false, false);
        o[pp] = nosent(u32x4{r0_[0], r1_[0], r0_[1], r1_[1]});
        st16_sc1(ro, so[pp], sb0, o[pp]);
      }
      if (l < 3) {
        lds_st<u32x4>(lds0 + L_R2 + so[0], o[0]);
        lds_st<u32x4>(lds0 + L_R2 + so[1], o[1]);
        const f32x4 zero = {0.f, 0.f, 0.f, 0.f};
        const bf16x8 ones = {0x3f80, 0x3f80, 0x3f80, 0x3f80, 0x3f80, 0x3f80, 0x3f80, 0x3f80};
        const f32x4 a = MFMA(ones, as_frag(o[1]), MFMA(ones, as_frag(o[0]), zero));
        const f32x4 b = MFMA(as_frag(o[1]), as_frag(o[1]), MFMA(as_frag(o[0]), as_frag(o[0]), zero));
        const float q = (fr & 2) ? ((fr & 1) ? b[3] : b[2]) : ((fr & 1) ? b[1] : b[0]);
        if (fq == (fr >> 2)) st8_sc1(rs, ((hw * 2 + kh) * 16 + fr) * 8, st_off(g, l + 1) + j * 1024, u32x2{__float_as_uint(a[0]), __float_as_uint(q)});
      } else if (lead && blockIdx.x == 0 && lane == 0) {
        st8_sc1(rs, WS_EPOCH, 0, epoch);
      }
    }
  }
  asm volatile(".Lpf_end:");
}

extern "C" void kernel_launch(void* const* d_in, const int* in_sizes, int n_in, void* d_out, int out_size,
                              void* d_ws, size_t ws_size, hipStream_t stream) {
  Params p;
  p.x = (const u16*)d_in[0];
  p.wqkvA = (const u16*)d_in[1];
  p.wphi = (const u16*)d_in[2];
  p.woA = (const u16*)d_in[3];
  p.wqkvB = (const u16*)d_in[5];
  p.woB = (const u16*)d_in[6];
  p.out = (u16*)d_out;
  p.ws = (char*)d_ws + ((ws_size - WS_SPAN) & ~(size_t)0xffff);
  (void)hipFuncSetAttribute((const void*)mega_kernel, hipFuncAttributeMaxDynamicSharedMemorySize, L_TOTAL);
  mega_kernel<<<256, 512, L_TOTAL, stream>>>(p);
}
```

```cpp
#include <hip/hip_runtime.h>
#include <stdint.h>
#include <type_traits>
constexpr int LOOKAHEAD = 2;
constexpr int NVAL = 4;
constexpr int WT = 1;
constexpr int W1S = 6;
constexpr int NJ = 8;
constexpr int KX = 8;
constexpr int RX = 8;
constexpr unsigned TS = 17664;
#define XD 3
#define H1S 6
#define H2D 5
#define BD 8
#define PACE 4

typedef unsigned short u16;
typedef unsigned long long u64;
typedef __attribute__((ext_vector_type(8))) short bf16x8;
typedef __attribute__((ext_vector_type(4))) short s16x4;
typedef __attribute__((ext_vector_type(4))) float f32x4;
typedef __attribute__((ext_vector_type(2))) float f32x2;
typedef __attribute__((ext_vector_type(4))) unsigned u32x4;
typedef __attribute__((ext_vector_type(2))) unsigned u32x2;
typedef __attribute__((ext_vector_type(2))) __bf16 bf16x2_t;
#define LAS __attribute__((address_space(3)))
typedef LAS s16x4* lds_s16x4_ptr;

constexpr unsigned NREP = 8, REP_STRIDE = 17 * 2048;
enum { FL_X = 0, FL_CS = 4, FL_CLR = 5, FL_CLC = 9, FL_PK = 13, FL_WO = 14 };
__device__ __forceinline__ unsigned fl_line(int set, int a) { return (unsigned)(set * 16 + a) * 128; }
constexpr unsigned WS_EPOCH = 512u << 10;
constexpr unsigned WS_CS = 1u << 20;
constexpr unsigned WS_KVP = 4u << 20;
constexpr unsigned KVP_G = 32768, KVP_J = 16 * KVP_G, KVP_L = 16384, KVP_Z = 8192, KVP_SUM = 8704, KVP_HALO = 12u << 10;
constexpr unsigned SENT = 0xffffffffu;
constexpr unsigned WS_X = 14u << 20, WS_O = 29u << 20, WS_ST = 2u << 20;
__device__ __forceinline__ unsigned xb_off(int g, int l) { return WS_X + (unsigned)(l * 16 + g) * (16 * TS); }
__device__ __forceinline__ unsigned ob_off(int g, int j, int l) { return WS_O + (unsigned)((l * 16 + g) * 16 + j) * TS; }
__device__ __forceinline__ unsigned st_off(int g, int l) { return WS_ST + (unsigned)(l * 16 + g) * (16u << 10); }
constexpr unsigned WS_WP = 48u << 20;
constexpr unsigned WS_WO = 60u << 20;
constexpr unsigned WS_SPAN = 64u << 20;

constexpr unsigned L_R1 = 0;
constexpr unsigned L_R2 = 98304, XS = 16384;
constexpr unsigned L_MISC = 147456;
constexpr unsigned L_MEAN = L_MISC;
constexpr unsigned L_RSTD = L_MISC + 576;
constexpr unsigned L_CS = L_MISC + 1152;
constexpr unsigned L_WPHI = L_MISC + 2688;
constexpr unsigned L_SINK = L_MISC + 6912;
constexpr unsigned L_ZF = L_MISC + 7168;
constexpr unsigned L_PREV = L_MISC + 8192;
constexpr unsigned L_TOTAL = 163840;

#define JROT(j) ((j) & ~(NJ - 1))
constexpr int NX = NJ / 2;
#define KB0(g, j) (JROT(j) + (16 / NX) * ((g) & (NX - 1)))
#define JOBC(j, w) (((j) & ~3) + (((j) + (w) + 1) & 3))

#define SLOWC(m, jc) ((slowm >> ((m) * 8 + ((jc) & 7))) & 1)

struct Params {
  const u16* x;
  const u16* wqkvA;
  const u16* wphi;
  const u16* woA;
  const u16* wqkvB;
  const u16* woB;
  u16* out;
  char* ws;
};

#define SCHED __builtin_amdgcn_sched_barrier(0)
#define BAR __builtin_amdgcn_s_barrier()
#define SGB(m, n) __builtin_amdgcn_sched_group_barrier(m, n, 0)
#define WAITV(n) asm volatile("s_waitcnt vmcnt(%0)" ::"n"(n) : "memory")
#define WAITL0 asm volatile("s_waitcnt lgkmcnt(0)" ::: "memory")

#define TRV(slot, val) do {} while (0)
#define TRQ(slot) do {} while (0)
#define TRC(slot) do {} while (0)
#define TRR(slot) do {} while (0)
#define STAMP_INIT do {} while (0)
#define STAMP_DUMP do {} while (0)

__device__ __forceinline__ unsigned pk2(float a, float b) {
  f32x2 v = {a, b};
  return __builtin_bit_cast(unsigned, __builtin_convertvector(v, bf16x2_t));
}
__device__ __forceinline__ float lo16(unsigned u) { return __uint_as_float(u << 16); }
__device__ __forceinline__ float hi16(unsigned u) { return __uint_as_float(u & 0xffff0000u); }

template <int A, int B, typename F> __device__ __forceinline__ void static_for(F f) {
  if constexpr (A < B) { f(std::integral_constant<int, A>{}); static_for<A + 1, B>(f); }
}
typedef __amdgpu_buffer_rsrc_t rsrc_t;
__device__ __forceinline__ rsrc_t mk_rsrc(const void* p, unsigned bytes) { return __builtin_amdgcn_make_buffer_rsrc((void*)p, 0, bytes, 0x00020000); }
#define UNI(x) __builtin_amdgcn_readfirstlane(x)
#define DMA_HEAD "s_mov_b32 m0, %4\n\ts_mov_b32 %0, %3\n\ts_nop 2\n\tbuffer_load_dwordx4 %1, %2, %0 offen sc1 lds\n\t"
#define DMA_NEXT "s_add_u32 m0, m0, %6\n\ts_add_u32 %0, %0, %5\n\tbuffer_load_dwordx4 %1, %2, %0 offen sc1 lds\n\t"
#define DMA_OPS : "=&s"(t) : "v"(voff), "s"(r), "s"(UNI(soff)), "s"(UNI(lds)), "s"(UNI(sstep)), "i"(LSTEP) : "memory", "scc", "m0"
template <int LSTEP> __device__ __forceinline__ void dma4(rsrc_t r, unsigned voff, unsigned soff, unsigned sstep, unsigned lds) {
  unsigned t;
  asm volatile(DMA_HEAD DMA_NEXT DMA_NEXT DMA_NEXT DMA_OPS);
}
template <int LSTEP> __device__ __forceinline__ void dma3(rsrc_t r, unsigned voff, unsigned soff, unsigned sstep, unsigned lds) {
  unsigned t;
  asm volatile(DMA_HEAD DMA_NEXT DMA_NEXT DMA_OPS);
}
template <int LSTEP, int N> __device__ __forceinline__ void bar_dma4(rsrc_t r, unsigned voff, unsigned soff, unsigned sstep, unsigned lds) {
  unsigned t;
  asm volatile("s_mov_b32 m0, %4\n\ts_mov_b32 %0, %3\n\ts_nop 0\n\ts_waitcnt vmcnt(%7) lgkmcnt(0)\n\ts_barrier\n\t"
               "buffer_load_dwordx4 %1, %2, %0 offen sc1 lds\n\t" DMA_NEXT DMA_NEXT DMA_NEXT
               : "=&s"(t) : "v"(voff), "s"(r), "s"(UNI(soff)), "s"(UNI(lds)), "s"(UNI(sstep)), "i"(LSTEP), "n"(N) : "memory", "scc", "m0");
}
__device__ __forceinline__ void dma1(rsrc_t r, unsigned voff, unsigned soff, unsigned lds) {
  asm volatile("s_mov_b32 m0, %3\n\ts_nop 3\n\tbuffer_load_dwordx4 %0, %1, %2 offen sc1 lds" ::"v"(voff), "s"(r), "s"(UNI(soff)), "s"(UNI(lds)) : "memory", "m0");
}
__device__ __forceinline__ void touch(rsrc_t r, unsigned voff, unsigned lds) {
  asm volatile("s_mov_b32 m0, %2\n\ts_nop 3\n\tbuffer_load_dword %0, %1, 0 offen lds" ::"v"(voff), "s"(r), "s"(UNI(lds)) : "memory", "m0");
}
__device__ __forceinline__ void ld8_asm(u32x2& d, rsrc_t r, unsigned voff) {
  asm volatile("s_nop 4\n\tbuffer_load_dwordx2 %0, %1, %2, 0 offen sc1" : "=v"(d) : "v"(voff), "s"(r) : "memory");
}

__device__ __forceinline__ void ld16in_asm(u32x4& d, rsrc_t r, unsigned voff, unsigned soff) {
  asm volatile("s_nop 4\n\tbuffer_load_dwordx4 %0, %1, %2, %3 offen" : "=v"(d) : "v"(voff), "s"(r), "s"(UNI(soff)) : "memory");
}
__device__ __forceinline__ void ld16_asm(f32x4& d, rsrc_t r, unsigned voff, unsigned soff) {
  asm volatile("s_nop 4\n\tbuffer_load_dwordx4 %0, %1, %2, %3 offen sc1" : "=v"(d) : "v"(voff), "s"(r), "s"(UNI(soff)) : "memory");
}

template <typename T> __device__ __forceinline__ T lds_ld(unsigned a) { return *(LAS T*)(a); }
template <typename T> __device__ __forceinline__ void lds_st(unsigned a, T v) { *(LAS T*)(a) = v; }
__device__ __forceinline__ bf16x8 tr2(unsigned a0, unsigned a1) {
  s16x4 v0 = __builtin_amdgcn_ds_read_tr16_b64_v4i16((lds_s16x4_ptr)(a0));
  s16x4 v1 = __builtin_amdgcn_ds_read_tr16_b64_v4i16((lds_s16x4_ptr)(a1));
  return __builtin_shufflevector(v0, v1, 0, 1, 2, 3, 4, 5, 6, 7);
}
__device__ __forceinline__ bf16x8 as_frag(u32x4 v) { return __builtin_bit_cast(bf16x8, v); }
#define MFMA(a, b, c) __builtin_amdgcn_mfma_f32_16x16x32_bf16(a, b, c, 0, 0, 0)

__device__ __forceinline__ float vmax2(float a, float b) { float inf = __builtin_inff(); asm("" : "+s"(inf)); return __builtin_amdgcn_fmed3f(a, b, inf); }
#define SWAP16(a, b) __builtin_amdgcn_permlane16_swap(__float_as_uint(a), __float_as_uint(b), false, false)
#define SWAP32(a, b) __builtin_amdgcn_permlane32_swap(__float_as_uint(a), __float_as_uint(b), false, false)
__device__ __forceinline__ void red2_max(float& a, float& b) {
  auto r = SWAP16(a, b);
  float x = vmax2(__uint_as_float(r[0]), __uint_as_float(r[1]));
  r = SWAP32(x, x);
  x = vmax2(__uint_as_float(r[0]), __uint_as_float(r[1]));
  r = SWAP16(x, x);
  a = __uint_as_float(r[0]); b = __uint_as_float(r[1]);
}
__device__ __forceinline__ void red2_add(float& a, float& b) {
  auto r = SWAP16(a, b);
  float x = __uint_as_float(r[0]) + __uint_as_float(r[1]);
  r = SWAP32(x, x);
  x = __uint_as_float(r[0]) + __uint_as_float(r[1]);
  r = SWAP16(x, x);
  a = __uint_as_float(r[0]); b = __uint_as_float(r[1]);
}
template <bool MAX, typename M> __device__ __forceinline__ void red4(float& a, float& b, float& c, float& d, M mid) {
  auto op = [](unsigned x, unsigned y) { return MAX ? vmax2(__uint_as_float(x), __uint_as_float(y)) : __uint_as_float(x) + __uint_as_float(y); };
  auto r = SWAP16(a, b), q = SWAP16(c, d);
  const float x = op(r[0], r[1]), y = op(q[0], q[1]);
  r = SWAP32(x, y);
  const float w = mid(op(r[0], r[1]));
  r = SWAP32(w, w);
  q = __builtin_amdgcn_permlane16_swap(r[1], r[1], false, false);
  r = __builtin_amdgcn_permlane16_swap(r[0], r[0], false, false);
  a = __uint_as_float(r[0]); b = __uint_as_float(r[1]); c = __uint_as_float(q[0]); d = __uint_as_float(q[1]);
}
__device__ __forceinline__ float red_max4(float x) {
  auto r = __builtin_amdgcn_permlane16_swap(__float_as_uint(x), __float_as_uint(x), false, false);
  x = vmax2(__uint_as_float(r[0]), __uint_as_float(r[1]));
  r = __builtin_amdgcn_permlane32_swap(__float_as_uint(x), __float_as_uint(x), false, false);
  return vmax2(__uint_as_float(r[0]), __uint_as_float(r[1]));
}
__device__ __forceinline__ float red_add4(float x) {
  auto r = __builtin_amdgcn_permlane16_swap(__float_as_uint(x), __float_as_uint(x), false, false);
  x = __uint_as_float(r[0]) + __uint_as_float(r[1]);
  r = __builtin_amdgcn_permlane32_swap(__float_as_uint(x), __float_as_uint(x), false, false);
  return __uint_as_float(r[0]) + __uint_as_float(r[1]);
}

__device__ __forceinline__ void st16_sc1(rsrc_t r, unsigned voff, unsigned soff, u32x4 v) {
  __builtin_amdgcn_raw_buffer_store_b128(v, r, voff, soff, 16);
  asm volatile("s_nop 1" ::"v"(v) : "memory");
}
__device__ __forceinline__ void st8_sc1(rsrc_t r, unsigned voff, unsigned soff, u32x2 v) { __builtin_amdgcn_raw_buffer_store_b64(v, r, voff, soff, 16); }
__device__ __forceinline__ u32x4 ld16_sc1(rsrc_t r, unsigned voff, unsigned soff) { return __builtin_amdgcn_raw_buffer_load_b128(r, voff, soff, 16); }
__device__ __forceinline__ u32x2 ld8_sc1(rsrc_t r, unsigned voff, unsigned soff) { return __builtin_amdgcn_raw_buffer_load_b64(r, voff, soff, 16); }
__device__ __forceinline__ f32x4 ld4f_sc1(rsrc_t r, unsigned voff, unsigned soff) { return __builtin_bit_cast(f32x4, __builtin_amdgcn_raw_buffer_load_b128(r, voff, soff, 16)); }
#define OPAQUE_V(x) asm volatile("" : "+v"(x))
#define OPAQUE_S(x) asm volatile("" : "+s"(x))

#define CLOBBER asm volatile("" ::: "memory")
constexpr unsigned SPIN_CAP = 1u << 17;
__device__ __forceinline__ bool is_set(u32x2 v, u32x2 epoch) { return v[0] == epoch[0] && v[1] == epoch[1]; }
__device__ __forceinline__ void set_flags(rsrc_t rs, unsigned lineA, int colA, unsigned lineB, int colB, u32x2 epoch, int lane) {
  const unsigned off = (lane & 7) * REP_STRIDE + ((lane & 8) ? lineB + colB * 8 : lineA + colA * 8);
  if (lane < 16) st8_sc1(rs, off, 0, epoch);
}
__device__ __forceinline__ unsigned poll_need(rsrc_t rs, unsigned voff, u32x2 epoch, unsigned ready, unsigned need, bool& dead) {
  for (unsigned spins = 0; !dead; ++spins) {
    CLOBBER;
    ready |= (unsigned)__ballot(is_set(ld8_sc1(rs, voff, 0), epoch));
    if ((ready & need) == need) break;
    if (spins > SPIN_CAP) dead = true;
    __builtin_amdgcn_s_sleep(1);
  }
  CLOBBER;
  return dead ? ~0u : ready;
}

__device__ __forceinline__ void poll_all(rsrc_t rs, unsigned voff, u32x2 epoch, bool& dead) {
  for (unsigned spins = 0; !dead; ++spins) {
    CLOBBER;
    if (__all(is_set(ld8_sc1(rs, voff, 0), epoch))) break;
    if (spins > SPIN_CAP) dead = true;
    __builtin_amdgcn_s_sleep(1);
  }
  CLOBBER;
}

constexpr unsigned SC_VF = L_R1 + 32768;
constexpr unsigned SC_PHIK = L_R1 + 49152;
constexpr unsigned SC_XCH = L_R2 + XS;
constexpr unsigned wo_slot(int s) {
  return s < 2 ? L_R2 + (unsigned)(s + 1) * XS : (s < 6 ? L_R1 + (unsigned)(s - 2) * 16384 : (s == 6 ? L_R1 + 81920 : L_R2));
}
__device__ __forceinline__ unsigned wo_src(int s, int j) { return (unsigned)(((JROT(j) >> 1) + NJ / 2 + s) & 7); }
__device__ __forceinline__ unsigned wo_swz(int lane) { return (unsigned)(((lane & 7) ^ ((lane >> 3) & 6)) * 16); }
__device__ __forceinline__ unsigned umax3(unsigned a, unsigned b, unsigned c) { return max(max(a, b), c); }
__device__ __forceinline__ unsigned umax4v(unsigned m, const u32x4& v) { return umax3(umax3(m, v[0], v[1]), v[2], v[3]); }
__device__ __forceinline__ u32x4 nosent(u32x4 v) { return u32x4{min(v[0], SENT - 1), min(v[1], SENT - 1), min(v[2], SENT - 1), min(v[3], SENT - 1)}; }

template <typename P, typename F>
__device__ __forceinline__ void qkv_loop(int isb, int wm, int h, int lane, bool first, int& trv, P pre, F fin) {
  constexpr unsigned lds0 = 0;
  f32x4 acc[4][6];
  const int fr = lane & 15, fq = lane >> 4;
  unsigned ab[2], bb[2];
  {
    const int r = wm * 64 + fr;
#pragma unroll
    for (int ks = 0; ks < 2; ++ks) ab[ks] = lds0 + L_R2 + r * 128 + (((ks * 4 + fq) ^ ((fr >> 1) & 7)) * 16);
    const int kr = fq * 8 + (fr >> 2);
    const int gs = ((kr >> 1) & 1) | (((kr >> 3) & 1) << 1);
#pragma unroll
    for (int c = 0; c < 2; ++c) bb[c] = lds0 + L_R1 + kr * 128 + (((h * 2 + c) ^ gs) * 32) + (lane & 3) * 8;
  }
#pragma unroll
  for (int m = 0; m < 4; ++m)
#pragma unroll
    for (int n = 0; n < 6; ++n) acc[m][n] = f32x4{0.f, 0.f, 0.f, 0.f};
  bf16x8 af[2][4], bf[2][6];
  auto rds = [&](int ks, int p, int rot = 0) {
#pragma unroll
    for (int m = 0; m < 4; ++m) af[p][m] = lds_ld<bf16x8>(ab[ks] + m * 2048);
#pragma unroll
    for (int nn = 0; nn < 6; ++nn) {
      const int n = (nn + rot) % 6;
      const unsigned a = bb[n & 1] + (n >> 1) * 8192 + ks * 4096;
      bf[p][n] = tr2(a, a + 512);
    }
  };
  auto mms = [&](int p, int rot = 0, int cnt = 6) {
#pragma unroll
    for (int nn = 0; nn < cnt; ++nn)
#pragma unroll
      for (int mm = 0; mm < 4; ++mm) { const int n = (nn + rot) % 6, m = (n & 1) ? 3 - mm : mm; acc[m][n] = n < 4 ? MFMA(bf[p][n], af[p][m], acc[m][n]) : MFMA(af[p][m], bf[p][n], acc[m][n]); }
  };
  auto ilv2 = [&]() {
#pragma unroll
    for (int i = 0; i < 16; ++i) { SGB(0x008, 1); SGB(0x100, 1); }
#pragma unroll
    for (int i = 0; i < 4; ++i) { SGB(0x008, 1); SGB(0x002, 1); }
    SGB(0x008, 4);
  };
  auto ilv3 = [&]() {
#pragma unroll
    for (int i = 0; i < 16; ++i) { SGB(0x008, 1); SGB(0x100, 1); }
#pragma unroll
    for (int i = 0; i < 7; ++i) { SGB(0x008, 1); SGB(0x004, 2); }
    SGB(0x008, 1);
  };
#pragma unroll
  for (int i = 0; i < 24; ++i) OPAQUE_V(acc[i / 6][i % 6]);
  OPAQUE_V(ab[0]); OPAQUE_V(ab[1]); OPAQUE_V(bb[0]); OPAQUE_V(bb[1]);
  if (!first) { WAITV(3); WAITL0; }
  TRQ(1); BAR;
  int tslot = 2;
  rds(0, 0);
  int sa = 0, sb = 0;
  int da = XS, db = 24576;
  auto step = [&]() {
    ab[0] += da; ab[1] += da;
    bb[0] += db; bb[1] += db;
  };
  auto ring = [&]() {
    sa = (sa == 2) ? 0 : sa + 1;
    sb = (sb == 3) ? 0 : sb + 1;
    da = (sa == 2) ? -2 * (int)XS : (int)XS;
    db = (sb == 3) ? -3 * 24576 : 24576;
  };
#pragma unroll 1
  for (int t = 14; t > 0; --t) {
    SCHED;
    rds(1, 1); mms(0);
    step();
    ilv2();
    SCHED;
    __builtin_amdgcn_s_waitcnt(0x0070);
    TRQ(tslot); tslot++; BAR;
    SCHED;
    rds(0, 0);
    ring();
    mms(1); ilv3();
  }
  SCHED;
  f32x4 cs[4], nm4[4];
  float nmean[4], csv[2];
  auto fold = [&](int n) {
#pragma unroll
    for (int mm = 0; mm < 4; ++mm) {
      const int m = (n & 1) ? 3 - mm : mm;
#pragma unroll
      for (int e = 0; e < 4; ++e) {
        float t = n < 4 ? __builtin_fmaf(nmean[m], cs[n & 3][e], acc[m][n][e]) : __builtin_fmaf(nm4[m][e], csv[n & 1], acc[m][n][e]);
        asm("" : "+v"(t));
        acc[m][n][e] = t;
      }
    }
  };
  {
    unsigned pm = lds0 + L_MEAN + (16 + wm * 64 + fr) * 4, pc = lds0 + L_CS + isb * 768 + (h * 32 + fq * 4) * 4;
    unsigned pm4 = lds0 + L_MEAN + (16 + wm * 64 + fq * 4) * 4, pcv = lds0 + L_CS + isb * 768 + (128 + h * 32 + fr) * 4;
    asm("" : "+v"(pm)); asm("" : "+v"(pc)); asm("" : "+v"(pm4)); asm("" : "+v"(pcv));
#pragma unroll
    for (int m = 0; m < 4; ++m) nmean[m] = lds_ld<float>(pm + m * 64);
#pragma unroll
    for (int n = 0; n < 4; ++n) cs[n ^ 2] = lds_ld<f32x4>(pc + (((n ^ 2) >> 1) * 64 + (n & 1) * 16) * 4);
#pragma unroll
    for (int m = 0; m < 4; ++m) nm4[m] = lds_ld<f32x4>(pm4 + m * 64);
#pragma unroll
    for (int n = 0; n < 2; ++n) csv[n] = lds_ld<float>(pcv + n * 64);
    rds(1, 1, 4); mms(0, 2);
    step();
    fold(2); fold(3);
    SGB(0x002, 8);
#pragma unroll
    for (int i = 0; i < 8; ++i) { SGB(0x008, 1); SGB(0x100, 2); }
#pragma unroll
    for (int i = 0; i < 11; ++i) { SGB(0x008, 1); SGB(0x100, 1); SGB(0x002, 2); }
#pragma unroll
    for (int i = 0; i < 5; ++i) { SGB(0x008, 1); SGB(0x002, 3); }
    SCHED;
    __builtin_amdgcn_s_waitcnt(0xC07F);
    TRQ(16); BAR;
    SCHED;
    rds(0, 0, 2);
    mms(1, 4); fold(4); fold(5);
#pragma unroll
    for (int i = 0; i < 8; ++i) { SGB(0x008, 1); SGB(0x100, 1); SGB(0x002, 1); }
#pragma unroll
    for (int i = 0; i < 8; ++i) { SGB(0x008, 1); SGB(0x100, 1); SGB(0x002, 2); }
#pragma unroll
    for (int i = 0; i < 8; ++i) { SGB(0x008, 1); SGB(0x002, 2); }
    SCHED;
    pre(0);
    SCHED;
    rds(1, 1, 2);
    mms(0, 2, 4); fold(0); fold(1);
    pre(1);
#pragma unroll
    for (int i = 0; i < 16; ++i) { SGB(0x008, 1); SGB(0x100, 1); SGB(0x002, 2); SGB(0x004, 1); }
  }
  SCHED;
  auto mm6 = [&](int m, int n0 = 0, int n1 = 8) {
#pragma unroll
    for (int nn = n0; nn < n1; ++nn) {
      const int n = nn < 4 ? nn + 2 : nn & 1, p = (nn >> 1) != 2;
      acc[m][n] = n < 4 ? MFMA(bf[p][n], af[p][m], acc[m][n]) : MFMA(af[p][m], bf[p][n], acc[m][n]);
    }
  };
  if (!isb) {
    fin(std::false_type{}, acc, mm6);
#pragma unroll
    for (int i = 0; i < 24; ++i) asm volatile("" : "=v"(acc[i / 6][i % 6]));
#pragma unroll
    for (int i = 0; i < 6; ++i) asm volatile("" : "=v"(bf[1][i]));
#pragma unroll
    for (int i = 0; i < 8; ++i) asm volatile("" : "=v"(af[i >> 2][i & 3]));
    asm volatile("" : "=v"(bf[0][0])); asm volatile("" : "=v"(bf[0][1]));
  }
  OPAQUE_S(isb);
  if (isb) fin(std::true_type{}, acc, mm6);
}
__device__ __forceinline__ u32x4 pack(const f32x4& a, const f32x4& b) { return u32x4{pk2(a[0], a[1]), pk2(a[2], a[3]), pk2(b[0], b[1]), pk2(b[2], b[3])}; }

__device__ __forceinline__ u32x2 nosent2(const f32x4& a) { return u32x2{min(pk2(a[0], a[1]), SENT - 1), min(pk2(a[2], a[3]), SENT - 1)}; }
template <int N> __device__ __forceinline__ void hedgehog(const f32x4 (&r)[4][2], const float (&c)[4], float (&ep)[4][8], float (&en)[4][8]) {
  float mx[4], nm[4];
#pragma unroll
  for (int m = 0; m < N; ++m) {
    const f32x4 a = r[m][0], b = r[m][1];
    mx[m] = vmax2(fmaxf(fmaxf(a[3], b[0]), b[1]), fmaxf(fmaxf(fmaxf(fmaxf(a[0], a[1]), a[2]), b[2]), b[3]));
    nm[m] = vmax2(fmaxf(fmaxf(-a[3], -b[0]), -b[1]), fmaxf(fmaxf(fmaxf(fmaxf(-a[0], -a[1]), -a[2]), -b[2]), -b[3]));
  }
  SCHED;
  red4<true>(mx[0], mx[1], nm[0], nm[1], [](float x) { return x; });
  if (N == 4) red4<true>(mx[2], mx[3], nm[2], nm[3], [](float x) { return x; });
#pragma unroll
  for (int m = 0; m < N; ++m) { mx[m] *= -c[m]; nm[m] *= -c[m]; }
  SCHED;
#pragma unroll
  for (int m = 0; m < N; ++m) {
#pragma unroll
    for (int i = 0; i < 8; ++i) { ep[m][i] = __builtin_fmaf(r[m][i >> 2][i & 3], c[m], mx[m]); en[m][i] = __builtin_fmaf(r[m][i >> 2][i & 3], -c[m], nm[m]); }
    SCHED;
#pragma unroll
    for (int i = 0; i < 8; ++i) { ep[m][i] = __builtin_amdgcn_exp2f(ep[m][i]); en[m][i] = __builtin_amdgcn_exp2f(en[m][i]); }
    SCHED;
  }
}
__device__ __forceinline__ u32x4 pack8(const float (&e)[8], float f) {
  return u32x4{pk2(e[0] * f, e[1] * f), pk2(e[2] * f, e[3] * f), pk2(e[4] * f, e[5] * f), pk2(e[6] * f, e[7] * f)};
}
__device__ __forceinline__ u32x4 pack8(const float (&e)[8]) { return u32x4{pk2(e[0], e[1]), pk2(e[2], e[3]), pk2(e[4], e[5]), pk2(e[6], e[7])}; }

constexpr u64 window_mask(int t, int e) {
  u64 mk = 0;
  for (int ln = 0; ln < 64; ++ln) {
    const int delta = (ln & 15) + 16 - t * 16 - (ln >> 4) * 4 - e;
    if (delta >= 0 && delta <= 14 && !(delta & 1)) mk |= 1ull << ln;
  }
  return mk;
}
__device__ __forceinline__ void phik2(const f32x4 (&r)[4][2], const float (&c)[4], unsigned pw) {
  const f32x4 zero = {0.f, 0.f, 0.f, 0.f};
  const bf16x8 ones = {0x3f80, 0x3f80, 0x3f80, 0x3f80, 0x3f80, 0x3f80, 0x3f80, 0x3f80};
  float ep[4][8], en[4][8];
  f32x4 sp[2], sn[2];
  hedgehog<2>(r, c, ep, en);
#pragma unroll
  for (int m = 0; m < 2; ++m) { sp[m] = MFMA(ones, as_frag(pack8(ep[m])), zero); sn[m] = MFMA(ones, as_frag(pack8(en[m])), zero); }
#pragma unroll
  for (int m = 0; m < 2; ++m) { asm("" : "+v"(sp[m])); asm("" : "+v"(sn[m])); }
  SCHED;
#pragma unroll
  for (int m = 0; m < 2; ++m) {
    const float fp = __builtin_amdgcn_rcpf(sp[m][0]), fn = __builtin_amdgcn_rcpf(sn[m][0]);
#pragma unroll
    for (int i = 0; i < 8; ++i) { ep[m][i] *= fp; en[m][i] *= fn; }
    SCHED;
    const u32x4 pp = pack8(ep[m]), pn = pack8(en[m]);
    const unsigned d = pw + m * (16 * 160);
    lds_st<u32x2>(d, u32x2{pp[0], pp[1]});
    lds_st<u32x2>(d + 32, u32x2{pp[2], pp[3]});
    lds_st<u32x2>(d + 64, u32x2{pn[0], pn[1]});
    lds_st<u32x2>(d + 96, u32x2{pn[2], pn[3]});
    SCHED;
  }
}
__device__ __forceinline__ bf16x8 rowsum_sel(int lane) {
  const int fr = lane & 15;
  const unsigned one = (fr < 8) ? (0x3f80u << (16 * (fr & 1))) : 0u;
  const int e = (fr >> 1) & 3;
  return as_frag(u32x4{e == 0 ? one : 0u, e == 1 ? one : 0u, e == 2 ? one : 0u, e == 3 ? one : 0u});
}
__device__ __forceinline__ f32x4 rowsum_fin(f32x4 c) {
#pragma unroll
  for (int k = 0; k < 4; ++k) {
    const float a = c[k];
    c[k] = a + __int_as_float(__builtin_amdgcn_update_dpp(0, __float_as_int(a), 0x128  , 0xf, 0xf, false));
  }
  return c;
}
__device__ __forceinline__ void job_colsum(rsrc_t rs, const u32x4 (&w)[8], int mat, int g, int jc, int hw, int lane) {
  const bf16x8 sel = rowsum_sel(lane);
  f32x4 c = {0.f, 0.f, 0.f, 0.f};
#pragma unroll
  for (int i = 0; i < 8; ++i) c = MFMA(sel, as_frag(w[i]), c);
  const u32x4 w4 = __builtin_bit_cast(u32x4, rowsum_fin(c));
  unsigned so = WS_CS + (unsigned)(((mat * 16 + jc) * 16 + g) * 768);
  OPAQUE_S(so);
  if ((lane & 40) == 0) st16_sc1(rs, (unsigned)(hw * 64 + (lane & 7) * 8 + (lane >> 4) * 4) * 4, so, w4);
}
__device__ __forceinline__ unsigned img_off(int i, int lane) { return (lane >> 3) * 128 + (((lane & 7) ^ ((((lane >> 4) & 1) | ((i & 1) << 1)) << 1)) * 16); }
__device__ __forceinline__ void job_pack(rsrc_t rs, u32x4 w, int i, int mat, int g, int jc, int hw, int lane) {
  st16_sc1(rs, img_off(i, lane),
           WS_WP + (unsigned)(((mat * 16 + jc) * 16 + g) * 24576 + hw * 8192 + i * 1024), w);
}

__global__ __launch_bounds__(512, 2) void mega_kernel(Params p) {
  extern __shared__ __attribute__((aligned(16))) char smem[];
  const int tid = threadIdx.x;
  const int wave0 = __builtin_amdgcn_readfirstlane(tid >> 6);
  const int xx = blockIdx.x & 7, rr = blockIdx.x >> 3;
  const int g0 = (rr / NJ) * NX + (xx & (NX - 1)), j0 = (xx / NX) * NJ + (rr & (NJ - 1));
  constexpr unsigned lds0 = 0, lds00 = 0;
  if ((unsigned)(size_t)(LAS char*)smem != 0u) return;
#define LOCALS0 int g = g0, j = j0, l = l0, wave = wave0;                                              \
  OPAQUE_S(g); OPAQUE_S(j); OPAQUE_S(l); OPAQUE_S(wave);                                               \
  const int hw = wave & 3, wm = wave & 1, h = (wave >> 1) & 1;                                         \
  const unsigned myrep = (unsigned)(JROT(j) / 2 + (g & (NX - 1))) * REP_STRIDE;                              \
  (void)hw; (void)wm; (void)h; (void)myrep;                                                            \
  int lane = tid; OPAQUE_V(lane); lane &= 63
#define LOCALS LOCALS0; const bool isb = l & 1, lead = (wave == 4); const int half = wave >> 2; (void)isb; (void)lead; (void)half
  const rsrc_t rs = mk_rsrc(p.ws, WS_SPAN);
  const bool cw = wave0 < 4;
  int trv = 0, trA = 0, trB = 0, trC = 0;
  STAMP_INIT;
  TRC(0); TRR(62);
  if (wave0 == 3) {
    const char* pc;
    unsigned len;
    asm volatile("s_getpc_b64 %0\n.Lpf_base:\n\ts_mov_b32 %1, .Lpf_end-.Lpf_base" : "=s"(pc), "=s"(len));
    const unsigned off = ((unsigned)rr * 16 + (tid & 15)) * 128;
    touch(mk_rsrc(pc, len), ((int)len > 0 && off + 4 <= len) ? off : 0u, lds00 + L_SINK);
  }
  if (wave0 == 2) touch(rs, WS_EPOCH, lds00 + L_SINK);
  if (wave0 == 4) touch(mk_rsrc(p.wphi, 32 * 2048), j0 * 4096, lds00 + L_SINK);
  if (wave0 == 5) touch(mk_rsrc(p.woA, 1024 * 2048), (tid & 1) << 20, lds00 + L_SINK);
  if (wave0 == 6) touch(mk_rsrc(p.wqkvB, 1024 * 6144), min(tid & 7, 5) << 20, lds00 + L_SINK);
  if (wave0 == 7) touch(mk_rsrc(p.woB, 1024 * 2048), (tid & 1) << 20, lds00 + L_SINK);
  auto slowbit = [](const void* q) { return 1u << ((3 - ((unsigned)(size_t)q >> 7)) & 7); };
  unsigned slowm = slowbit(p.wqkvA) | (slowbit(p.wqkvB) << 8) | (slowbit(p.woA) << 16) | (slowbit(p.woB) << 24);
  OPAQUE_S(slowm);
  u32x2 epoch = {0u, 0u};
  bool dead = false;
      if (cw) {
        const int l0 = 0;
        LOCALS;
        u32x4 v[2][8];
        if (wave < 3) {
          const int jc = JOBC(j, wave), kt = (KB0(g, j) + (unsigned)g / NX) & 15;
          const rsrc_t ra = mk_rsrc(p.wqkvA, 1024 * 6144);
#pragma unroll
          for (int i = 0; i < 8; ++i)
            v[0][i] = __builtin_amdgcn_raw_buffer_load_b128(ra, (lane >> 3) * 6144 + (lane & 7) * 16, (kt * 64 + i * 8) * 6144 + wave * 2048 + jc * 128, 0);
          SCHED;
          TRC(44); BAR; SCHED;
          job_colsum(rs, v[0], 0, g, jc, wave, lane);
          if (SLOWC(0, jc)) {
#pragma unroll
            for (int i = 0; i < 8; ++i) job_pack(rs, v[0][i], i, 0, kt, jc, wave, lane);
          }
        } else {
          const int fr = lane & 15, fq = lane >> 4;
          const rsrc_t rx = mk_rsrc(p.x, 2048 * 2048);
#pragma unroll
          for (int i = 0; i < 16; ++i)
            v[i & 1][i >> 1] = __builtin_amdgcn_raw_buffer_load_b128(rx, fr * 2048 + fq * 16, (g * 128 + (j & (NJ - 1)) * (128 / NJ) + ((i >> 1) % (8 / NJ)) * 16) * 2048 + ((KB0(g, j) + (i >> 1) / (8 / NJ)) & 15) * 128 + (i & 1) * 64, 0);
          SCHED;
          TRC(44); BAR; SCHED;
          const f32x4 zero = {0.f, 0.f, 0.f, 0.f};
          const bf16x8 ones = {0x3f80, 0x3f80, 0x3f80, 0x3f80, 0x3f80, 0x3f80, 0x3f80, 0x3f80};
          float sm[8], q[8];
#pragma unroll
          for (int m = 0; m < 8; ++m) {
            const f32x4 a = MFMA(ones, as_frag(v[1][m]), MFMA(ones, as_frag(v[0][m]), zero));
            const f32x4 b = MFMA(as_frag(v[1][m]), as_frag(v[1][m]), MFMA(as_frag(v[0][m]), as_frag(v[0][m]), zero));
            sm[m] = a[0];
            q[m] = (fr & 2) ? ((fr & 1) ? b[3] : b[2]) : ((fr & 1) ? b[1] : b[0]);
          }
          if (fq == (fr >> 2)) {
#pragma unroll
            for (int m = 0; m < 8; ++m) st8_sc1(rs, ((m % (8 / NJ)) * 16 + fr) * 8 + (m / (8 / NJ)) * 1024, st_off(g, 0) + JROT(j) * 1024 + (j & (NJ - 1)) * (1024 / NJ), u32x2{__float_as_uint(sm[m]), __float_as_uint(q[m])});
          }
        }
      }
  const char *pA = (const char*)p.x, *pW = (const char*)p.wqkvA;
  unsigned zA = 2048 * 2048, bA = g0 * (128 * 2048), kA = 128, pcA = 32 * 2048, vA;
  unsigned zW = 1024 * 6144, bW = j0 * 128, kW = 64 * 6144, pcW = 2048, hW = 32 * 6144, vW;
  int kb = KB0(g0, j0), dj = -1;
  {
    const int lane = tid & 63, hw = wave0 & 3;
    vA = (hw * 8 + (lane >> 3)) * 2048 + (((lane & 7) ^ (((hw & 1) * 4 + (lane >> 4)) & 7)) * 16);
    vW = (hw * 8 + (lane >> 3)) * 6144 + (((lane & 7) ^ ((((lane >> 4) & 1) | ((hw & 1) << 1)) << 1)) * 16);
  }

#pragma unroll 1
  for (int l0 = 0; l0 < 4; ++l0) {
    if (l0) { TRC(0); TRR(62); }

    f32x4 acc[2][4];
    auto kvpart = [&](unsigned va, unsigned pa, unsigned pb, unsigned kvb, int lane, int wave) {
      const f32x4 zero = {0.f, 0.f, 0.f, 0.f};
      const bf16x8 ones = {0x3f80, 0x3f80, 0x3f80, 0x3f80, 0x3f80, 0x3f80, 0x3f80, 0x3f80};
      f32x4 c0 = zero, c1 = zero, z0 = zero;
#pragma unroll
      for (int ks = 0; ks < 4; ++ks) {
        const bf16x8 v = lds_ld<bf16x8>(va + ks * 2048);
        const bf16x8 a0 = tr2(pa + ks * 32 * 160, pa + (ks * 32 + 16) * 160);
        const bf16x8 a1 = tr2(pb + ks * 32 * 160, pb + (ks * 32 + 16) * 160);
        c0 = MFMA(a0, v, c0);
        c1 = MFMA(a1, v, c1);
        z0 = MFMA(a0, ones, z0);
      }
      const u32x4 c = nosent(pack(c0, c1));
      const bool dt = wave & 2;
      st16_sc1(rs, lane * 16, kvb + (unsigned)wave * 1024, u32x4{dt ? c[2] : c[0], dt ? c[3] : c[1], dt ? c[0] : c[2], dt ? c[1] : c[3]});
      if ((lane & 15) == 0)
        st8_sc1(rs, lane, kvb + KVP_Z + (wave >> 2) * 128 + (wave & 1) * 64 + (wave & 2) * 4, nosent2(z0));
    };
#define KV_ADDR                                                                                                            \
    unsigned va = lds0 + SC_VF + (wave >> 2) * 8192 + ((wave >> 1) & 1) * 1024 + lane * 16;                                \
    unsigned pa = lds0 + SC_PHIK + ((wave >> 2) * 128 + fq * 4 + (fr >> 2)) * 160 + (wave & 1) * 64 + (lane & 3) * 8;      \
    unsigned kvj = WS_KVP + (unsigned)j * KVP_J + (unsigned)(l >> 1) * KVP_L;                                              \
    unsigned pb = pa + 32 - (wave & 2) * 16;                                                                               \
    pa += (wave & 2) * 16;                                                                                                 \
    OPAQUE_V(va); OPAQUE_V(pa); OPAQUE_V(pb); OPAQUE_S(kvj)
    if (cw) {
      LOCALS;
      unsigned otile, kvo, l16, pr1, pr4;
      int g2, j2, l2, ln2;
      qkv_loop(l & 1, wm, h, lane, l == 0, trv, [&](int st) {
        if (st == 0) {
          g2 = g0; j2 = j0; l2 = l0; ln2 = tid;
          OPAQUE_S(g2); OPAQUE_S(j2); OPAQUE_S(l2); OPAQUE_V(ln2);
          return;
        }
        otile = ob_off(g2, j2, l2) + (unsigned)(wm * 8 + h) * 1024;
        kvo = WS_KVP + (unsigned)j2 * KVP_J + (unsigned)g2 * KVP_G + (unsigned)(l2 >> 1) * KVP_L;
        l16 = (ln2 & 63) * 16;
        pr1 = lds0 + L_RSTD + (16 + wm * 64 + (ln2 & 15)) * 4;
        pr4 = lds0 + L_RSTD + wm * 256 + (ln2 & 48);
        asm("" : "+s"(otile)); asm("" : "+s"(kvo)); asm("" : "+v"(l16)); asm("" : "+v"(pr1)); asm("" : "+v"(pr4));
      }, [&](auto isb_c, f32x4 (&a)[4][6], auto& mm6) {
        const f32x4 zero = {0.f, 0.f, 0.f, 0.f};
        constexpr float L2E = 1.4426950408889634f;
        float rq[4];
#define ilv8(nv) do { _Pragma("unroll") for (int i_ = 0; i_ < 8; ++i_) { SGB(0x008, 1); SGB(0x100, 2); SGB(0x002, nv); } SCHED; } while (0)
        if constexpr (decltype(isb_c)::value) {
          constexpr float QS = 0.17677669529663687f * L2E, NEG = -3.0e38f;
          const bf16x8 ones = {0x3f80, 0x3f80, 0x3f80, 0x3f80, 0x3f80, 0x3f80, 0x3f80, 0x3f80};
          u32x4 kf[5], vf[5], qf[4], pf[4];
          f32x4 rs4[4], bias[2], s0[4], s1[4], dn[4];
          float x[4][4], mx[4];
          unsigned psel0, psel1;
          auto pkkv = [&](int m) { kf[m + 1] = pack(a[m][2] * rq[m], a[m][3] * rq[m]); vf[m + 1] = pack(a[m][4] * rs4[m], a[m][5] * rs4[m]); };
          auto pkq = [&](int m) { qf[m] = pack(a[m][0], a[m][1]); };
          auto st1 = [&](int m) {
            x[m][0] = vmax2(s0[m][0], s0[m][1]); x[m][1] = vmax2(s0[m][2], s0[m][3]); x[m][2] = vmax2(s1[m][0], s1[m][1]); x[m][3] = vmax2(s1[m][2], s1[m][3]);
            mx[m] = vmax2(vmax2(x[m][0], x[m][1]), vmax2(x[m][2], x[m][3]));
          };
          auto st2 = [&](int m) {
            const float mxl = mx[m] * -rq[m];
#pragma unroll
            for (int e = 0; e < 4; ++e) x[m][e] = __builtin_fmaf(x[m][e], rq[m], mxl);
          };
          auto st3 = [&](int m) {
#pragma unroll
            for (int e = 0; e < 4; ++e) x[m][e] = __builtin_amdgcn_exp2f(x[m][e]);
          };
          auto st5 = [&](int m) {
            const unsigned p0 = pk2(x[m][0], x[m][1]), p1 = pk2(x[m][2], x[m][3]);
            pf[m] = u32x4{__builtin_amdgcn_perm(0u, p0, psel0), __builtin_amdgcn_perm(0u, p0, psel1), __builtin_amdgcn_perm(0u, p1, psel0), __builtin_amdgcn_perm(0u, p1, psel1)};
          };
          auto st6 = [&](int m) {
            dn[m] = MFMA(ones, as_frag(pf[m]), zero);
            s0[m] = MFMA(as_frag(u32x4{vf[m][0], vf[m][1], vf[m + 1][0], vf[m + 1][1]}), as_frag(pf[m]), zero);
            s1[m] = MFMA(as_frag(u32x4{vf[m][2], vf[m][3], vf[m + 1][2], vf[m + 1][3]}), as_frag(pf[m]), zero);
            asm("" : "+v"(dn[m]));
          };
          auto st7 = [&](int m) { const float inv = __builtin_amdgcn_rcpf(dn[m][0]); pf[m] = nosent(pack(s0[m] * inv, s1[m] * inv)); };
          mm6(3);
#pragma unroll
          for (int m = 0; m < 4; ++m) rq[m] = lds_ld<float>(pr1 + m * 64);
#pragma unroll
          for (int m = 3; m >= 0; --m) rs4[m] = lds_ld<f32x4>(pr4 + 64 + m * 64);
          unsigned pv_ = l16 + lds0 + L_PREV + h * 2048;
          asm("" : "+v"(pv_));
          static_for<0, 8>([&](auto i_c) __attribute__((always_inline)) {
            constexpr int i = decltype(i_c)::value;
            constexpr u64 mk = window_mask(i >> 2, i & 3);
            bias[i >> 2][i & 3] = __builtin_amdgcn_inverse_ballot_w64(mk) ? 0.f : NEG;
          });
          asm("" : "+v"(bias[0])); asm("" : "+v"(bias[1]));
          const unsigned hso = kvo + KVP_HALO + h * 2048, hsi = hso - (g > 0 ? KVP_G : 0u);
          ilv8(2);
          mm6(2);
          pkkv(3);
          ilv8(2);
          OPAQUE_V(kf[4]); OPAQUE_V(vf[4]);
          if (wm) {
            st16_sc1(rs, l16, hso, nosent(kf[4]));
            st16_sc1(rs, l16, hso + 1024, nosent(vf[4]));
          } else {
            lds_st<u32x4>(pv_, kf[4]);
            lds_st<u32x4>(pv_ + 1024, vf[4]);
          }
          SCHED;
          mm6(1);
          pkq(3); pkkv(2);
          ilv8(3);
          WAITL0;
          TRQ(17); BAR;
          SCHED;
          mm6(0);
          pkq(2); pkkv(1);
          ilv8(3);
          s0[3] = MFMA(as_frag(kf[3]), as_frag(qf[3]), bias[0]); s1[3] = MFMA(as_frag(kf[4]), as_frag(qf[3]), bias[1]);
          s0[2] = MFMA(as_frag(kf[2]), as_frag(qf[2]), bias[0]); s1[2] = MFMA(as_frag(kf[3]), as_frag(qf[2]), bias[1]);
          pkq(1); pkkv(0);
#pragma unroll
          for (int i = 0; i < 4; ++i) { SGB(0x008, 1); SGB(0x002, 5); }
          SCHED;
          const bool halo = wm == 0 && g > 0;
          const u32x4 hk = ld16_sc1(rs, l16, hsi), hv = ld16_sc1(rs, l16, hsi + 1024);
          SCHED;
          s0[1] = MFMA(as_frag(kf[1]), as_frag(qf[1]), bias[0]); s1[1] = MFMA(as_frag(kf[2]), as_frag(qf[1]), bias[1]);
          st1(3); st1(2);
#pragma unroll
          for (int m = 0; m < 4; ++m) rq[m] *= QS;
          SCHED;
          st1(1);
          pkq(0);
          psel0 = __builtin_amdgcn_inverse_ballot_w64(0xaaaaaaaaaaaaaaaaull) ? 0x01000c0cu : 0x0c0c0100u;
          psel1 = __builtin_amdgcn_inverse_ballot_w64(0xaaaaaaaaaaaaaaaaull) ? 0x03020c0cu : 0x0c0c0302u;
          { float d = mx[1]; red4<true>(mx[3], mx[2], mx[1], d, [](float v) { return v; }); }
          s1[0] = MFMA(as_frag(kf[1]), as_frag(qf[0]), bias[1]);
          SCHED;
          st2(3); st2(2); st2(1);
          SCHED;
          st3(3); st3(2); st3(1);
          SCHED;
          st5(3); st6(3); st5(2); st6(2); st5(1); st6(1);
          SCHED;
          TRR(60);
          TRC(18);
          u32x4 pk = lds_ld<u32x4>(pv_), pv = lds_ld<u32x4>(pv_ + 1024);
          st7(3); st7(2); st7(1);
          SCHED;
#pragma unroll
          for (int m = 3; m > 0; --m) st16_sc1(rs, l16, otile + m * 2048, pf[m]);
          if (!wm && g == 0) bias[0] = f32x4{NEG, NEG, NEG, NEG};
          SCHED;
          TRV(55, 0); TRC(21);
          if (halo) {
            pk = hk; pv = hv;
            for (unsigned spins = 0; !dead && __any(umax4v(umax4v(0u, pk), pv) == SENT); ++spins) {
              if (spins > SPIN_CAP) { dead = true; break; }
              CLOBBER;
              TRV(55, spins + 1);
              pk = ld16_sc1(rs, l16, hsi); pv = ld16_sc1(rs, l16, hsi + 1024);
              OPAQUE_V(pk); OPAQUE_V(pv);
            }
          }
          vf[0] = pv;
          s0[0] = MFMA(as_frag(pk), as_frag(qf[0]), bias[0]);
          st1(0);
          mx[0] = red_max4(mx[0]);
          st2(0); st3(0); st5(0); st6(0); st7(0);
          st16_sc1(rs, l16, otile, pf[0]);
        } else {
          LOCALS;
          const int fr = lane & 15, fq = lane >> 4;
          f32x4 rs4[4], r[4][2];
          const bf16x8 ones = {0x3f80, 0x3f80, 0x3f80, 0x3f80, 0x3f80, 0x3f80, 0x3f80, 0x3f80};
          auto proj = [&](int m, int n, const bf16x8& w0, const bf16x8& w1) {
            const u32x4 f = pack(a[m][n], a[m][n + 1]);
            r[m][0] = MFMA(w0, as_frag(f), zero); r[m][1] = MFMA(w1, as_frag(f), zero);
          };
          mm6(3, 0, 2); mm6(2, 0, 2); mm6(1, 0, 2); mm6(0, 0, 2);
          unsigned wa = lds0 + L_WPHI + h * 2048 + (fq * 4 + (fr >> 2)) * 64 + (lane & 3) * 8, kw = lds0 + L_PREV + hw * 2048 + l16;
          asm("" : "+v"(wa)); asm("" : "+v"(kw));
#pragma unroll
          for (int m = 0; m < 4; ++m) rq[m] = lds_ld<float>(pr1 + m * 64);
#pragma unroll
          for (int m = 0; m < 4; ++m) rs4[m] = lds_ld<f32x4>(pr4 + 64 + m * 64);
          const bf16x8 w0 = tr2(wa, wa + 1024);
          const bf16x8 w1 = tr2(wa + 32, wa + 32 + 1024);
#pragma unroll
          for (int i = 0; i < 8; ++i) { SGB(0x008, 1); SGB(0x100, 2); SGB(0x002, 2); }
          SCHED;
          mm6(3, 2, 4); mm6(2, 2, 4);
          lds_st<u32x4>(kw + 1024, pack(a[3][2], a[3][3]));
          lds_st<u32x4>(kw, pack(a[2][2], a[2][3]));
#pragma unroll
          for (int i = 0; i < 2; ++i) { SGB(0x008, 1); SGB(0x002, 2); SGB(0x008, 1); SGB(0x002, 2); SGB(0x200, 1); }
          SCHED;
          mm6(1, 2, 4); mm6(0, 2, 4);
          SCHED;
          WAITL0;
          TRQ(17); BAR;
          SCHED;
          proj(0, 2, w0, w1); proj(1, 2, w0, w1);
          unsigned pw = lds0 + SC_PHIK + (h * 128 + wm * 64 + fr) * 160 + fq * 8, vw = lds0 + SC_VF + h * 8192 + wm * 4096 + l16;
          asm("" : "+v"(pw)); asm("" : "+v"(vw));
#pragma unroll
          for (int m = 0; m < 4; ++m) rq[m] *= L2E;
#pragma unroll
          for (int i = 0; i < 4; ++i)
            lds_st<u32x4>(vw + i * 1024, pack(a[i & 2][4 + (i & 1)] * rs4[i & 2], a[(i & 2) + 1][4 + (i & 1)] * rs4[(i & 2) + 1]));
          SCHED;
          phik2(r, rq, pw);
          KV_ADDR;
          WAITL0;
          TRR(60);
          TRC(19); BAR;
          kvpart(va, pa, pb, kvo, lane, wave);
          SCHED;
#pragma unroll
          for (int m = 0; m < 4; ++m) mm6(m, 4, 8);
          SCHED;
          CLOBBER;
          TRC(20); BAR;
          u32x4 qp[4], qn[4];
          float sqp[4], sqn[4];
#pragma unroll
          for (int m = 0; m < 4; ++m) proj(m, 0, w0, w1);
          SCHED;
          float ep[4][8], en[4][8];
          hedgehog<4>(r, rq, ep, en);
          const unsigned zo = (lane >> 3) * KVP_G + (lane & 7) * 16, zs = kvj + KVP_Z + h * 128;
          u32x4 zv[2] = {ld16_sc1(rs, zo, zs), ld16_sc1(rs, zo, zs + 8 * KVP_G)};
          SCHED;
#pragma unroll
          for (int m = 0; m < 4; ++m) {
            qp[m] = pack8(ep[m]); qn[m] = pack8(en[m]);
            sqp[m] = MFMA(ones, as_frag(qp[m]), zero)[0];
            sqn[m] = MFMA(ones, as_frag(qn[m]), zero)[0];
          }
          SCHED;
#pragma unroll
          for (int m = 0; m < 4; ++m) { sqp[m] = __builtin_amdgcn_rcpf(sqp[m]); sqn[m] = __builtin_amdgcn_rcpf(sqn[m]); }
#pragma unroll
          for (int m = 0; m < 4; ++m) { OPAQUE_V(qp[m]); OPAQUE_V(qn[m]); OPAQUE_V(sqp[m]); OPAQUE_V(sqn[m]); }
          unsigned ko = (lane >> 5) * KVP_G + (lane & 31) * 16, ks = kvj + (unsigned)((j ^ RX) - j) * KVP_J + KVP_SUM + h * (8 * KVP_G);
          bf16x8 sel = rowsum_sel(lane);
          OPAQUE_V(ko); OPAQUE_S(ks); OPAQUE_V(sel);
          TRC(21); TRV(50, 0);
          for (unsigned spins = 0; !dead && __any(umax4v(umax4v(0u, zv[0]), zv[1]) == SENT); ++spins) {
            if (spins > SPIN_CAP) { dead = true; break; }
            CLOBBER;
            TRV(50, spins + 1);
            zv[0] = ld16_sc1(rs, zo, zs); zv[1] = ld16_sc1(rs, zo, zs + 8 * KVP_G);
          }
          TRC(22);
          {
            const f32x4 z = rowsum_fin(MFMA(sel, as_frag(zv[1]), MFMA(sel, as_frag(zv[0]), zero)));
            if ((lane & 40) == 0) lds_st<u32x2>(lds0 + L_ZF + hw * 128 + fr * 16 + fq * 8, u32x2{pk2(z[0], z[1]), pk2(z[2], z[3])});
            WAITL0;
            const bf16x8 zf0 = lds_ld<bf16x8>(lds0 + L_ZF + hw * 128 + fq * 16), zf1 = lds_ld<bf16x8>(lds0 + L_ZF + hw * 128 + 64 + fq * 16);
            float dp[4], dn[4];
#pragma unroll
            for (int m = 0; m < 4; ++m) { dp[m] = MFMA(zf0, as_frag(qp[m]), zero)[0]; dn[m] = MFMA(zf1, as_frag(qn[m]), zero)[0]; }
            SCHED;
#pragma unroll
            for (int m = 0; m < 4; ++m) {
              const float inv = __builtin_amdgcn_rcpf(__builtin_fmaf(dp[m], sqp[m], __builtin_fmaf(dn[m], sqn[m], 1e-6f)));
              sqp[m] *= inv; sqn[m] *= inv;
              OPAQUE_V(sqp[m]); OPAQUE_V(sqn[m]);
            }
          }
          TRV(51, 0);
          TRC(43); BAR;
          __builtin_amdgcn_s_sleep(H2D);
          u32x4 kv[4];
#pragma unroll
          for (int i = 0; i < 4; ++i) kv[i] = ld16_sc1(rs, ko, ks + i * (2 * KVP_G));
          SCHED;
#pragma unroll
          for (int m = 0; m < 4; ++m) { qp[m] = pack8(ep[m], sqp[m]); qn[m] = pack8(en[m], sqn[m]); }
#pragma unroll
          for (int m = 0; m < 4; ++m) { OPAQUE_V(qp[m]); OPAQUE_V(qn[m]); }
          SCHED;
          for (unsigned spins = 0; !dead && __any(max(umax4v(umax4v(0u, kv[0]), kv[1]), umax4v(umax4v(0u, kv[2]), kv[3])) == SENT); ++spins) {
            if (spins > SPIN_CAP) { dead = true; break; }
            CLOBBER;
            TRV(51, spins + 1);
#pragma unroll
            for (int i = 0; i < 4; ++i) kv[i] = ld16_sc1(rs, ko, ks + i * (2 * KVP_G));
          }
          TRC(23);
          f32x4 o[4][2];
#pragma unroll
          for (int m = 0; m < 4; ++m)
#pragma unroll
            for (int i = 0; i < 2; ++i) o[m][i] = MFMA(as_frag(kv[2 * i + 1]), as_frag(qn[m]), MFMA(as_frag(kv[2 * i]), as_frag(qp[m]), zero));
#pragma unroll
          for (int m = 0; m < 4; ++m) st16_sc1(rs, l16, otile + m * 2048, nosent(pack(o[m][0], o[m][1])));
        }
      });
#pragma unroll
      for (int i = 0; i < 8; ++i) asm volatile("" : "=v"(acc[i >> 2][i & 3]));
    } else {
      __builtin_amdgcn_s_setprio(3);
      if (l0) {
        WAITV(3);
        WAITL0;
        TRC(1); BAR;
      }
      LOCALS;
      u32x4 xr[2][2];
      const unsigned lin = lane * 16, vlin = lin + hw * 1024;
      const int jr = j & ~3;
      u32x4 sent4 = {SENT, SENT, SENT, SENT};
      OPAQUE_V(sent4);
      const unsigned cb0 = ob_off(g, j, l), cb2 = xb_off(g, l) + j * TS;
      const unsigned cb1 = WS_KVP + (unsigned)j * KVP_J + (unsigned)g * KVP_G + (unsigned)(l >> 1) * KVP_L;
      unsigned ra0;
      int tr = 0;
      auto ldres = [&](int t) {
        if (tr == t) {
#pragma unroll
          for (int i = 0; i < 4; ++i) xr[i >> 1][i & 1] = lds_ld<u32x4>((ra0 ^ ((i & 1) * 64)) + (t % 3) * XS + ((i >> 1) ^ 1) * 2048);
        }
      };
      auto aslot = [&](int t) { return lds0 + L_R2 + (t % 3) * XS + hw * 1024; };
      auto wslot = [&](int t) { return lds0 + L_R1 + (t & 3) * 24576 + hw * 1024; };
      const bool l0f = l == 0;
      const rsrc_t rA = mk_rsrc(pA, zA);
      auto kseq = [&](int t) { return (kb + t - (t <= dj ? 1 : 0)) & 15; };
      auto offA = [&](int t) { return bA + (unsigned)kseq(t) * kA; };
      auto offW = [&](int t) { return bW + (unsigned)kseq(t) * kW; };
      auto ldA = [&](int t) { dma4<4096>(rA, vA, offA(t), pcA, aslot(t)); };
      auto ldWh = [&](int t, unsigned o, int hf) { dma3<8192>(mk_rsrc(pW, zW), vW, o + (hf ? hW : 0u), pcW, wslot(t) + hf * 4096); };
      const bool slowj = l0f && SLOWC(0, j);
      auto packedW = [&]() {
        pW = p.ws; zW = WS_SPAN; bW = WS_WP + (unsigned)j * (16 * 24576); kW = 24576; pcW = 8192; hW = 4096; vW = vlin;
      };
      auto ldW = [&](int t) { const unsigned o = offW(t); ldWh(t, o, 0); ldWh(t, o, 1); };
      constexpr auto spread = [](int n) { unsigned m = 0; for (int t = 0; t < 26; ++t) if ((t + 1) * n / 13 > t * n / 13) m |= 1u << t; return m; };
      constexpr unsigned sp4 = spread(4), sp8 = spread(8), sp12 = spread(12);
      const unsigned smask = (isb ? (l == 3 ? sp4 : sp8) : sp12) >> ((((g + j * 5) & 15) * 13) >> 4);
      const unsigned cbm = isb ? cb2 : cb1;
      auto clean = [&](int t) __attribute__((always_inline)) {
        if (t < 13 && ((smask >> t) & 1)) {
          const unsigned cc = __builtin_popcount(smask & ((1u << t) - 1));
          const unsigned cb = cb0 + (cc >= 4 ? cbm - cb0 : 0u) + (cc >= 8 ? cb2 - cbm : 0u), pc = (cc + g + j) & 3;
          st16_sc1(rs, (lane >> 4) * 4096 + (lane & 15) * 16, cb + hw * 1024 + pc * 256, sent4);
        }
      };
      unsigned flo;
      unsigned ready = ~0u;
      u32x4 cv[4];
#pragma unroll
      for (int ii = 0; ii < 4; ++ii) asm volatile("" : "=v"(cv[ii]));
      auto chkrd = [&](int t) {
#pragma unroll
        for (int ii = 0; ii < 4; ++ii) cv[ii] = lds_ld<u32x4>(aslot(t) + lin + ii * 4096);
      };
      auto chkbad = [&]() { return __any(max(umax4v(umax4v(0u, cv[0]), cv[1]), umax4v(umax4v(0u, cv[2]), cv[3])) == SENT) && !l0f; };
      auto refetch = [&](int t) __attribute__((always_inline)) {
        for (unsigned spins = 0; !dead; ++spins) {
          if (spins > SPIN_CAP) { dead = true; break; }
          WAITV(0);
          TRV(t == 1 ? 56 : 57, spins + 1);
          ldA(t);
          ldA(t + 1);
          WAITV(0);
          chkrd(t);
          if (!chkbad()) break;
          __builtin_amdgcn_s_sleep(1);
        }
      };
      f32x4 jc[4];
#pragma unroll
      for (int i = 0; i < 4; ++i) asm volatile("" : "=v"(jc[i]));
      auto csld = [&]() {
#pragma unroll
        for (int i = 0; i < 4; ++i)
          ld16_asm(jc[i], rs, (lane >> 4) * 768 + hw * 192 + ((lane & 15) < 12 ? (lane & 15) : 0) * 16, WS_CS + (unsigned)((l * 16 + j) * 16 + i * 4) * 768);
      };
      auto csred = [&]() {
#pragma unroll
        for (int i = 0; i < 4; ++i) OPAQUE_V(jc[i]);
        const f32x4 s = (jc[0] + jc[1]) + (jc[2] + jc[3]);
        auto r = SWAP16(s[0], s[1]), q = SWAP16(s[2], s[3]);
        r = SWAP32(__uint_as_float(r[0]) + __uint_as_float(r[1]), __uint_as_float(q[0]) + __uint_as_float(q[1]));
        if ((lane & 15) < 12) lds_st<float>(lds0 + L_CS + l * 768 + hw * 192 + (lane & 15) * 16 + (lane >> 4) * 4, __uint_as_float(r[0]) + __uint_as_float(r[1]));
      };
      if (l0f) {
        u32x2 gw2;
        ld8_asm(gw2, rs, WS_EPOCH + 0u * lin);
        TRC(44); BAR;
        ldW(0); ldA(0);
        dma1(mk_rsrc(p.wphi, 32 * 2048), vlin, j * 4096, lds0 + L_WPHI + hw * 1024);
        ldW(1);
        WAITV(17);
        OPAQUE_V(gw2);
        const u64 gword = ((u64)(unsigned)UNI(gw2[1]) << 32) | (unsigned)UNI(gw2[0]);
        u64 z = gword + 0x9e3779b97f4a7c15ull;
        z = (z ^ (z >> 30)) * 0xbf58476d1ce4e5b9ull;
        z = (z ^ (z >> 27)) * 0x94d049bb133111ebull;
        z = z ^ (z >> 31);
        z = (z == gword) ? ~z : z;
        epoch = u32x2{(unsigned)z, (unsigned)(z >> 32)};
        tr = (j - kb) & 15;
        WAITV(7);
        TRC(1); BAR;
        ldA(1);
      } else {
        ldW(1);
        if (l == 1) csld();
        else __builtin_amdgcn_s_sleep(XD);
        ldA(1);
      }
      ldA(2);
      TRC(40);
      ldW(2);
#pragma unroll
      for (int i = 0; i < 4; ++i) asm volatile("" : "=v"(xr[i >> 1][i & 1]));
      unsigned oA = offA(3), oW = 0;
      OPAQUE_S(oA);
      TRV(56, 0); TRV(57, 0);
      ra0 = lds0 + L_R2 + (hw * 32 + (lane & 15)) * 128 + (((((lane >> 4) & 1) * 2 + (lane >> 5)) ^ ((lane >> 1) & 7)) * 16);
      OPAQUE_V(ra0);
      ldres(0);
      clean(0);
      if (l == 1) { WAITV(14); csred(); }
      WAITV(10);
      chkrd(1);
      if (__builtin_expect(chkbad(), 0)) refetch(1);
      const int fr = lane & 15, fq = lane >> 4;
      u32x2 fl;
      f32x4 js[4];
      float rb[4];
      const bool slowo = SLOWC(2 + (l & 1), j);
      const char* pO;
      unsigned zO, bO, pcO, vO, clo;
      u32x4 wo2[2];
      asm volatile("" : "=v"(wo2[0])); asm volatile("" : "=v"(wo2[1]));
      const unsigned fwo = myrep + fl_line(FL_WO + (isb ? 1 : 0), jr + (lane >> 4)) + (lane & 15) * 8;
      static_for<1, 14>([&](auto t_c) __attribute__((always_inline)) {
        constexpr int t = decltype(t_c)::value;
        constexpr auto rest = [](int s) { return 6 + (s == NVAL - 2 || s == 5 || s == 11) + (s == 8 ? 4 : 0); };
        constexpr int NW = t == 1 ? 63 : 4 + rest(t - 1);
        TRC(1 + t);
        if (t >= NVAL && t <= 6 && __builtin_expect((ready & (1u << kseq(t + 2))) == 0, 0)) {
          WAITV(NW); WAITL0; BAR;
          ready = poll_need(rs, flo, epoch, ready, 1u << kseq(t + 2), dead);
          dma4<4096>(rA, vA, oA, pcA, aslot(t + 2));
        } else if (t == 7 && __builtin_expect(ready != ~0u, 0)) {
          WAITV(NW); WAITL0; BAR;
          poll_all(rs, flo, epoch, dead);
          dma4<4096>(rA, vA, oA, pcA, aslot(t + 2));
        } else {
          bar_dma4<4096, NW>(rA, vA, oA, pcA, aslot(t + 2));
        }
        if (t == 1 && lead) set_flags(rs, fl_line(FL_X + l, g), j, l0f ? fl_line(FL_CS, j) : fl_line(FL_X + l, g), l0f ? g : j, epoch, lane);
        if (t == 7 && slowj) packedW();
        oW = offW(t + 2);
        if (t < 13) oA = offA(t + 3);
        OPAQUE_S(oA); OPAQUE_S(oW);
        clean(t);
        if (t <= NVAL && !l0f) { WAITV(rest(t - 1) + 4); chkrd(t + 1); }
        if (t == WT + 3 && l0f && SLOWC(2, jr + hw)) {
          OPAQUE_V(wo2[0]); OPAQUE_V(wo2[1]);
#pragma unroll
          for (int i = 0; i < 2; ++i) st16_sc1(rs, lin, WS_WO + (unsigned)((jr + hw) * 131072 + g * 8192 + ((j & 3) * 2 + i) * 1024), wo2[i]);
        }
        if (t == WT + 6 && l0f && lead) set_flags(rs, fl_line(FL_WO, j), g, fl_line(FL_WO, j), g, epoch, lane);
        ldres(t);
        if (t == 8) {
#pragma unroll
          for (int i = 0; i < 4; ++i) ld16_asm(js[i], rs, fq * 1024 + hw * 256 + fr * 16, st_off(g, l) + i * 4096);
        }
        SCHED;
        ldWh(t + 2, oW, 0);
        SCHED;
        bool bad = false;
        if (t <= NVAL) bad = chkbad();
        if (t == 1) {
          const unsigned fx = fl_line(FL_X + l, g), fc = fl_line(FL_CS, jr) + ((j + fq) & 3) * 128;
          flo = myrep + ((l0f && lane >= 16) ? fc : fx) + fr * 8;
          OPAQUE_V(flo);
        }
        if (t == 9) {
          pO = slowo ? p.ws : (const char*)(isb ? p.woB : p.woA);
          zO = slowo ? WS_SPAN : 1024 * 2048; bO = slowo ? WS_WO + (unsigned)((isb ? 16 : 0) + j) * 131072 : j * 128; pcO = slowo ? 4096 : 32 * 2048;
          vO = slowo ? vlin : (hw * 8 + (lane >> 3)) * 2048 + wo_swz(lane);
          clo = myrep + ((lane & 16) ? fl_line(FL_CLC + l, (lane & 32) ? j ^ RX : j) : fl_line(FL_CLR + l, g)) + fr * 8;
          OPAQUE_S(pO); OPAQUE_S(zO); OPAQUE_S(bO); OPAQUE_S(pcO); OPAQUE_V(vO); OPAQUE_V(clo);
        }
        if (t == 10 && l0f) csred();
        if (t == 11) {
#pragma unroll
          for (int i = 0; i < 4; ++i) OPAQUE_V(js[i]);
          const f32x4 s = (js[0] + js[1]) + (js[2] + js[3]);
          const auto r = SWAP16(s[0], s[2]), q = SWAP16(s[1], s[3]);
          rb[0] = __uint_as_float(r[0]) + __uint_as_float(r[1]); rb[1] = __uint_as_float(q[0]) + __uint_as_float(q[1]);
          OPAQUE_V(rb[0]); OPAQUE_V(rb[1]);
        }
        if (t == 12) {
          const auto r = SWAP32(rb[0], rb[0]), q = SWAP32(rb[1], rb[1]);
          const float m = (__uint_as_float(r[0]) + __uint_as_float(r[1])) * (-1.0f / 1024), q2 = __uint_as_float(q[0]) + __uint_as_float(q[1]);
          const float rstd = __builtin_amdgcn_rsqf(fmaxf(__builtin_fmaf(q2, 1.0f / 1024, -m * m), 0.f) + 1e-5f);
          lds_st<float>(lds0 + L_MEAN + 64 + hw * 128 + fr * 8 + (fq & 1) * 4 + (fq >> 1) * (L_RSTD - L_MEAN), (lane & 32) ? rstd : m);
        }
        SCHED;
        ldWh(t + 2, oW, 1);
        if (t == NVAL - 1 || t == 6) {
          WAITV(10);
          OPAQUE_V(fl);
          if (t == 6) ready = __all(is_set(fl, epoch)) ? ~0u : 0u;
          else {
            ready = l0f ? ~0u : (unsigned)__ballot(is_set(fl, epoch));
            if (slowj && __all(is_set(fl, epoch))) packedW();
          }
        }
        if (t == 12) { WAITV(10); OPAQUE_V(fl); }
        if (t == 13 && slowo && __builtin_expect(!__all(is_set(fl, epoch)), 0)) poll_all(rs, fwo, epoch, dead);
        if (t == NVAL - 2 || t == 5) ld8_asm(fl, rs, flo);
        if (t == 11) ld8_asm(fl, rs, fwo);
        if (t == 7 && l0f) csld();
        if (t == WT && l0f) {
          if (hw == 0) touch(mk_rsrc(p.ws, NREP * REP_STRIDE), min((g * 16 + j) * 9 + (lane < 9 ? lane : 0), (int)(NREP * REP_STRIDE / 128 - 1)) * 128, lds0 + L_SINK);
#pragma unroll
          for (int i = 0; i < 2; ++i) ld16in_asm(wo2[i], mk_rsrc(p.woA, 1024 * 2048), (lane >> 3) * 2048 + wo_swz(lane), (g * 64 + (j & 3) * 16 + i * 8) * 2048 + (jr + hw) * 128);
        }
        if constexpr (t <= NVAL) { if (__builtin_expect(bad, 0)) refetch(t + 1); }
      });
      auto stO = [&](int i) { dma4<4096>(mk_rsrc(pO, zO), vO, bO + wo_src(i, j) * (4 * pcO), pcO, lds0 + wo_slot(i) + hw * 1024); };
      auto clcheck = [&]() {
        OPAQUE_V(fl);
        TRV(58, 0);
        if (__builtin_expect(!__all(is_set(fl, epoch)), 0)) { TRV(58, 1); poll_all(rs, clo, epoch, dead); }
      };
      auto accinit = [&]() {
#pragma unroll
        for (int m = 0; m < 2; ++m)
#pragma unroll
          for (int pp = 0; pp < 2; ++pp) {
            auto r0_ = __builtin_amdgcn_permlane16_swap(xr[m][pp][0], xr[m][pp][2], false, false);
            auto r1_ = __builtin_amdgcn_permlane16_swap(xr[m][pp][1], xr[m][pp][3], false, false);
            acc[m][2 * pp] = f32x4{lo16(r0_[0]), hi16(r0_[0]), lo16(r1_[0]), hi16(r1_[0])};
            acc[m][2 * pp + 1] = f32x4{lo16(r0_[1]), hi16(r0_[1]), lo16(r1_[1]), hi16(r1_[1])};
          }
#pragma unroll
        for (int i = 0; i < 8; ++i) OPAQUE_V(acc[i >> 2][i & 3]);
      };
      WAITV(10);
      WAITL0;
      TRC(15); BAR;
      stO(0);
      if (lead) set_flags(rs, fl_line(FL_CLR + l, g), j, fl_line(FL_CLC + l, j), g, epoch, lane);
      if (isb) {
        stO(2); stO(3);
        WAITV(12);
        TRC(16); BAR;
        stO(1); stO(4); stO(5);
        __builtin_amdgcn_s_setprio(0);
        TRC(17); BAR;
        __builtin_amdgcn_s_sleep(BD);
        stO(7); stO(6);
        ld8_asm(fl, rs, clo);
        accinit();
        WAITV(0);
        clcheck();
      } else {
        u32x4 vb[8];
#pragma unroll
        for (int i = 0; i < 8; ++i) asm volatile("" : "=v"(vb[i]));
        stO(2); stO(3);
        ldres(14);
        WAITV(12);
        WAITL0;
        TRC(16); BAR;
        stO(1);
        ldres(15);
        if (l0f) {
          const bool wj = hw != 0;
          const char* bp = (const char*)(wj ? p.wqkvB : p.woB);
          unsigned rs_ = wj ? 6144 : 2048, sz = rs_ * 1024, ss = rs_ * 8, s2 = wj ? 16 * 6144 : 128;
          unsigned sb = g * 64 * rs_ + (wj ? (hw - 1) * 2048 + JOBC(j, hw - 1) * 128 : (j & 3) * (16 * 2048) + jr * 128);
          OPAQUE_S(bp); OPAQUE_S(rs_); OPAQUE_S(sz); OPAQUE_S(sb); OPAQUE_S(ss); OPAQUE_S(s2);
          const rsrc_t rb = mk_rsrc(bp, sz);
#pragma unroll
          for (int i = 0; i < 8; ++i) ld16in_asm(vb[i], rb, (lane >> 3) * rs_ + (lane & 7) * 16, sb + (i & 1) * ss + (i >> 1) * s2);
        }
        __builtin_amdgcn_s_setprio(0);
        KV_ADDR;
        bf16x8 sel = rowsum_sel(lane);
        unsigned h1 = (lane >> 3) * KVP_G + (lane & 7) * 16;
        unsigned pw = lds0 + SC_PHIK + (h * 128 + wm * 64 + 32 + fr) * 160 + fq * 8, kw = lds0 + L_PREV + hw * 2048 + lin;
        const unsigned wa = lds0 + L_WPHI + h * 2048 + (fq * 4 + (fr >> 2)) * 64 + (lane & 3) * 8;
        bf16x8 w0 = tr2(wa, wa + 1024), w1 = tr2(wa + 32, wa + 32 + 1024);
        float cq[4];
#pragma unroll
        for (int m = 0; m < 2; ++m) { cq[m] = lds_ld<float>(lds0 + L_RSTD + (16 + wm * 64 + 32 + m * 16 + fr) * 4) * 1.4426950408889634f; OPAQUE_V(cq[m]); }
        OPAQUE_V(sel); OPAQUE_V(h1); OPAQUE_V(pw); OPAQUE_V(kw); OPAQUE_V(w0); OPAQUE_V(w1);
        TRC(17); BAR;
        CLOBBER;
        {
          const f32x4 zero = {0.f, 0.f, 0.f, 0.f};
          f32x4 r[4][2];
#pragma unroll
          for (int m = 0; m < 2; ++m) {
            const bf16x8 f = lds_ld<bf16x8>(kw + m * 1024);
            r[m][0] = MFMA(w0, f, zero); r[m][1] = MFMA(w1, f, zero);
          }
          ld8_asm(fl, rs, clo);
          phik2(r, cq, pw);
        }
        WAITL0;
        TRC(19); BAR;
        kvpart(va, pa, pb, kvj + (unsigned)g * KVP_G, lane, wave);
        WAITV(2);
#pragma unroll
        for (int i = 0; i < 8; ++i) OPAQUE_V(vb[i]);
        clcheck();
        TRC(20); BAR;
        __builtin_amdgcn_s_setprio(3);
        stO(4); stO(5);
        const f32x4 zero = {0.f, 0.f, 0.f, 0.f};
        const unsigned s1 = kvj + (unsigned)((j ^ RX) - j) * KVP_J + g * 512 + hw * 128;
        __builtin_amdgcn_s_sleep(H1S);
        u32x4 v[2] = {ld16_sc1(rs, h1, s1), ld16_sc1(rs, h1, s1 + 8 * KVP_G)};
        TRC(21); TRV(50, 0);
        for (unsigned spins = 0; !dead && __any(umax4v(umax4v(0u, v[0]), v[1]) == SENT); ++spins) {
          if (spins > SPIN_CAP) { dead = true; break; }
          CLOBBER;
          TRV(50, spins + 1);
          v[0] = ld16_sc1(rs, h1, s1); v[1] = ld16_sc1(rs, h1, s1 + 8 * KVP_G);
        }
        TRC(22);
        const f32x4 c = rowsum_fin(MFMA(sel, as_frag(v[1]), MFMA(sel, as_frag(v[0]), zero)));
        if ((lane & 40) == 0)
          st8_sc1(rs, fr * 16 + fq * 8, kvj + (unsigned)g * KVP_G + KVP_SUM + hw * 128, nosent2(c));
        CLOBBER;
        __builtin_amdgcn_s_setprio(0);
        TRC(43); BAR;
        SCHED;
        accinit();
        stO(6); stO(7);
        if (l0f) {
          if (hw) {
            job_colsum(rs, vb, 1, g, JOBC(j, hw - 1), hw - 1, lane);
            if (SLOWC(1, JOBC(j, hw - 1))) {
#pragma unroll
              for (int i = 0; i < 8; ++i) job_pack(rs, vb[i], i, 1, g, JOBC(j, hw - 1), hw - 1, lane);
            }
          } else {
#pragma unroll
            for (int i = 0; i < 8; ++i)
              if (SLOWC(3, jr + (i >> 1))) st16_sc1(rs, (lane >> 3) * 128 + wo_swz(lane), WS_WO + (unsigned)((16 + jr + (i >> 1)) * 131072 + g * 8192 + ((j & 3) * 2 + (i & 1)) * 1024), vb[i]);
          }
        }
        WAITV(0);
      }
    }
    {
      LOCALS;
      const int fr = lane & 15, fq = lane >> 4;
      const int kh = half;
      unsigned v0 = lane * 16 + hw * 4096 + kh * 2048, v1 = lane * 16 + hw * 4096 + (kh ^ 1) * 2048;
      const int r0 = (JROT(j) >> 1) + NJ / 2;
      const unsigned ob0 = ob_off(g, kh, l);
      const unsigned pko = myrep + fl_line(FL_PK, JOBC(j, fq - 1)) + fr * 8;
      u32x4 R[8][4];
      u32x2 pkf;
      auto issue_ = [&](int i) {
        const unsigned so = ob0 + (unsigned)((r0 + i) & 7) * (2 * TS);
        R[i][0] = ld16_sc1(rs, v0, so); R[i][1] = ld16_sc1(rs, v1, so);
        R[i][2] = ld16_sc1(rs, v0 + 1024, so); R[i][3] = ld16_sc1(rs, v1 + 1024, so);
      };
      auto issue = [&](int i) { issue_(i); SCHED; };
      constexpr int LA = LOOKAHEAD;
      TRV(52, 0); TRV(53, 0); TRV(54, 0);
      TRR(61);
      __builtin_amdgcn_s_setprio(3);
      unsigned so0 = ob0 + (unsigned)(r0 & 7) * (2 * TS);
      OPAQUE_V(v0); OPAQUE_V(v1); OPAQUE_S(so0);
      TRC(25); BAR;
      SCHED;
      R[0][0] = ld16_sc1(rs, v0, so0); R[0][1] = ld16_sc1(rs, v1, so0);
      R[0][2] = ld16_sc1(rs, v0 + 1024, so0); R[0][3] = ld16_sc1(rs, v1 + 1024, so0);
      SCHED;
      __builtin_amdgcn_s_setprio(2);
      if (l == 0 && lead) set_flags(rs, fl_line(FL_PK, j), g, fl_line(FL_WO + 1, j), g, epoch, lane);

      auto vmax = [&](int i) { return umax4v(umax4v(umax4v(umax4v(0u, R[i][0]), R[i][1]), R[i][2]), R[i][3]); };
      auto bad = [&](int i) { return __any(umax4v(umax4v(umax4v(umax4v(0u, R[i][0]), R[i][1]), R[i][2]), R[i][3]) == SENT); };
      auto poll = [&](int i, int nfl) {
        for (unsigned spins = 0; !dead; ++spins) {
          if (spins > SPIN_CAP) { dead = true; break; }
          __builtin_amdgcn_s_sleep(2);
          CLOBBER;
          TRV(52 + (i > 2 ? 2 : i), spins + 1);
          issue(i);
#pragma unroll
          for (int k = i + 1; k < i + nfl && k < 8; ++k)
            if (bad(k)) issue(k);
          WAITV(0);
#pragma unroll
          for (int k = i + 1; k < i + nfl && k < 8; ++k)
#pragma unroll
            for (int f = 0; f < 4; ++f) OPAQUE_V(R[k][f]);
          if (!bad(i)) break;
        }
      };
      unsigned bb[4];
      {
        const int kr = fq * 4 + (fr >> 2);
        const int gs = (kr >> 1) & 3;
#pragma unroll
        for (int c = 0; c < 4; ++c) bb[c] = lds0 + kh * 8192 + kr * 128 + ((c ^ gs) * 32) + (lane & 3) * 8;
      }
      bf16x8 bf[2][4];
      auto rdB = [&](int i, int hh) {
        if (hh == 0) {
#pragma unroll
          for (int c = 0; c < 4; ++c) { bb[c] += wo_slot(i) - (i ? wo_slot(i - 1) : 0u); asm("" : "+v"(bb[c])); }
        }
#pragma unroll
        for (int c = 0; c < 4; ++c) bf[hh][c] = tr2(bb[c] + hh * 4096, bb[c] + hh * 4096 + 2048);
      };
      auto mms = [&](int i, int hh) {
#pragma unroll
        for (int n = 0; n < 4; ++n)
#pragma unroll
          for (int m = 0; m < 2; ++m) acc[m][n] = MFMA(bf[hh][n], as_frag(R[i][hh * 2 + m]), acc[m][n]);
      };
      if (!half) {
#pragma unroll
        for (int i = 0; i < 8; ++i) acc[i >> 2][i & 3] = f32x4{0.f, 0.f, 0.f, 0.f};
      }
      const char* obase = (l == 3) ? (const char*)p.out : (const char*)p.ws;
      unsigned osize = (l == 3) ? 2048 * 2048 : WS_SPAN;
      const bool slown = SLOWC((l & 1) ^ 1, j);
      pW = slown ? p.ws : (const char*)(isb ? p.wqkvA : p.wqkvB);
      bW = slown ? WS_WP + (unsigned)((isb ? 0 : 16) + j) * (16 * 24576) : j * 128;
      kW = slown ? 24576 : 64 * 6144; zW = slown ? WS_SPAN : 1024 * 6144; pcW = slown ? 8192 : 2048; hW = slown ? 4096 : 32 * 6144;
      vA = hw * 1024 + lane * 16;
      vW = slown ? vA : (hw * 8 + (lane >> 3)) * 6144 + (((lane & 7) ^ ((((lane >> 4) & 1) | ((hw & 1) << 1)) << 1)) * 16);
      pA = p.ws; zA = WS_SPAN; bA = xb_off(g, l); kA = TS; pcA = 4096; kb = JROT(j) + KX; dj = (j - kb) & 15;
      OPAQUE_S(pW); OPAQUE_S(bW); OPAQUE_S(kW); OPAQUE_S(zW); OPAQUE_S(pcW); OPAQUE_S(hW); OPAQUE_V(vA); OPAQUE_V(vW); OPAQUE_S(bA);
      unsigned sq = bW + j * kW + kh * hW, dst = lds0 + L_R1 + kh * 4096 + hw * 1024;
      OPAQUE_S(obase); OPAQUE_S(osize); OPAQUE_S(sq); OPAQUE_S(dst);
      const rsrc_t ro = mk_rsrc(obase, osize);
      unsigned so[2];
      unsigned xw = lds0 + SC_XCH + wave * 4096 + lane * 16;
      unsigned xa = lds0 + SC_XCH + (wave ^ 4) * 4096 + lane * 16;
      unsigned sb0 = (l == 3) ? g * (128 * 2048) + j * 128 : xb_off(g, l) + j * TS;
      {
        const int row = (hw * 2 + kh) * 16 + fr, ch = (fq & 1) * 2 + (fq >> 1);
        const unsigned sw = (l == 3) ? 0u : (unsigned)((row >> 1) & 7), rb = (unsigned)row << ((l == 3) ? 11 : 7);
#pragma unroll
        for (int pp = 0; pp < 2; ++pp) { so[pp] = rb + (((ch + 4 * pp) ^ sw) * 16); OPAQUE_V(so[pp]); }
        OPAQUE_V(xa); OPAQUE_V(xw); OPAQUE_S(sb0);
      }
      SCHED;
#pragma unroll
      for (int i = 1; i < LA; ++i) issue(i);
      rdB(0, 0);
      unsigned mx = vmax(0);
#pragma unroll
      for (int i = 0; i < 8; ++i) {
        SCHED;
        if (__builtin_expect(__any(mx == SENT), 0)) poll(i, LA);
        if (i == 0) TRC(27); else TRQ(27 + i);
        if (i == W1S - 2) { pkf = ld8_sc1(rs, pko, 0); SCHED; }
        if (i == W1S) {
          OPAQUE_V(pkf);
          if (l == 0 && lead && !__all(is_set(pkf, epoch))) poll_all(rs, pko, epoch, dead);
          TRC(35); BAR;
        }
        if (i == 7 && l < 3) dma3<8192>(mk_rsrc(pW, zW), vW, sq, pcW, dst);
        if (i < 4 && !isb) __builtin_amdgcn_s_sleep(PACE);
        if (i == 2) __builtin_amdgcn_s_setprio(1);
        if (i == 5) __builtin_amdgcn_s_setprio(0);
        SCHED;
        rdB(i, 1);
        if (i == 7) break;
        if (i + LA < 8) issue_(i + LA);
        mms(i, 0);
#pragma unroll
        for (int k = 0; k < 4; ++k) { SGB(0x008, 1); SGB(0x100, 1); SGB(0x020, 1); SGB(0x008, 1); SGB(0x100, 1); }
        SCHED;
        rdB(i + 1, 0); mx = vmax(i + 1);
        mms(i, 1);
#pragma unroll
        for (int k = 0; k < 4; ++k) { SGB(0x008, 1); SGB(0x100, 2); }
#pragma unroll
        for (int k = 4; k < 8; ++k) { SGB(0x008, 1); SGB(0x002, 8 / (8 - 4)); }
      }
#pragma unroll
      for (int n = 0; n < 4; ++n) acc[1][n] = MFMA(bf[0][n], as_frag(R[7][1]), acc[1][n]);
#pragma unroll
      for (int n = 0; n < 4; ++n) acc[0][n] = MFMA(bf[0][n], as_frag(R[7][0]), acc[0][n]);
#pragma unroll
      for (int k = 0; k < 8; ++k) { SGB(0x008, 1); SGB(0x100, 1); }
      SCHED;
#pragma unroll
      for (int n = 0; n < 4; ++n) acc[1][n] = MFMA(bf[1][n], as_frag(R[7][3]), acc[1][n]);
#pragma unroll
      for (int n = 0; n < 4; ++n) acc[0][n] = MFMA(bf[1][n], as_frag(R[7][2]), acc[0][n]);
#pragma unroll
      for (int n = 0; n < 4; ++n) lds_st<f32x4>(xw + n * 1024, acc[1][n]);
      SGB(0x008, 4);
#pragma unroll
      for (int k = 0; k < 4; ++k) { SGB(0x008, 1); SGB(0x200, 1); }
      SCHED;
      TRC(36);
      WAITL0;
      TRC(37); BAR;
      SCHED;
      f32x4 pa[4];
#pragma unroll
      for (int n = 0; n < 4; ++n) pa[n] = lds_ld<f32x4>(xa + n * 1024);
      SCHED;
      u32x4 o[2];
#pragma unroll
      for (int pp = 0; pp < 2; ++pp) {
        const f32x4 a = acc[0][2 * pp] + pa[2 * pp], b = acc[0][2 * pp + 1] + pa[2 * pp + 1];
        auto r0_ = __builtin_amdgcn_permlane16_swap(pk2(a[0], a[1]), pk2(b[0], b[1]), false, false);
        auto r1_ = __builtin_amdgcn_permlane16_swap(pk2(a[2], a[3]), pk2(b[2], b[3]), false, false);
        o[pp] = nosent(u32x4{r0_[0], r1_[0], r0_[1], r1_[1]});
        st16_sc1(ro, so[pp], sb0, o[pp]);
      }
      if (l < 3) {
        lds_st<u32x4>(lds0 + L_R2 + so[0], o[0]);
        lds_st<u32x4>(lds0 + L_R2 + so[1], o[1]);
        const f32x4 zero = {0.f, 0.f, 0.f, 0.f};
        const bf16x8 ones = {0x3f80, 0x3f80, 0x3f80, 0x3f80, 0x3f80, 0x3f80, 0x3f80, 0x3f80};
        const f32x4 a = MFMA(ones, as_frag(o[1]), MFMA(ones, as_frag(o[0]), zero));
        const f32x4 b = MFMA(as_frag(o[1]), as_frag(o[1]), MFMA(as_frag(o[0]), as_frag(o[0]), zero));
        const float q = (fr & 2) ? ((fr & 1) ? b[3] : b[2]) : ((fr & 1) ? b[1] : b[0]);
        if (fq == (fr >> 2)) st8_sc1(rs, ((hw * 2 + kh) * 16 + fr) * 8, st_off(g, l + 1) + j * 1024, u32x2{__float_as_uint(a[0]), __float_as_uint(q)});
      } else if (lead && blockIdx.x == 0 && lane == 0) {
        st8_sc1(rs, WS_EPOCH, 0, epoch);
      }
    }
    TRC(38); TRR(63);
  }
  STAMP_DUMP;
  asm volatile(".Lpf_end:");
}

extern "C" void kernel_launch(void* const* d_in, const int* in_sizes, int n_in, void* d_out, int out_size,
                              void* d_ws, size_t ws_size, hipStream_t stream) {
  Params p;
  p.x = (const u16*)d_in[0];
  p.wqkvA = (const u16*)d_in[1];
  p.wphi = (const u16*)d_in[2];
  p.woA = (const u16*)d_in[3];
  p.wqkvB = (const u16*)d_in[5];
  p.woB = (const u16*)d_in[6];
  p.out = (u16*)d_out;
  p.ws = (char*)d_ws + ((ws_size - WS_SPAN) & ~(size_t)0xffff);
  (void)hipFuncSetAttribute((const void*)mega_kernel, hipFuncAttributeMaxDynamicSharedMemorySize, L_TOTAL);
  mega_kernel<<<256, 512, L_TOTAL, stream>>>(p);
}
```

```cpp
#include <hip/hip_runtime.h>
#include <stdint.h>
#include <type_traits>
constexpr int LOOKAHEAD = 2;
constexpr int NVAL = 4;
constexpr int WT = 1;
constexpr int W1S = 6;
constexpr int NJ = 8;
constexpr int KX = 8;
constexpr int RX = 8;
constexpr unsigned TS = 17664;
#define XD 3
#define H1S 6
#define H2D 5
#define BD 8
#define PACE 4

typedef unsigned short u16;
typedef unsigned long long u64;
typedef __attribute__((ext_vector_type(8))) short bf16x8;
typedef __attribute__((ext_vector_type(4))) short s16x4;
typedef __attribute__((ext_vector_type(4))) float f32x4;
typedef __attribute__((ext_vector_type(2))) float f32x2;
typedef __attribute__((ext_vector_type(4))) unsigned u32x4;
typedef __attribute__((ext_vector_type(2))) unsigned u32x2;
typedef __attribute__((ext_vector_type(2))) __bf16 bf16x2_t;
#define LAS __attribute__((address_space(3)))
typedef LAS s16x4* lds_s16x4_ptr;

constexpr unsigned NREP = 8, REP_STRIDE = 17 * 2048;
enum { FL_X = 0, FL_CS = 4, FL_CLR = 5, FL_CLC = 9, FL_PK = 13, FL_WO = 14 };
__device__ __forceinline__ unsigned fl_line(int set, int a) { return (unsigned)(set * 16 + a) * 128; }
constexpr unsigned WS_EPOCH = 512u << 10;
constexpr unsigned WS_CS = 1u << 20;
constexpr unsigned WS_KVP = 4u << 20;
constexpr unsigned KVP_G = 32768, KVP_J = 16 * KVP_G, KVP_L = 16384, KVP_Z = 8192, KVP_SUM = 8704, KVP_HALO = 12u << 10;
constexpr unsigned SENT = 0xffffffffu;
constexpr unsigned WS_X = 14u << 20, WS_O = 29u << 20, WS_ST = 2u << 20;
__device__ __forceinline__ unsigned xb_off(int g, int l) { return WS_X + (unsigned)(l * 16 + g) * (16 * TS); }
__device__ __forceinline__ unsigned ob_off(int g, int j, int l) { return WS_O + (unsigned)((l * 16 + g) * 16 + j) * TS; }
__device__ __forceinline__ unsigned st_off(int g, int l) { return WS_ST + (unsigned)(l * 16 + g) * (16u << 10); }
constexpr unsigned WS_WP = 48u << 20;
constexpr unsigned WS_WO = 60u << 20;
constexpr unsigned WS_SPAN = 64u << 20;

constexpr unsigned L_R1 = 0;
constexpr unsigned L_R2 = 98304, XS = 16384;
constexpr unsigned L_MISC = 147456;
constexpr unsigned L_MEAN = L_MISC;
constexpr unsigned L_RSTD = L_MISC + 576;
constexpr unsigned L_CS = L_MISC + 1152;
constexpr unsigned L_WPHI = L_MISC + 2688;
constexpr unsigned L_SINK = L_MISC + 6912;
constexpr unsigned L_ZF = L_MISC + 7168;
constexpr unsigned L_PREV = L_MISC + 8192;
constexpr unsigned L_TOTAL = 163840;

#define JROT(j) ((j) & ~(NJ - 1))
constexpr int NX = NJ / 2;
#define KB0(g, j) (JROT(j) + (16 / NX) * ((g) & (NX - 1)))
#define JOBC(j, w) (((j) & ~3) + (((j) + (w) + 1) & 3))

#define SLOWC(m, jc) ((slowm >> ((m) * 8 + ((jc) & 7))) & 1)

struct Params {
  const u16* x;
  const u16* wqkvA;
  const u16* wphi;
  const u16* woA;
  const u16* wqkvB;
  const u16* woB;
  u16* out;
  char* ws;
};

#define SCHED __builtin_amdgcn_sched_barrier(0)
#define BAR __builtin_amdgcn_s_barrier()
#define SGB(m, n) __builtin_amdgcn_sched_group_barrier(m, n, 0)
#define WAITV(n) asm volatile("s_waitcnt vmcnt(%0)" ::"n"(n) : "memory")
#define WAITL0 asm volatile("s_waitcnt lgkmcnt(0)" ::: "memory")

#define TRV(slot, val) do {} while (0)
#define TRQ(slot) do {} while (0)
#define TRC(slot) do {} while (0)
#define TRR(slot) do {} while (0)
#define STAMP_INIT do {} while (0)
#define STAMP_DUMP do {} while (0)

__device__ __forceinline__ unsigned pk2(float a, float b) {
  f32x2 v = {a, b};
  return __builtin_bit_cast(unsigned, __builtin_convertvector(v, bf16x2_t));
}
__device__ __forceinline__ float lo16(unsigned u) { return __uint_as_float(u << 16); }
__device__ __forceinline__ float hi16(unsigned u) { return __uint_as_float(u & 0xffff0000u); }

template <int A, int B, typename F> __device__ __forceinline__ void static_for(F f) {
  if constexpr (A < B) { f(std::integral_constant<int, A>{}); static_for<A + 1, B>(f); }
}
typedef __amdgpu_buffer_rsrc_t rsrc_t;
__device__ __forceinline__ rsrc_t mk_rsrc(const void* p, unsigned bytes) { return __builtin_amdgcn_make_buffer_rsrc((void*)p, 0, bytes, 0x00020000); }
#define UNI(x) __builtin_amdgcn_readfirstlane(x)
#define DMA_HEAD "s_mov_b32 m0, %4\n\ts_mov_b32 %0, %3\n\ts_nop 2\n\tbuffer_load_dwordx4 %1, %2, %0 offen sc1 lds\n\t"
#define DMA_NEXT "s_add_u32 m0, m0, %6\n\ts_add_u32 %0, %0, %5\n\tbuffer_load_dwordx4 %1, %2, %0 offen sc1 lds\n\t"
#define DMA_OPS : "=&s"(t) : "v"(voff), "s"(r), "s"(UNI(soff)), "s"(UNI(lds)), "s"(UNI(sstep)), "i"(LSTEP) : "memory", "scc", "m0"
template <int LSTEP> __device__ __forceinline__ void dma4(rsrc_t r, unsigned voff, unsigned soff, unsigned sstep, unsigned lds) {
  unsigned t;
  asm volatile(DMA_HEAD DMA_NEXT DMA_NEXT DMA_NEXT DMA_OPS);
}
template <int LSTEP> __device__ __forceinline__ void dma3(rsrc_t r, unsigned voff, unsigned soff, unsigned sstep, unsigned lds) {
  unsigned t;
  asm volatile(DMA_HEAD DMA_NEXT DMA_NEXT DMA_OPS);
}
template <int LSTEP, int N> __device__ __forceinline__ void bar_dma4(rsrc_t r, unsigned voff, unsigned soff, unsigned sstep, unsigned lds) {
  unsigned t;
  asm volatile("s_mov_b32 m0, %4\n\ts_mov_b32 %0, %3\n\ts_nop 0\n\ts_waitcnt vmcnt(%7) lgkmcnt(0)\n\ts_barrier\n\t"
               "buffer_load_dwordx4 %1, %2, %0 offen sc1 lds\n\t" DMA_NEXT DMA_NEXT DMA_NEXT
               : "=&s"(t) : "v"(voff), "s"(r), "s"(UNI(soff)), "s"(UNI(lds)), "s"(UNI(sstep)), "i"(LSTEP), "n"(N) : "memory", "scc", "m0");
}
__device__ __forceinline__ void dma1(rsrc_t r, unsigned voff, unsigned soff, unsigned lds) {
  asm volatile("s_mov_b32 m0, %3\n\ts_nop 3\n\tbuffer_load_dwordx4 %0, %1, %2 offen sc1 lds" ::"v"(voff), "s"(r), "s"(UNI(soff)), "s"(UNI(lds)) : "memory", "m0");
}
__device__ __forceinline__ void touch(rsrc_t r, unsigned voff, unsigned lds) {
  asm volatile("s_mov_b32 m0, %2\n\ts_nop 3\n\tbuffer_load_dword %0, %1, 0 offen lds" ::"v"(voff), "s"(r), "s"(UNI(lds)) : "memory", "m0");
}
__device__ __forceinline__ void ld8_asm(u32x2& d, rsrc_t r, unsigned voff) {
  asm volatile("s_nop 4\n\tbuffer_load_dwordx2 %0, %1, %2, 0 offen sc1" : "=v"(d) : "v"(voff), "s"(r) : "memory");
}

__device__ __forceinline__ void ld16in_asm(u32x4& d, rsrc_t r, unsigned voff, unsigned soff) {
  asm volatile("s_nop 4\n\tbuffer_load_dwordx4 %0, %1, %2, %3 offen" : "=v"(d) : "v"(voff), "s"(r), "s"(UNI(soff)) : "memory");
}
__device__ __forceinline__ void ld16_asm(f32x4& d, rsrc_t r, unsigned voff, unsigned soff) {
  asm volatile("s_nop 4\n\tbuffer_load_dwordx4 %0, %1, %2, %3 offen sc1" : "=v"(d) : "v"(voff), "s"(r), "s"(UNI(soff)) : "memory");
}

template <typename T> __device__ __forceinline__ T lds_ld(unsigned a) { return *(LAS T*)(a); }
template <typename T> __device__ __forceinline__ void lds_st(unsigned a, T v) { *(LAS T*)(a) = v; }
__device__ __forceinline__ bf16x8 tr2(unsigned a0, unsigned a1) {
  s16x4 v0 = __builtin_amdgcn_ds_read_tr16_b64_v4i16((lds_s16x4_ptr)(a0));
  s16x4 v1 = __builtin_amdgcn_ds_read_tr16_b64_v4i16((lds_s16x4_ptr)(a1));
  return __builtin_shufflevector(v0, v1, 0, 1, 2, 3, 4, 5, 6, 7);
}
__device__ __forceinline__ bf16x8 as_frag(u32x4 v) { return __builtin_bit_cast(bf16x8, v); }
#define MFMA(a, b, c) __builtin_amdgcn_mfma_f32_16x16x32_bf16(a, b, c, 0, 0, 0)

__device__ __forceinline__ float vmax2(float a, float b) { float inf = __builtin_inff(); asm("" : "+s"(inf)); return __builtin_amdgcn_fmed3f(a, b, inf); }
#define SWAP16(a, b) __builtin_amdgcn_permlane16_swap(__float_as_uint(a), __float_as_uint(b), false, false)
#define SWAP32(a, b) __builtin_amdgcn_permlane32_swap(__float_as_uint(a), __float_as_uint(b), false, false)
__device__ __forceinline__ void red2_max(float& a, float& b) {
  auto r = SWAP16(a, b);
  float x = vmax2(__uint_as_float(r[0]), __uint_as_float(r[1]));
  r = SWAP32(x, x);
  x = vmax2(__uint_as_float(r[0]), __uint_as_float(r[1]));
  r = SWAP16(x, x);
  a = __uint_as_float(r[0]); b = __uint_as_float(r[1]);
}
__device__ __forceinline__ void red2_add(float& a, float& b) {
  auto r = SWAP16(a, b);
  float x = __uint_as_float(r[0]) + __uint_as_float(r[1]);
  r = SWAP32(x, x);
  x = __uint_as_float(r[0]) + __uint_as_float(r[1]);
  r = SWAP16(x, x);
  a = __uint_as_float(r[0]); b = __uint_as_float(r[1]);
}
template <bool MAX, typename M> __device__ __forceinline__ void red4(float& a, float& b, float& c, float& d, M mid) {
  auto op = [](unsigned x, unsigned y) { return MAX ? vmax2(__uint_as_float(x), __uint_as_float(y)) : __uint_as_float(x) + __uint_as_float(y); };
  auto r = SWAP16(a, b), q = SWAP16(c, d);
  const float x = op(r[0], r[1]), y = op(q[0], q[1]);
  r = SWAP32(x, y);
  const float w = mid(op(r[0], r[1]));
  r = SWAP32(w, w);
  q = __builtin_amdgcn_permlane16_swap(r[1], r[1], false, false);
  r = __builtin_amdgcn_permlane16_swap(r[0], r[0], false, false);
  a = __uint_as_float(r[0]); b = __uint_as_float(r[1]); c = __uint_as_float(q[0]); d = __uint_as_float(q[1]);
}
__device__ __forceinline__ float red_max4(float x) {
  auto r = __builtin_amdgcn_permlane16_swap(__float_as_uint(x), __float_as_uint(x), false, false);
  x = vmax2(__uint_as_float(r[0]), __uint_as_float(r[1]));
  r = __builtin_amdgcn_permlane32_swap(__float_as_uint(x), __float_as_uint(x), false, false);
  return vmax2(__uint_as_float(r[0]), __uint_as_float(r[1]));
}
__device__ __forceinline__ float red_add4(float x) {
  auto r = __builtin_amdgcn_permlane16_swap(__float_as_uint(x), __float_as_uint(x), false, false);
  x = __uint_as_float(r[0]) + __uint_as_float(r[1]);
  r = __builtin_amdgcn_permlane32_swap(__float_as_uint(x), __float_as_uint(x), false, false);
  return __uint_as_float(r[0]) + __uint_as_float(r[1]);
}

__device__ __forceinline__ void st16_sc1(rsrc_t r, unsigned voff, unsigned soff, u32x4 v) {
  __builtin_amdgcn_raw_buffer_store_b128(v, r, voff, soff, 16);
  asm volatile("s_nop 1" ::"v"(v) : "memory");
}
__device__ __forceinline__ void st8_sc1(rsrc_t r, unsigned voff, unsigned soff, u32x2 v) { __builtin_amdgcn_raw_buffer_store_b64(v, r, voff, soff, 16); }
__device__ __forceinline__ u32x4 ld16_sc1(rsrc_t r, unsigned voff, unsigned soff) { return __builtin_amdgcn_raw_buffer_load_b128(r, voff, soff, 16); }
__device__ __forceinline__ u32x2 ld8_sc1(rsrc_t r, unsigned voff, unsigned soff) { return __builtin_amdgcn_raw_buffer_load_b64(r, voff, soff, 16); }
__device__ __forceinline__ f32x4 ld4f_sc1(rsrc_t r, unsigned voff, unsigned soff) { return __builtin_bit_cast(f32x4, __builtin_amdgcn_raw_buffer_load_b128(r, voff, soff, 16)); }
#define OPAQUE_V(x) asm volatile("" : "+v"(x))
#define OPAQUE_S(x) asm volatile("" : "+s"(x))

#define CLOBBER asm volatile("" ::: "memory")
constexpr unsigned SPIN_CAP = 1u << 17;
__device__ __forceinline__ bool is_set(u32x2 v, u32x2 epoch) { return v[0] == epoch[0] && v[1] == epoch[1]; }
__device__ __forceinline__ void set_flags(rsrc_t rs, unsigned lineA, int colA, unsigned lineB, int colB, u32x2 epoch, int lane) {
  const unsigned off = (lane & 7) * REP_STRIDE + ((lane & 8) ? lineB + colB * 8 : lineA + colA * 8);
  if (lane < 16) st8_sc1(rs, off, 0, epoch);
}
__device__ __forceinline__ unsigned poll_need(rsrc_t rs, unsigned voff, u32x2 epoch, unsigned ready, unsigned need, bool& dead) {
  for (unsigned spins = 0; !dead; ++spins) {
    CLOBBER;
    ready |= (unsigned)__ballot(is_set(ld8_sc1(rs, voff, 0), epoch));
    if ((ready & need) == need) break;
    if (spins > SPIN_CAP) dead = true;
    __builtin_amdgcn_s_sleep(1);
  }
  CLOBBER;
  return dead ? ~0u : ready;
}

__device__ __forceinline__ void poll_all(rsrc_t rs, unsigned voff, u32x2 epoch, bool& dead) {
  for (unsigned spins = 0; !dead; ++spins) {
    CLOBBER;
    if (__all(is_set(ld8_sc1(rs, voff, 0), epoch))) break;
    if (spins > SPIN_CAP) dead = true;
    __builtin_amdgcn_s_sleep(1);
  }
  CLOBBER;
}

constexpr unsigned SC_VF = L_R1 + 32768;
constexpr unsigned SC_PHIK = L_R1 + 49152;
constexpr unsigned SC_XCH = L_R2 + XS;
constexpr unsigned wo_slot(int s) {
  return s < 2 ? L_R2 + (unsigned)(s + 1) * XS : (s < 6 ? L_R1 + (unsigned)(s - 2) * 16384 : (s == 6 ? L_R1 + 81920 : L_R2));
}
__device__ __forceinline__ unsigned wo_src(int s, int j) { return (unsigned)(((JROT(j) >> 1) + NJ / 2 + s) & 7); }
__device__ __forceinline__ unsigned wo_swz(int lane) { return (unsigned)(((lane & 7) ^ ((lane >> 3) & 6)) * 16); }
__device__ __forceinline__ unsigned umax3(unsigned a, unsigned b, unsigned c) { return max(max(a, b), c); }
__device__ __forceinline__ unsigned umax4v(unsigned m, const u32x4& v) { return umax3(umax3(m, v[0], v[1]), v[2], v[3]); }
__device__ __forceinline__ u32x4 nosent(u32x4 v) { return u32x4{min(v[0], SENT - 1), min(v[1], SENT - 1), min(v[2], SENT - 1), min(v[3], SENT - 1)}; }

template <typename P, typename F>
__device__ __forceinline__ void qkv_loop(int isb, int wm, int h, int lane, bool first, int& trv, P pre, F fin) {
  constexpr unsigned lds0 = 0;
  f32x4 acc[4][6];
  const int fr = lane & 15, fq = lane >> 4;
  unsigned ab[2], bb[2];
  {
    const int r = wm * 64 + fr;
#pragma unroll
    for (int ks = 0; ks < 2; ++ks) ab[ks] = lds0 + L_R2 + r * 128 + (((ks * 4 + fq) ^ ((fr >> 1) & 7)) * 16);
    const int kr = fq * 8 + (fr >> 2);
    const int gs = ((kr >> 1) & 1) | (((kr >> 3) & 1) << 1);
#pragma unroll
    for (int c = 0; c < 2; ++c) bb[c] = lds0 + L_R1 + kr * 128 + (((h * 2 + c) ^ gs) * 32) + (lane & 3) * 8;
  }
#pragma unroll
  for (int m = 0; m < 4; ++m)
#pragma unroll
    for (int n = 0; n < 6; ++n) acc[m][n] = f32x4{0.f, 0.f, 0.f, 0.f};
  bf16x8 af[2][4], bf[2][6];
  auto rds = [&](int ks, int p, int rot = 0) {
#pragma unroll
    for (int m = 0; m < 4; ++m) af[p][m] = lds_ld<bf16x8>(ab[ks] + m * 2048);
#pragma unroll
    for (int nn = 0; nn < 6; ++nn) {
      const int n = (nn + rot) % 6;
      const unsigned a = bb[n & 1] + (n >> 1) * 8192 + ks * 4096;
      bf[p][n] = tr2(a, a + 512);
    }
  };
  auto mms = [&](int p, int rot = 0, int cnt = 6) {
#pragma unroll
    for (int nn = 0; nn < cnt; ++nn)
#pragma unroll
      for (int mm = 0; mm < 4; ++mm) { const int n = (nn + rot) % 6, m = (n & 1) ? 3 - mm : mm; acc[m][n] = n < 4 ? MFMA(bf[p][n], af[p][m], acc[m][n]) : MFMA(af[p][m], bf[p][n], acc[m][n]); }
  };
  auto ilv2 = [&]() {
#pragma unroll
    for (int i = 0; i < 16; ++i) { SGB(0x008, 1); SGB(0x100, 1); }
#pragma unroll
    for (int i = 0; i < 4; ++i) { SGB(0x008, 1); SGB(0x002, 1); }
    SGB(0x008, 4);
  };
  auto ilv3 = [&]() {
#pragma unroll
    for (int i = 0; i < 16; ++i) { SGB(0x008, 1); SGB(0x100, 1); }
#pragma unroll
    for (int i = 0; i < 7; ++i) { SGB(0x008, 1); SGB(0x004, 2); }
    SGB(0x008, 1);
  };
#pragma unroll
  for (int i = 0; i < 24; ++i) OPAQUE_V(acc[i / 6][i % 6]);
  OPAQUE_V(ab[0]); OPAQUE_V(ab[1]); OPAQUE_V(bb[0]); OPAQUE_V(bb[1]);
  if (!first) { WAITV(3); WAITL0; }
  TRQ(1); BAR;
  int tslot = 2;
  rds(0, 0);
  int sa = 0, sb = 0;
  int da = XS, db = 24576;
  auto step = [&]() {
    ab[0] += da; ab[1] += da;
    bb[0] += db; bb[1] += db;
  };
  auto ring = [&]() {
    sa = (sa == 2) ? 0 : sa + 1;
    sb = (sb == 3) ? 0 : sb + 1;
    da = (sa == 2) ? -2 * (int)XS : (int)XS;
    db = (sb == 3) ? -3 * 24576 : 24576;
  };
#pragma unroll 1
  for (int t = 14; t > 0; --t) {
    SCHED;
    rds(1, 1); mms(0);
    step();
    ilv2();
    SCHED;
    __builtin_amdgcn_s_waitcnt(0x0070);
    TRQ(tslot); tslot++; BAR;
    SCHED;
    rds(0, 0);
    ring();
    mms(1); ilv3();
  }
  SCHED;
  f32x4 cs[4], nm4[4];
  float nmean[4], csv[2];
  auto fold = [&](int n) {
#pragma unroll
    for (int mm = 0; mm < 4; ++mm) {
      const int m = (n & 1) ? 3 - mm : mm;
#pragma unroll
      for (int e = 0; e < 4; ++e) {
        float t = n < 4 ? __builtin_fmaf(nmean[m], cs[n & 3][e], acc[m][n][e]) : __builtin_fmaf(nm4[m][e], csv[n & 1], acc[m][n][e]);
        asm("" : "+v"(t));
        acc[m][n][e] = t;
      }
    }
  };
  {
    unsigned pm = lds0 + L_MEAN + (16 + wm * 64 + fr) * 4, pc = lds0 + L_CS + isb * 768 + (h * 32 + fq * 4) * 4;
    unsigned pm4 = lds0 + L_MEAN + (16 + wm * 64 + fq * 4) * 4, pcv = lds0 + L_CS + isb * 768 + (128 + h * 32 + fr) * 4;
    asm("" : "+v"(pm)); asm("" : "+v"(pc)); asm("" : "+v"(pm4)); asm("" : "+v"(pcv));
#pragma unroll
    for (int m = 0; m < 4; ++m) nmean[m] = lds_ld<float>(pm + m * 64);
#pragma unroll
    for (int n = 0; n < 4; ++n) cs[n ^ 2] = lds_ld<f32x4>(pc + (((n ^ 2) >> 1) * 64 + (n & 1) * 16) * 4);
#pragma unroll
    for (int m = 0; m < 4; ++m) nm4[m] = lds_ld<f32x4>(pm4 + m * 64);
#pragma unroll
    for (int n = 0; n < 2; ++n) csv[n] = lds_ld<float>(pcv + n * 64);
    rds(1, 1, 4); mms(0, 2);
    step();
    fold(2); fold(3);
    SGB(0x002, 8);
#pragma unroll
    for (int i = 0; i < 8; ++i) { SGB(0x008, 1); SGB(0x100, 2); }
#pragma unroll
    for (int i = 0; i < 11; ++i) { SGB(0x008, 1); SGB(0x100, 1); SGB(0x002, 2); }
#pragma unroll
    for (int i = 0; i < 5; ++i) { SGB(0x008, 1); SGB(0x002, 3); }
    SCHED;
    __builtin_amdgcn_s_waitcnt(0xC07F);
    TRQ(16); BAR;
    SCHED;
    rds(0, 0, 2);
    mms(1, 4); fold(4); fold(5);
#pragma unroll
    for (int i = 0; i < 8; ++i) { SGB(0x008, 1); SGB(0x100, 1); SGB(0x002, 1); }
#pragma unroll
    for (int i = 0; i < 8; ++i) { SGB(0x008, 1); SGB(0x100, 1); SGB(0x002, 2); }
#pragma unroll
    for (int i = 0; i < 8; ++i) { SGB(0x008, 1); SGB(0x002, 2); }
    SCHED;
    pre(0);
    SCHED;
    rds(1, 1, 2);
    mms(0, 2, 4); fold(0); fold(1);
    pre(1);
#pragma unroll
    for (int i = 0; i < 16; ++i) { SGB(0x008, 1); SGB(0x100, 1); SGB(0x002, 2); SGB(0x004, 1); }
  }
  SCHED;
  auto mm6 = [&](int m, int n0 = 0, int n1 = 8) {
#pragma unroll
    for (int nn = n0; nn < n1; ++nn) {
      const int n = nn < 4 ? nn + 2 : nn & 1, p = (nn >> 1) != 2;
      acc[m][n] = n < 4 ? MFMA(bf[p][n], af[p][m], acc[m][n]) : MFMA(af[p][m], bf[p][n], acc[m][n]);
    }
  };
  if (!isb) {
    fin(std::false_type{}, acc, mm6);
#pragma unroll
    for (int i = 0; i < 24; ++i) asm volatile("" : "=v"(acc[i / 6][i % 6]));
#pragma unroll
    for (int i = 0; i < 6; ++i) asm volatile("" : "=v"(bf[1][i]));
#pragma unroll
    for (int i = 0; i < 8; ++i) asm volatile("" : "=v"(af[i >> 2][i & 3]));
    asm volatile("" : "=v"(bf[0][0])); asm volatile("" : "=v"(bf[0][1]));
  }
  OPAQUE_S(isb);
  if (isb) fin(std::true_type{}, acc, mm6);
}
__device__ __forceinline__ u32x4 pack(const f32x4& a, const f32x4& b) { return u32x4{pk2(a[0], a[1]), pk2(a[2], a[3]), pk2(b[0], b[1]), pk2(b[2], b[3])}; }

__device__ __forceinline__ u32x2 nosent2(const f32x4& a) { return u32x2{min(pk2(a[0], a[1]), SENT - 1), min(pk2(a[2], a[3]), SENT - 1)}; }
template <int N> __device__ __forceinline__ void hedgehog(const f32x4 (&r)[4][2], const float (&c)[4], float (&ep)[4][8], float (&en)[4][8]) {
  float mx[4], nm[4];
#pragma unroll
  for (int m = 0; m < N; ++m) {
    const f32x4 a = r[m][0], b = r[m][1];
    mx[m] = vmax2(fmaxf(fmaxf(a[3], b[0]), b[1]), fmaxf(fmaxf(fmaxf(fmaxf(a[0], a[1]), a[2]), b[2]), b[3]));
    nm[m] = vmax2(fmaxf(fmaxf(-a[3], -b[0]), -b[1]), fmaxf(fmaxf(fmaxf(fmaxf(-a[0], -a[1]), -a[2]), -b[2]), -b[3]));
  }
  SCHED;
  red4<true>(mx[0], mx[1], nm[0], nm[1], [](float x) { return x; });
  if (N == 4) red4<true>(mx[2], mx[3], nm[2], nm[3], [](float x) { return x; });
#pragma unroll
  for (int m = 0; m < N; ++m) { mx[m] *= -c[m]; nm[m] *= -c[m]; }
  SCHED;
#pragma unroll
  for (int m = 0; m < N; ++m) {
#pragma unroll
    for (int i = 0; i < 8; ++i) { ep[m][i] = __builtin_fmaf(r[m][i >> 2][i & 3], c[m], mx[m]); en[m][i] = __builtin_fmaf(r[m][i >> 2][i & 3], -c[m], nm[m]); }
    SCHED;
#pragma unroll
    for (int i = 0; i < 8; ++i) { ep[m][i] = __builtin_amdgcn_exp2f(ep[m][i]); en[m][i] = __builtin_amdgcn_exp2f(en[m][i]); }
    SCHED;
  }
}
__device__ __forceinline__ u32x4 pack8(const float (&e)[8], float f) {
  return u32x4{pk2(e[0] * f, e[1] * f), pk2(e[2] * f, e[3] * f), pk2(e[4] * f, e[5] * f), pk2(e[6] * f, e[7] * f)};
}
__device__ __forceinline__ u32x4 pack8(const float (&e)[8]) { return u32x4{pk2(e[0], e[1]), pk2(e[2], e[3]), pk2(e[4], e[5]), pk2(e[6], e[7])}; }

constexpr u64 window_mask(int t, int e) {
  u64 mk = 0;
  for (int ln = 0; ln < 64; ++ln) {
    const int delta = (ln & 15) + 16 - t * 16 - (ln >> 4) * 4 - e;
    if (delta >= 0 && delta <= 14 && !(delta & 1)) mk |= 1ull << ln;
  }
  return mk;
}
__device__ __forceinline__ void phik2(const f32x4 (&r)[4][2], const float (&c)[4], unsigned pw) {
  const f32x4 zero = {0.f, 0.f, 0.f, 0.f};
  const bf16x8 ones = {0x3f80, 0x3f80, 0x3f80, 0x3f80, 0x3f80, 0x3f80, 0x3f80, 0x3f80};
  float ep[4][8], en[4][8];
  f32x4 sp[2], sn[2];
  hedgehog<2>(r, c, ep, en);
#pragma unroll
  for (int m = 0; m < 2; ++m) { sp[m] = MFMA(ones, as_frag(pack8(ep[m])), zero); sn[m] = MFMA(ones, as_frag(pack8(en[m])), zero); }
#pragma unroll
  for (int m = 0; m < 2; ++m) { asm("" : "+v"(sp[m])); asm("" : "+v"(sn[m])); }
  SCHED;
#pragma unroll
  for (int m = 0; m < 2; ++m) {
    const float fp = __builtin_amdgcn_rcpf(sp[m][0]), fn = __builtin_amdgcn_rcpf(sn[m][0]);
#pragma unroll
    for (int i = 0; i < 8; ++i) { ep[m][i] *= fp; en[m][i] *= fn; }
    SCHED;
    const u32x4 pp = pack8(ep[m]), pn = pack8(en[m]);
    const unsigned d = pw + m * (16 * 160);
    lds_st<u32x2>(d, u32x2{pp[0], pp[1]});
    lds_st<u32x2>(d + 32, u32x2{pp[2], pp[3]});
    lds_st<u32x2>(d + 64, u32x2{pn[0], pn[1]});
    lds_st<u32x2>(d + 96, u32x2{pn[2], pn[3]});
    SCHED;
  }
}
__device__ __forceinline__ bf16x8 rowsum_sel(int lane) {
  const int fr = lane & 15;
  const unsigned one = (fr < 8) ? (0x3f80u << (16 * (fr & 1))) : 0u;
  const int e = (fr >> 1) & 3;
  return as_frag(u32x4{e == 0 ? one : 0u, e == 1 ? one : 0u, e == 2 ? one : 0u, e == 3 ? one : 0u});
}
__device__ __forceinline__ f32x4 rowsum_fin(f32x4 c) {
#pragma unroll
  for (int k = 0; k < 4; ++k) {
    const float a = c[k];
    c[k] = a + __int_as_float(__builtin_amdgcn_update_dpp(0, __float_as_int(a), 0x128  , 0xf, 0xf, false));
  }
  return c;
}
__device__ __forceinline__ void job_colsum(rsrc_t rs, const u32x4 (&w)[8], int mat, int g, int jc, int hw, int lane) {
  const bf16x8 sel = rowsum_sel(lane);
  f32x4 c = {0.f, 0.f, 0.f, 0.f};
#pragma unroll
  for (int i = 0; i < 8; ++i) c = MFMA(sel, as_frag(w[i]), c);
  const u32x4 w4 = __builtin_bit_cast(u32x4, rowsum_fin(c));
  unsigned so = WS_CS + (unsigned)(((mat * 16 + jc) * 16 + g) * 768);
  OPAQUE_S(so);
  if ((lane & 40) == 0) st16_sc1(rs, (unsigned)(hw * 64 + (lane & 7) * 8 + (lane >> 4) * 4) * 4, so, w4);
}
__device__ __forceinline__ unsigned img_off(int i, int lane) { return (lane >> 3) * 128 + (((lane & 7) ^ ((((lane >> 4) & 1) | ((i & 1) << 1)) << 1)) * 16); }
__device__ __forceinline__ void job_pack(rsrc_t rs, u32x4 w, int i, int mat, int g, int jc, int hw, int lane) {
  st16_sc1(rs, img_off(i, lane),
           WS_WP + (unsigned)(((mat * 16 + jc) * 16 + g) * 24576 + hw * 8192 + i * 1024), w);
}

__global__ __launch_bounds__(512, 2) void mega_kernel(Params p) {
  extern __shared__ __attribute__((aligned(16))) char smem[];
  const int tid = threadIdx.x;
  const int wave0 = __builtin_amdgcn_readfirstlane(tid >> 6);
  const int xx = blockIdx.x & 7, rr = blockIdx.x >> 3;
  const int g0 = (rr / NJ) * NX + (xx & (NX - 1)), j0 = (xx / NX) * NJ + (rr & (NJ - 1));
  constexpr unsigned lds0 = 0, lds00 = 0;
  if ((unsigned)(size_t)(LAS char*)smem != 0u) return;
#define LOCALS0 int g = g0, j = j0, l = l0, wave = wave0;                                              \
  OPAQUE_S(g); OPAQUE_S(j); OPAQUE_S(l); OPAQUE_S(wave);                                               \
  const int hw = wave & 3, wm = wave & 1, h = (wave >> 1) & 1;                                         \
  const unsigned myrep = (unsigned)(JROT(j) / 2 + (g & (NX - 1))) * REP_STRIDE;                              \
  (void)hw; (void)wm; (void)h; (void)myrep;                                                            \
  int lane = tid; OPAQUE_V(lane); lane &= 63
#define LOCALS LOCALS0; const bool isb = l & 1, lead = (wave == 4); const int half = wave >> 2; (void)isb; (void)lead; (void)half
  const rsrc_t rs = mk_rsrc(p.ws, WS_SPAN);
  const bool cw = wave0 < 4;
  int trv = 0, trA = 0, trB = 0, trC = 0;
  STAMP_INIT;
  TRC(0); TRR(62);
  if (wave0 == 3) {
    const char* pc;
    unsigned len;
    asm volatile("s_getpc_b64 %0\n.Lpf_base:\n\ts_mov_b32 %1, .Lpf_end-.Lpf_base" : "=s"(pc), "=s"(len));
    const unsigned off = ((unsigned)rr * 16 + (tid & 15)) * 128;
    touch(mk_rsrc(pc, len), ((int)len > 0 && off + 4 <= len) ? off : 0u, lds00 + L_SINK);
  }
  if (wave0 == 4) {
    const int q = rr & 15;
    const void* bp = q < 6 ? (const void*)p.wqkvB : (q < 8 ? (const void*)p.woA : (q < 10 ? (const void*)p.woB : (q < 11 ? (const void*)p.wphi : (const void*)p.ws)));
    const unsigned off = q < 6 ? q << 20 : (q < 10 ? (q & 1) << 20 : (q < 11 ? 0u : (q - 11) << 20));
    touch(mk_rsrc(bp, off + 4), off, lds00 + L_SINK);
  }
  auto slowbit = [](const void* q) { return 1u << ((3 - ((unsigned)(size_t)q >> 7)) & 7); };
  unsigned slowm = slowbit(p.wqkvA) | (slowbit(p.wqkvB) << 8) | (slowbit(p.woA) << 16) | (slowbit(p.woB) << 24);
  OPAQUE_S(slowm);
  u32x2 epoch = {0u, 0u};
  bool dead = false;
      if (cw) {
        const int l0 = 0;
        LOCALS;
        u32x4 v[2][8];
        if (wave < 3) {
          const int jc = JOBC(j, wave), kt = (KB0(g, j) + (unsigned)g / NX) & 15;
          const rsrc_t ra = mk_rsrc(p.wqkvA, 1024 * 6144);
#pragma unroll
          for (int i = 0; i < 8; ++i)
            v[0][i] = __builtin_amdgcn_raw_buffer_load_b128(ra, (lane >> 3) * 6144 + (lane & 7) * 16, (kt * 64 + i * 8) * 6144 + wave * 2048 + jc * 128, 0);
          SCHED;
          TRC(44); BAR; SCHED;
          job_colsum(rs, v[0], 0, g, jc, wave, lane);
          if (SLOWC(0, jc)) {
#pragma unroll
            for (int i = 0; i < 8; ++i) job_pack(rs, v[0][i], i, 0, kt, jc, wave, lane);
          }
        } else {
          const int fr = lane & 15, fq = lane >> 4;
          const rsrc_t rx = mk_rsrc(p.x, 2048 * 2048);
#pragma unroll
          for (int i = 0; i < 16; ++i)
            v[i & 1][i >> 1] = __builtin_amdgcn_raw_buffer_load_b128(rx, fr * 2048 + fq * 16, (g * 128 + (j & (NJ - 1)) * (128 / NJ) + ((i >> 1) % (8 / NJ)) * 16) * 2048 + ((KB0(g, j) + (i >> 1) / (8 / NJ)) & 15) * 128 + (i & 1) * 64, 0);
          SCHED;
          TRC(44); BAR; SCHED;
          const f32x4 zero = {0.f, 0.f, 0.f, 0.f};
          const bf16x8 ones = {0x3f80, 0x3f80, 0x3f80, 0x3f80, 0x3f80, 0x3f80, 0x3f80, 0x3f80};
          float sm[8], q[8];
#pragma unroll
          for (int m = 0; m < 8; ++m) {
            const f32x4 a = MFMA(ones, as_frag(v[1][m]), MFMA(ones, as_frag(v[0][m]), zero));
            const f32x4 b = MFMA(as_frag(v[1][m]), as_frag(v[1][m]), MFMA(as_frag(v[0][m]), as_frag(v[0][m]), zero));
            sm[m] = a[0];
            q[m] = (fr & 2) ? ((fr & 1) ? b[3] : b[2]) : ((fr & 1) ? b[1] : b[0]);
          }
          if (fq == (fr >> 2)) {
#pragma unroll
            for (int m = 0; m < 8; ++m) st8_sc1(rs, ((m % (8 / NJ)) * 16 + fr) * 8 + (m / (8 / NJ)) * 1024, st_off(g, 0) + JROT(j) * 1024 + (j & (NJ - 1)) * (1024 / NJ), u32x2{__float_as_uint(sm[m]), __float_as_uint(q[m])});
          }
        }
      }
  const char *pA = (const char*)p.x, *pW = (const char*)p.wqkvA;
  unsigned zA = 2048 * 2048, bA = g0 * (128 * 2048), kA = 128, pcA = 32 * 2048, vA;
  unsigned zW = 1024 * 6144, bW = j0 * 128, kW = 64 * 6144, pcW = 2048, hW = 32 * 6144, vW;
  int kb = KB0(g0, j0), dj = -1;
  {
    const int lane = tid & 63, hw = wave0 & 3;
    vA = (hw * 8 + (lane >> 3)) * 2048 + (((lane & 7) ^ (((hw & 1) * 4 + (lane >> 4)) & 7)) * 16);
    vW = (hw * 8 + (lane >> 3)) * 6144 + (((lane & 7) ^ ((((lane >> 4) & 1) | ((hw & 1) << 1)) << 1)) * 16);
  }

#pragma unroll 1
  for (int l0 = 0; l0 < 4; ++l0) {
    if (l0) { TRC(0); TRR(62); }

    f32x4 acc[2][4];
    auto kvpart = [&](unsigned va, unsigned pa, unsigned pb, unsigned kvb, int lane, int wave) {
      const f32x4 zero = {0.f, 0.f, 0.f, 0.f};
      const bf16x8 ones = {0x3f80, 0x3f80, 0x3f80, 0x3f80, 0x3f80, 0x3f80, 0x3f80, 0x3f80};
      f32x4 c0 = zero, c1 = zero, z0 = zero;
#pragma unroll
      for (int ks = 0; ks < 4; ++ks) {
        const bf16x8 v = lds_ld<bf16x8>(va + ks * 2048);
        const bf16x8 a0 = tr2(pa + ks * 32 * 160, pa + (ks * 32 + 16) * 160);
        const bf16x8 a1 = tr2(pb + ks * 32 * 160, pb + (ks * 32 + 16) * 160);
        c0 = MFMA(a0, v, c0);
        c1 = MFMA(a1, v, c1);
        z0 = MFMA(a0, ones, z0);
      }
      const u32x4 c = nosent(pack(c0, c1));
      const bool dt = wave & 2;
      st16_sc1(rs, lane * 16, kvb + (unsigned)wave * 1024, u32x4{dt ? c[2] : c[0], dt ? c[3] : c[1], dt ? c[0] : c[2], dt ? c[1] : c[3]});
      if ((lane & 15) == 0)
        st8_sc1(rs, lane, kvb + KVP_Z + (wave >> 2) * 128 + (wave & 1) * 64 + (wave & 2) * 4, nosent2(z0));
    };
#define KV_ADDR                                                                                                            \
    unsigned va = lds0 + SC_VF + (wave >> 2) * 8192 + ((wave >> 1) & 1) * 1024 + lane * 16;                                \
    unsigned pa = lds0 + SC_PHIK + ((wave >> 2) * 128 + fq * 4 + (fr >> 2)) * 160 + (wave & 1) * 64 + (lane & 3) * 8;      \
    unsigned kvj = WS_KVP + (unsigned)j * KVP_J + (unsigned)(l >> 1) * KVP_L;                                              \
    unsigned pb = pa + 32 - (wave & 2) * 16;                                                                               \
    pa += (wave & 2) * 16;                                                                                                 \
    OPAQUE_V(va); OPAQUE_V(pa); OPAQUE_V(pb); OPAQUE_S(kvj)
    if (cw) {
      LOCALS;
      unsigned otile, kvo, l16, pr1, pr4;
      int g2, j2, l2, ln2;
      qkv_loop(l & 1, wm, h, lane, l == 0, trv, [&](int st) {
        if (st == 0) {
          g2 = g0; j2 = j0; l2 = l0; ln2 = tid;
          OPAQUE_S(g2); OPAQUE_S(j2); OPAQUE_S(l2); OPAQUE_V(ln2);
          return;
        }
        otile = ob_off(g2, j2, l2) + (unsigned)(wm * 8 + h) * 1024;
        kvo = WS_KVP + (unsigned)j2 * KVP_J + (unsigned)g2 * KVP_G + (unsigned)(l2 >> 1) * KVP_L;
        l16 = (ln2 & 63) * 16;
        pr1 = lds0 + L_RSTD + (16 + wm * 64 + (ln2 & 15)) * 4;
        pr4 = lds0 + L_RSTD + wm * 256 + (ln2 & 48);
        asm("" : "+s"(otile)); asm("" : "+s"(kvo)); asm("" : "+v"(l16)); asm("" : "+v"(pr1)); asm("" : "+v"(pr4));
      }, [&](auto isb_c, f32x4 (&a)[4][6], auto& mm6) {
        const f32x4 zero = {0.f, 0.f, 0.f, 0.f};
        constexpr float L2E = 1.4426950408889634f;
        float rq[4];
#define ilv8(nv) do { _Pragma("unroll") for (int i_ = 0; i_ < 8; ++i_) { SGB(0x008, 1); SGB(0x100, 2); SGB(0x002, nv); } SCHED; } while (0)
        if constexpr (decltype(isb_c)::value) {
          constexpr float QS = 0.17677669529663687f * L2E, NEG = -3.0e38f;
          const bf16x8 ones = {0x3f80, 0x3f80, 0x3f80, 0x3f80, 0x3f80, 0x3f80, 0x3f80, 0x3f80};
          u32x4 kf[5], vf[5], qf[4], pf[4];
          f32x4 rs4[4], bias[2], s0[4], s1[4], dn[4];
          float x[4][4], mx[4];
          unsigned psel0, psel1;
          auto pkkv = [&](int m) { kf[m + 1] = pack(a[m][2] * rq[m], a[m][3] * rq[m]); vf[m + 1] = pack(a[m][4] * rs4[m], a[m][5] * rs4[m]); };
          auto pkq = [&](int m) { qf[m] = pack(a[m][0], a[m][1]); };
          auto st1 = [&](int m) {
            x[m][0] = vmax2(s0[m][0], s0[m][1]); x[m][1] = vmax2(s0[m][2], s0[m][3]); x[m][2] = vmax2(s1[m][0], s1[m][1]); x[m][3] = vmax2(s1[m][2], s1[m][3]);
            mx[m] = vmax2(vmax2(x[m][0], x[m][1]), vmax2(x[m][2], x[m][3]));
          };
          auto st2 = [&](int m) {
            const float mxl = mx[m] * -rq[m];
#pragma unroll
            for (int e = 0; e < 4; ++e) x[m][e] = __builtin_fmaf(x[m][e], rq[m], mxl);
          };
          auto st3 = [&](int m) {
#pragma unroll
            for (int e = 0; e < 4; ++e) x[m][e] = __builtin_amdgcn_exp2f(x[m][e]);
          };
          auto st5 = [&](int m) {
            const unsigned p0 = pk2(x[m][0], x[m][1]), p1 = pk2(x[m][2], x[m][3]);
            pf[m] = u32x4{__builtin_amdgcn_perm(0u, p0, psel0), __builtin_amdgcn_perm(0u, p0, psel1), __builtin_amdgcn_perm(0u, p1, psel0), __builtin_amdgcn_perm(0u, p1, psel1)};
          };
          auto st6 = [&](int m) {
            dn[m] = MFMA(ones, as_frag(pf[m]), zero);
            s0[m] = MFMA(as_frag(u32x4{vf[m][0], vf[m][1], vf[m + 1][0], vf[m + 1][1]}), as_frag(pf[m]), zero);
            s1[m] = MFMA(as_frag(u32x4{vf[m][2], vf[m][3], vf[m + 1][2], vf[m + 1][3]}), as_frag(pf[m]), zero);
            asm("" : "+v"(dn[m]));
          };
          auto st7 = [&](int m) { const float inv = __builtin_amdgcn_rcpf(dn[m][0]); pf[m] = nosent(pack(s0[m] * inv, s1[m] * inv)); };
          mm6(3);
#pragma unroll
          for (int m = 0; m < 4; ++m) rq[m] = lds_ld<float>(pr1 + m * 64);
#pragma unroll
          for (int m = 3; m >= 0; --m) rs4[m] = lds_ld<f32x4>(pr4 + 64 + m * 64);
          unsigned pv_ = l16 + lds0 + L_PREV + h * 2048;
          asm("" : "+v"(pv_));
          static_for<0, 8>([&](auto i_c) __attribute__((always_inline)) {
            constexpr int i = decltype(i_c)::value;
            constexpr u64 mk = window_mask(i >> 2, i & 3);
            bias[i >> 2][i & 3] = __builtin_amdgcn_inverse_ballot_w64(mk) ? 0.f : NEG;
          });
          asm("" : "+v"(bias[0])); asm("" : "+v"(bias[1]));
          const unsigned hso = kvo + KVP_HALO + h * 2048, hsi = hso - (g > 0 ? KVP_G : 0u);
          ilv8(2);
          mm6(2);
          pkkv(3);
          ilv8(2);
          OPAQUE_V(kf[4]); OPAQUE_V(vf[4]);
          if (wm) {
            st16_sc1(rs, l16, hso, nosent(kf[4]));
            st16_sc1(rs, l16, hso + 1024, nosent(vf[4]));
          } else {
            lds_st<u32x4>(pv_, kf[4]);
            lds_st<u32x4>(pv_ + 1024, vf[4]);
          }
          SCHED;
          mm6(1);
          pkq(3); pkkv(2);
          ilv8(3);
          WAITL0;
          TRQ(17); BAR;
          SCHED;
          mm6(0);
          pkq(2); pkkv(1);
          ilv8(3);
          s0[3] = MFMA(as_frag(kf[3]), as_frag(qf[3]), bias[0]); s1[3] = MFMA(as_frag(kf[4]), as_frag(qf[3]), bias[1]);
          s0[2] = MFMA(as_frag(kf[2]), as_frag(qf[2]), bias[0]); s1[2] = MFMA(as_frag(kf[3]), as_frag(qf[2]), bias[1]);
          pkq(1); pkkv(0);
#pragma unroll
          for (int i = 0; i < 4; ++i) { SGB(0x008, 1); SGB(0x002, 5); }
          SCHED;
          const bool halo = wm == 0 && g > 0;
          const u32x4 hk = ld16_sc1(rs, l16, hsi), hv = ld16_sc1(rs, l16, hsi + 1024);
          SCHED;
          s0[1] = MFMA(as_frag(kf[1]), as_frag(qf[1]), bias[0]); s1[1] = MFMA(as_frag(kf[2]), as_frag(qf[1]), bias[1]);
          st1(3); st1(2);
#pragma unroll
          for (int m = 0; m < 4; ++m) rq[m] *= QS;
          SCHED;
          st1(1);
          pkq(0);
          psel0 = __builtin_amdgcn_inverse_ballot_w64(0xaaaaaaaaaaaaaaaaull) ? 0x01000c0cu : 0x0c0c0100u;
          psel1 = __builtin_amdgcn_inverse_ballot_w64(0xaaaaaaaaaaaaaaaaull) ? 0x03020c0cu : 0x0c0c0302u;
          { float d = mx[1]; red4<true>(mx[3], mx[2], mx[1], d, [](float v) { return v; }); }
          s1[0] = MFMA(as_frag(kf[1]), as_frag(qf[0]), bias[1]);
          SCHED;
          st2(3); st2(2); st2(1);
          SCHED;
          st3(3); st3(2); st3(1);
          SCHED;
          st5(3); st6(3); st5(2); st6(2); st5(1); st6(1);
          SCHED;
          TRR(60);
          TRC(18);
          u32x4 pk = lds_ld<u32x4>(pv_), pv = lds_ld<u32x4>(pv_ + 1024);
          st7(3); st7(2); st7(1);
          SCHED;
#pragma unroll
          for (int m = 3; m > 0; --m) st16_sc1(rs, l16, otile + m * 2048, pf[m]);
          if (!wm && g == 0) bias[0] = f32x4{NEG, NEG, NEG, NEG};
          SCHED;
          TRV(55, 0); TRC(21);
          if (halo) {
            pk = hk; pv = hv;
            for (unsigned spins = 0; !dead && __any(umax4v(umax4v(0u, pk), pv) == SENT); ++spins) {
              if (spins > SPIN_CAP) { dead = true; break; }
              CLOBBER;
              TRV(55, spins + 1);
              pk = ld16_sc1(rs, l16, hsi); pv = ld16_sc1(rs, l16, hsi + 1024);
              OPAQUE_V(pk); OPAQUE_V(pv);
            }
          }
          vf[0] = pv;
          s0[0] = MFMA(as_frag(pk), as_frag(qf[0]), bias[0]);
          st1(0);
          mx[0] = red_max4(mx[0]);
          st2(0); st3(0); st5(0); st6(0); st7(0);
          st16_sc1(rs, l16, otile, pf[0]);
        } else {
          LOCALS;
          const int fr = lane & 15, fq = lane >> 4;
          f32x4 rs4[4], r[4][2];
          const bf16x8 ones = {0x3f80, 0x3f80, 0x3f80, 0x3f80, 0x3f80, 0x3f80, 0x3f80, 0x3f80};
          auto proj = [&](int m, int n, const bf16x8& w0, const bf16x8& w1) {
            const u32x4 f = pack(a[m][n], a[m][n + 1]);
            r[m][0] = MFMA(w0, as_frag(f), zero); r[m][1] = MFMA(w1, as_frag(f), zero);
          };
          mm6(3, 0, 2); mm6(2, 0, 2); mm6(1, 0, 2); mm6(0, 0, 2);
          unsigned wa = lds0 + L_WPHI + h * 2048 + (fq * 4 + (fr >> 2)) * 64 + (lane & 3) * 8, kw = lds0 + L_PREV + hw * 2048 + l16;
          asm("" : "+v"(wa)); asm("" : "+v"(kw));
#pragma unroll
          for (int m = 0; m < 4; ++m) rq[m] = lds_ld<float>(pr1 + m * 64);
#pragma unroll
          for (int m = 0; m < 4; ++m) rs4[m] = lds_ld<f32x4>(pr4 + 64 + m * 64);
          const bf16x8 w0 = tr2(wa, wa + 1024);
          const bf16x8 w1 = tr2(wa + 32, wa + 32 + 1024);
#pragma unroll
          for (int i = 0; i < 8; ++i) { SGB(0x008, 1); SGB(0x100, 2); SGB(0x002, 2); }
          SCHED;
          mm6(3, 2, 4); mm6(2, 2, 4);
          lds_st<u32x4>(kw + 1024, pack(a[3][2], a[3][3]));
          lds_st<u32x4>(kw, pack(a[2][2], a[2][3]));
#pragma unroll
          for (int i = 0; i < 2; ++i) { SGB(0x008, 1); SGB(0x002, 2); SGB(0x008, 1); SGB(0x002, 2); SGB(0x200, 1); }
          SCHED;
          mm6(1, 2, 4); mm6(0, 2, 4);
          SCHED;
          WAITL0;
          TRQ(17); BAR;
          SCHED;
          proj(0, 2, w0, w1); proj(1, 2, w0, w1);
          unsigned pw = lds0 + SC_PHIK + (h * 128 + wm * 64 + fr) * 160 + fq * 8, vw = lds0 + SC_VF + h * 8192 + wm * 4096 + l16;
          asm("" : "+v"(pw)); asm("" : "+v"(vw));
#pragma unroll
          for (int m = 0; m < 4; ++m) rq[m] *= L2E;
#pragma unroll
          for (int i = 0; i < 4; ++i)
            lds_st<u32x4>(vw + i * 1024, pack(a[i & 2][4 + (i & 1)] * rs4[i & 2], a[(i & 2) + 1][4 + (i & 1)] * rs4[(i & 2) + 1]));
          SCHED;
          phik2(r, rq, pw);
          KV_ADDR;
          WAITL0;
          TRR(60);
          TRC(19); BAR;
          kvpart(va, pa, pb, kvo, lane, wave);
          SCHED;
#pragma unroll
          for (int m = 0; m < 4; ++m) mm6(m, 4, 8);
          SCHED;
          CLOBBER;
          TRC(20); BAR;
          u32x4 qp[4], qn[4];
          float sqp[4], sqn[4];
#pragma unroll
          for (int m = 0; m < 4; ++m) proj(m, 0, w0, w1);
          SCHED;
          float ep[4][8], en[4][8];
          hedgehog<4>(r, rq, ep, en);
          const unsigned zo = (lane >> 3) * KVP_G + (lane & 7) * 16, zs = kvj + KVP_Z + h * 128;
          u32x4 zv[2] = {ld16_sc1(rs, zo, zs), ld16_sc1(rs, zo, zs + 8 * KVP_G)};
          SCHED;
#pragma unroll
          for (int m = 0; m < 4; ++m) {
            qp[m] = pack8(ep[m]); qn[m] = pack8(en[m]);
            sqp[m] = MFMA(ones, as_frag(qp[m]), zero)[0];
            sqn[m] = MFMA(ones, as_frag(qn[m]), zero)[0];
          }
          SCHED;
#pragma unroll
          for (int m = 0; m < 4; ++m) { sqp[m] = __builtin_amdgcn_rcpf(sqp[m]); sqn[m] = __builtin_amdgcn_rcpf(sqn[m]); }
#pragma unroll
          for (int m = 0; m < 4; ++m) { OPAQUE_V(qp[m]); OPAQUE_V(qn[m]); OPAQUE_V(sqp[m]); OPAQUE_V(sqn[m]); }
          unsigned ko = (lane >> 5) * KVP_G + (lane & 31) * 16, ks = kvj + (unsigned)((j ^ RX) - j) * KVP_J + KVP_SUM + h * (8 * KVP_G);
          bf16x8 sel = rowsum_sel(lane);
          OPAQUE_V(ko); OPAQUE_S(ks); OPAQUE_V(sel);
          TRC(21); TRV(50, 0);
          for (unsigned spins = 0; !dead && __any(umax4v(umax4v(0u, zv[0]), zv[1]) == SENT); ++spins) {
            if (spins > SPIN_CAP) { dead = true; break; }
            CLOBBER;
            TRV(50, spins + 1);
            zv[0] = ld16_sc1(rs, zo, zs); zv[1] = ld16_sc1(rs, zo, zs + 8 * KVP_G);
          }
          TRC(22);
          {
            const f32x4 z = rowsum_fin(MFMA(sel, as_frag(zv[1]), MFMA(sel, as_frag(zv[0]), zero)));
            if ((lane & 40) == 0) lds_st<u32x2>(lds0 + L_ZF + hw * 128 + fr * 16 + fq * 8, u32x2{pk2(z[0], z[1]), pk2(z[2], z[3])});
            WAITL0;
            const bf16x8 zf0 = lds_ld<bf16x8>(lds0 + L_ZF + hw * 128 + fq * 16), zf1 = lds_ld<bf16x8>(lds0 + L_ZF + hw * 128 + 64 + fq * 16);
            float dp[4], dn[4];
#pragma unroll
            for (int m = 0; m < 4; ++m) { dp[m] = MFMA(zf0, as_frag(qp[m]), zero)[0]; dn[m] = MFMA(zf1, as_frag(qn[m]), zero)[0]; }
            SCHED;
#pragma unroll
            for (int m = 0; m < 4; ++m) {
              const float inv = __builtin_amdgcn_rcpf(__builtin_fmaf(dp[m], sqp[m], __builtin_fmaf(dn[m], sqn[m], 1e-6f)));
              sqp[m] *= inv; sqn[m] *= inv;
              OPAQUE_V(sqp[m]); OPAQUE_V(sqn[m]);
            }
          }
          TRV(51, 0);
          TRC(43); BAR;
          __builtin_amdgcn_s_sleep(H2D);
          u32x4 kv[4];
#pragma unroll
          for (int i = 0; i < 4; ++i) kv[i] = ld16_sc1(rs, ko, ks + i * (2 * KVP_G));
          SCHED;
#pragma unroll
          for (int m = 0; m < 4; ++m) { qp[m] = pack8(ep[m], sqp[m]); qn[m] = pack8(en[m], sqn[m]); }
#pragma unroll
          for (int m = 0; m < 4; ++m) { OPAQUE_V(qp[m]); OPAQUE_V(qn[m]); }
          SCHED;
          for (unsigned spins = 0; !dead && __any(max(umax4v(umax4v(0u, kv[0]), kv[1]), umax4v(umax4v(0u, kv[2]), kv[3])) == SENT); ++spins) {
            if (spins > SPIN_CAP) { dead = true; break; }
            CLOBBER;
            TRV(51, spins + 1);
#pragma unroll
            for (int i = 0; i < 4; ++i) kv[i] = ld16_sc1(rs, ko, ks + i * (2 * KVP_G));
          }
          TRC(23);
          f32x4 o[4][2];
#pragma unroll
          for (int m = 0; m < 4; ++m)
#pragma unroll
            for (int i = 0; i < 2; ++i) o[m][i] = MFMA(as_frag(kv[2 * i + 1]), as_frag(qn[m]), MFMA(as_frag(kv[2 * i]), as_frag(qp[m]), zero));
#pragma unroll
          for (int m = 0; m < 4; ++m) st16_sc1(rs, l16, otile + m * 2048, nosent(pack(o[m][0], o[m][1])));
        }
      });
#pragma unroll
      for (int i = 0; i < 8; ++i) asm volatile("" : "=v"(acc[i >> 2][i & 3]));
    } else {
      __builtin_amdgcn_s_setprio(3);
      if (l0) {
        WAITV(3);
        WAITL0;
        TRC(1); BAR;
      }
      LOCALS;
      u32x4 xr[2][2];
      const unsigned lin = lane * 16, vlin = lin + hw * 1024;
      const int jr = j & ~3;
      u32x4 sent4 = {SENT, SENT, SENT, SENT};
      OPAQUE_V(sent4);
      const unsigned cb0 = ob_off(g, j, l), cb2 = xb_off(g, l) + j * TS;
      const unsigned cb1 = WS_KVP + (unsigned)j * KVP_J + (unsigned)g * KVP_G + (unsigned)(l >> 1) * KVP_L;
      unsigned ra0;
      int tr = 0;
      auto ldres = [&](int t) {
        if (tr == t) {
#pragma unroll
          for (int i = 0; i < 4; ++i) xr[i >> 1][i & 1] = lds_ld<u32x4>((ra0 ^ ((i & 1) * 64)) + (t % 3) * XS + ((i >> 1) ^ 1) * 2048);
        }
      };
      auto aslot = [&](int t) { return lds0 + L_R2 + (t % 3) * XS + hw * 1024; };
      auto wslot = [&](int t) { return lds0 + L_R1 + (t & 3) * 24576 + hw * 1024; };
      const bool l0f = l == 0;
      const rsrc_t rA = mk_rsrc(pA, zA);
      auto kseq = [&](int t) { return (kb + t - (t <= dj ? 1 : 0)) & 15; };
      auto offA = [&](int t) { return bA + (unsigned)kseq(t) * kA; };
      auto offW = [&](int t) { return bW + (unsigned)kseq(t) * kW; };
      auto ldA = [&](int t) { dma4<4096>(rA, vA, offA(t), pcA, aslot(t)); };
      auto ldWh = [&](int t, unsigned o, int hf) { dma3<8192>(mk_rsrc(pW, zW), vW, o + (hf ? hW : 0u), pcW, wslot(t) + hf * 4096); };
      const bool slowj = l0f && SLOWC(0, j);
      auto packedW = [&]() {
        pW = p.ws; zW = WS_SPAN; bW = WS_WP + (unsigned)j * (16 * 24576); kW = 24576; pcW = 8192; hW = 4096; vW = vlin;
      };
      auto ldW = [&](int t) { const unsigned o = offW(t); ldWh(t, o, 0); ldWh(t, o, 1); };
      constexpr auto spread = [](int n) { unsigned m = 0; for (int t = 0; t < 26; ++t) if ((t + 1) * n / 13 > t * n / 13) m |= 1u << t; return m; };
      constexpr unsigned sp4 = spread(4), sp8 = spread(8), sp12 = spread(12);
      const unsigned smask = (isb ? (l == 3 ? sp4 : sp8) : sp12) >> ((((g + j * 5) & 15) * 13) >> 4);
      const unsigned cbm = isb ? cb2 : cb1;
      auto clean = [&](int t) __attribute__((always_inline)) {
        if (t < 13 && ((smask >> t) & 1)) {
          const unsigned cc = __builtin_popcount(smask & ((1u << t) - 1));
          const unsigned cb = cb0 + (cc >= 4 ? cbm - cb0 : 0u) + (cc >= 8 ? cb2 - cbm : 0u), pc = (cc + g + j) & 3;
          st16_sc1(rs, (lane >> 4) * 4096 + (lane & 15) * 16, cb + hw * 1024 + pc * 256, sent4);
        }
      };
      unsigned flo;
      unsigned ready = ~0u;
      u32x4 cv[4];
#pragma unroll
      for (int ii = 0; ii < 4; ++ii) asm volatile("" : "=v"(cv[ii]));
      auto chkrd = [&](int t) {
#pragma unroll
        for (int ii = 0; ii < 4; ++ii) cv[ii] = lds_ld<u32x4>(aslot(t) + lin + ii * 4096);
      };
      auto chkbad = [&]() { return __any(max(umax4v(umax4v(0u, cv[0]), cv[1]), umax4v(umax4v(0u, cv[2]), cv[3])) == SENT) && !l0f; };
      auto refetch = [&](int t) __attribute__((always_inline)) {
        for (unsigned spins = 0; !dead; ++spins) {
          if (spins > SPIN_CAP) { dead = true; break; }
          WAITV(0);
          TRV(t == 1 ? 56 : 57, spins + 1);
          ldA(t);
          ldA(t + 1);
          WAITV(0);
          chkrd(t);
          if (!chkbad()) break;
          __builtin_amdgcn_s_sleep(1);
        }
      };
      f32x4 jc[4];
#pragma unroll
      for (int i = 0; i < 4; ++i) asm volatile("" : "=v"(jc[i]));
      auto csld = [&]() {
#pragma unroll
        for (int i = 0; i < 4; ++i)
          ld16_asm(jc[i], rs, (lane >> 4) * 768 + hw * 192 + ((lane & 15) < 12 ? (lane & 15) : 0) * 16, WS_CS + (unsigned)((l * 16 + j) * 16 + i * 4) * 768);
      };
      auto csred = [&]() {
#pragma unroll
        for (int i = 0; i < 4; ++i) OPAQUE_V(jc[i]);
        const f32x4 s = (jc[0] + jc[1]) + (jc[2] + jc[3]);
        auto r = SWAP16(s[0], s[1]), q = SWAP16(s[2], s[3]);
        r = SWAP32(__uint_as_float(r[0]) + __uint_as_float(r[1]), __uint_as_float(q[0]) + __uint_as_float(q[1]));
        if ((lane & 15) < 12) lds_st<float>(lds0 + L_CS + l * 768 + hw * 192 + (lane & 15) * 16 + (lane >> 4) * 4, __uint_as_float(r[0]) + __uint_as_float(r[1]));
      };
      if (l0f) {
        u32x2 gw2;
        ld8_asm(gw2, rs, WS_EPOCH + 0u * lin);
        TRC(44); BAR;
        ldW(0); ldA(0);
        dma1(mk_rsrc(p.wphi, 32 * 2048), vlin, j * 4096, lds0 + L_WPHI + hw * 1024);
        ldW(1);
        WAITV(17);
        OPAQUE_V(gw2);
        const u64 gword = ((u64)(unsigned)UNI(gw2[1]) << 32) | (unsigned)UNI(gw2[0]);
        u64 z = gword + 0x9e3779b97f4a7c15ull;
        z = (z ^ (z >> 30)) * 0xbf58476d1ce4e5b9ull;
        z = (z ^ (z >> 27)) * 0x94d049bb133111ebull;
        z = z ^ (z >> 31);
        z = (z == gword) ? ~z : z;
        epoch = u32x2{(unsigned)z, (unsigned)(z >> 32)};
        tr = (j - kb) & 15;
        WAITV(7);
        TRC(1); BAR;
        ldA(1);
      } else {
        ldW(1);
        if (l == 1) csld();
        else __builtin_amdgcn_s_sleep(XD);
        ldA(1);
      }
      ldA(2);
      TRC(40);
      ldW(2);
#pragma unroll
      for (int i = 0; i < 4; ++i) asm volatile("" : "=v"(xr[i >> 1][i & 1]));
      unsigned oA = offA(3), oW = 0;
      OPAQUE_S(oA);
      TRV(56, 0); TRV(57, 0);
      ra0 = lds0 + L_R2 + (hw * 32 + (lane & 15)) * 128 + (((((lane >> 4) & 1) * 2 + (lane >> 5)) ^ ((lane >> 1) & 7)) * 16);
      OPAQUE_V(ra0);
      ldres(0);
      clean(0);
      if (l == 1) { WAITV(14); csred(); }
      WAITV(10);
      chkrd(1);
      if (__builtin_expect(chkbad(), 0)) refetch(1);
      const int fr = lane & 15, fq = lane >> 4;
      u32x2 fl;
      f32x4 js[4];
      float rb[4];
      const bool slowo = SLOWC(2 + (l & 1), j);
      const char* pO;
      unsigned zO, bO, pcO, vO, clo;
      u32x4 wo2[2];
      asm volatile("" : "=v"(wo2[0])); asm volatile("" : "=v"(wo2[1]));
      const unsigned fwo = myrep + fl_line(FL_WO + (isb ? 1 : 0), jr + (lane >> 4)) + (lane & 15) * 8;
      static_for<1, 14>([&](auto t_c) __attribute__((always_inline)) {
        constexpr int t = decltype(t_c)::value;
        constexpr auto rest = [](int s) { return 6 + (s == NVAL - 2 || s == 5 || s == 11) + (s == 8 ? 4 : 0); };
        constexpr int NW = t == 1 ? 63 : 4 + rest(t - 1);
        TRC(1 + t);
        if (t >= NVAL && t <= 6 && __builtin_expect((ready & (1u << kseq(t + 2))) == 0, 0)) {
          WAITV(NW); WAITL0; BAR;
          ready = poll_need(rs, flo, epoch, ready, 1u << kseq(t + 2), dead);
          dma4<4096>(rA, vA, oA, pcA, aslot(t + 2));
        } else if (t == 7 && __builtin_expect(ready != ~0u, 0)) {
          WAITV(NW); WAITL0; BAR;
          poll_all(rs, flo, epoch, dead);
          dma4<4096>(rA, vA, oA, pcA, aslot(t + 2));
        } else {
          bar_dma4<4096, NW>(rA, vA, oA, pcA, aslot(t + 2));
        }
        if (t == 1 && lead) set_flags(rs, fl_line(FL_X + l, g), j, l0f ? fl_line(FL_CS, j) : fl_line(FL_X + l, g), l0f ? g : j, epoch, lane);
        if (t == 7 && slowj) packedW();
        oW = offW(t + 2);
        if (t < 13) oA = offA(t + 3);
        OPAQUE_S(oA); OPAQUE_S(oW);
        clean(t);
        if (t <= NVAL && !l0f) { WAITV(rest(t - 1) + 4); chkrd(t + 1); }
        if (t == WT + 3 && l0f && SLOWC(2, jr + hw)) {
          OPAQUE_V(wo2[0]); OPAQUE_V(wo2[1]);
#pragma unroll
          for (int i = 0; i < 2; ++i) st16_sc1(rs, lin, WS_WO + (unsigned)((jr + hw) * 131072 + g * 8192 + ((j & 3) * 2 + i) * 1024), wo2[i]);
        }
        if (t == WT + 6 && l0f && lead) set_flags(rs, fl_line(FL_WO, j), g, fl_line(FL_WO, j), g, epoch, lane);
        ldres(t);
        if (t == 8) {
#pragma unroll
          for (int i = 0; i < 4; ++i) ld16_asm(js[i], rs, fq * 1024 + hw * 256 + fr * 16, st_off(g, l) + i * 4096);
        }
        SCHED;
        ldWh(t + 2, oW, 0);
        SCHED;
        bool bad = false;
        if (t <= NVAL) bad = chkbad();
        if (t == 1) {
          const unsigned fx = fl_line(FL_X + l, g), fc = fl_line(FL_CS, jr) + ((j + fq) & 3) * 128;
          flo = myrep + ((l0f && lane >= 16) ? fc : fx) + fr * 8;
          OPAQUE_V(flo);
        }
        if (t == 9) {
          pO = slowo ? p.ws : (const char*)(isb ? p.woB : p.woA);
          zO = slowo ? WS_SPAN : 1024 * 2048; bO = slowo ? WS_WO + (unsigned)((isb ? 16 : 0) + j) * 131072 : j * 128; pcO = slowo ? 4096 : 32 * 2048;
          vO = slowo ? vlin : (hw * 8 + (lane >> 3)) * 2048 + wo_swz(lane);
          clo = myrep + ((lane & 16) ? fl_line(FL_CLC + l, (lane & 32) ? j ^ RX : j) : fl_line(FL_CLR + l, g)) + fr * 8;
          OPAQUE_S(pO); OPAQUE_S(zO); OPAQUE_S(bO); OPAQUE_S(pcO); OPAQUE_V(vO); OPAQUE_V(clo);
        }
        if (t == 10 && l0f) csred();
        if (t == 11) {
#pragma unroll
          for (int i = 0; i < 4; ++i) OPAQUE_V(js[i]);
          const f32x4 s = (js[0] + js[1]) + (js[2] + js[3]);
          const auto r = SWAP16(s[0], s[2]), q = SWAP16(s[1], s[3]);
          rb[0] = __uint_as_float(r[0]) + __uint_as_float(r[1]); rb[1] = __uint_as_float(q[0]) + __uint_as_float(q[1]);
          OPAQUE_V(rb[0]); OPAQUE_V(rb[1]);
        }
        if (t == 12) {
          const auto r = SWAP32(rb[0], rb[0]), q = SWAP32(rb[1], rb[1]);
          const float m = (__uint_as_float(r[0]) + __uint_as_float(r[1])) * (-1.0f / 1024), q2 = __uint_as_float(q[0]) + __uint_as_float(q[1]);
          const float rstd = __builtin_amdgcn_rsqf(fmaxf(__builtin_fmaf(q2, 1.0f / 1024, -m * m), 0.f) + 1e-5f);
          lds_st<float>(lds0 + L_MEAN + 64 + hw * 128 + fr * 8 + (fq & 1) * 4 + (fq >> 1) * (L_RSTD - L_MEAN), (lane & 32) ? rstd : m);
        }
        SCHED;
        ldWh(t + 2, oW, 1);
        if (t == NVAL - 1 || t == 6) {
          WAITV(10);
          OPAQUE_V(fl);
          if (t == 6) ready = __all(is_set(fl, epoch)) ? ~0u : 0u;
          else {
            ready = l0f ? ~0u : (unsigned)__ballot(is_set(fl, epoch));
            if (slowj && __all(is_set(fl, epoch))) packedW();
          }
        }
        if (t == 12) { WAITV(10); OPAQUE_V(fl); }
        if (t == 13 && slowo && __builtin_expect(!__all(is_set(fl, epoch)), 0)) poll_all(rs, fwo, epoch, dead);
        if (t == NVAL - 2 || t == 5) ld8_asm(fl, rs, flo);
        if (t == 11) ld8_asm(fl, rs, fwo);
        if (t == 7 && l0f) csld();
        if (t == WT && l0f) {
          if (hw == 0) touch(mk_rsrc(p.ws, NREP * REP_STRIDE), min((g * 16 + j) * 9 + (lane < 9 ? lane : 0), (int)(NREP * REP_STRIDE / 128 - 1)) * 128, lds0 + L_SINK);
#pragma unroll
          for (int i = 0; i < 2; ++i) ld16in_asm(wo2[i], mk_rsrc(p.woA, 1024 * 2048), (lane >> 3) * 2048 + wo_swz(lane), (g * 64 + (j & 3) * 16 + i * 8) * 2048 + (jr + hw) * 128);
        }
        if constexpr (t <= NVAL) { if (__builtin_expect(bad, 0)) refetch(t + 1); }
      });
      auto stO = [&](int i) { dma4<4096>(mk_rsrc(pO, zO), vO, bO + wo_src(i, j) * (4 * pcO), pcO, lds0 + wo_slot(i) + hw * 1024); };
      auto clcheck = [&]() {
        OPAQUE_V(fl);
        TRV(58, 0);
        if (__builtin_expect(!__all(is_set(fl, epoch)), 0)) { TRV(58, 1); poll_all(rs, clo, epoch, dead); }
      };
      auto accinit = [&]() {
#pragma unroll
        for (int m = 0; m < 2; ++m)
#pragma unroll
          for (int pp = 0; pp < 2; ++pp) {
            auto r0_ = __builtin_amdgcn_permlane16_swap(xr[m][pp][0], xr[m][pp][2], false, false);
            auto r1_ = __builtin_amdgcn_permlane16_swap(xr[m][pp][1], xr[m][pp][3], false, false);
            acc[m][2 * pp] = f32x4{lo16(r0_[0]), hi16(r0_[0]), lo16(r1_[0]), hi16(r1_[0])};
            acc[m][2 * pp + 1] = f32x4{lo16(r0_[1]), hi16(r0_[1]), lo16(r1_[1]), hi16(r1_[1])};
          }
#pragma unroll
        for (int i = 0; i < 8; ++i) OPAQUE_V(acc[i >> 2][i & 3]);
      };
      WAITV(10);
      WAITL0;
      TRC(15); BAR;
      stO(0);
      if (lead) set_flags(rs, fl_line(FL_CLR + l, g), j, fl_line(FL_CLC + l, j), g, epoch, lane);
      if (isb) {
        stO(2); stO(3);
        WAITV(12);
        TRC(16); BAR;
        stO(1); stO(4); stO(5);
        __builtin_amdgcn_s_setprio(0);
        TRC(17); BAR;
        __builtin_amdgcn_s_sleep(BD);
        stO(7); stO(6);
        ld8_asm(fl, rs, clo);
        accinit();
        WAITV(0);
        clcheck();
      } else {
        u32x4 vb[8];
#pragma unroll
        for (int i = 0; i < 8; ++i) asm volatile("" : "=v"(vb[i]));
        stO(2); stO(3);
        ldres(14);
        WAITV(12);
        WAITL0;
        TRC(16); BAR;
        stO(1);
        ldres(15);
        if (l0f) {
          const bool wj = hw != 0;
          const char* bp = (const char*)(wj ? p.wqkvB : p.woB);
          unsigned rs_ = wj ? 6144 : 2048, sz = rs_ * 1024, ss = rs_ * 8, s2 = wj ? 16 * 6144 : 128;
          unsigned sb = g * 64 * rs_ + (wj ? (hw - 1) * 2048 + JOBC(j, hw - 1) * 128 : (j & 3) * (16 * 2048) + jr * 128);
          OPAQUE_S(bp); OPAQUE_S(rs_); OPAQUE_S(sz); OPAQUE_S(sb); OPAQUE_S(ss); OPAQUE_S(s2);
          const rsrc_t rb = mk_rsrc(bp, sz);
#pragma unroll
          for (int i = 0; i < 8; ++i) ld16in_asm(vb[i], rb, (lane >> 3) * rs_ + (lane & 7) * 16, sb + (i & 1) * ss + (i >> 1) * s2);
        }
        __builtin_amdgcn_s_setprio(0);
        KV_ADDR;
        bf16x8 sel = rowsum_sel(lane);
        unsigned h1 = (lane >> 3) * KVP_G + (lane & 7) * 16;
        unsigned pw = lds0 + SC_PHIK + (h * 128 + wm * 64 + 32 + fr) * 160 + fq * 8, kw = lds0 + L_PREV + hw * 2048 + lin;
        const unsigned wa = lds0 + L_WPHI + h * 2048 + (fq * 4 + (fr >> 2)) * 64 + (lane & 3) * 8;
        bf16x8 w0 = tr2(wa, wa + 1024), w1 = tr2(wa + 32, wa + 32 + 1024);
        float cq[4];
#pragma unroll
        for (int m = 0; m < 2; ++m) { cq[m] = lds_ld<float>(lds0 + L_RSTD + (16 + wm * 64 + 32 + m * 16 + fr) * 4) * 1.4426950408889634f; OPAQUE_V(cq[m]); }
        OPAQUE_V(sel); OPAQUE_V(h1); OPAQUE_V(pw); OPAQUE_V(kw); OPAQUE_V(w0); OPAQUE_V(w1);
        TRC(17); BAR;
        CLOBBER;
        {
          const f32x4 zero = {0.f, 0.f, 0.f, 0.f};
          f32x4 r[4][2];
#pragma unroll
          for (int m = 0; m < 2; ++m) {
            const bf16x8 f = lds_ld<bf16x8>(kw + m * 1024);
            r[m][0] = MFMA(w0, f, zero); r[m][1] = MFMA(w1, f, zero);
          }
          ld8_asm(fl, rs, clo);
          phik2(r, cq, pw);
        }
        WAITL0;
        TRC(19); BAR;
        kvpart(va, pa, pb, kvj + (unsigned)g * KVP_G, lane, wave);
        WAITV(2);
#pragma unroll
        for (int i = 0; i < 8; ++i) OPAQUE_V(vb[i]);
        clcheck();
        TRC(20); BAR;
        __builtin_amdgcn_s_setprio(3);
        stO(4); stO(5);
        const f32x4 zero = {0.f, 0.f, 0.f, 0.f};
        const unsigned s1 = kvj + (unsigned)((j ^ RX) - j) * KVP_J + g * 512 + hw * 128;
        __builtin_amdgcn_s_sleep(H1S);
        u32x4 v[2] = {ld16_sc1(rs, h1, s1), ld16_sc1(rs, h1, s1 + 8 * KVP_G)};
        TRC(21); TRV(50, 0);
        for (unsigned spins = 0; !dead && __any(umax4v(umax4v(0u, v[0]), v[1]) == SENT); ++spins) {
          if (spins > SPIN_CAP) { dead = true; break; }
          CLOBBER;
          TRV(50, spins + 1);
          v[0] = ld16_sc1(rs, h1, s1); v[1] = ld16_sc1(rs, h1, s1 + 8 * KVP_G);
        }
        TRC(22);
        const f32x4 c = rowsum_fin(MFMA(sel, as_frag(v[1]), MFMA(sel, as_frag(v[0]), zero)));
        if ((lane & 40) == 0)
          st8_sc1(rs, fr * 16 + fq * 8, kvj + (unsigned)g * KVP_G + KVP_SUM + hw * 128, nosent2(c));
        CLOBBER;
        __builtin_amdgcn_s_setprio(0);
        TRC(43); BAR;
        SCHED;
        accinit();
        stO(6); stO(7);
        if (l0f) {
          if (hw) {
            job_colsum(rs, vb, 1, g, JOBC(j, hw - 1), hw - 1, lane);
            if (SLOWC(1, JOBC(j, hw - 1))) {
#pragma unroll
              for (int i = 0; i < 8; ++i) job_pack(rs, vb[i], i, 1, g, JOBC(j, hw - 1), hw - 1, lane);
            }
          } else {
#pragma unroll
            for (int i = 0; i < 8; ++i)
              if (SLOWC(3, jr + (i >> 1))) st16_sc1(rs, (lane >> 3) * 128 + wo_swz(lane), WS_WO + (unsigned)((16 + jr + (i >> 1)) * 131072 + g * 8192 + ((j & 3) * 2 + (i & 1)) * 1024), vb[i]);
          }
        }
        WAITV(0);
      }
    }
    {
      LOCALS;
      const int fr = lane & 15, fq = lane >> 4;
      const int kh = half;
      unsigned v0 = lane * 16 + hw * 4096 + kh * 2048, v1 = lane * 16 + hw * 4096 + (kh ^ 1) * 2048;
      const int r0 = (JROT(j) >> 1) + NJ / 2;
      const unsigned ob0 = ob_off(g, kh, l);
      const unsigned pko = myrep + fl_line(FL_PK, JOBC(j, fq - 1)) + fr * 8;
      u32x4 R[8][4];
      u32x2 pkf;
      auto issue_ = [&](int i) {
        const unsigned so = ob0 + (unsigned)((r0 + i) & 7) * (2 * TS);
        R[i][0] = ld16_sc1(rs, v0, so); R[i][1] = ld16_sc1(rs, v1, so);
        R[i][2] = ld16_sc1(rs, v0 + 1024, so); R[i][3] = ld16_sc1(rs, v1 + 1024, so);
      };
      auto issue = [&](int i) { issue_(i); SCHED; };
      constexpr int LA = LOOKAHEAD;
      TRV(52, 0); TRV(53, 0); TRV(54, 0);
      TRR(61);
      __builtin_amdgcn_s_setprio(3);
      unsigned so0 = ob0 + (unsigned)(r0 & 7) * (2 * TS);
      OPAQUE_V(v0); OPAQUE_V(v1); OPAQUE_S(so0);
      TRC(25); BAR;
      SCHED;
      R[0][0] = ld16_sc1(rs, v0, so0); R[0][1] = ld16_sc1(rs, v1, so0);
      R[0][2] = ld16_sc1(rs, v0 + 1024, so0); R[0][3] = ld16_sc1(rs, v1 + 1024, so0);
      SCHED;
      __builtin_amdgcn_s_setprio(2);
      if (l == 0 && lead) set_flags(rs, fl_line(FL_PK, j), g, fl_line(FL_WO + 1, j), g, epoch, lane);

      auto vmax = [&](int i) { return umax4v(umax4v(umax4v(umax4v(0u, R[i][0]), R[i][1]), R[i][2]), R[i][3]); };
      auto bad = [&](int i) { return __any(umax4v(umax4v(umax4v(umax4v(0u, R[i][0]), R[i][1]), R[i][2]), R[i][3]) == SENT); };
      auto poll = [&](int i, int nfl) {
        for (unsigned spins = 0; !dead; ++spins) {
          if (spins > SPIN_CAP) { dead = true; break; }
          __builtin_amdgcn_s_sleep(2);
          CLOBBER;
          TRV(52 + (i > 2 ? 2 : i), spins + 1);
          issue(i);
#pragma unroll
          for (int k = i + 1; k < i + nfl && k < 8; ++k)
            if (bad(k)) issue(k);
          WAITV(0);
#pragma unroll
          for (int k = i + 1; k < i + nfl && k < 8; ++k)
#pragma unroll
            for (int f = 0; f < 4; ++f) OPAQUE_V(R[k][f]);
          if (!bad(i)) break;
        }
      };
      unsigned bb[4];
      {
        const int kr = fq * 4 + (fr >> 2);
        const int gs = (kr >> 1) & 3;
#pragma unroll
        for (int c = 0; c < 4; ++c) bb[c] = lds0 + kh * 8192 + kr * 128 + ((c ^ gs) * 32) + (lane & 3) * 8;
      }
      bf16x8 bf[2][4];
      auto rdB = [&](int i, int hh) {
        if (hh == 0) {
#pragma unroll
          for (int c = 0; c < 4; ++c) { bb[c] += wo_slot(i) - (i ? wo_slot(i - 1) : 0u); asm("" : "+v"(bb[c])); }
        }
#pragma unroll
        for (int c = 0; c < 4; ++c) bf[hh][c] = tr2(bb[c] + hh * 4096, bb[c] + hh * 4096 + 2048);
      };
      auto mms = [&](int i, int hh) {
#pragma unroll
        for (int n = 0; n < 4; ++n)
#pragma unroll
          for (int m = 0; m < 2; ++m) acc[m][n] = MFMA(bf[hh][n], as_frag(R[i][hh * 2 + m]), acc[m][n]);
      };
      if (!half) {
#pragma unroll
        for (int i = 0; i < 8; ++i) acc[i >> 2][i & 3] = f32x4{0.f, 0.f, 0.f, 0.f};
      }
      const char* obase = (l == 3) ? (const char*)p.out : (const char*)p.ws;
      unsigned osize = (l == 3) ? 2048 * 2048 : WS_SPAN;
      const bool slown = SLOWC((l & 1) ^ 1, j);
      pW = slown ? p.ws : (const char*)(isb ? p.wqkvA : p.wqkvB);
      bW = slown ? WS_WP + (unsigned)((isb ? 0 : 16) + j) * (16 * 24576) : j * 128;
      kW = slown ? 24576 : 64 * 6144; zW = slown ? WS_SPAN : 1024 * 6144; pcW = slown ? 8192 : 2048; hW = slown ? 4096 : 32 * 6144;
      vA = hw * 1024 + lane * 16;
      vW = slown ? vA : (hw * 8 + (lane >> 3)) * 6144 + (((lane & 7) ^ ((((lane >> 4) & 1) | ((hw & 1) << 1)) << 1)) * 16);
      pA = p.ws; zA = WS_SPAN; bA = xb_off(g, l); kA = TS; pcA = 4096; kb = JROT(j) + KX; dj = (j - kb) & 15;
      OPAQUE_S(pW); OPAQUE_S(bW); OPAQUE_S(kW); OPAQUE_S(zW); OPAQUE_S(pcW); OPAQUE_S(hW); OPAQUE_V(vA); OPAQUE_V(vW); OPAQUE_S(bA);
      unsigned sq = bW + j * kW + kh * hW, dst = lds0 + L_R1 + kh * 4096 + hw * 1024;
      OPAQUE_S(obase); OPAQUE_S(osize); OPAQUE_S(sq); OPAQUE_S(dst);
      const rsrc_t ro = mk_rsrc(obase, osize);
      unsigned so[2];
      unsigned xw = lds0 + SC_XCH + wave * 4096 + lane * 16;
      unsigned xa = lds0 + SC_XCH + (wave ^ 4) * 4096 + lane * 16;
      unsigned sb0 = (l == 3) ? g * (128 * 2048) + j * 128 : xb_off(g, l) + j * TS;
      {
        const int row = (hw * 2 + kh) * 16 + fr, ch = (fq & 1) * 2 + (fq >> 1);
        const unsigned sw = (l == 3) ? 0u : (unsigned)((row >> 1) & 7), rb = (unsigned)row << ((l == 3) ? 11 : 7);
#pragma unroll
        for (int pp = 0; pp < 2; ++pp) { so[pp] = rb + (((ch + 4 * pp) ^ sw) * 16); OPAQUE_V(so[pp]); }
        OPAQUE_V(xa); OPAQUE_V(xw); OPAQUE_S(sb0);
      }
      SCHED;
#pragma unroll
      for (int i = 1; i < LA; ++i) issue(i);
      rdB(0, 0);
      unsigned mx = vmax(0);
#pragma unroll
      for (int i = 0; i < 8; ++i) {
        SCHED;
        if (__builtin_expect(__any(mx == SENT), 0)) poll(i, LA);
        if (i == 0) TRC(27); else TRQ(27 + i);
        if (i == W1S - 2) { pkf = ld8_sc1(rs, pko, 0); SCHED; }
        if (i == W1S) {
          OPAQUE_V(pkf);
          if (l == 0 && lead && !__all(is_set(pkf, epoch))) poll_all(rs, pko, epoch, dead);
          TRC(35); BAR;
        }
        if (i == 7 && l < 3) dma3<8192>(mk_rsrc(pW, zW), vW, sq, pcW, dst);
        if (i < 4 && !isb) __builtin_amdgcn_s_sleep(PACE);
        if (i == 2) __builtin_amdgcn_s_setprio(1);
        if (i == 5) __builtin_amdgcn_s_setprio(0);
        SCHED;
        rdB(i, 1);
        if (i == 7) break;
        if (i + LA < 8) issue_(i + LA);
        mms(i, 0);
#pragma unroll
        for (int k = 0; k < 4; ++k) { SGB(0x008, 1); SGB(0x100, 1); SGB(0x020, 1); SGB(0x008, 1); SGB(0x100, 1); }
        SCHED;
        rdB(i + 1, 0); mx = vmax(i + 1);
        mms(i, 1);
#pragma unroll
        for (int k = 0; k < 4; ++k) { SGB(0x008, 1); SGB(0x100, 2); }
#pragma unroll
        for (int k = 4; k < 8; ++k) { SGB(0x008, 1); SGB(0x002, 8 / (8 - 4)); }
      }
#pragma unroll
      for (int n = 0; n < 4; ++n) acc[1][n] = MFMA(bf[0][n], as_frag(R[7][1]), acc[1][n]);
#pragma unroll
      for (int n = 0; n < 4; ++n) acc[0][n] = MFMA(bf[0][n], as_frag(R[7][0]), acc[0][n]);
#pragma unroll
      for (int k = 0; k < 8; ++k) { SGB(0x008, 1); SGB(0x100, 1); }
      SCHED;
#pragma unroll
      for (int n = 0; n < 4; ++n) acc[1][n] = MFMA(bf[1][n], as_frag(R[7][3]), acc[1][n]);
#pragma unroll
      for (int n = 0; n < 4; ++n) acc[0][n] = MFMA(bf[1][n], as_frag(R[7][2]), acc[0][n]);
#pragma unroll
      for (int n = 0; n < 4; ++n) lds_st<f32x4>(xw + n * 1024, acc[1][n]);
      SGB(0x008, 4);
#pragma unroll
      for (int k = 0; k < 4; ++k) { SGB(0x008, 1); SGB(0x200, 1); }
      SCHED;
      TRC(36);
      WAITL0;
      TRC(37); BAR;
      SCHED;
      f32x4 pa[4];
#pragma unroll
      for (int n = 0; n < 4; ++n) pa[n] = lds_ld<f32x4>(xa + n * 1024);
      SCHED;
      u32x4 o[2];
#pragma unroll
      for (int pp = 0; pp < 2; ++pp) {
        const f32x4 a = acc[0][2 * pp] + pa[2 * pp], b = acc[0][2 * pp + 1] + pa[2 * pp + 1];
        auto r0_ = __builtin_amdgcn_permlane16_swap(pk2(a[0], a[1]), pk2(b[0], b[1]), false, false);
        auto r1_ = __builtin_amdgcn_permlane16_swap(pk2(a[2], a[3]), pk2(b[2], b[3]), false, false);
        o[pp] = nosent(u32x4{r0_[0], r1_[0], r0_[1], r1_[1]});
        st16_sc1(ro, so[pp], sb0, o[pp]);
      }
      if (l < 3) {
        lds_st<u32x4>(lds0 + L_R2 + so[0], o[0]);
        lds_st<u32x4>(lds0 + L_R2 + so[1], o[1]);
        const f32x4 zero = {0.f, 0.f, 0.f, 0.f};
        const bf16x8 ones = {0x3f80, 0x3f80, 0x3f80, 0x3f80, 0x3f80, 0x3f80, 0x3f80, 0x3f80};
        const f32x4 a = MFMA(ones, as_frag(o[1]), MFMA(ones, as_frag(o[0]), zero));
        const f32x4 b = MFMA(as_frag(o[1]), as_frag(o[1]), MFMA(as_frag(o[0]), as_frag(o[0]), zero));
        const float q = (fr & 2) ? ((fr & 1) ? b[3] : b[2]) : ((fr & 1) ? b[1] : b[0]);
        if (fq == (fr >> 2)) st8_sc1(rs, ((hw * 2 + kh) * 16 + fr) * 8, st_off(g, l + 1) + j * 1024, u32x2{__float_as_uint(a[0]), __float_as_uint(q)});
      } else if (lead && blockIdx.x == 0 && lane == 0) {
        st8_sc1(rs, WS_EPOCH, 0, epoch);
      }
    }
    TRC(38); TRR(63);
  }
  STAMP_DUMP;
  asm volatile(".Lpf_end:");
}

extern "C" void kernel_launch(void* const* d_in, const int* in_sizes, int n_in, void* d_out, int out_size,
                              void* d_ws, size_t ws_size, hipStream_t stream) {
  Params p;
  p.x = (const u16*)d_in[0];
  p.wqkvA = (const u16*)d_in[1];
  p.wphi = (const u16*)d_in[2];
  p.woA = (const u16*)d_in[3];
  p.wqkvB = (const u16*)d_in[5];
  p.woB = (const u16*)d_in[6];
  p.out = (u16*)d_out;
  p.ws = (char*)d_ws + ((ws_size - WS_SPAN) & ~(size_t)0xffff);
  (void)hipFuncSetAttribute((const void*)mega_kernel, hipFuncAttributeMaxDynamicSharedMemorySize, L_TOTAL);
  mega_kernel<<<256, 512, L_TOTAL, stream>>>(p);
}
```

```cpp
#include <hip/hip_runtime.h>
#include <stdint.h>
#include <type_traits>
constexpr int LOOKAHEAD = 2;
constexpr int NVAL = 4;
constexpr int WT = 1;
constexpr int W1S = 6;
constexpr int NJ = 8;
constexpr int KX = 8;
constexpr int RX = 8;
constexpr unsigned TS = 17664;
#define XD 3
#define H1S 6
#define H2D 5
#define BD 8
#define PACE 4

typedef unsigned short u16;
typedef unsigned long long u64;
typedef __attribute__((ext_vector_type(8))) short bf16x8;
typedef __attribute__((ext_vector_type(4))) short s16x4;
typedef __attribute__((ext_vector_type(4))) float f32x4;
typedef __attribute__((ext_vector_type(2))) float f32x2;
typedef __attribute__((ext_vector_type(4))) unsigned u32x4;
typedef __attribute__((ext_vector_type(2))) unsigned u32x2;
typedef __attribute__((ext_vector_type(2))) __bf16 bf16x2_t;
#define LAS __attribute__((address_space(3)))
typedef LAS s16x4* lds_s16x4_ptr;

constexpr unsigned NREP = 8, REP_STRIDE = 17 * 2048;
enum { FL_X = 0, FL_CS = 4, FL_CLR = 5, FL_CLC = 9, FL_PK = 13, FL_WO = 14 };
__device__ __forceinline__ unsigned fl_line(int set, int a) { return (unsigned)(set * 16 + a) * 128; }
constexpr unsigned WS_EPOCH = 512u << 10;
constexpr unsigned WS_CS = 1u << 20;
constexpr unsigned WS_KVP = 4u << 20;
constexpr unsigned KVP_G = 32768, KVP_J = 16 * KVP_G, KVP_L = 16384, KVP_Z = 8192, KVP_SUM = 8704, KVP_HALO = 12u << 10;
constexpr unsigned SENT = 0xffffffffu;
constexpr unsigned WS_X = 14u << 20, WS_O = 29u << 20, WS_ST = 2u << 20;
__device__ __forceinline__ unsigned xb_off(int g, int l) { return WS_X + (unsigned)(l * 16 + g) * (16 * TS); }
__device__ __forceinline__ unsigned ob_off(int g, int j, int l) { return WS_O + (unsigned)((l * 16 + g) * 16 + j) * TS; }
__device__ __forceinline__ unsigned st_off(int g, int l) { return WS_ST + (unsigned)(l * 16 + g) * (16u << 10); }
constexpr unsigned WS_WP = 48u << 20;
constexpr unsigned WS_WO = 60u << 20;
constexpr unsigned WS_SPAN = 64u << 20;

constexpr unsigned L_R1 = 0;
constexpr unsigned L_R2 = 98304, XS = 16384;
constexpr unsigned L_MISC = 147456;
constexpr unsigned L_MEAN = L_MISC;
constexpr unsigned L_RSTD = L_MISC + 576;
constexpr unsigned L_CS = L_MISC + 1152;
constexpr unsigned L_WPHI = L_MISC + 2688;
constexpr unsigned L_SINK = L_MISC + 6912;
constexpr unsigned L_ZF = L_MISC + 7168;
constexpr unsigned L_PREV = L_MISC + 8192;
constexpr unsigned L_TOTAL = 163840;

#define JROT(j) ((j) & ~(NJ - 1))
constexpr int NX = NJ / 2;
#define KB0(g, j) (JROT(j) + (16 / NX) * ((g) & (NX - 1)))
#define JOBC(j, w) (((j) & ~3) + (((j) + (w) + 1) & 3))

#define SLOWC(m, jc) ((slowm >> ((m) * 8 + ((jc) & 7))) & 1)

struct Params {
  const u16* x;
  const u16* wqkvA;
  const u16* wphi;
  const u16* woA;
  const u16* wqkvB;
  const u16* woB;
  u16* out;
  char* ws;
};

#define SCHED __builtin_amdgcn_sched_barrier(0)
#define BAR __builtin_amdgcn_s_barrier()
#define SGB(m, n) __builtin_amdgcn_sched_group_barrier(m, n, 0)
#define WAITV(n) asm volatile("s_waitcnt vmcnt(%0)" ::"n"(n) : "memory")
#define WAITL0 asm volatile("s_waitcnt lgkmcnt(0)" ::: "memory")

__device__ __forceinline__ unsigned pk2(float a, float b) {
  f32x2 v = {a, b};
  return __builtin_bit_cast(unsigned, __builtin_convertvector(v, bf16x2_t));
}
__device__ __forceinline__ float lo16(unsigned u) { return __uint_as_float(u << 16); }
__device__ __forceinline__ float hi16(unsigned u) { return __uint_as_float(u & 0xffff0000u); }

template <int A, int B, typename F> __device__ __forceinline__ void static_for(F f) {
  if constexpr (A < B) { f(std::integral_constant<int, A>{}); static_for<A + 1, B>(f); }
}
typedef __amdgpu_buffer_rsrc_t rsrc_t;
__device__ __forceinline__ rsrc_t mk_rsrc(const void* p, unsigned bytes) { return __builtin_amdgcn_make_buffer_rsrc((void*)p, 0, bytes, 0x00020000); }
#define UNI(x) __builtin_amdgcn_readfirstlane(x)
#define DMA_HEAD "s_mov_b32 m0, %4\n\ts_mov_b32 %0, %3\n\ts_nop 2\n\tbuffer_load_dwordx4 %1, %2, %0 offen sc1 lds\n\t"
#define DMA_NEXT "s_add_u32 m0, m0, %6\n\ts_add_u32 %0, %0, %5\n\tbuffer_load_dwordx4 %1, %2, %0 offen sc1 lds\n\t"
#define DMA_OPS : "=&s"(t) : "v"(voff), "s"(r), "s"(UNI(soff)), "s"(UNI(lds)), "s"(UNI(sstep)), "i"(LSTEP) : "memory", "scc", "m0"
template <int LSTEP> __device__ __forceinline__ void dma4(rsrc_t r, unsigned voff, unsigned soff, unsigned sstep, unsigned lds) {
  unsigned t;
  asm volatile(DMA_HEAD DMA_NEXT DMA_NEXT DMA_NEXT DMA_OPS);
}
template <int LSTEP> __device__ __forceinline__ void dma3(rsrc_t r, unsigned voff, unsigned soff, unsigned sstep, unsigned lds) {
  unsigned t;
  asm volatile(DMA_HEAD DMA_NEXT DMA_NEXT DMA_OPS);
}
template <int LSTEP, int N> __device__ __forceinline__ void bar_dma4(rsrc_t r, unsigned voff, unsigned soff, unsigned sstep, unsigned lds) {
  unsigned t;
  asm volatile("s_mov_b32 m0, %4\n\ts_mov_b32 %0, %3\n\ts_nop 0\n\ts_waitcnt vmcnt(%7) lgkmcnt(0)\n\ts_barrier\n\t"
               "buffer_load_dwordx4 %1, %2, %0 offen sc1 lds\n\t" DMA_NEXT DMA_NEXT DMA_NEXT
               : "=&s"(t) : "v"(voff), "s"(r), "s"(UNI(soff)), "s"(UNI(lds)), "s"(UNI(sstep)), "i"(LSTEP), "n"(N) : "memory", "scc", "m0");
}
__device__ __forceinline__ void dma1(rsrc_t r, unsigned voff, unsigned soff, unsigned lds) {
  asm volatile("s_mov_b32 m0, %3\n\ts_nop 3\n\tbuffer_load_dwordx4 %0, %1, %2 offen sc1 lds" ::"v"(voff), "s"(r), "s"(UNI(soff)), "s"(UNI(lds)) : "memory", "m0");
}
__device__ __forceinline__ void touch(rsrc_t r, unsigned voff, unsigned lds) {
  asm volatile("s_mov_b32 m0, %2\n\ts_nop 3\n\tbuffer_load_dword %0, %1, 0 offen lds" ::"v"(voff), "s"(r), "s"(UNI(lds)) : "memory", "m0");
}
__device__ __forceinline__ void ld8_asm(u32x2& d, rsrc_t r, unsigned voff) {
  asm volatile("s_nop 4\n\tbuffer_load_dwordx2 %0, %1, %2, 0 offen sc1" : "=v"(d) : "v"(voff), "s"(r) : "memory");
}

__device__ __forceinline__ void ld16in_asm(u32x4& d, rsrc_t r, unsigned voff, unsigned soff) {
  asm volatile("s_nop 4\n\tbuffer_load_dwordx4 %0, %1, %2, %3 offen" : "=v"(d) : "v"(voff), "s"(r), "s"(UNI(soff)) : "memory");
}
__device__ __forceinline__ void ld16_asm(f32x4& d, rsrc_t r, unsigned voff, unsigned soff) {
  asm volatile("s_nop 4\n\tbuffer_load_dwordx4 %0, %1, %2, %3 offen sc1" : "=v"(d) : "v"(voff), "s"(r), "s"(UNI(soff)) : "memory");
}

template <typename T> __device__ __forceinline__ T lds_ld(unsigned a) { return *(LAS T*)(a); }
template <typename T> __device__ __forceinline__ void lds_st(unsigned a, T v) { *(LAS T*)(a) = v; }
__device__ __forceinline__ bf16x8 tr2(unsigned a0, unsigned a1) {
  s16x4 v0 = __builtin_amdgcn_ds_read_tr16_b64_v4i16((lds_s16x4_ptr)(a0));
  s16x4 v1 = __builtin_amdgcn_ds_read_tr16_b64_v4i16((lds_s16x4_ptr)(a1));
  return __builtin_shufflevector(v0, v1, 0, 1, 2, 3, 4, 5, 6, 7);
}
__device__ __forceinline__ bf16x8 as_frag(u32x4 v) { return __builtin_bit_cast(bf16x8, v); }
#define MFMA(a, b, c) __builtin_amdgcn_mfma_f32_16x16x32_bf16(a, b, c, 0, 0, 0)

__device__ __forceinline__ float vmax2(float a, float b) { float inf = __builtin_inff(); asm("" : "+s"(inf)); return __builtin_amdgcn_fmed3f(a, b, inf); }
#define SWAP16(a, b) __builtin_amdgcn_permlane16_swap(__float_as_uint(a), __float_as_uint(b), false, false)
#define SWAP32(a, b) __builtin_amdgcn_permlane32_swap(__float_as_uint(a), __float_as_uint(b), false, false)
__device__ __forceinline__ void red2_max(float& a, float& b) {
  auto r = SWAP16(a, b);
  float x = vmax2(__uint_as_float(r[0]), __uint_as_float(r[1]));
  r = SWAP32(x, x);
  x = vmax2(__uint_as_float(r[0]), __uint_as_float(r[1]));
  r = SWAP16(x, x);
  a = __uint_as_float(r[0]); b = __uint_as_float(r[1]);
}
__device__ __forceinline__ void red2_add(float& a, float& b) {
  auto r = SWAP16(a, b);
  float x = __uint_as_float(r[0]) + __uint_as_float(r[1]);
  r = SWAP32(x, x);
  x = __uint_as_float(r[0]) + __uint_as_float(r[1]);
  r = SWAP16(x, x);
  a = __uint_as_float(r[0]); b = __uint_as_float(r[1]);
}
template <bool MAX, typename M> __device__ __forceinline__ void red4(float& a, float& b, float& c, float& d, M mid) {
  auto op = [](unsigned x, unsigned y) { return MAX ? vmax2(__uint_as_float(x), __uint_as_float(y)) : __uint_as_float(x) + __uint_as_float(y); };
  auto r = SWAP16(a, b), q = SWAP16(c, d);
  const float x = op(r[0], r[1]), y = op(q[0], q[1]);
  r = SWAP32(x, y);
  const float w = mid(op(r[0], r[1]));
  r = SWAP32(w, w);
  q = __builtin_amdgcn_permlane16_swap(r[1], r[1], false, false);
  r = __builtin_amdgcn_permlane16_swap(r[0], r[0], false, false);
  a = __uint_as_float(r[0]); b = __uint_as_float(r[1]); c = __uint_as_float(q[0]); d = __uint_as_float(q[1]);
}
__device__ __forceinline__ float red_max4(float x) {
  auto r = __builtin_amdgcn_permlane16_swap(__float_as_uint(x), __float_as_uint(x), false, false);
  x = vmax2(__uint_as_float(r[0]), __uint_as_float(r[1]));
  r = __builtin_amdgcn_permlane32_swap(__float_as_uint(x), __float_as_uint(x), false, false);
  return vmax2(__uint_as_float(r[0]), __uint_as_float(r[1]));
}
__device__ __forceinline__ float red_add4(float x) {
  auto r = __builtin_amdgcn_permlane16_swap(__float_as_uint(x), __float_as_uint(x), false, false);
  x = __uint_as_float(r[0]) + __uint_as_float(r[1]);
  r = __builtin_amdgcn_permlane32_swap(__float_as_uint(x), __float_as_uint(x), false, false);
  return __uint_as_float(r[0]) + __uint_as_float(r[1]);
}

__device__ __forceinline__ void st16_sc1(rsrc_t r, unsigned voff, unsigned soff, u32x4 v) {
  __builtin_amdgcn_raw_buffer_store_b128(v, r, voff, soff, 16);
  asm volatile("s_nop 1" ::"v"(v) : "memory");
}
__device__ __forceinline__ void st8_sc1(rsrc_t r, unsigned voff, unsigned soff, u32x2 v) { __builtin_amdgcn_raw_buffer_store_b64(v, r, voff, soff, 16); }
__device__ __forceinline__ u32x4 ld16_sc1(rsrc_t r, unsigned voff, unsigned soff) { return __builtin_amdgcn_raw_buffer_load_b128(r, voff, soff, 16); }
__device__ __forceinline__ u32x2 ld8_sc1(rsrc_t r, unsigned voff, unsigned soff) { return __builtin_amdgcn_raw_buffer_load_b64(r, voff, soff, 16); }
__device__ __forceinline__ f32x4 ld4f_sc1(rsrc_t r, unsigned voff, unsigned soff) { return __builtin_bit_cast(f32x4, __builtin_amdgcn_raw_buffer_load_b128(r, voff, soff, 16)); }
#define OPAQUE_V(x) asm volatile("" : "+v"(x))
#define OPAQUE_S(x) asm volatile("" : "+s"(x))

#define CLOBBER asm volatile("" ::: "memory")
constexpr unsigned SPIN_CAP = 1u << 17;
__device__ __forceinline__ bool is_set(u32x2 v, u32x2 epoch) { return v[0] == epoch[0] && v[1] == epoch[1]; }
__device__ __forceinline__ void set_flags(rsrc_t rs, unsigned lineA, int colA, unsigned lineB, int colB, u32x2 epoch, int lane) {
  const unsigned off = (lane & 7) * REP_STRIDE + ((lane & 8) ? lineB + colB * 8 : lineA + colA * 8);
  if (lane < 16) st8_sc1(rs, off, 0, epoch);
}
__device__ __forceinline__ unsigned poll_need(rsrc_t rs, unsigned voff, u32x2 epoch, unsigned ready, unsigned need, bool& dead) {
  for (unsigned spins = 0; !dead; ++spins) {
    CLOBBER;
    ready |= (unsigned)__ballot(is_set(ld8_sc1(rs, voff, 0), epoch));
    if ((ready & need) == need) break;
    if (spins > SPIN_CAP) dead = true;
    __builtin_amdgcn_s_sleep(1);
  }
  CLOBBER;
  return dead ? ~0u : ready;
}

__device__ __forceinline__ void poll_all(rsrc_t rs, unsigned voff, u32x2 epoch, bool& dead) {
  for (unsigned spins = 0; !dead; ++spins) {
    CLOBBER;
    if (__all(is_set(ld8_sc1(rs, voff, 0), epoch))) break;
    if (spins > SPIN_CAP) dead = true;
    __builtin_amdgcn_s_sleep(1);
  }
  CLOBBER;
}

constexpr unsigned SC_VF = L_R1 + 32768;
constexpr unsigned SC_PHIK = L_R1 + 49152;
constexpr unsigned SC_XCH = L_R2 + XS;
constexpr unsigned wo_slot(int s) {
  return s < 2 ? L_R2 + (unsigned)(s + 1) * XS : (s < 6 ? L_R1 + (unsigned)(s - 2) * 16384 : (s == 6 ? L_R1 + 81920 : L_R2));
}
__device__ __forceinline__ unsigned wo_src(int s, int j) { return (unsigned)(((JROT(j) >> 1) + NJ / 2 + s) & 7); }
__device__ __forceinline__ unsigned wo_swz(int lane) { return (unsigned)(((lane & 7) ^ ((lane >> 3) & 6)) * 16); }
__device__ __forceinline__ unsigned umax3(unsigned a, unsigned b, unsigned c) { return max(max(a, b), c); }
__device__ __forceinline__ unsigned umax4v(unsigned m, const u32x4& v) { return umax3(umax3(m, v[0], v[1]), v[2], v[3]); }
__device__ __forceinline__ u32x4 nosent(u32x4 v) { return u32x4{min(v[0], SENT - 1), min(v[1], SENT - 1), min(v[2], SENT - 1), min(v[3], SENT - 1)}; }

template <typename P, typename F>
__device__ __forceinline__ void qkv_loop(int isb, int wm, int h, int lane, bool first, P pre, F fin) {
  constexpr unsigned lds0 = 0;
  f32x4 acc[4][6];
  const int fr = lane & 15, fq = lane >> 4;
  unsigned ab[2], bb[2];
  {
    const int r = wm * 64 + fr;
#pragma unroll
    for (int ks = 0; ks < 2; ++ks) ab[ks] = lds0 + L_R2 + r * 128 + (((ks * 4 + fq) ^ ((fr >> 1) & 7)) * 16);
    const int kr = fq * 8 + (fr >> 2);
    const int gs = ((kr >> 1) & 1) | (((kr >> 3) & 1) << 1);
#pragma unroll
    for (int c = 0; c < 2; ++c) bb[c] = lds0 + L_R1 + kr * 128 + (((h * 2 + c) ^ gs) * 32) + (lane & 3) * 8;
  }
#pragma unroll
  for (int m = 0; m < 4; ++m)
#pragma unroll
    for (int n = 0; n < 6; ++n) acc[m][n] = f32x4{0.f, 0.f, 0.f, 0.f};
  bf16x8 af[2][4], bf[2][6];
  auto rds = [&](int ks, int p, int rot = 0) {
#pragma unroll
    for (int m = 0; m < 4; ++m) af[p][m] = lds_ld<bf16x8>(ab[ks] + m * 2048);
#pragma unroll
    for (int nn = 0; nn < 6; ++nn) {
      const int n = (nn + rot) % 6;
      const unsigned a = bb[n & 1] + (n >> 1) * 8192 + ks * 4096;
      bf[p][n] = tr2(a, a + 512);
    }
  };
  auto mms = [&](int p, int rot = 0, int cnt = 6) {
#pragma unroll
    for (int nn = 0; nn < cnt; ++nn)
#pragma unroll
      for (int mm = 0; mm < 4; ++mm) { const int n = (nn + rot) % 6, m = (n & 1) ? 3 - mm : mm; acc[m][n] = n < 4 ? MFMA(bf[p][n], af[p][m], acc[m][n]) : MFMA(af[p][m], bf[p][n], acc[m][n]); }
  };
  auto ilv2 = [&]() {
#pragma unroll
    for (int i = 0; i < 16; ++i) { SGB(0x008, 1); SGB(0x100, 1); }
#pragma unroll
    for (int i = 0; i < 4; ++i) { SGB(0x008, 1); SGB(0x002, 1); }
    SGB(0x008, 4);
  };
  auto ilv3 = [&]() {
#pragma unroll
    for (int i = 0; i < 16; ++i) { SGB(0x008, 1); SGB(0x100, 1); }
#pragma unroll
    for (int i = 0; i < 7; ++i) { SGB(0x008, 1); SGB(0x004, 2); }
    SGB(0x008, 1);
  };
#pragma unroll
  for (int i = 0; i < 24; ++i) OPAQUE_V(acc[i / 6][i % 6]);
  OPAQUE_V(ab[0]); OPAQUE_V(ab[1]); OPAQUE_V(bb[0]); OPAQUE_V(bb[1]);
  if (!first) { WAITV(3); WAITL0; }
  BAR;
  rds(0, 0);
  int sa = 0, sb = 0;
  int da = XS, db = 24576;
  auto step = [&]() {
    ab[0] += da; ab[1] += da;
    bb[0] += db; bb[1] += db;
  };
  auto ring = [&]() {
    sa = (sa == 2) ? 0 : sa + 1;
    sb = (sb == 3) ? 0 : sb + 1;
    da = (sa == 2) ? -2 * (int)XS : (int)XS;
    db = (sb == 3) ? -3 * 24576 : 24576;
  };
#pragma unroll 1
  for (int t = 14; t > 0; --t) {
    SCHED;
    rds(1, 1); mms(0);
    step();
    ilv2();
    SCHED;
    __builtin_amdgcn_s_waitcnt(0x0070);
    BAR;
    SCHED;
    rds(0, 0);
    ring();
    mms(1); ilv3();
  }
  SCHED;
  f32x4 cs[4], nm4[4];
  float nmean[4], csv[2];
  auto fold = [&](int n) {
#pragma unroll
    for (int mm = 0; mm < 4; ++mm) {
      const int m = (n & 1) ? 3 - mm : mm;
#pragma unroll
      for (int e = 0; e < 4; ++e) {
        float t = n < 4 ? __builtin_fmaf(nmean[m], cs[n & 3][e], acc[m][n][e]) : __builtin_fmaf(nm4[m][e], csv[n & 1], acc[m][n][e]);
        asm("" : "+v"(t));
        acc[m][n][e] = t;
      }
    }
  };
  {
    unsigned pm = lds0 + L_MEAN + (16 + wm * 64 + fr) * 4, pc = lds0 + L_CS + isb * 768 + (h * 32 + fq * 4) * 4;
    unsigned pm4 = lds0 + L_MEAN + (16 + wm * 64 + fq * 4) * 4, pcv = lds0 + L_CS + isb * 768 + (128 + h * 32 + fr) * 4;
    asm("" : "+v"(pm)); asm("" : "+v"(pc)); asm("" : "+v"(pm4)); asm("" : "+v"(pcv));
#pragma unroll
    for (int m = 0; m < 4; ++m) nmean[m] = lds_ld<float>(pm + m * 64);
#pragma unroll
    for (int n = 0; n < 4; ++n) cs[n ^ 2] = lds_ld<f32x4>(pc + (((n ^ 2) >> 1) * 64 + (n & 1) * 16) * 4);
#pragma unroll
    for (int m = 0; m < 4; ++m) nm4[m] = lds_ld<f32x4>(pm4 + m * 64);
#pragma unroll
    for (int n = 0; n < 2; ++n) csv[n] = lds_ld<float>(pcv + n * 64);
    rds(1, 1, 4); mms(0, 2);
    step();
    fold(2); fold(3);
    SGB(0x002, 8);
#pragma unroll
    for (int i = 0; i < 8; ++i) { SGB(0x008, 1); SGB(0x100, 2); }
#pragma unroll
    for (int i = 0; i < 11; ++i) { SGB(0x008, 1); SGB(0x100, 1); SGB(0x002, 2); }
#pragma unroll
    for (int i = 0; i < 5; ++i) { SGB(0x008, 1); SGB(0x002, 3); }
    SCHED;
    __builtin_amdgcn_s_waitcnt(0xC07F);
    BAR;
    SCHED;
    rds(0, 0, 2);
    mms(1, 4); fold(4); fold(5);
#pragma unroll
    for (int i = 0; i < 8; ++i) { SGB(0x008, 1); SGB(0x100, 1); SGB(0x002, 1); }
#pragma unroll
    for (int i = 0; i < 8; ++i) { SGB(0x008, 1); SGB(0x100, 1); SGB(0x002, 2); }
#pragma unroll
    for (int i = 0; i < 8; ++i) { SGB(0x008, 1); SGB(0x002, 2); }
    SCHED;
    pre(0);
    SCHED;
    rds(1, 1, 2);
    mms(0, 2, 4); fold(0); fold(1);
    pre(1);
#pragma unroll
    for (int i = 0; i < 16; ++i) { SGB(0x008, 1); SGB(0x100, 1); SGB(0x002, 2); SGB(0x004, 1); }
  }
  SCHED;
  auto mm6 = [&](int m, int n0 = 0, int n1 = 8) {
#pragma unroll
    for (int nn = n0; nn < n1; ++nn) {
      const int n = nn < 4 ? nn + 2 : nn & 1, p = (nn >> 1) != 2;
      acc[m][n] = n < 4 ? MFMA(bf[p][n], af[p][m], acc[m][n]) : MFMA(af[p][m], bf[p][n], acc[m][n]);
    }
  };
  if (!isb) {
    fin(std::false_type{}, acc, mm6);
#pragma unroll
    for (int i = 0; i < 24; ++i) asm volatile("" : "=v"(acc[i / 6][i % 6]));
#pragma unroll
    for (int i = 0; i < 6; ++i) asm volatile("" : "=v"(bf[1][i]));
#pragma unroll
    for (int i = 0; i < 8; ++i) asm volatile("" : "=v"(af[i >> 2][i & 3]));
    asm volatile("" : "=v"(bf[0][0])); asm volatile("" : "=v"(bf[0][1]));
  }
  OPAQUE_S(isb);
  if (isb) fin(std::true_type{}, acc, mm6);
}
__device__ __forceinline__ u32x4 pack(const f32x4& a, const f32x4& b) { return u32x4{pk2(a[0], a[1]), pk2(a[2], a[3]), pk2(b[0], b[1]), pk2(b[2], b[3])}; }

__device__ __forceinline__ u32x2 nosent2(const f32x4& a) { return u32x2{min(pk2(a[0], a[1]), SENT - 1), min(pk2(a[2], a[3]), SENT - 1)}; }
template <int N> __device__ __forceinline__ void hedgehog(const f32x4 (&r)[4][2], const float (&c)[4], float (&ep)[4][8], float (&en)[4][8]) {
  float mx[4], nm[4];
#pragma unroll
  for (int m = 0; m < N; ++m) {
    const f32x4 a = r[m][0], b = r[m][1];
    mx[m] = vmax2(fmaxf(fmaxf(a[3], b[0]), b[1]), fmaxf(fmaxf(fmaxf(fmaxf(a[0], a[1]), a[2]), b[2]), b[3]));
    nm[m] = vmax2(fmaxf(fmaxf(-a[3], -b[0]), -b[1]), fmaxf(fmaxf(fmaxf(fmaxf(-a[0], -a[1]), -a[2]), -b[2]), -b[3]));
  }
  SCHED;
  red4<true>(mx[0], mx[1], nm[0], nm[1], [](float x) { return x; });
  if (N == 4) red4<true>(mx[2], mx[3], nm[2], nm[3], [](float x) { return x; });
#pragma unroll
  for (int m = 0; m < N; ++m) { mx[m] *= -c[m]; nm[m] *= -c[m]; }
  SCHED;
#pragma unroll
  for (int m = 0; m < N; ++m) {
#pragma unroll
    for (int i = 0; i < 8; ++i) { ep[m][i] = __builtin_fmaf(r[m][i >> 2][i & 3], c[m], mx[m]); en[m][i] = __builtin_fmaf(r[m][i >> 2][i & 3], -c[m], nm[m]); }
    SCHED;
#pragma unroll
    for (int i = 0; i < 8; ++i) { ep[m][i] = __builtin_amdgcn_exp2f(ep[m][i]); en[m][i] = __builtin_amdgcn_exp2f(en[m][i]); }
    SCHED;
  }
}
__device__ __forceinline__ u32x4 pack8(const float (&e)[8], float f) {
  return u32x4{pk2(e[0] * f, e[1] * f), pk2(e[2] * f, e[3] * f), pk2(e[4] * f, e[5] * f), pk2(e[6] * f, e[7] * f)};
}
__device__ __forceinline__ u32x4 pack8(const float (&e)[8]) { return u32x4{pk2(e[0], e[1]), pk2(e[2], e[3]), pk2(e[4], e[5]), pk2(e[6], e[7])}; }

constexpr u64 window_mask(int t, int e) {
  u64 mk = 0;
  for (int ln = 0; ln < 64; ++ln) {
    const int delta = (ln & 15) + 16 - t * 16 - (ln >> 4) * 4 - e;
    if (delta >= 0 && delta <= 14 && !(delta & 1)) mk |= 1ull << ln;
  }
  return mk;
}
__device__ __forceinline__ void phik2(const f32x4 (&r)[4][2], const float (&c)[4], unsigned pw) {
  const f32x4 zero = {0.f, 0.f, 0.f, 0.f};
  const bf16x8 ones = {0x3f80, 0x3f80, 0x3f80, 0x3f80, 0x3f80, 0x3f80, 0x3f80, 0x3f80};
  float ep[4][8], en[4][8];
  f32x4 sp[2], sn[2];
  hedgehog<2>(r, c, ep, en);
#pragma unroll
  for (int m = 0; m < 2; ++m) { sp[m] = MFMA(ones, as_frag(pack8(ep[m])), zero); sn[m] = MFMA(ones, as_frag(pack8(en[m])), zero); }
#pragma unroll
  for (int m = 0; m < 2; ++m) { asm("" : "+v"(sp[m])); asm("" : "+v"(sn[m])); }
  SCHED;
#pragma unroll
  for (int m = 0; m < 2; ++m) {
    const float fp = __builtin_amdgcn_rcpf(sp[m][0]), fn = __builtin_amdgcn_rcpf(sn[m][0]);
#pragma unroll
    for (int i = 0; i < 8; ++i) { ep[m][i] *= fp; en[m][i] *= fn; }
    SCHED;
    const u32x4 pp = pack8(ep[m]), pn = pack8(en[m]);
    const unsigned d = pw + m * (16 * 160);
    lds_st<u32x2>(d, u32x2{pp[0], pp[1]});
    lds_st<u32x2>(d + 32, u32x2{pp[2], pp[3]});
    lds_st<u32x2>(d + 64, u32x2{pn[0], pn[1]});
    lds_st<u32x2>(d + 96, u32x2{pn[2], pn[3]});
    SCHED;
  }
}
__device__ __forceinline__ bf16x8 rowsum_sel(int lane) {
  const int fr = lane & 15;
  const unsigned one = (fr < 8) ? (0x3f80u << (16 * (fr & 1))) : 0u;
  const int e = (fr >> 1) & 3;
  return as_frag(u32x4{e == 0 ? one : 0u, e == 1 ? one : 0u, e == 2 ? one : 0u, e == 3 ? one : 0u});
}
__device__ __forceinline__ f32x4 rowsum_fin(f32x4 c) {
#pragma unroll
  for (int k = 0; k < 4; ++k) {
    const float a = c[k];
    c[k] = a + __int_as_float(__builtin_amdgcn_update_dpp(0, __float_as_int(a), 0x128  , 0xf, 0xf, false));
  }
  return c;
}
__device__ __forceinline__ void job_colsum(rsrc_t rs, const u32x4 (&w)[8], int mat, int g, int jc, int hw, int lane) {
  const bf16x8 sel = rowsum_sel(lane);
  f32x4 c = {0.f, 0.f, 0.f, 0.f};
#pragma unroll
  for (int i = 0; i < 8; ++i) c = MFMA(sel, as_frag(w[i]), c);
  const u32x4 w4 = __builtin_bit_cast(u32x4, rowsum_fin(c));
  unsigned so = WS_CS + (unsigned)(((mat * 16 + jc) * 16 + g) * 768);
  OPAQUE_S(so);
  if ((lane & 40) == 0) st16_sc1(rs, (unsigned)(hw * 64 + (lane & 7) * 8 + (lane >> 4) * 4) * 4, so, w4);
}
__device__ __forceinline__ unsigned img_off(int i, int lane) { return (lane >> 3) * 128 + (((lane & 7) ^ ((((lane >> 4) & 1) | ((i & 1) << 1)) << 1)) * 16); }
__device__ __forceinline__ void job_pack(rsrc_t rs, u32x4 w, int i, int mat, int g, int jc, int hw, int lane) {
  st16_sc1(rs, img_off(i, lane),
           WS_WP + (unsigned)(((mat * 16 + jc) * 16 + g) * 24576 + hw * 8192 + i * 1024), w);
}

__global__ __launch_bounds__(512, 2) void mega_kernel(Params p) {
  extern __shared__ __attribute__((aligned(16))) char smem[];
  const int tid = threadIdx.x;
  const int wave0 = __builtin_amdgcn_readfirstlane(tid >> 6);
  const int xx = blockIdx.x & 7, rr = blockIdx.x >> 3;
  const int g0 = (rr / NJ) * NX + (xx & (NX - 1)), j0 = (xx / NX) * NJ + (rr & (NJ - 1));
  constexpr unsigned lds0 = 0, lds00 = 0;
  if ((unsigned)(size_t)(LAS char*)smem != 0u) return;
#define LOCALS0 int g = g0, j = j0, l = l0, wave = wave0;                                              \
  OPAQUE_S(g); OPAQUE_S(j); OPAQUE_S(l); OPAQUE_S(wave);                                               \
  const int hw = wave & 3, wm = wave & 1, h = (wave >> 1) & 1;                                         \
  const unsigned myrep = (unsigned)(JROT(j) / 2 + (g & (NX - 1))) * REP_STRIDE;                              \
  (void)hw; (void)wm; (void)h; (void)myrep;                                                            \
  int lane = tid; OPAQUE_V(lane); lane &= 63
#define LOCALS LOCALS0; const bool isb = l & 1, lead = (wave == 4); const int half = wave >> 2; (void)isb; (void)lead; (void)half
  const rsrc_t rs = mk_rsrc(p.ws, WS_SPAN);
  const bool cw = wave0 < 4;
  if (wave0 == 3) {
    const char* pc;
    unsigned len;
    asm volatile("s_getpc_b64 %0\n.Lpf_base:\n\ts_mov_b32 %1, .Lpf_end-.Lpf_base" : "=s"(pc), "=s"(len));
    const unsigned off = ((unsigned)rr * 16 + (tid & 15)) * 128;
    touch(mk_rsrc(pc, len), ((int)len > 0 && off + 4 <= len) ? off : 0u, lds00 + L_SINK);
  }
  if (wave0 == 4) {
    const int q = rr & 15;
    const void* bp = q < 6 ? (const void*)p.wqkvB : (q < 8 ? (const void*)p.woA : (q < 10 ? (const void*)p.woB : (q < 11 ? (const void*)p.wphi : (const void*)p.ws)));
    const unsigned off = q < 6 ? q << 20 : (q < 10 ? (q & 1) << 20 : (q < 11 ? 0u : (q - 11) << 20));
    touch(mk_rsrc(bp, off + 4), off, lds00 + L_SINK);
  }
  auto slowbit = [](const void* q) { return 1u << ((3 - ((unsigned)(size_t)q >> 7)) & 7); };
  unsigned slowm = slowbit(p.wqkvA) | (slowbit(p.wqkvB) << 8) | (slowbit(p.woA) << 16) | (slowbit(p.woB) << 24);
  OPAQUE_S(slowm);
  u32x2 epoch = {0u, 0u};
  bool dead = false;
      if (cw) {
        const int l0 = 0;
        LOCALS;
        u32x4 v[2][8];
        if (wave < 3) {
          const int jc = JOBC(j, wave), kt = (KB0(g, j) + (unsigned)g / NX) & 15;
          const rsrc_t ra = mk_rsrc(p.wqkvA, 1024 * 6144);
#pragma unroll
          for (int i = 0; i < 8; ++i)
            v[0][i] = __builtin_amdgcn_raw_buffer_load_b128(ra, (lane >> 3) * 6144 + (lane & 7) * 16, (kt * 64 + i * 8) * 6144 + wave * 2048 + jc * 128, 0);
          SCHED;
          BAR; SCHED;
          job_colsum(rs, v[0], 0, g, jc, wave, lane);
          if (SLOWC(0, jc)) {
#pragma unroll
            for (int i = 0; i < 8; ++i) job_pack(rs, v[0][i], i, 0, kt, jc, wave, lane);
          }
        } else {
          const int fr = lane & 15, fq = lane >> 4;
          const rsrc_t rx = mk_rsrc(p.x, 2048 * 2048);
#pragma unroll
          for (int i = 0; i < 16; ++i)
            v[i & 1][i >> 1] = __builtin_amdgcn_raw_buffer_load_b128(rx, fr * 2048 + fq * 16, (g * 128 + (j & (NJ - 1)) * (128 / NJ) + ((i >> 1) % (8 / NJ)) * 16) * 2048 + ((KB0(g, j) + (i >> 1) / (8 / NJ)) & 15) * 128 + (i & 1) * 64, 0);
          SCHED;
          BAR; SCHED;
          const f32x4 zero = {0.f, 0.f, 0.f, 0.f};
          const bf16x8 ones = {0x3f80, 0x3f80, 0x3f80, 0x3f80, 0x3f80, 0x3f80, 0x3f80, 0x3f80};
          float sm[8], q[8];
#pragma unroll
          for (int m = 0; m < 8; ++m) {
            const f32x4 a = MFMA(ones, as_frag(v[1][m]), MFMA(ones, as_frag(v[0][m]), zero));
            const f32x4 b = MFMA(as_frag(v[1][m]), as_frag(v[1][m]), MFMA(as_frag(v[0][m]), as_frag(v[0][m]), zero));
            sm[m] = a[0];
            q[m] = (fr & 2) ? ((fr & 1) ? b[3] : b[2]) : ((fr & 1) ? b[1] : b[0]);
          }
          if (fq == (fr >> 2)) {
#pragma unroll
            for (int m = 0; m < 8; ++m) st8_sc1(rs, ((m % (8 / NJ)) * 16 + fr) * 8 + (m / (8 / NJ)) * 1024, st_off(g, 0) + JROT(j) * 1024 + (j & (NJ - 1)) * (1024 / NJ), u32x2{__float_as_uint(sm[m]), __float_as_uint(q[m])});
          }
        }
      }
  const char *pA = (const char*)p.x, *pW = (const char*)p.wqkvA;
  unsigned zA = 2048 * 2048, bA = g0 * (128 * 2048), kA = 128, pcA = 32 * 2048, vA;
  unsigned zW = 1024 * 6144, bW = j0 * 128, kW = 64 * 6144, pcW = 2048, hW = 32 * 6144, vW;
  int kb = KB0(g0, j0), dj = -1;
  {
    const int lane = tid & 63, hw = wave0 & 3;
    vA = (hw * 8 + (lane >> 3)) * 2048 + (((lane & 7) ^ (((hw & 1) * 4 + (lane >> 4)) & 7)) * 16);
    vW = (hw * 8 + (lane >> 3)) * 6144 + (((lane & 7) ^ ((((lane >> 4) & 1) | ((hw & 1) << 1)) << 1)) * 16);
  }

#pragma unroll 1
  for (int l0 = 0; l0 < 4; ++l0) {
    f32x4 acc[2][4];
    auto kvpart = [&](unsigned va, unsigned pa, unsigned pb, unsigned kvb, int lane, int wave) {
      const f32x4 zero = {0.f, 0.f, 0.f, 0.f};
      const bf16x8 ones = {0x3f80, 0x3f80, 0x3f80, 0x3f80, 0x3f80, 0x3f80, 0x3f80, 0x3f80};
      f32x4 c0 = zero, c1 = zero, z0 = zero;
#pragma unroll
      for (int ks = 0; ks < 4; ++ks) {
        const bf16x8 v = lds_ld<bf16x8>(va + ks * 2048);
        const bf16x8 a0 = tr2(pa + ks * 32 * 160, pa + (ks * 32 + 16) * 160);
        const bf16x8 a1 = tr2(pb + ks * 32 * 160, pb + (ks * 32 + 16) * 160);
        c0 = MFMA(a0, v, c0);
        c1 = MFMA(a1, v, c1);
        z0 = MFMA(a0, ones, z0);
      }
      const u32x4 c = nosent(pack(c0, c1));
      const bool dt = wave & 2;
      st16_sc1(rs, lane * 16, kvb + (unsigned)wave * 1024, u32x4{dt ? c[2] : c[0], dt ? c[3] : c[1], dt ? c[0] : c[2], dt ? c[1] : c[3]});
      if ((lane & 15) == 0)
        st8_sc1(rs, lane, kvb + KVP_Z + (wave >> 2) * 128 + (wave & 1) * 64 + (wave & 2) * 4, nosent2(z0));
    };
#define KV_ADDR                                                                                                            \
    unsigned va = lds0 + SC_VF + (wave >> 2) * 8192 + ((wave >> 1) & 1) * 1024 + lane * 16;                                \
    unsigned pa = lds0 + SC_PHIK + ((wave >> 2) * 128 + fq * 4 + (fr >> 2)) * 160 + (wave & 1) * 64 + (lane & 3) * 8;      \
    unsigned kvj = WS_KVP + (unsigned)j * KVP_J + (unsigned)(l >> 1) * KVP_L;                                              \
    unsigned pb = pa + 32 - (wave & 2) * 16;                                                                               \
    pa += (wave & 2) * 16;                                                                                                 \
    OPAQUE_V(va); OPAQUE_V(pa); OPAQUE_V(pb); OPAQUE_S(kvj)
    if (cw) {
      LOCALS;
      unsigned otile, kvo, l16, pr1, pr4;
      int g2, j2, l2, ln2;
      qkv_loop(l & 1, wm, h, lane, l == 0, [&](int st) {
        if (st == 0) {
          g2 = g0; j2 = j0; l2 = l0; ln2 = tid;
          OPAQUE_S(g2); OPAQUE_S(j2); OPAQUE_S(l2); OPAQUE_V(ln2);
          return;
        }
        otile = ob_off(g2, j2, l2) + (unsigned)(wm * 8 + h) * 1024;
        kvo = WS_KVP + (unsigned)j2 * KVP_J + (unsigned)g2 * KVP_G + (unsigned)(l2 >> 1) * KVP_L;
        l16 = (ln2 & 63) * 16;
        pr1 = lds0 + L_RSTD + (16 + wm * 64 + (ln2 & 15)) * 4;
        pr4 = lds0 + L_RSTD + wm * 256 + (ln2 & 48);
        asm("" : "+s"(otile)); asm("" : "+s"(kvo)); asm("" : "+v"(l16)); asm("" : "+v"(pr1)); asm("" : "+v"(pr4));
      }, [&](auto isb_c, f32x4 (&a)[4][6], auto& mm6) {
        const f32x4 zero = {0.f, 0.f, 0.f, 0.f};
        constexpr float L2E = 1.4426950408889634f;
        float rq[4];
#define ilv8(nv) do { _Pragma("unroll") for (int i_ = 0; i_ < 8; ++i_) { SGB(0x008, 1); SGB(0x100, 2); SGB(0x002, nv); } SCHED; } while (0)
        if constexpr (decltype(isb_c)::value) {
          constexpr float QS = 0.17677669529663687f * L2E, NEG = -3.0e38f;
          const bf16x8 ones = {0x3f80, 0x3f80, 0x3f80, 0x3f80, 0x3f80, 0x3f80, 0x3f80, 0x3f80};
          u32x4 kf[5], vf[5], qf[4], pf[4];
          f32x4 rs4[4], bias[2], s0[4], s1[4], dn[4];
          float x[4][4], mx[4];
          unsigned psel0, psel1;
          auto pkkv = [&](int m) { kf[m + 1] = pack(a[m][2] * rq[m], a[m][3] * rq[m]); vf[m + 1] = pack(a[m][4] * rs4[m], a[m][5] * rs4[m]); };
          auto pkq = [&](int m) { qf[m] = pack(a[m][0], a[m][1]); };
          auto st1 = [&](int m) {
            x[m][0] = vmax2(s0[m][0], s0[m][1]); x[m][1] = vmax2(s0[m][2], s0[m][3]); x[m][2] = vmax2(s1[m][0], s1[m][1]); x[m][3] = vmax2(s1[m][2], s1[m][3]);
            mx[m] = vmax2(vmax2(x[m][0], x[m][1]), vmax2(x[m][2], x[m][3]));
          };
          auto st2 = [&](int m) {
            const float mxl = mx[m] * -rq[m];
#pragma unroll
            for (int e = 0; e < 4; ++e) x[m][e] = __builtin_fmaf(x[m][e], rq[m], mxl);
          };
          auto st3 = [&](int m) {
#pragma unroll
            for (int e = 0; e < 4; ++e) x[m][e] = __builtin_amdgcn_exp2f(x[m][e]);
          };
          auto st5 = [&](int m) {
            const unsigned p0 = pk2(x[m][0], x[m][1]), p1 = pk2(x[m][2], x[m][3]);
            pf[m] = u32x4{__builtin_amdgcn_perm(0u, p0, psel0), __builtin_amdgcn_perm(0u, p0, psel1), __builtin_amdgcn_perm(0u, p1, psel0), __builtin_amdgcn_perm(0u, p1, psel1)};
          };
          auto st6 = [&](int m) {
            dn[m] = MFMA(ones, as_frag(pf[m]), zero);
            s0[m] = MFMA(as_frag(u32x4{vf[m][0], vf[m][1], vf[m + 1][0], vf[m + 1][1]}), as_frag(pf[m]), zero);
            s1[m] = MFMA(as_frag(u32x4{vf[m][2], vf[m][3], vf[m + 1][2], vf[m + 1][3]}), as_frag(pf[m]), zero);
            asm("" : "+v"(dn[m]));
          };
          auto st7 = [&](int m) { const float inv = __builtin_amdgcn_rcpf(dn[m][0]); pf[m] = nosent(pack(s0[m] * inv, s1[m] * inv)); };
          mm6(3);
#pragma unroll
          for (int m = 0; m < 4; ++m) rq[m] = lds_ld<float>(pr1 + m * 64);
#pragma unroll
          for (int m = 3; m >= 0; --m) rs4[m] = lds_ld<f32x4>(pr4 + 64 + m * 64);
          unsigned pv_ = l16 + lds0 + L_PREV + h * 2048;
          asm("" : "+v"(pv_));
          static_for<0, 8>([&](auto i_c) __attribute__((always_inline)) {
            constexpr int i = decltype(i_c)::value;
            constexpr u64 mk = window_mask(i >> 2, i & 3);
            bias[i >> 2][i & 3] = __builtin_amdgcn_inverse_ballot_w64(mk) ? 0.f : NEG;
          });
          asm("" : "+v"(bias[0])); asm("" : "+v"(bias[1]));
          const unsigned hso = kvo + KVP_HALO + h * 2048, hsi = hso - (g > 0 ? KVP_G : 0u);
          ilv8(2);
          mm6(2);
          pkkv(3);
          ilv8(2);
          OPAQUE_V(kf[4]); OPAQUE_V(vf[4]);
          if (wm) {
            st16_sc1(rs, l16, hso, nosent(kf[4]));
            st16_sc1(rs, l16, hso + 1024, nosent(vf[4]));
          } else {
            lds_st<u32x4>(pv_, kf[4]);
            lds_st<u32x4>(pv_ + 1024, vf[4]);
          }
          SCHED;
          mm6(1);
          pkq(3); pkkv(2);
          ilv8(3);
          WAITL0;
          BAR;
          SCHED;
          mm6(0);
          pkq(2); pkkv(1);
          ilv8(3);
          s0[3] = MFMA(as_frag(kf[3]), as_frag(qf[3]), bias[0]); s1[3] = MFMA(as_frag(kf[4]), as_frag(qf[3]), bias[1]);
          s0[2] = MFMA(as_frag(kf[2]), as_frag(qf[2]), bias[0]); s1[2] = MFMA(as_frag(kf[3]), as_frag(qf[2]), bias[1]);
          pkq(1); pkkv(0);
#pragma unroll
          for (int i = 0; i < 4; ++i) { SGB(0x008, 1); SGB(0x002, 5); }
          SCHED;
          const bool halo = wm == 0 && g > 0;
          const u32x4 hk = ld16_sc1(rs, l16, hsi), hv = ld16_sc1(rs, l16, hsi + 1024);
          SCHED;
          s0[1] = MFMA(as_frag(kf[1]), as_frag(qf[1]), bias[0]); s1[1] = MFMA(as_frag(kf[2]), as_frag(qf[1]), bias[1]);
          st1(3); st1(2);
#pragma unroll
          for (int m = 0; m < 4; ++m) rq[m] *= QS;
          SCHED;
          st1(1);
          pkq(0);
          psel0 = __builtin_amdgcn_inverse_ballot_w64(0xaaaaaaaaaaaaaaaaull) ? 0x01000c0cu : 0x0c0c0100u;
          psel1 = __builtin_amdgcn_inverse_ballot_w64(0xaaaaaaaaaaaaaaaaull) ? 0x03020c0cu : 0x0c0c0302u;
          { float d = mx[1]; red4<true>(mx[3], mx[2], mx[1], d, [](float v) { return v; }); }
          s1[0] = MFMA(as_frag(kf[1]), as_frag(qf[0]), bias[1]);
          SCHED;
          st2(3); st2(2); st2(1);
          SCHED;
          st3(3); st3(2); st3(1);
          SCHED;
          st5(3); st6(3); st5(2); st6(2); st5(1); st6(1);
          SCHED;
          u32x4 pk = lds_ld<u32x4>(pv_), pv = lds_ld<u32x4>(pv_ + 1024);
          st7(3); st7(2); st7(1);
          SCHED;
#pragma unroll
          for (int m = 3; m > 0; --m) st16_sc1(rs, l16, otile + m * 2048, pf[m]);
          if (!wm && g == 0) bias[0] = f32x4{NEG, NEG, NEG, NEG};
          SCHED;
          if (halo) {
            pk = hk; pv = hv;
            for (unsigned spins = 0; !dead && __any(umax4v(umax4v(0u, pk), pv) == SENT); ++spins) {
              if (spins > SPIN_CAP) { dead = true; break; }
              CLOBBER;
              pk = ld16_sc1(rs, l16, hsi); pv = ld16_sc1(rs, l16, hsi + 1024);
              OPAQUE_V(pk); OPAQUE_V(pv);
            }
          }
          vf[0] = pv;
          s0[0] = MFMA(as_frag(pk), as_frag(qf[0]), bias[0]);
          st1(0);
          mx[0] = red_max4(mx[0]);
          st2(0); st3(0); st5(0); st6(0); st7(0);
          st16_sc1(rs, l16, otile, pf[0]);
        } else {
          LOCALS;
          const int fr = lane & 15, fq = lane >> 4;
          f32x4 rs4[4], r[4][2];
          const bf16x8 ones = {0x3f80, 0x3f80, 0x3f80, 0x3f80, 0x3f80, 0x3f80, 0x3f80, 0x3f80};
          auto proj = [&](int m, int n, const bf16x8& w0, const bf16x8& w1) {
            const u32x4 f = pack(a[m][n], a[m][n + 1]);
            r[m][0] = MFMA(w0, as_frag(f), zero); r[m][1] = MFMA(w1, as_frag(f), zero);
          };
          mm6(3, 0, 2); mm6(2, 0, 2); mm6(1, 0, 2); mm6(0, 0, 2);
          unsigned wa = lds0 + L_WPHI + h * 2048 + (fq * 4 + (fr >> 2)) * 64 + (lane & 3) * 8, kw = lds0 + L_PREV + hw * 2048 + l16;
          asm("" : "+v"(wa)); asm("" : "+v"(kw));
#pragma unroll
          for (int m = 0; m < 4; ++m) rq[m] = lds_ld<float>(pr1 + m * 64);
#pragma unroll
          for (int m = 0; m < 4; ++m) rs4[m] = lds_ld<f32x4>(pr4 + 64 + m * 64);
          const bf16x8 w0 = tr2(wa, wa + 1024);
          const bf16x8 w1 = tr2(wa + 32, wa + 32 + 1024);
#pragma unroll
          for (int i = 0; i < 8; ++i) { SGB(0x008, 1); SGB(0x100, 2); SGB(0x002, 2); }
          SCHED;
          mm6(3, 2, 4); mm6(2, 2, 4);
          lds_st<u32x4>(kw + 1024, pack(a[3][2], a[3][3]));
          lds_st<u32x4>(kw, pack(a[2][2], a[2][3]));
#pragma unroll
          for (int i = 0; i < 2; ++i) { SGB(0x008, 1); SGB(0x002, 2); SGB(0x008, 1); SGB(0x002, 2); SGB(0x200, 1); }
          SCHED;
          mm6(1, 2, 4); mm6(0, 2, 4);
          SCHED;
          WAITL0;
          BAR;
          SCHED;
          proj(0, 2, w0, w1); proj(1, 2, w0, w1);
          unsigned pw = lds0 + SC_PHIK + (h * 128 + wm * 64 + fr) * 160 + fq * 8, vw = lds0 + SC_VF + h * 8192 + wm * 4096 + l16;
          asm("" : "+v"(pw)); asm("" : "+v"(vw));
#pragma unroll
          for (int m = 0; m < 4; ++m) rq[m] *= L2E;
#pragma unroll
          for (int i = 0; i < 4; ++i)
            lds_st<u32x4>(vw + i * 1024, pack(a[i & 2][4 + (i & 1)] * rs4[i & 2], a[(i & 2) + 1][4 + (i & 1)] * rs4[(i & 2) + 1]));
          SCHED;
          phik2(r, rq, pw);
          KV_ADDR;
          WAITL0;
          BAR;
          kvpart(va, pa, pb, kvo, lane, wave);
          SCHED;
#pragma unroll
          for (int m = 0; m < 4; ++m) mm6(m, 4, 8);
          SCHED;
          CLOBBER;
          BAR;
          u32x4 qp[4], qn[4];
          float sqp[4], sqn[4];
#pragma unroll
          for (int m = 0; m < 4; ++m) proj(m, 0, w0, w1);
          SCHED;
          float ep[4][8], en[4][8];
          hedgehog<4>(r, rq, ep, en);
          const unsigned zo = (lane >> 3) * KVP_G + (lane & 7) * 16, zs = kvj + KVP_Z + h * 128;
          u32x4 zv[2] = {ld16_sc1(rs, zo, zs), ld16_sc1(rs, zo, zs + 8 * KVP_G)};
          SCHED;
#pragma unroll
          for (int m = 0; m < 4; ++m) {
            qp[m] = pack8(ep[m]); qn[m] = pack8(en[m]);
            sqp[m] = MFMA(ones, as_frag(qp[m]), zero)[0];
            sqn[m] = MFMA(ones, as_frag(qn[m]), zero)[0];
          }
          SCHED;
#pragma unroll
          for (int m = 0; m < 4; ++m) { sqp[m] = __builtin_amdgcn_rcpf(sqp[m]); sqn[m] = __builtin_amdgcn_rcpf(sqn[m]); }
#pragma unroll
          for (int m = 0; m < 4; ++m) { OPAQUE_V(qp[m]); OPAQUE_V(qn[m]); OPAQUE_V(sqp[m]); OPAQUE_V(sqn[m]); }
          unsigned ko = (lane >> 5) * KVP_G + (lane & 31) * 16, ks = kvj + (unsigned)((j ^ RX) - j) * KVP_J + KVP_SUM + h * (8 * KVP_G);
          bf16x8 sel = rowsum_sel(lane);
          OPAQUE_V(ko); OPAQUE_S(ks); OPAQUE_V(sel);
          for (unsigned spins = 0; !dead && __any(umax4v(umax4v(0u, zv[0]), zv[1]) == SENT); ++spins) {
            if (spins > SPIN_CAP) { dead = true; break; }
            CLOBBER;
            zv[0] = ld16_sc1(rs, zo, zs); zv[1] = ld16_sc1(rs, zo, zs + 8 * KVP_G);
          }
          {
            const f32x4 z = rowsum_fin(MFMA(sel, as_frag(zv[1]), MFMA(sel, as_frag(zv[0]), zero)));
            if ((lane & 40) == 0) lds_st<u32x2>(lds0 + L_ZF + hw * 128 + fr * 16 + fq * 8, u32x2{pk2(z[0], z[1]), pk2(z[2], z[3])});
            WAITL0;
            const bf16x8 zf0 = lds_ld<bf16x8>(lds0 + L_ZF + hw * 128 + fq * 16), zf1 = lds_ld<bf16x8>(lds0 + L_ZF + hw * 128 + 64 + fq * 16);
            float dp[4], dn[4];
#pragma unroll
            for (int m = 0; m < 4; ++m) { dp[m] = MFMA(zf0, as_frag(qp[m]), zero)[0]; dn[m] = MFMA(zf1, as_frag(qn[m]), zero)[0]; }
            SCHED;
#pragma unroll
            for (int m = 0; m < 4; ++m) {
              const float inv = __builtin_amdgcn_rcpf(__builtin_fmaf(dp[m], sqp[m], __builtin_fmaf(dn[m], sqn[m], 1e-6f)));
              sqp[m] *= inv; sqn[m] *= inv;
              OPAQUE_V(sqp[m]); OPAQUE_V(sqn[m]);
            }
          }
          BAR;
          __builtin_amdgcn_s_sleep(H2D);
          u32x4 kv[4];
#pragma unroll
          for (int i = 0; i < 4; ++i) kv[i] = ld16_sc1(rs, ko, ks + i * (2 * KVP_G));
          SCHED;
#pragma unroll
          for (int m = 0; m < 4; ++m) { qp[m] = pack8(ep[m], sqp[m]); qn[m] = pack8(en[m], sqn[m]); }
#pragma unroll
          for (int m = 0; m < 4; ++m) { OPAQUE_V(qp[m]); OPAQUE_V(qn[m]); }
          SCHED;
          for (unsigned spins = 0; !dead && __any(max(umax4v(umax4v(0u, kv[0]), kv[1]), umax4v(umax4v(0u, kv[2]), kv[3])) == SENT); ++spins) {
            if (spins > SPIN_CAP) { dead = true; break; }
            CLOBBER;
#pragma unroll
            for (int i = 0; i < 4; ++i) kv[i] = ld16_sc1(rs, ko, ks + i * (2 * KVP_G));
          }
          f32x4 o[4][2];
#pragma unroll
          for (int m = 0; m < 4; ++m)
#pragma unroll
            for (int i = 0; i < 2; ++i) o[m][i] = MFMA(as_frag(kv[2 * i + 1]), as_frag(qn[m]), MFMA(as_frag(kv[2 * i]), as_frag(qp[m]), zero));
#pragma unroll
          for (int m = 0; m < 4; ++m) st16_sc1(rs, l16, otile + m * 2048, nosent(pack(o[m][0], o[m][1])));
        }
      });
#pragma unroll
      for (int i = 0; i < 8; ++i) asm volatile("" : "=v"(acc[i >> 2][i & 3]));
    } else {
      __builtin_amdgcn_s_setprio(3);
      if (l0) {
        WAITV(3);
        WAITL0;
        BAR;
      }
      LOCALS;
      u32x4 xr[2][2];
      const unsigned lin = lane * 16, vlin = lin + hw * 1024;
      const int jr = j & ~3;
      u32x4 sent4 = {SENT, SENT, SENT, SENT};
      OPAQUE_V(sent4);
      const unsigned cb0 = ob_off(g, j, l), cb2 = xb_off(g, l) + j * TS;
      const unsigned cb1 = WS_KVP + (unsigned)j * KVP_J + (unsigned)g * KVP_G + (unsigned)(l >> 1) * KVP_L;
      unsigned ra0;
      int tr = 0;
      auto ldres = [&](int t) {
        if (tr == t) {
#pragma unroll
          for (int i = 0; i < 4; ++i) xr[i >> 1][i & 1] = lds_ld<u32x4>((ra0 ^ ((i & 1) * 64)) + (t % 3) * XS + ((i >> 1) ^ 1) * 2048);
        }
      };
      auto aslot = [&](int t) { return lds0 + L_R2 + (t % 3) * XS + hw * 1024; };
      auto wslot = [&](int t) { return lds0 + L_R1 + (t & 3) * 24576 + hw * 1024; };
      const bool l0f = l == 0;
      const rsrc_t rA = mk_rsrc(pA, zA);
      auto kseq = [&](int t) { return (kb + t - (t <= dj ? 1 : 0)) & 15; };
      auto offA = [&](int t) { return bA + (unsigned)kseq(t) * kA; };
      auto offW = [&](int t) { return bW + (unsigned)kseq(t) * kW; };
      auto ldA = [&](int t) { dma4<4096>(rA, vA, offA(t), pcA, aslot(t)); };
      auto ldWh = [&](int t, unsigned o, int hf) { dma3<8192>(mk_rsrc(pW, zW), vW, o + (hf ? hW : 0u), pcW, wslot(t) + hf * 4096); };
      const bool slowj = l0f && SLOWC(0, j);
      auto packedW = [&]() {
        pW = p.ws; zW = WS_SPAN; bW = WS_WP + (unsigned)j * (16 * 24576); kW = 24576; pcW = 8192; hW = 4096; vW = vlin;
      };
      auto ldW = [&](int t) { const unsigned o = offW(t); ldWh(t, o, 0); ldWh(t, o, 1); };
      constexpr auto spread = [](int n) { unsigned m = 0; for (int t = 0; t < 26; ++t) if ((t + 1) * n / 13 > t * n / 13) m |= 1u << t; return m; };
      constexpr unsigned sp4 = spread(4), sp8 = spread(8), sp12 = spread(12);
      const unsigned smask = (isb ? (l == 3 ? sp4 : sp8) : sp12) >> ((((g + j * 5) & 15) * 13) >> 4);
      const unsigned cbm = isb ? cb2 : cb1;
      auto clean = [&](int t) __attribute__((always_inline)) {
        if (t < 13 && ((smask >> t) & 1)) {
          const unsigned cc = __builtin_popcount(smask & ((1u << t) - 1));
          const unsigned cb = cb0 + (cc >= 4 ? cbm - cb0 : 0u) + (cc >= 8 ? cb2 - cbm : 0u), pc = (cc + g + j) & 3;
          st16_sc1(rs, (lane >> 4) * 4096 + (lane & 15) * 16, cb + hw * 1024 + pc * 256, sent4);
        }
      };
      unsigned flo;
      unsigned ready = ~0u;
      u32x4 cv[4];
#pragma unroll
      for (int ii = 0; ii < 4; ++ii) asm volatile("" : "=v"(cv[ii]));
      auto chkrd = [&](int t) {
#pragma unroll
        for (int ii = 0; ii < 4; ++ii) cv[ii] = lds_ld<u32x4>(aslot(t) + lin + ii * 4096);
      };
      auto chkbad = [&]() { return __any(max(umax4v(umax4v(0u, cv[0]), cv[1]), umax4v(umax4v(0u, cv[2]), cv[3])) == SENT) && !l0f; };
      auto refetch = [&](int t) __attribute__((always_inline)) {
        for (unsigned spins = 0; !dead; ++spins) {
          if (spins > SPIN_CAP) { dead = true; break; }
          WAITV(0);
          ldA(t);
          ldA(t + 1);
          WAITV(0);
          chkrd(t);
          if (!chkbad()) break;
          __builtin_amdgcn_s_sleep(1);
        }
      };
      f32x4 jc[4];
#pragma unroll
      for (int i = 0; i < 4; ++i) asm volatile("" : "=v"(jc[i]));
      auto csld = [&]() {
#pragma unroll
        for (int i = 0; i < 4; ++i)
          ld16_asm(jc[i], rs, (lane >> 4) * 768 + hw * 192 + ((lane & 15) < 12 ? (lane & 15) : 0) * 16, WS_CS + (unsigned)((l * 16 + j) * 16 + i * 4) * 768);
      };
      auto csred = [&]() {
#pragma unroll
        for (int i = 0; i < 4; ++i) OPAQUE_V(jc[i]);
        const f32x4 s = (jc[0] + jc[1]) + (jc[2] + jc[3]);
        auto r = SWAP16(s[0], s[1]), q = SWAP16(s[2], s[3]);
        r = SWAP32(__uint_as_float(r[0]) + __uint_as_float(r[1]), __uint_as_float(q[0]) + __uint_as_float(q[1]));
        if ((lane & 15) < 12) lds_st<float>(lds0 + L_CS + l * 768 + hw * 192 + (lane & 15) * 16 + (lane >> 4) * 4, __uint_as_float(r[0]) + __uint_as_float(r[1]));
      };
      if (l0f) {
        u32x2 gw2;
        ld8_asm(gw2, rs, WS_EPOCH + 0u * lin);
        BAR;
        ldW(0); ldA(0);
        dma1(mk_rsrc(p.wphi, 32 * 2048), vlin, j * 4096, lds0 + L_WPHI + hw * 1024);
        ldW(1);
        WAITV(17);
        OPAQUE_V(gw2);
        const u64 gword = ((u64)(unsigned)UNI(gw2[1]) << 32) | (unsigned)UNI(gw2[0]);
        u64 z = gword + 0x9e3779b97f4a7c15ull;
        z = (z ^ (z >> 30)) * 0xbf58476d1ce4e5b9ull;
        z = (z ^ (z >> 27)) * 0x94d049bb133111ebull;
        z = z ^ (z >> 31);
        z = (z == gword) ? ~z : z;
        epoch = u32x2{(unsigned)z, (unsigned)(z >> 32)};
        tr = (j - kb) & 15;
        WAITV(7);
        BAR;
        ldA(1);
      } else {
        ldW(1);
        if (l == 1) csld();
        else __builtin_amdgcn_s_sleep(XD);
        ldA(1);
      }
      ldA(2);
      ldW(2);
#pragma unroll
      for (int i = 0; i < 4; ++i) asm volatile("" : "=v"(xr[i >> 1][i & 1]));
      unsigned oA = offA(3), oW = 0;
      OPAQUE_S(oA);
      ra0 = lds0 + L_R2 + (hw * 32 + (lane & 15)) * 128 + (((((lane >> 4) & 1) * 2 + (lane >> 5)) ^ ((lane >> 1) & 7)) * 16);
      OPAQUE_V(ra0);
      ldres(0);
      clean(0);
      if (l == 1) { WAITV(14); csred(); }
      WAITV(10);
      chkrd(1);
      if (__builtin_expect(chkbad(), 0)) refetch(1);
      const int fr = lane & 15, fq = lane >> 4;
      u32x2 fl;
      f32x4 js[4];
      float rb[4];
      const bool slowo = SLOWC(2 + (l & 1), j);
      const char* pO;
      unsigned zO, bO, pcO, vO, clo;
      u32x4 wo2[2];
      asm volatile("" : "=v"(wo2[0])); asm volatile("" : "=v"(wo2[1]));
      const unsigned fwo = myrep + fl_line(FL_WO + (isb ? 1 : 0), jr + (lane >> 4)) + (lane & 15) * 8;
      static_for<1, 14>([&](auto t_c) __attribute__((always_inline)) {
        constexpr int t = decltype(t_c)::value;
        constexpr auto rest = [](int s) { return 6 + (s == NVAL - 2 || s == 5 || s == 11) + (s == 8 ? 4 : 0); };
        constexpr int NW = t == 1 ? 63 : 4 + rest(t - 1);
        if (t >= NVAL && t <= 6 && __builtin_expect((ready & (1u << kseq(t + 2))) == 0, 0)) {
          WAITV(NW); WAITL0; BAR;
          ready = poll_need(rs, flo, epoch, ready, 1u << kseq(t + 2), dead);
          dma4<4096>(rA, vA, oA, pcA, aslot(t + 2));
        } else if (t == 7 && __builtin_expect(ready != ~0u, 0)) {
          WAITV(NW); WAITL0; BAR;
          poll_all(rs, flo, epoch, dead);
          dma4<4096>(rA, vA, oA, pcA, aslot(t + 2));
        } else {
          bar_dma4<4096, NW>(rA, vA, oA, pcA, aslot(t + 2));
        }
        if (t == 1 && lead) set_flags(rs, fl_line(FL_X + l, g), j, l0f ? fl_line(FL_CS, j) : fl_line(FL_X + l, g), l0f ? g : j, epoch, lane);
        if (t == 7 && slowj) packedW();
        oW = offW(t + 2);
        if (t < 13) oA = offA(t + 3);
        OPAQUE_S(oA); OPAQUE_S(oW);
        clean(t);
        if (t <= NVAL && !l0f) { WAITV(rest(t - 1) + 4); chkrd(t + 1); }
        if (t == WT + 3 && l0f && SLOWC(2, jr + hw)) {
          OPAQUE_V(wo2[0]); OPAQUE_V(wo2[1]);
#pragma unroll
          for (int i = 0; i < 2; ++i) st16_sc1(rs, lin, WS_WO + (unsigned)((jr + hw) * 131072 + g * 8192 + ((j & 3) * 2 + i) * 1024), wo2[i]);
        }
        if (t == WT + 6 && l0f && lead) set_flags(rs, fl_line(FL_WO, j), g, fl_line(FL_WO, j), g, epoch, lane);
        ldres(t);
        if (t == 8) {
#pragma unroll
          for (int i = 0; i < 4; ++i) ld16_asm(js[i], rs, fq * 1024 + hw * 256 + fr * 16, st_off(g, l) + i * 4096);
        }
        SCHED;
        ldWh(t + 2, oW, 0);
        SCHED;
        bool bad = false;
        if (t <= NVAL) bad = chkbad();
        if (t == 1) {
          const unsigned fx = fl_line(FL_X + l, g), fc = fl_line(FL_CS, jr) + ((j + fq) & 3) * 128;
          flo = myrep + ((l0f && lane >= 16) ? fc : fx) + fr * 8;
          OPAQUE_V(flo);
        }
        if (t == 9) {
          pO = slowo ? p.ws : (const char*)(isb ? p.woB : p.woA);
          zO = slowo ? WS_SPAN : 1024 * 2048; bO = slowo ? WS_WO + (unsigned)((isb ? 16 : 0) + j) * 131072 : j * 128; pcO = slowo ? 4096 : 32 * 2048;
          vO = slowo ? vlin : (hw * 8 + (lane >> 3)) * 2048 + wo_swz(lane);
          clo = myrep + ((lane & 16) ? fl_line(FL_CLC + l, (lane & 32) ? j ^ RX : j) : fl_line(FL_CLR + l, g)) + fr * 8;
          OPAQUE_S(pO); OPAQUE_S(zO); OPAQUE_S(bO); OPAQUE_S(pcO); OPAQUE_V(vO); OPAQUE_V(clo);
        }
        if (t == 10 && l0f) csred();
        if (t == 11) {
#pragma unroll
          for (int i = 0; i < 4; ++i) OPAQUE_V(js[i]);
          const f32x4 s = (js[0] + js[1]) + (js[2] + js[3]);
          const auto r = SWAP16(s[0], s[2]), q = SWAP16(s[1], s[3]);
          rb[0] = __uint_as_float(r[0]) + __uint_as_float(r[1]); rb[1] = __uint_as_float(q[0]) + __uint_as_float(q[1]);
          OPAQUE_V(rb[0]); OPAQUE_V(rb[1]);
        }
        if (t == 12) {
          const auto r = SWAP32(rb[0], rb[0]), q = SWAP32(rb[1], rb[1]);
          const float m = (__uint_as_float(r[0]) + __uint_as_float(r[1])) * (-1.0f / 1024), q2 = __uint_as_float(q[0]) + __uint_as_float(q[1]);
          const float rstd = __builtin_amdgcn_rsqf(fmaxf(__builtin_fmaf(q2, 1.0f / 1024, -m * m), 0.f) + 1e-5f);
          lds_st<float>(lds0 + L_MEAN + 64 + hw * 128 + fr * 8 + (fq & 1) * 4 + (fq >> 1) * (L_RSTD - L_MEAN), (lane & 32) ? rstd : m);
        }
        SCHED;
        ldWh(t + 2, oW, 1);
        if (t == NVAL - 1 || t == 6) {
          WAITV(10);
          OPAQUE_V(fl);
          if (t == 6) ready = __all(is_set(fl, epoch)) ? ~0u : 0u;
          else {
            ready = l0f ? ~0u : (unsigned)__ballot(is_set(fl, epoch));
            if (slowj && __all(is_set(fl, epoch))) packedW();
          }
        }
        if (t == 12) { WAITV(10); OPAQUE_V(fl); }
        if (t == 13 && slowo && __builtin_expect(!__all(is_set(fl, epoch)), 0)) poll_all(rs, fwo, epoch, dead);
        if (t == NVAL - 2 || t == 5) ld8_asm(fl, rs, flo);
        if (t == 11) ld8_asm(fl, rs, fwo);
        if (t == 7 && l0f) csld();
        if (t == WT && l0f) {
          if (hw == 0) touch(mk_rsrc(p.ws, NREP * REP_STRIDE), min((g * 16 + j) * 9 + (lane < 9 ? lane : 0), (int)(NREP * REP_STRIDE / 128 - 1)) * 128, lds0 + L_SINK);
#pragma unroll
          for (int i = 0; i < 2; ++i) ld16in_asm(wo2[i], mk_rsrc(p.woA, 1024 * 2048), (lane >> 3) * 2048 + wo_swz(lane), (g * 64 + (j & 3) * 16 + i * 8) * 2048 + (jr + hw) * 128);
        }
        if constexpr (t <= NVAL) { if (__builtin_expect(bad, 0)) refetch(t + 1); }
      });
      auto stO = [&](int i) { dma4<4096>(mk_rsrc(pO, zO), vO, bO + wo_src(i, j) * (4 * pcO), pcO, lds0 + wo_slot(i) + hw * 1024); };
      auto clcheck = [&]() {
        OPAQUE_V(fl);
        if (__builtin_expect(!__all(is_set(fl, epoch)), 0)) poll_all(rs, clo, epoch, dead);
      };
      auto accinit = [&]() {
#pragma unroll
        for (int m = 0; m < 2; ++m)
#pragma unroll
          for (int pp = 0; pp < 2; ++pp) {
            auto r0_ = __builtin_amdgcn_permlane16_swap(xr[m][pp][0], xr[m][pp][2], false, false);
            auto r1_ = __builtin_amdgcn_permlane16_swap(xr[m][pp][1], xr[m][pp][3], false, false);
            acc[m][2 * pp] = f32x4{lo16(r0_[0]), hi16(r0_[0]), lo16(r1_[0]), hi16(r1_[0])};
            acc[m][2 * pp + 1] = f32x4{lo16(r0_[1]), hi16(r0_[1]), lo16(r1_[1]), hi16(r1_[1])};
          }
#pragma unroll
        for (int i = 0; i < 8; ++i) OPAQUE_V(acc[i >> 2][i & 3]);
      };
      WAITV(10);
      WAITL0;
      BAR;
      stO(0);
      if (lead) set_flags(rs, fl_line(FL_CLR + l, g), j, fl_line(FL_CLC + l, j), g, epoch, lane);
      if (isb) {
        stO(2); stO(3);
        WAITV(12);
        BAR;
        stO(1); stO(4); stO(5);
        __builtin_amdgcn_s_setprio(0);
        BAR;
        __builtin_amdgcn_s_sleep(BD);
        stO(7); stO(6);
        ld8_asm(fl, rs, clo);
        accinit();
        WAITV(0);
        clcheck();
      } else {
        u32x4 vb[8];
#pragma unroll
        for (int i = 0; i < 8; ++i) asm volatile("" : "=v"(vb[i]));
        stO(2); stO(3);
        ldres(14);
        WAITV(12);
        WAITL0;
        BAR;
        stO(1);
        ldres(15);
        if (l0f) {
          const bool wj = hw != 0;
          const char* bp = (const char*)(wj ? p.wqkvB : p.woB);
          unsigned rs_ = wj ? 6144 : 2048, sz = rs_ * 1024, ss = rs_ * 8, s2 = wj ? 16 * 6144 : 128;
          unsigned sb = g * 64 * rs_ + (wj ? (hw - 1) * 2048 + JOBC(j, hw - 1) * 128 : (j & 3) * (16 * 2048) + jr * 128);
          OPAQUE_S(bp); OPAQUE_S(rs_); OPAQUE_S(sz); OPAQUE_S(sb); OPAQUE_S(ss); OPAQUE_S(s2);
          const rsrc_t rb = mk_rsrc(bp, sz);
#pragma unroll
          for (int i = 0; i < 8; ++i) ld16in_asm(vb[i], rb, (lane >> 3) * rs_ + (lane & 7) * 16, sb + (i & 1) * ss + (i >> 1) * s2);
        }
        __builtin_amdgcn_s_setprio(0);
        KV_ADDR;
        bf16x8 sel = rowsum_sel(lane);
        unsigned h1 = (lane >> 3) * KVP_G + (lane & 7) * 16;
        unsigned pw = lds0 + SC_PHIK + (h * 128 + wm * 64 + 32 + fr) * 160 + fq * 8, kw = lds0 + L_PREV + hw * 2048 + lin;
        const unsigned wa = lds0 + L_WPHI + h * 2048 + (fq * 4 + (fr >> 2)) * 64 + (lane & 3) * 8;
        bf16x8 w0 = tr2(wa, wa + 1024), w1 = tr2(wa + 32, wa + 32 + 1024);
        float cq[4];
#pragma unroll
        for (int m = 0; m < 2; ++m) { cq[m] = lds_ld<float>(lds0 + L_RSTD + (16 + wm * 64 + 32 + m * 16 + fr) * 4) * 1.4426950408889634f; OPAQUE_V(cq[m]); }
        OPAQUE_V(sel); OPAQUE_V(h1); OPAQUE_V(pw); OPAQUE_V(kw); OPAQUE_V(w0); OPAQUE_V(w1);
        BAR;
        CLOBBER;
        {
          const f32x4 zero = {0.f, 0.f, 0.f, 0.f};
          f32x4 r[4][2];
#pragma unroll
          for (int m = 0; m < 2; ++m) {
            const bf16x8 f = lds_ld<bf16x8>(kw + m * 1024);
            r[m][0] = MFMA(w0, f, zero); r[m][1] = MFMA(w1, f, zero);
          }
          ld8_asm(fl, rs, clo);
          phik2(r, cq, pw);
        }
        WAITL0;
        BAR;
        kvpart(va, pa, pb, kvj + (unsigned)g * KVP_G, lane, wave);
        WAITV(2);
#pragma unroll
        for (int i = 0; i < 8; ++i) OPAQUE_V(vb[i]);
        clcheck();
        BAR;
        __builtin_amdgcn_s_setprio(3);
        stO(4); stO(5);
        const f32x4 zero = {0.f, 0.f, 0.f, 0.f};
        const unsigned s1 = kvj + (unsigned)((j ^ RX) - j) * KVP_J + g * 512 + hw * 128;
        __builtin_amdgcn_s_sleep(H1S);
        u32x4 v[2] = {ld16_sc1(rs, h1, s1), ld16_sc1(rs, h1, s1 + 8 * KVP_G)};
        for (unsigned spins = 0; !dead && __any(umax4v(umax4v(0u, v[0]), v[1]) == SENT); ++spins) {
          if (spins > SPIN_CAP) { dead = true; break; }
          CLOBBER;
          v[0] = ld16_sc1(rs, h1, s1); v[1] = ld16_sc1(rs, h1, s1 + 8 * KVP_G);
        }
        const f32x4 c = rowsum_fin(MFMA(sel, as_frag(v[1]), MFMA(sel, as_frag(v[0]), zero)));
        if ((lane & 40) == 0)
          st8_sc1(rs, fr * 16 + fq * 8, kvj + (unsigned)g * KVP_G + KVP_SUM + hw * 128, nosent2(c));
        CLOBBER;
        __builtin_amdgcn_s_setprio(0);
        BAR;
        SCHED;
        accinit();
        stO(6); stO(7);
        if (l0f) {
          if (hw) {
            job_colsum(rs, vb, 1, g, JOBC(j, hw - 1), hw - 1, lane);
            if (SLOWC(1, JOBC(j, hw - 1))) {
#pragma unroll
              for (int i = 0; i < 8; ++i) job_pack(rs, vb[i], i, 1, g, JOBC(j, hw - 1), hw - 1, lane);
            }
          } else {
#pragma unroll
            for (int i = 0; i < 8; ++i)
              if (SLOWC(3, jr + (i >> 1))) st16_sc1(rs, (lane >> 3) * 128 + wo_swz(lane), WS_WO + (unsigned)((16 + jr + (i >> 1)) * 131072 + g * 8192 + ((j & 3) * 2 + (i & 1)) * 1024), vb[i]);
          }
        }
        WAITV(0);
      }
    }
    {
      LOCALS;
      const int fr = lane & 15, fq = lane >> 4;
      const int kh = half;
      unsigned v0 = lane * 16 + hw * 4096 + kh * 2048, v1 = lane * 16 + hw * 4096 + (kh ^ 1) * 2048;
      const int r0 = (JROT(j) >> 1) + NJ / 2;
      const unsigned ob0 = ob_off(g, kh, l);
      const unsigned pko = myrep + fl_line(FL_PK, JOBC(j, fq - 1)) + fr * 8;
      u32x4 R[8][4];
      u32x2 pkf;
      auto issue_ = [&](int i) {
        const unsigned so = ob0 + (unsigned)((r0 + i) & 7) * (2 * TS);
        R[i][0] = ld16_sc1(rs, v0, so); R[i][1] = ld16_sc1(rs, v1, so);
        R[i][2] = ld16_sc1(rs, v0 + 1024, so); R[i][3] = ld16_sc1(rs, v1 + 1024, so);
      };
      auto issue = [&](int i) { issue_(i); SCHED; };
      constexpr int LA = LOOKAHEAD;
      __builtin_amdgcn_s_setprio(3);
      unsigned so0 = ob0 + (unsigned)(r0 & 7) * (2 * TS);
      OPAQUE_V(v0); OPAQUE_V(v1); OPAQUE_S(so0);
      BAR;
      SCHED;
      R[0][0] = ld16_sc1(rs, v0, so0); R[0][1] = ld16_sc1(rs, v1, so0);
      R[0][2] = ld16_sc1(rs, v0 + 1024, so0); R[0][3] = ld16_sc1(rs, v1 + 1024, so0);
      SCHED;
      __builtin_amdgcn_s_setprio(2);
      if (l == 0 && lead) set_flags(rs, fl_line(FL_PK, j), g, fl_line(FL_WO + 1, j), g, epoch, lane);

      auto vmax = [&](int i) { return umax4v(umax4v(umax4v(umax4v(0u, R[i][0]), R[i][1]), R[i][2]), R[i][3]); };
      auto bad = [&](int i) { return __any(umax4v(umax4v(umax4v(umax4v(0u, R[i][0]), R[i][1]), R[i][2]), R[i][3]) == SENT); };
      auto poll = [&](int i, int nfl) {
        for (unsigned spins = 0; !dead; ++spins) {
          if (spins > SPIN_CAP) { dead = true; break; }
          __builtin_amdgcn_s_sleep(2);
          CLOBBER;
          issue(i);
#pragma unroll
          for (int k = i + 1; k < i + nfl && k < 8; ++k)
            if (bad(k)) issue(k);
          WAITV(0);
#pragma unroll
          for (int k = i + 1; k < i + nfl && k < 8; ++k)
#pragma unroll
            for (int f = 0; f < 4; ++f) OPAQUE_V(R[k][f]);
          if (!bad(i)) break;
        }
      };
      unsigned bb[4];
      {
        const int kr = fq * 4 + (fr >> 2);
        const int gs = (kr >> 1) & 3;
#pragma unroll
        for (int c = 0; c < 4; ++c) bb[c] = lds0 + kh * 8192 + kr * 128 + ((c ^ gs) * 32) + (lane & 3) * 8;
      }
      bf16x8 bf[2][4];
      auto rdB = [&](int i, int hh) {
        if (hh == 0) {
#pragma unroll
          for (int c = 0; c < 4; ++c) { bb[c] += wo_slot(i) - (i ? wo_slot(i - 1) : 0u); asm("" : "+v"(bb[c])); }
        }
#pragma unroll
        for (int c = 0; c < 4; ++c) bf[hh][c] = tr2(bb[c] + hh * 4096, bb[c] + hh * 4096 + 2048);
      };
      auto mms = [&](int i, int hh) {
#pragma unroll
        for (int n = 0; n < 4; ++n)
#pragma unroll
          for (int m = 0; m < 2; ++m) acc[m][n] = MFMA(bf[hh][n], as_frag(R[i][hh * 2 + m]), acc[m][n]);
      };
      if (!half) {
#pragma unroll
        for (int i = 0; i < 8; ++i) acc[i >> 2][i & 3] = f32x4{0.f, 0.f, 0.f, 0.f};
      }
      const char* obase = (l == 3) ? (const char*)p.out : (const char*)p.ws;
      unsigned osize = (l == 3) ? 2048 * 2048 : WS_SPAN;
      const bool slown = SLOWC((l & 1) ^ 1, j);
      pW = slown ? p.ws : (const char*)(isb ? p.wqkvA : p.wqkvB);
      bW = slown ? WS_WP + (unsigned)((isb ? 0 : 16) + j) * (16 * 24576) : j * 128;
      kW = slown ? 24576 : 64 * 6144; zW = slown ? WS_SPAN : 1024 * 6144; pcW = slown ? 8192 : 2048; hW = slown ? 4096 : 32 * 6144;
      vA = hw * 1024 + lane * 16;
      vW = slown ? vA : (hw * 8 + (lane >> 3)) * 6144 + (((lane & 7) ^ ((((lane >> 4) & 1) | ((hw & 1) << 1)) << 1)) * 16);
      pA = p.ws; zA = WS_SPAN; bA = xb_off(g, l); kA = TS; pcA = 4096; kb = JROT(j) + KX; dj = (j - kb) & 15;
      OPAQUE_S(pW); OPAQUE_S(bW); OPAQUE_S(kW); OPAQUE_S(zW); OPAQUE_S(pcW); OPAQUE_S(hW); OPAQUE_V(vA); OPAQUE_V(vW); OPAQUE_S(bA);
      unsigned sq = bW + j * kW + kh * hW, dst = lds0 + L_R1 + kh * 4096 + hw * 1024;
      OPAQUE_S(obase); OPAQUE_S(osize); OPAQUE_S(sq); OPAQUE_S(dst);
      const rsrc_t ro = mk_rsrc(obase, osize);
      unsigned so[2];
      unsigned xw = lds0 + SC_XCH + wave * 4096 + lane * 16;
      unsigned xa = lds0 + SC_XCH + (wave ^ 4) * 4096 + lane * 16;
      unsigned sb0 = (l == 3) ? g * (128 * 2048) + j * 128 : xb_off(g, l) + j * TS;
      {
        const int row = (hw * 2 + kh) * 16 + fr, ch = (fq & 1) * 2 + (fq >> 1);
        const unsigned sw = (l == 3) ? 0u : (unsigned)((row >> 1) & 7), rb = (unsigned)row << ((l == 3) ? 11 : 7);
#pragma unroll
        for (int pp = 0; pp < 2; ++pp) { so[pp] = rb + (((ch + 4 * pp) ^ sw) * 16); OPAQUE_V(so[pp]); }
        OPAQUE_V(xa); OPAQUE_V(xw); OPAQUE_S(sb0);
      }
      SCHED;
#pragma unroll
      for (int i = 1; i < LA; ++i) issue(i);
      rdB(0, 0);
      unsigned mx = vmax(0);
#pragma unroll
      for (int i = 0; i < 8; ++i) {
        SCHED;
        if (__builtin_expect(__any(mx == SENT), 0)) poll(i, LA);
        if (i == W1S - 2) { pkf = ld8_sc1(rs, pko, 0); SCHED; }
        if (i == W1S) {
          OPAQUE_V(pkf);
          if (l == 0 && lead && !__all(is_set(pkf, epoch))) poll_all(rs, pko, epoch, dead);
          BAR;
        }
        if (i == 7 && l < 3) dma3<8192>(mk_rsrc(pW, zW), vW, sq, pcW, dst);
        if (i < 4 && !isb) __builtin_amdgcn_s_sleep(PACE);
        if (i == 2) __builtin_amdgcn_s_setprio(1);
        if (i == 5) __builtin_amdgcn_s_setprio(0);
        SCHED;
        rdB(i, 1);
        if (i == 7) break;
        if (i + LA < 8) issue_(i + LA);
        mms(i, 0);
#pragma unroll
        for (int k = 0; k < 4; ++k) { SGB(0x008, 1); SGB(0x100, 1); SGB(0x020, 1); SGB(0x008, 1); SGB(0x100, 1); }
        SCHED;
        rdB(i + 1, 0); mx = vmax(i + 1);
        mms(i, 1);
#pragma unroll
        for (int k = 0; k < 4; ++k) { SGB(0x008, 1); SGB(0x100, 2); }
#pragma unroll
        for (int k = 4; k < 8; ++k) { SGB(0x008, 1); SGB(0x002, 8 / (8 - 4)); }
      }
#pragma unroll
      for (int n = 0; n < 4; ++n) acc[1][n] = MFMA(bf[0][n], as_frag(R[7][1]), acc[1][n]);
#pragma unroll
      for (int n = 0; n < 4; ++n) acc[0][n] = MFMA(bf[0][n], as_frag(R[7][0]), acc[0][n]);
#pragma unroll
      for (int k = 0; k < 8; ++k) { SGB(0x008, 1); SGB(0x100, 1); }
      SCHED;
#pragma unroll
      for (int n = 0; n < 4; ++n) acc[1][n] = MFMA(bf[1][n], as_frag(R[7][3]), acc[1][n]);
#pragma unroll
      for (int n = 0; n < 4; ++n) acc[0][n] = MFMA(bf[1][n], as_frag(R[7][2]), acc[0][n]);
#pragma unroll
      for (int n = 0; n < 4; ++n) lds_st<f32x4>(xw + n * 1024, acc[1][n]);
      SGB(0x008, 4);
#pragma unroll
      for (int k = 0; k < 4; ++k) { SGB(0x008, 1); SGB(0x200, 1); }
      SCHED;
      WAITL0;
      BAR;
      SCHED;
      f32x4 pa[4];
#pragma unroll
      for (int n = 0; n < 4; ++n) pa[n] = lds_ld<f32x4>(xa + n * 1024);
      SCHED;
      u32x4 o[2];
#pragma unroll
      for (int pp = 0; pp < 2; ++pp) {
        const f32x4 a = acc[0][2 * pp] + pa[2 * pp], b = acc[0][2 * pp + 1] + pa[2 * pp + 1];
        auto r0_ = __builtin_amdgcn_permlane16_swap(pk2(a[0], a[1]), pk2(b[0], b[1]), false, false);
        auto r1_ = __builtin_amdgcn_permlane16_swap(pk2(a[2], a[3]), pk2(b[2], b[3]), false, false);
        o[pp] = nosent(u32x4{r0_[0], r1_[0], r0_[1], r1_[1]});
        st16_sc1(ro, so[pp], sb0, o[pp]);
      }
      if (l < 3) {
        lds_st<u32x4>(lds0 + L_R2 + so[0], o[0]);
        lds_st<u32x4>(lds0 + L_R2 + so[1], o[1]);
        const f32x4 zero = {0.f, 0.f, 0.f, 0.f};
        const bf16x8 ones = {0x3f80, 0x3f80, 0x3f80, 0x3f80, 0x3f80, 0x3f80, 0x3f80, 0x3f80};
        const f32x4 a = MFMA(ones, as_frag(o[1]), MFMA(ones, as_frag(o[0]), zero));
        const f32x4 b = MFMA(as_frag(o[1]), as_frag(o[1]), MFMA(as_frag(o[0]), as_frag(o[0]), zero));
        const float q = (fr & 2) ? ((fr & 1) ? b[3] : b[2]) : ((fr & 1) ? b[1] : b[0]);
        if (fq == (fr >> 2)) st8_sc1(rs, ((hw * 2 + kh) * 16 + fr) * 8, st_off(g, l + 1) + j * 1024, u32x2{__float_as_uint(a[0]), __float_as_uint(q)});
      } else if (lead && blockIdx.x == 0 && lane == 0) {
        st8_sc1(rs, WS_EPOCH, 0, epoch);
      }
    }
  }
  asm volatile(".Lpf_end:");
}

extern "C" void kernel_launch(void* const* d_in, const int* in_sizes, int n_in, void* d_out, int out_size,
                              void* d_ws, size_t ws_size, hipStream_t stream) {
  Params p;
  p.x = (const u16*)d_in[0];
  p.wqkvA = (const u16*)d_in[1];
  p.wphi = (const u16*)d_in[2];
  p.woA = (const u16*)d_in[3];
  p.wqkvB = (const u16*)d_in[5];
  p.woB = (const u16*)d_in[6];
  p.out = (u16*)d_out;
  p.ws = (char*)d_ws + ((ws_size - WS_SPAN) & ~(size_t)0xffff);
  (void)hipFuncSetAttribute((const void*)mega_kernel, hipFuncAttributeMaxDynamicSharedMemorySize, L_TOTAL);
  mega_kernel<<<256, 512, L_TOTAL, stream>>>(p);
}
```
